```python
import torch

def _softmax(x, axis=-1):
    return torch.softmax(x, dim=axis)

def _gelu(x):
    return torch.nn.functional.gelu(x, approximate='tanh')

def _silu(x):
    return torch.nn.functional.silu(x)

def _sigmoid(x):
    return torch.sigmoid(x)

def _layernorm(x, axis=-1, eps=1e-05):
    m = x.mean(dim=axis, keepdim=True)
    v = x.var(dim=axis, keepdim=True, unbiased=False)
    return (x - m) / torch.sqrt(v + eps)

def _scatter_add(tgt, idx, src, axis=0):
    return tgt.clone().index_add_(axis, idx, src)

def _segment_sum(src, seg, n):
    out = torch.zeros((n,) + src.shape[1:], dtype=src.dtype)
    return out.index_add_(0, seg, src)

def _pad(x, pw):
    flat = tuple((v for pair in reversed(pw) for v in pair))
    return torch.nn.functional.pad(x, flat)
kernel_name = 'parallel_sum__memory_aug_recurrent_mem_tr__glu_gated_reglu__prompt'

def setup_inputs():

    _w = lambda a: (lambda t: t.to(torch.bfloat16) if t.is_floating_point() else t)(torch.as_tensor(a))

    def _A_setup():
        B, S, Dm = (1, 2048, 512)
        H, Dh = (32, 128)
        M, seg = (8, 32)
        seg = min(seg, S)
        S = S // seg * seg
        return {'x': _w((lambda *s: torch.randn(*s, dtype=torch.bfloat16) * 0.05)(B, S, Dm)), 'mem0': _w((lambda *s: torch.randn(*s, dtype=torch.bfloat16) * 0.2)(B, M, Dm)), 'Wq': _w((lambda *s: torch.randn(*s, dtype=torch.bfloat16) * 1.6)(Dm, H, Dh) * Dm ** (-0.5)), 'Wk': _w((lambda *s: torch.randn(*s, dtype=torch.bfloat16) * 1.6)(Dm, H, Dh) * Dm ** (-0.5)), 'Wv': _w((lambda *s: torch.randn(*s, dtype=torch.bfloat16) * 0.2)(Dm, H, Dh) * Dm ** (-0.5)), 'Wo': _w((lambda *s: torch.randn(*s, dtype=torch.bfloat16) * 0.2)(H, Dh, Dm) * (H * Dh) ** (-0.5)), 'seg_len': seg}
    _A_in = _A_setup()

    def _B_setup():
        B, S, D, F = (1, 2048, 512, 1792)
        return {'x': _w((lambda *s: torch.randn(*s, dtype=torch.bfloat16) * 0.05)(B, S, D)), 'W_gate': _w((lambda *s: torch.randn(*s, dtype=torch.bfloat16) * 0.2)(D, F) * (2.0 / D) ** 0.5), 'W_up': _w((lambda *s: torch.randn(*s, dtype=torch.bfloat16) * 0.2)(D, F) * (2.0 / D) ** 0.5), 'W_down': _w((lambda *s: torch.randn(*s, dtype=torch.bfloat16) * 0.2)(F, D) * (1.0 / F) ** 0.5)}
    _B_in = _B_setup()
    return {'A_x': _A_in['x'], 'A_mem0': _A_in['mem0'], 'A_Wq': _A_in['Wq'], 'A_Wk': _A_in['Wk'], 'A_Wv': _A_in['Wv'], 'A_Wo': _A_in['Wo'], 'A_seg_len': _A_in['seg_len'], 'B_x': _B_in['x'], 'B_W_gate': _B_in['W_gate'], 'B_W_up': _B_in['W_up'], 'B_W_down': _B_in['W_down']}

def reference(A_x, A_mem0, A_Wq, A_Wk, A_Wv, A_Wo, A_seg_len, B_x, B_W_gate, B_W_up, B_W_down):

    def _A_ref(x, mem0, Wq, Wk, Wv, Wo, seg_len):
        B, S, Dm = x.shape
        H, Dh = (Wq.shape[1], Wq.shape[2])
        M = mem0.shape[1]
        scale = Dh ** (-0.5)
        n_seg = S // seg_len
        L = M + seg_len
        idx = torch.arange(L)
        mask = (idx[:, None] < M) | (idx[:, None] >= idx[None, :])
        mem = mem0
        outs = []
        for s in range(n_seg):
            xs = x[:, s * seg_len:(s + 1) * seg_len, :].clone()
            h = torch.concatenate([mem, xs], axis=1)
            q = torch.einsum('btd,dhe->bhte', h, Wq)
            k = torch.einsum('btd,dhe->bhte', h, Wk)
            v = torch.einsum('btd,dhe->bhte', h, Wv)
            scores = torch.einsum('bhqe,bhke->bhqk', q, k) * scale
            scores = torch.where(mask[None, None], scores, -1000000000.0)
            p = _softmax(scores, axis=-1)
            o = torch.einsum('bhqk,bhke->bhqe', p, v)
            o = torch.einsum('bhte,hed->btd', o, Wo)
            mem = o[:, :M, :].clone()
            outs.append(o[:, M:, :])
        return torch.concatenate(outs, axis=1)

    def _B_ref(x, W_gate, W_up, W_down):
        gate = torch.einsum('bsd,df->bsf', x, W_gate)
        up = torch.einsum('bsd,df->bsf', x, W_up)
        gate = gate * (lambda x, dt: x.to(dt))(gate > 0, gate.dtype)
        h = gate * up
        return torch.einsum('bsf,fd->bsd', h, W_down)
    x = A_x
    xn = _layernorm(x, axis=-1)
    return x + _A_ref(xn, A_mem0, A_Wq, A_Wk, A_Wv, A_Wo, A_seg_len) + _B_ref(xn, B_W_gate, B_W_up, B_W_down)
```

```cpp
#include <hip/hip_runtime.h>
#include <hip/hip_bf16.h>
#include <stdint.h>
#include <stddef.h>

typedef unsigned short u16;
typedef unsigned v4u __attribute__((ext_vector_type(4)));

struct Params {
    const char* Wq; const char* Wk; const char* Wv; const char* Wo;
    char* Kf; char* Vf; char* memb; char* part;
    unsigned* ctl;
    int D1, D2, up1, up2;
    int dn1, dn2;
    const char* x; const char* mem0; char* xn;
    char* Qf; char* PO; unsigned* flg; unsigned* cnt; unsigned* prog;
    const char* Wg; const char* Wu; const char* Wd;
    char* h; char* Bo; char* out;
    unsigned* f1c; unsigned* f2f;
    int roles; int pad0;
    unsigned* kvp;
    unsigned* lat;
    int halfM, halfP;
};

__device__ __forceinline__ float bf2f(u16 v) { return __uint_as_float((unsigned)v << 16); }
__device__ __forceinline__ u16 f2bf(float f) { __hip_bfloat16 b = __float2bfloat16(f); return *(u16*)&b; }
__device__ __forceinline__ float rbf(float f) { return bf2f(f2bf(f)); }

typedef short s16x4 __attribute__((ext_vector_type(4)));
typedef short s16x8 __attribute__((ext_vector_type(8)));
typedef float f32x16 __attribute__((ext_vector_type(16)));
typedef float f32x4 __attribute__((ext_vector_type(4)));
typedef unsigned v2u __attribute__((ext_vector_type(2)));
#define LAS __attribute__((address_space(3)))

__device__ __forceinline__ unsigned pk2(float lo, float hi) { unsigned r; asm("v_cvt_pk_bf16_f32 %0, %1, %2" : "=v"(r) : "v"(lo), "v"(hi)); return r; }
__device__ __forceinline__ void dma16(const void* g, unsigned ldsaddr) {
    asm volatile("s_mov_b32 m0, %1\n\ts_nop 0\n\tglobal_load_lds_dwordx4 %0, off" :: "v"(g), "s"(ldsaddr) : "memory");
}
__device__ __forceinline__ void st16_sc1(void* p, v4u v) { asm volatile("global_store_dwordx4 %0, %1, off sc1\n\ts_nop 1" :: "v"(p), "v"(v) : "memory"); }
__device__ __forceinline__ void st8_sc1(void* p, v2u v) { asm volatile("global_store_dwordx2 %0, %1, off sc1" :: "v"(p), "v"(v) : "memory"); }

enum { EPI_QKV = 0, EPI_F1 = 1, EPI_F2 = 2 };
template <class T> __device__ __forceinline__ T* uni(T* p) {
    const unsigned long long v = (unsigned long long)p;
    const unsigned lo = __builtin_amdgcn_readfirstlane((unsigned)v), hi = __builtin_amdgcn_readfirstlane((unsigned)(v >> 32));
    return (T*)(((unsigned long long)hi << 32) | lo);
}

struct GTask {
    const char* W0;
    ptrdiff_t dW;
    int ldw;
    const char* X;
    int ldx;
    int xkstep;
    int nk;
    char* out;
    int tok0, f0;
    int far;
};

__device__ __forceinline__ void kloop_QKV44(f32x16 (&acc)[4][4], const char* W, const char* X, const char* Wn, const char* Xn, unsigned ctl, int w, unsigned vW, unsigned vX, unsigned aw, unsigned ax, unsigned* flag, ptrdiff_t dW) {
    asm volatile(
        "s_mov_b64 s[60:61], %16\n"
        "s_mov_b64 s[62:63], %17\n"
        "s_mov_b64 s[64:65], %18\n"
        "s_mov_b64 s[66:67], %19\n"
        "s_mov_b64 s[84:85], %26\n"
        "s_and_b32 s70, %20, 0xff\n"
        "s_bfe_u32 s76, %20, 0x2000a\n"
        "s_lshl_b32 s76, s76, 15\n"
        "s_bfe_u32 s77, %20, 0x80010\n"
        "s_mul_i32 s72, %21, 4096\n"
        "s_mul_i32 s73, %21, 4096\n"
        "s_add_u32 s73, s73, 16384\n"
        "v_mov_b32 v200, %22\n"
        "v_mov_b32 v201, %23\n"
        "s_bitcmp1_b32 %20, 8\n"
        "s_cbranch_scc1 QKV44_PRE_%=\n"
        "s_waitcnt vmcnt(0)\n"
        "s_barrier\n"
        "s_mov_b32 s78, s76\n"
        "s_mov_b32 s71, 0\n"
        "s_cmp_lt_u32 s71, s70\n"
        "s_cselect_b32 s80, s60, s64\n"
        "s_cselect_b32 s81, s61, s65\n"
        "s_cselect_b32 s82, s62, s66\n"
        "s_cselect_b32 s83, s63, s67\n"
        "s_cselect_b32 s90, 0, s70\n"
        "s_sub_u32 s90, s71, s90\n"
        "s_mul_i32 s91, s90, 262144\n"
        "s_add_u32 s80, s80, s91\n"
        "s_addc_u32 s81, s81, 0\n"
        "s_lshl_b32 s91, s90, 6\n"
        "s_add_u32 s82, s82, s91\n"
        "s_addc_u32 s83, s83, 0\n"
        "s_add_u32 s74, s80, 0\n"
        "s_addc_u32 s75, s81, 0\n"
        "s_add_u32 m0, s78, s72\n"
        "s_add_u32 m0, m0, 0\n"
        "s_nop 0\n"
        "global_load_lds_dwordx4 v200, s[74:75]\n"
        "s_add_u32 s74, s82, 0\n"
        "s_addc_u32 s75, s83, 0\n"
        "s_add_u32 m0, s78, s73\n"
        "s_add_u32 m0, m0, 0\n"
        "s_nop 0\n"
        "global_load_lds_dwordx4 v201, s[74:75]\n"
        "s_add_u32 s74, s80, 16384\n"
        "s_addc_u32 s75, s81, 0\n"
        "s_add_u32 m0, s78, s72\n"
        "s_add_u32 m0, m0, 1024\n"
        "s_nop 0\n"
        "global_load_lds_dwordx4 v200, s[74:75]\n"
        "s_add_u32 s74, s82, 16384\n"
        "s_addc_u32 s75, s83, 0\n"
        "s_add_u32 m0, s78, s73\n"
        "s_add_u32 m0, m0, 1024\n"
        "s_nop 0\n"
        "global_load_lds_dwordx4 v201, s[74:75]\n"
        "s_add_u32 s74, s80, 32768\n"
        "s_addc_u32 s75, s81, 0\n"
        "s_add_u32 m0, s78, s72\n"
        "s_add_u32 m0, m0, 2048\n"
        "s_nop 0\n"
        "global_load_lds_dwordx4 v200, s[74:75]\n"
        "s_add_u32 s74, s82, 32768\n"
        "s_addc_u32 s75, s83, 0\n"
        "s_add_u32 m0, s78, s73\n"
        "s_add_u32 m0, m0, 2048\n"
        "s_nop 0\n"
        "global_load_lds_dwordx4 v201, s[74:75]\n"
        "s_add_u32 s74, s80, 49152\n"
        "s_addc_u32 s75, s81, 0\n"
        "s_add_u32 m0, s78, s72\n"
        "s_add_u32 m0, m0, 3072\n"
        "s_nop 0\n"
        "global_load_lds_dwordx4 v200, s[74:75]\n"
        "s_add_u32 s74, s82, 49152\n"
        "s_addc_u32 s75, s83, 0\n"
        "s_add_u32 m0, s78, s73\n"
        "s_add_u32 m0, m0, 3072\n"
        "s_nop 0\n"
        "global_load_lds_dwordx4 v201, s[74:75]\n"
        "s_add_u32 s78, s78, 32768\n"
        "s_and_b32 s78, s78, 131071\n"
        "s_mov_b32 s71, 1\n"
        "s_cmp_lt_u32 s71, s70\n"
        "s_cselect_b32 s80, s60, s64\n"
        "s_cselect_b32 s81, s61, s65\n"
        "s_cselect_b32 s82, s62, s66\n"
        "s_cselect_b32 s83, s63, s67\n"
        "s_cselect_b32 s90, 0, s70\n"
        "s_sub_u32 s90, s71, s90\n"
        "s_mul_i32 s91, s90, 262144\n"
        "s_add_u32 s80, s80, s91\n"
        "s_addc_u32 s81, s81, 0\n"
        "s_lshl_b32 s91, s90, 6\n"
        "s_add_u32 s82, s82, s91\n"
        "s_addc_u32 s83, s83, 0\n"
        "s_add_u32 s74, s80, 0\n"
        "s_addc_u32 s75, s81, 0\n"
        "s_add_u32 m0, s78, s72\n"
        "s_add_u32 m0, m0, 0\n"
        "s_nop 0\n"
        "global_load_lds_dwordx4 v200, s[74:75]\n"
        "s_add_u32 s74, s82, 0\n"
        "s_addc_u32 s75, s83, 0\n"
        "s_add_u32 m0, s78, s73\n"
        "s_add_u32 m0, m0, 0\n"
        "s_nop 0\n"
        "global_load_lds_dwordx4 v201, s[74:75]\n"
        "s_add_u32 s74, s80, 16384\n"
        "s_addc_u32 s75, s81, 0\n"
        "s_add_u32 m0, s78, s72\n"
        "s_add_u32 m0, m0, 1024\n"
        "s_nop 0\n"
        "global_load_lds_dwordx4 v200, s[74:75]\n"
        "s_add_u32 s74, s82, 16384\n"
        "s_addc_u32 s75, s83, 0\n"
        "s_add_u32 m0, s78, s73\n"
        "s_add_u32 m0, m0, 1024\n"
        "s_nop 0\n"
        "global_load_lds_dwordx4 v201, s[74:75]\n"
        "s_add_u32 s74, s80, 32768\n"
        "s_addc_u32 s75, s81, 0\n"
        "s_add_u32 m0, s78, s72\n"
        "s_add_u32 m0, m0, 2048\n"
        "s_nop 0\n"
        "global_load_lds_dwordx4 v200, s[74:75]\n"
        "s_add_u32 s74, s82, 32768\n"
        "s_addc_u32 s75, s83, 0\n"
        "s_add_u32 m0, s78, s73\n"
        "s_add_u32 m0, m0, 2048\n"
        "s_nop 0\n"
        "global_load_lds_dwordx4 v201, s[74:75]\n"
        "s_add_u32 s74, s80, 49152\n"
        "s_addc_u32 s75, s81, 0\n"
        "s_add_u32 m0, s78, s72\n"
        "s_add_u32 m0, m0, 3072\n"
        "s_nop 0\n"
        "global_load_lds_dwordx4 v200, s[74:75]\n"
        "s_add_u32 s74, s82, 49152\n"
        "s_addc_u32 s75, s83, 0\n"
        "s_add_u32 m0, s78, s73\n"
        "s_add_u32 m0, m0, 3072\n"
        "s_nop 0\n"
        "global_load_lds_dwordx4 v201, s[74:75]\n"
        "s_add_u32 s78, s78, 32768\n"
        "s_and_b32 s78, s78, 131071\n"
        "s_mov_b32 s71, 2\n"
        "s_cmp_lt_u32 s71, s70\n"
        "s_cselect_b32 s80, s60, s64\n"
        "s_cselect_b32 s81, s61, s65\n"
        "s_cselect_b32 s82, s62, s66\n"
        "s_cselect_b32 s83, s63, s67\n"
        "s_cselect_b32 s90, 0, s70\n"
        "s_sub_u32 s90, s71, s90\n"
        "s_mul_i32 s91, s90, 262144\n"
        "s_add_u32 s80, s80, s91\n"
        "s_addc_u32 s81, s81, 0\n"
        "s_lshl_b32 s91, s90, 6\n"
        "s_add_u32 s82, s82, s91\n"
        "s_addc_u32 s83, s83, 0\n"
        "s_add_u32 s74, s80, 0\n"
        "s_addc_u32 s75, s81, 0\n"
        "s_add_u32 m0, s78, s72\n"
        "s_add_u32 m0, m0, 0\n"
        "s_nop 0\n"
        "global_load_lds_dwordx4 v200, s[74:75]\n"
        "s_add_u32 s74, s82, 0\n"
        "s_addc_u32 s75, s83, 0\n"
        "s_add_u32 m0, s78, s73\n"
        "s_add_u32 m0, m0, 0\n"
        "s_nop 0\n"
        "global_load_lds_dwordx4 v201, s[74:75]\n"
        "s_add_u32 s74, s80, 16384\n"
        "s_addc_u32 s75, s81, 0\n"
        "s_add_u32 m0, s78, s72\n"
        "s_add_u32 m0, m0, 1024\n"
        "s_nop 0\n"
        "global_load_lds_dwordx4 v200, s[74:75]\n"
        "s_add_u32 s74, s82, 16384\n"
        "s_addc_u32 s75, s83, 0\n"
        "s_add_u32 m0, s78, s73\n"
        "s_add_u32 m0, m0, 1024\n"
        "s_nop 0\n"
        "global_load_lds_dwordx4 v201, s[74:75]\n"
        "s_add_u32 s74, s80, 32768\n"
        "s_addc_u32 s75, s81, 0\n"
        "s_add_u32 m0, s78, s72\n"
        "s_add_u32 m0, m0, 2048\n"
        "s_nop 0\n"
        "global_load_lds_dwordx4 v200, s[74:75]\n"
        "s_add_u32 s74, s82, 32768\n"
        "s_addc_u32 s75, s83, 0\n"
        "s_add_u32 m0, s78, s73\n"
        "s_add_u32 m0, m0, 2048\n"
        "s_nop 0\n"
        "global_load_lds_dwordx4 v201, s[74:75]\n"
        "s_add_u32 s74, s80, 49152\n"
        "s_addc_u32 s75, s81, 0\n"
        "s_add_u32 m0, s78, s72\n"
        "s_add_u32 m0, m0, 3072\n"
        "s_nop 0\n"
        "global_load_lds_dwordx4 v200, s[74:75]\n"
        "s_add_u32 s74, s82, 49152\n"
        "s_addc_u32 s75, s83, 0\n"
        "s_add_u32 m0, s78, s73\n"
        "s_add_u32 m0, m0, 3072\n"
        "s_nop 0\n"
        "global_load_lds_dwordx4 v201, s[74:75]\n"
        "s_add_u32 s78, s78, 32768\n"
        "s_and_b32 s78, s78, 131071\n"
        "s_mov_b32 s71, 3\n"
        "s_cmp_lt_u32 s71, s70\n"
        "s_cselect_b32 s80, s60, s64\n"
        "s_cselect_b32 s81, s61, s65\n"
        "s_cselect_b32 s82, s62, s66\n"
        "s_cselect_b32 s83, s63, s67\n"
        "s_cselect_b32 s90, 0, s70\n"
        "s_sub_u32 s90, s71, s90\n"
        "s_mul_i32 s91, s90, 262144\n"
        "s_add_u32 s80, s80, s91\n"
        "s_addc_u32 s81, s81, 0\n"
        "s_lshl_b32 s91, s90, 6\n"
        "s_add_u32 s82, s82, s91\n"
        "s_addc_u32 s83, s83, 0\n"
        "s_add_u32 s74, s80, 0\n"
        "s_addc_u32 s75, s81, 0\n"
        "s_add_u32 m0, s78, s72\n"
        "s_add_u32 m0, m0, 0\n"
        "s_nop 0\n"
        "global_load_lds_dwordx4 v200, s[74:75]\n"
        "s_add_u32 s74, s82, 0\n"
        "s_addc_u32 s75, s83, 0\n"
        "s_add_u32 m0, s78, s73\n"
        "s_add_u32 m0, m0, 0\n"
        "s_nop 0\n"
        "global_load_lds_dwordx4 v201, s[74:75]\n"
        "s_add_u32 s74, s80, 16384\n"
        "s_addc_u32 s75, s81, 0\n"
        "s_add_u32 m0, s78, s72\n"
        "s_add_u32 m0, m0, 1024\n"
        "s_nop 0\n"
        "global_load_lds_dwordx4 v200, s[74:75]\n"
        "s_add_u32 s74, s82, 16384\n"
        "s_addc_u32 s75, s83, 0\n"
        "s_add_u32 m0, s78, s73\n"
        "s_add_u32 m0, m0, 1024\n"
        "s_nop 0\n"
        "global_load_lds_dwordx4 v201, s[74:75]\n"
        "s_add_u32 s74, s80, 32768\n"
        "s_addc_u32 s75, s81, 0\n"
        "s_add_u32 m0, s78, s72\n"
        "s_add_u32 m0, m0, 2048\n"
        "s_nop 0\n"
        "global_load_lds_dwordx4 v200, s[74:75]\n"
        "s_add_u32 s74, s82, 32768\n"
        "s_addc_u32 s75, s83, 0\n"
        "s_add_u32 m0, s78, s73\n"
        "s_add_u32 m0, m0, 2048\n"
        "s_nop 0\n"
        "global_load_lds_dwordx4 v201, s[74:75]\n"
        "s_add_u32 s74, s80, 49152\n"
        "s_addc_u32 s75, s81, 0\n"
        "s_add_u32 m0, s78, s72\n"
        "s_add_u32 m0, m0, 3072\n"
        "s_nop 0\n"
        "global_load_lds_dwordx4 v200, s[74:75]\n"
        "s_add_u32 s74, s82, 49152\n"
        "s_addc_u32 s75, s83, 0\n"
        "s_add_u32 m0, s78, s73\n"
        "s_add_u32 m0, m0, 3072\n"
        "s_nop 0\n"
        "global_load_lds_dwordx4 v201, s[74:75]\n"
        "s_add_u32 s78, s78, 32768\n"
        "s_and_b32 s78, s78, 131071\n"
        "s_waitcnt vmcnt(24)\n"
        "s_mov_b32 s77, 0\n"
        "s_branch QKV44_GO_%=\n"
        "QKV44_PRE_%=:\n"
        "s_cmp_eq_u32 s77, 8\n"
        "s_cbranch_scc0 QKV44_PN8_%=\n"
        "s_waitcnt vmcnt(32)\n"
        "s_branch QKV44_GO_%=\n"
        "QKV44_PN8_%=:\n"
        "s_cmp_eq_u32 s77, 16\n"
        "s_cbranch_scc0 QKV44_PN16_%=\n"
        "s_waitcnt vmcnt(40)\n"
        "s_branch QKV44_GO_%=\n"
        "QKV44_PN16_%=:\n"
        "s_cmp_eq_u32 s77, 32\n"
        "s_cbranch_scc0 QKV44_PN32_%=\n"
        "s_waitcnt vmcnt(56)\n"
        "s_branch QKV44_GO_%=\n"
        "QKV44_PN32_%=:\n"
        "s_waitcnt vmcnt(24)\n"
        "QKV44_GO_%=:\n"
        "s_barrier\n"
        "v_add_u32 v192, s76, %24\n"
        "v_add_u32 v196, s76, %25\n"
        "v_xor_b32 v197, 32, v196\n"
        "v_xor_b32 v193, 64, v192\n"
        "v_xor_b32 v194, 128, v192\n"
        "v_xor_b32 v195, 192, v192\n"
        "ds_read_b64_tr_b16 v[128:129], v192 offset:0\n"
        "ds_read_b64_tr_b16 v[130:131], v192 offset:1024\n"
        "ds_read_b64_tr_b16 v[132:133], v193 offset:0\n"
        "ds_read_b64_tr_b16 v[134:135], v193 offset:1024\n"
        "ds_read_b64_tr_b16 v[136:137], v194 offset:0\n"
        "ds_read_b64_tr_b16 v[138:139], v194 offset:1024\n"
        "ds_read_b64_tr_b16 v[140:141], v195 offset:0\n"
        "ds_read_b64_tr_b16 v[142:143], v195 offset:1024\n"
        "ds_read_b128 v[160:163], v196 offset:16384\n"
        "ds_read_b128 v[164:167], v196 offset:18432\n"
        "ds_read_b128 v[168:171], v196 offset:20480\n"
        "ds_read_b128 v[172:175], v196 offset:22528\n"
        "s_mov_b32 s71, 0\n"
        "s_waitcnt lgkmcnt(0)\n"
        "v_mfma_f32_32x32x16_bf16 %0, v[128:131], v[160:163], 0\n"
        "ds_read_b64_tr_b16 v[144:145], v192 offset:8192\n"
        "v_mfma_f32_32x32x16_bf16 %1, v[128:131], v[164:167], 0\n"
        "ds_read_b64_tr_b16 v[146:147], v192 offset:9216\n"
        "v_mfma_f32_32x32x16_bf16 %2, v[128:131], v[168:171], 0\n"
        "ds_read_b64_tr_b16 v[148:149], v193 offset:8192\n"
        "v_mfma_f32_32x32x16_bf16 %3, v[128:131], v[172:175], 0\n"
        "ds_read_b64_tr_b16 v[150:151], v193 offset:9216\n"
        "v_mfma_f32_32x32x16_bf16 %4, v[132:135], v[160:163], 0\n"
        "ds_read_b64_tr_b16 v[152:153], v194 offset:8192\n"
        "v_mfma_f32_32x32x16_bf16 %5, v[132:135], v[164:167], 0\n"
        "ds_read_b64_tr_b16 v[154:155], v194 offset:9216\n"
        "v_mfma_f32_32x32x16_bf16 %6, v[132:135], v[168:171], 0\n"
        "ds_read_b64_tr_b16 v[156:157], v195 offset:8192\n"
        "v_mfma_f32_32x32x16_bf16 %7, v[132:135], v[172:175], 0\n"
        "ds_read_b64_tr_b16 v[158:159], v195 offset:9216\n"
        "v_mfma_f32_32x32x16_bf16 %8, v[136:139], v[160:163], 0\n"
        "ds_read_b128 v[176:179], v197 offset:16384\n"
        "v_mfma_f32_32x32x16_bf16 %9, v[136:139], v[164:167], 0\n"
        "ds_read_b128 v[180:183], v197 offset:18432\n"
        "v_mfma_f32_32x32x16_bf16 %10, v[136:139], v[168:171], 0\n"
        "ds_read_b128 v[184:187], v197 offset:20480\n"
        "v_mfma_f32_32x32x16_bf16 %11, v[136:139], v[172:175], 0\n"
        "ds_read_b128 v[188:191], v197 offset:22528\n"
        "v_mfma_f32_32x32x16_bf16 %12, v[140:143], v[160:163], 0\n"
        "s_add_u32 s92, s71, 4\n"
        "s_cmp_lt_u32 s92, s70\n"
        "s_cselect_b32 s86, s60, s64\n"
        "s_cselect_b32 s87, s61, s65\n"
        "s_cselect_b32 s88, s62, s66\n"
        "s_cselect_b32 s89, s63, s67\n"
        "s_cselect_b32 s90, 0, s70\n"
        "s_sub_u32 s90, s92, s90\n"
        "s_mul_i32 s91, s90, 262144\n"
        "s_add_u32 s86, s86, s91\n"
        "s_addc_u32 s87, s87, 0\n"
        "s_lshl_b32 s91, s90, 6\n"
        "s_add_u32 s88, s88, s91\n"
        "s_addc_u32 s89, s89, 0\n"
        "v_mfma_f32_32x32x16_bf16 %13, v[140:143], v[164:167], 0\n"
        "v_mfma_f32_32x32x16_bf16 %14, v[140:143], v[168:171], 0\n"
        "v_mfma_f32_32x32x16_bf16 %15, v[140:143], v[172:175], 0\n"
        "s_branch QKV44_MID_%=\n"
        "QKV44_LOOP_%=:\n"
        "s_waitcnt lgkmcnt(0)\n"
        "v_mfma_f32_32x32x16_bf16 %0, v[128:131], v[160:163], %0\n"
        "s_add_u32 s74, s80, 32768\n"
        "s_addc_u32 s75, s81, 0\n"
        "s_add_u32 m0, s78, s72\n"
        "s_add_u32 m0, m0, 2048\n"
        "s_nop 0\n"
        "global_load_lds_dwordx4 v200, s[74:75]\n"
        "ds_read_b64_tr_b16 v[144:145], v192 offset:8192\n"
        "v_mfma_f32_32x32x16_bf16 %1, v[128:131], v[164:167], %1\n"
        "ds_read_b64_tr_b16 v[146:147], v192 offset:9216\n"
        "v_mfma_f32_32x32x16_bf16 %2, v[128:131], v[168:171], %2\n"
        "ds_read_b64_tr_b16 v[148:149], v193 offset:8192\n"
        "v_mfma_f32_32x32x16_bf16 %3, v[128:131], v[172:175], %3\n"
        "ds_read_b64_tr_b16 v[150:151], v193 offset:9216\n"
        "v_mfma_f32_32x32x16_bf16 %4, v[132:135], v[160:163], %4\n"
        "s_add_u32 s74, s82, 32768\n"
        "s_addc_u32 s75, s83, 0\n"
        "s_add_u32 m0, s78, s73\n"
        "s_add_u32 m0, m0, 2048\n"
        "s_nop 0\n"
        "global_load_lds_dwordx4 v201, s[74:75]\n"
        "ds_read_b64_tr_b16 v[152:153], v194 offset:8192\n"
        "v_mfma_f32_32x32x16_bf16 %5, v[132:135], v[164:167], %5\n"
        "ds_read_b64_tr_b16 v[154:155], v194 offset:9216\n"
        "v_mfma_f32_32x32x16_bf16 %6, v[132:135], v[168:171], %6\n"
        "ds_read_b64_tr_b16 v[156:157], v195 offset:8192\n"
        "v_mfma_f32_32x32x16_bf16 %7, v[132:135], v[172:175], %7\n"
        "ds_read_b64_tr_b16 v[158:159], v195 offset:9216\n"
        "v_mfma_f32_32x32x16_bf16 %8, v[136:139], v[160:163], %8\n"
        "s_add_u32 s74, s80, 49152\n"
        "s_addc_u32 s75, s81, 0\n"
        "s_add_u32 m0, s78, s72\n"
        "s_add_u32 m0, m0, 3072\n"
        "s_nop 0\n"
        "global_load_lds_dwordx4 v200, s[74:75]\n"
        "ds_read_b128 v[176:179], v197 offset:16384\n"
        "v_mfma_f32_32x32x16_bf16 %9, v[136:139], v[164:167], %9\n"
        "ds_read_b128 v[180:183], v197 offset:18432\n"
        "v_mfma_f32_32x32x16_bf16 %10, v[136:139], v[168:171], %10\n"
        "ds_read_b128 v[184:187], v197 offset:20480\n"
        "v_mfma_f32_32x32x16_bf16 %11, v[136:139], v[172:175], %11\n"
        "ds_read_b128 v[188:191], v197 offset:22528\n"
        "v_mfma_f32_32x32x16_bf16 %12, v[140:143], v[160:163], %12\n"
        "s_add_u32 s74, s82, 49152\n"
        "s_addc_u32 s75, s83, 0\n"
        "s_add_u32 m0, s78, s73\n"
        "s_add_u32 m0, m0, 3072\n"
        "s_nop 0\n"
        "global_load_lds_dwordx4 v201, s[74:75]\n"
        "s_add_u32 s92, s71, 4\n"
        "s_cmp_lt_u32 s92, s70\n"
        "s_cselect_b32 s86, s60, s64\n"
        "s_cselect_b32 s87, s61, s65\n"
        "s_cselect_b32 s88, s62, s66\n"
        "s_cselect_b32 s89, s63, s67\n"
        "s_cselect_b32 s90, 0, s70\n"
        "s_sub_u32 s90, s92, s90\n"
        "s_mul_i32 s91, s90, 262144\n"
        "s_add_u32 s86, s86, s91\n"
        "s_addc_u32 s87, s87, 0\n"
        "s_lshl_b32 s91, s90, 6\n"
        "s_add_u32 s88, s88, s91\n"
        "s_addc_u32 s89, s89, 0\n"
        "v_mfma_f32_32x32x16_bf16 %13, v[140:143], v[164:167], %13\n"
        "v_mfma_f32_32x32x16_bf16 %14, v[140:143], v[168:171], %14\n"
        "v_mfma_f32_32x32x16_bf16 %15, v[140:143], v[172:175], %15\n"
        "QKV44_MID_%=:\n"
        "s_cmp_lt_u32 s71, 4\n"
        "s_cbranch_scc1 QKV44_SPECIAL_%=\n"
        "s_waitcnt vmcnt(16) lgkmcnt(0)\n"
        "s_barrier\n"
        "QKV44_WGO_%=:\n"
        "s_mov_b64 s[80:81], s[86:87]\n"
        "s_mov_b64 s[82:83], s[88:89]\n"
        "s_mov_b32 s78, s76\n"
        "v_mfma_f32_32x32x16_bf16 %0, v[144:147], v[176:179], %0\n"
        "s_add_u32 s74, s80, 0\n"
        "s_addc_u32 s75, s81, 0\n"
        "s_add_u32 m0, s78, s72\n"
        "s_add_u32 m0, m0, 0\n"
        "s_nop 0\n"
        "global_load_lds_dwordx4 v200, s[74:75]\n"
        "s_add_u32 s79, s76, 32768\n"
        "s_and_b32 s79, s79, 131071\n"
        "v_mfma_f32_32x32x16_bf16 %1, v[144:147], v[180:183], %1\n"
        "v_add_u32 v192, s79, %24\n"
        "v_add_u32 v196, s79, %25\n"
        "v_mfma_f32_32x32x16_bf16 %2, v[144:147], v[184:187], %2\n"
        "v_xor_b32 v197, 32, v196\n"
        "v_xor_b32 v193, 64, v192\n"
        "v_mfma_f32_32x32x16_bf16 %3, v[144:147], v[188:191], %3\n"
        "v_xor_b32 v194, 128, v192\n"
        "v_xor_b32 v195, 192, v192\n"
        "v_mfma_f32_32x32x16_bf16 %4, v[148:151], v[176:179], %4\n"
        "s_add_u32 s74, s82, 0\n"
        "s_addc_u32 s75, s83, 0\n"
        "s_add_u32 m0, s78, s73\n"
        "s_add_u32 m0, m0, 0\n"
        "s_nop 0\n"
        "global_load_lds_dwordx4 v201, s[74:75]\n"
        "ds_read_b64_tr_b16 v[128:129], v192 offset:0\n"
        "ds_read_b64_tr_b16 v[130:131], v192 offset:1024\n"
        "v_mfma_f32_32x32x16_bf16 %5, v[148:151], v[180:183], %5\n"
        "ds_read_b64_tr_b16 v[132:133], v193 offset:0\n"
        "ds_read_b64_tr_b16 v[134:135], v193 offset:1024\n"
        "v_mfma_f32_32x32x16_bf16 %6, v[148:151], v[184:187], %6\n"
        "ds_read_b64_tr_b16 v[136:137], v194 offset:0\n"
        "ds_read_b64_tr_b16 v[138:139], v194 offset:1024\n"
        "v_mfma_f32_32x32x16_bf16 %7, v[148:151], v[188:191], %7\n"
        "ds_read_b64_tr_b16 v[140:141], v195 offset:0\n"
        "ds_read_b64_tr_b16 v[142:143], v195 offset:1024\n"
        "v_mfma_f32_32x32x16_bf16 %8, v[152:155], v[176:179], %8\n"
        "s_add_u32 s74, s80, 16384\n"
        "s_addc_u32 s75, s81, 0\n"
        "s_add_u32 m0, s78, s72\n"
        "s_add_u32 m0, m0, 1024\n"
        "s_nop 0\n"
        "global_load_lds_dwordx4 v200, s[74:75]\n"
        "ds_read_b128 v[160:163], v196 offset:16384\n"
        "ds_read_b128 v[164:167], v196 offset:18432\n"
        "v_mfma_f32_32x32x16_bf16 %9, v[152:155], v[180:183], %9\n"
        "ds_read_b128 v[168:171], v196 offset:20480\n"
        "ds_read_b128 v[172:175], v196 offset:22528\n"
        "v_mfma_f32_32x32x16_bf16 %10, v[152:155], v[184:187], %10\n"
        "s_add_u32 s76, s76, 32768\n"
        "s_and_b32 s76, s76, 131071\n"
        "v_mfma_f32_32x32x16_bf16 %11, v[152:155], v[188:191], %11\n"
        "s_add_u32 s71, s71, 1\n"
        "v_mfma_f32_32x32x16_bf16 %12, v[156:159], v[176:179], %12\n"
        "s_add_u32 s74, s82, 16384\n"
        "s_addc_u32 s75, s83, 0\n"
        "s_add_u32 m0, s78, s73\n"
        "s_add_u32 m0, m0, 1024\n"
        "s_nop 0\n"
        "global_load_lds_dwordx4 v201, s[74:75]\n"
        "v_mfma_f32_32x32x16_bf16 %13, v[156:159], v[180:183], %13\n"
        "v_mfma_f32_32x32x16_bf16 %14, v[156:159], v[184:187], %14\n"
        "v_mfma_f32_32x32x16_bf16 %15, v[156:159], v[188:191], %15\n"
        "s_cmp_lt_u32 s71, s70\n"
        "s_cbranch_scc1 QKV44_LOOP_%=\n"
        "s_branch QKV44_END_%=\n"
        "QKV44_SPECIAL_%=:\n"
        "s_waitcnt lgkmcnt(0)\n"
        "s_cmp_eq_u32 s71, 3\n"
        "s_cbranch_scc1 QKV44_SP3_%=\n"
        "s_cmp_eq_u32 s77, 8\n"
        "s_cbranch_scc0 QKV44_QN8_%=\n"
        "s_waitcnt vmcnt(24)\n"
        "s_barrier\n"
        "s_branch QKV44_WGO_%=\n"
        "QKV44_QN8_%=:\n"
        "s_cmp_eq_u32 s77, 16\n"
        "s_cbranch_scc0 QKV44_QN16_%=\n"
        "s_waitcnt vmcnt(32)\n"
        "s_barrier\n"
        "s_branch QKV44_WGO_%=\n"
        "QKV44_QN16_%=:\n"
        "s_cmp_eq_u32 s77, 32\n"
        "s_cbranch_scc0 QKV44_QN32_%=\n"
        "s_waitcnt vmcnt(48)\n"
        "s_barrier\n"
        "s_branch QKV44_WGO_%=\n"
        "QKV44_QN32_%=:\n"
        "s_waitcnt vmcnt(16)\n"
        "s_barrier\n"
        "s_branch QKV44_WGO_%=\n"
        "QKV44_SP3_%=:\n"
        "s_waitcnt vmcnt(16)\n"
        "s_barrier\n"
        "s_cmp_eq_u64 s[84:85], 0\n"
        "s_cbranch_scc1 QKV44_WGO_%=\n"
        "s_cmp_eq_u32 %21, 0\n"
        "s_cbranch_scc0 QKV44_WGO_%=\n"
        "s_bfe_u32 s90, %20, 0x40018\n"
        "v_mov_b32 v202, 0\n"
        "v_mov_b32 v203, s90\n"
        "s_mov_b64 exec, 1\n"
        "global_atomic_add v202, v203, s[84:85] sc1\n"
        "s_mov_b64 exec, -1\n"
        "s_branch QKV44_WGO_%=\n"
        "QKV44_END_%=:\n"
        "s_add_u32 s74, s80, 32768\n"
        "s_addc_u32 s75, s81, 0\n"
        "s_add_u32 m0, s78, s72\n"
        "s_add_u32 m0, m0, 2048\n"
        "s_nop 0\n"
        "global_load_lds_dwordx4 v200, s[74:75]\n"
        "s_add_u32 s74, s82, 32768\n"
        "s_addc_u32 s75, s83, 0\n"
        "s_add_u32 m0, s78, s73\n"
        "s_add_u32 m0, m0, 2048\n"
        "s_nop 0\n"
        "global_load_lds_dwordx4 v201, s[74:75]\n"
        "s_add_u32 s74, s80, 49152\n"
        "s_addc_u32 s75, s81, 0\n"
        "s_add_u32 m0, s78, s72\n"
        "s_add_u32 m0, m0, 3072\n"
        "s_nop 0\n"
        "global_load_lds_dwordx4 v200, s[74:75]\n"
        "s_add_u32 s74, s82, 49152\n"
        "s_addc_u32 s75, s83, 0\n"
        "s_add_u32 m0, s78, s73\n"
        "s_add_u32 m0, m0, 3072\n"
        "s_nop 0\n"
        "global_load_lds_dwordx4 v201, s[74:75]\n"
        "s_waitcnt lgkmcnt(0)\n"
        "s_nop 15\n"
        : "=&a"(acc[0][0]), "=&a"(acc[0][1]), "=&a"(acc[0][2]), "=&a"(acc[0][3]), "=&a"(acc[1][0]), "=&a"(acc[1][1]), "=&a"(acc[1][2]), "=&a"(acc[1][3]), "=&a"(acc[2][0]), "=&a"(acc[2][1]), "=&a"(acc[2][2]), "=&a"(acc[2][3]), "=&a"(acc[3][0]), "=&a"(acc[3][1]), "=&a"(acc[3][2]), "=&a"(acc[3][3])
        : "s"(W), "s"(X), "s"(Wn), "s"(Xn), "s"(ctl), "s"(w), "v"(vW), "v"(vX), "v"(aw), "v"(ax), "s"(flag)
        : "v128", "v129", "v130", "v131", "v132", "v133", "v134", "v135", "v136", "v137", "v138", "v139", "v140", "v141", "v142", "v143", "v144", "v145", "v146", "v147", "v148", "v149", "v150", "v151", "v152", "v153", "v154", "v155", "v156", "v157", "v158", "v159", "v160", "v161", "v162", "v163", "v164", "v165", "v166", "v167", "v168", "v169", "v170", "v171", "v172", "v173", "v174", "v175", "v176", "v177", "v178", "v179", "v180", "v181", "v182", "v183", "v184", "v185", "v186", "v187", "v188", "v189", "v190", "v191", "v192", "v193", "v194", "v195", "v196", "v197", "v198", "v199", "v200", "v201", "v202", "v203", "s60", "s61", "s62", "s63", "s64", "s65", "s66", "s67", "s68", "s69", "s70", "s71", "s72", "s73", "s74", "s75", "s76", "s77", "s78", "s79", "s80", "s81", "s82", "s83", "s84", "s85", "s86", "s87", "s88", "s89", "s90", "s91", "s92", "s93", "memory", "scc");
}

__device__ __forceinline__ void kloop_QKV22(f32x16 (&acc)[2][2], const char* W, const char* X, const char* Wn, const char* Xn, unsigned ctl, int w, unsigned vW, unsigned vX, unsigned aw, unsigned ax, unsigned* flag, ptrdiff_t dW) {
    asm volatile(
        "s_mov_b64 s[60:61], %4\n"
        "s_mov_b64 s[62:63], %5\n"
        "s_mov_b64 s[64:65], %6\n"
        "s_mov_b64 s[66:67], %7\n"
        "s_mov_b64 s[84:85], %14\n"
        "s_and_b32 s70, %8, 0xff\n"
        "s_bfe_u32 s76, %8, 0x2000a\n"
        "s_lshl_b32 s76, s76, 15\n"
        "s_bfe_u32 s77, %8, 0x80010\n"
        "s_mul_i32 s72, %9, 2048\n"
        "s_mul_i32 s73, %9, 2048\n"
        "s_add_u32 s73, s73, 8192\n"
        "v_mov_b32 v200, %10\n"
        "v_mov_b32 v201, %11\n"
        "s_bitcmp1_b32 %8, 8\n"
        "s_cbranch_scc1 QKV22_PRE_%=\n"
        "s_waitcnt vmcnt(0)\n"
        "s_barrier\n"
        "s_mov_b32 s78, s76\n"
        "s_mov_b32 s71, 0\n"
        "s_cmp_lt_u32 s71, s70\n"
        "s_cselect_b32 s80, s60, s64\n"
        "s_cselect_b32 s81, s61, s65\n"
        "s_cselect_b32 s82, s62, s66\n"
        "s_cselect_b32 s83, s63, s67\n"
        "s_cselect_b32 s90, 0, s70\n"
        "s_sub_u32 s90, s71, s90\n"
        "s_mul_i32 s91, s90, 262144\n"
        "s_add_u32 s80, s80, s91\n"
        "s_addc_u32 s81, s81, 0\n"
        "s_lshl_b32 s91, s90, 6\n"
        "s_add_u32 s82, s82, s91\n"
        "s_addc_u32 s83, s83, 0\n"
        "s_add_u32 s74, s80, 0\n"
        "s_addc_u32 s75, s81, 0\n"
        "s_add_u32 m0, s78, s72\n"
        "s_add_u32 m0, m0, 0\n"
        "s_nop 0\n"
        "global_load_lds_dwordx4 v200, s[74:75]\n"
        "s_add_u32 s74, s82, 0\n"
        "s_addc_u32 s75, s83, 0\n"
        "s_add_u32 m0, s78, s73\n"
        "s_add_u32 m0, m0, 0\n"
        "s_nop 0\n"
        "global_load_lds_dwordx4 v201, s[74:75]\n"
        "s_add_u32 s74, s80, 32768\n"
        "s_addc_u32 s75, s81, 0\n"
        "s_add_u32 m0, s78, s72\n"
        "s_add_u32 m0, m0, 1024\n"
        "s_nop 0\n"
        "global_load_lds_dwordx4 v200, s[74:75]\n"
        "s_add_u32 s74, s82, 16384\n"
        "s_addc_u32 s75, s83, 0\n"
        "s_add_u32 m0, s78, s73\n"
        "s_add_u32 m0, m0, 1024\n"
        "s_nop 0\n"
        "global_load_lds_dwordx4 v201, s[74:75]\n"
        "s_add_u32 s78, s78, 32768\n"
        "s_and_b32 s78, s78, 131071\n"
        "s_mov_b32 s71, 1\n"
        "s_cmp_lt_u32 s71, s70\n"
        "s_cselect_b32 s80, s60, s64\n"
        "s_cselect_b32 s81, s61, s65\n"
        "s_cselect_b32 s82, s62, s66\n"
        "s_cselect_b32 s83, s63, s67\n"
        "s_cselect_b32 s90, 0, s70\n"
        "s_sub_u32 s90, s71, s90\n"
        "s_mul_i32 s91, s90, 262144\n"
        "s_add_u32 s80, s80, s91\n"
        "s_addc_u32 s81, s81, 0\n"
        "s_lshl_b32 s91, s90, 6\n"
        "s_add_u32 s82, s82, s91\n"
        "s_addc_u32 s83, s83, 0\n"
        "s_add_u32 s74, s80, 0\n"
        "s_addc_u32 s75, s81, 0\n"
        "s_add_u32 m0, s78, s72\n"
        "s_add_u32 m0, m0, 0\n"
        "s_nop 0\n"
        "global_load_lds_dwordx4 v200, s[74:75]\n"
        "s_add_u32 s74, s82, 0\n"
        "s_addc_u32 s75, s83, 0\n"
        "s_add_u32 m0, s78, s73\n"
        "s_add_u32 m0, m0, 0\n"
        "s_nop 0\n"
        "global_load_lds_dwordx4 v201, s[74:75]\n"
        "s_add_u32 s74, s80, 32768\n"
        "s_addc_u32 s75, s81, 0\n"
        "s_add_u32 m0, s78, s72\n"
        "s_add_u32 m0, m0, 1024\n"
        "s_nop 0\n"
        "global_load_lds_dwordx4 v200, s[74:75]\n"
        "s_add_u32 s74, s82, 16384\n"
        "s_addc_u32 s75, s83, 0\n"
        "s_add_u32 m0, s78, s73\n"
        "s_add_u32 m0, m0, 1024\n"
        "s_nop 0\n"
        "global_load_lds_dwordx4 v201, s[74:75]\n"
        "s_add_u32 s78, s78, 32768\n"
        "s_and_b32 s78, s78, 131071\n"
        "s_mov_b32 s71, 2\n"
        "s_cmp_lt_u32 s71, s70\n"
        "s_cselect_b32 s80, s60, s64\n"
        "s_cselect_b32 s81, s61, s65\n"
        "s_cselect_b32 s82, s62, s66\n"
        "s_cselect_b32 s83, s63, s67\n"
        "s_cselect_b32 s90, 0, s70\n"
        "s_sub_u32 s90, s71, s90\n"
        "s_mul_i32 s91, s90, 262144\n"
        "s_add_u32 s80, s80, s91\n"
        "s_addc_u32 s81, s81, 0\n"
        "s_lshl_b32 s91, s90, 6\n"
        "s_add_u32 s82, s82, s91\n"
        "s_addc_u32 s83, s83, 0\n"
        "s_add_u32 s74, s80, 0\n"
        "s_addc_u32 s75, s81, 0\n"
        "s_add_u32 m0, s78, s72\n"
        "s_add_u32 m0, m0, 0\n"
        "s_nop 0\n"
        "global_load_lds_dwordx4 v200, s[74:75]\n"
        "s_add_u32 s74, s82, 0\n"
        "s_addc_u32 s75, s83, 0\n"
        "s_add_u32 m0, s78, s73\n"
        "s_add_u32 m0, m0, 0\n"
        "s_nop 0\n"
        "global_load_lds_dwordx4 v201, s[74:75]\n"
        "s_add_u32 s74, s80, 32768\n"
        "s_addc_u32 s75, s81, 0\n"
        "s_add_u32 m0, s78, s72\n"
        "s_add_u32 m0, m0, 1024\n"
        "s_nop 0\n"
        "global_load_lds_dwordx4 v200, s[74:75]\n"
        "s_add_u32 s74, s82, 16384\n"
        "s_addc_u32 s75, s83, 0\n"
        "s_add_u32 m0, s78, s73\n"
        "s_add_u32 m0, m0, 1024\n"
        "s_nop 0\n"
        "global_load_lds_dwordx4 v201, s[74:75]\n"
        "s_add_u32 s78, s78, 32768\n"
        "s_and_b32 s78, s78, 131071\n"
        "s_mov_b32 s71, 3\n"
        "s_cmp_lt_u32 s71, s70\n"
        "s_cselect_b32 s80, s60, s64\n"
        "s_cselect_b32 s81, s61, s65\n"
        "s_cselect_b32 s82, s62, s66\n"
        "s_cselect_b32 s83, s63, s67\n"
        "s_cselect_b32 s90, 0, s70\n"
        "s_sub_u32 s90, s71, s90\n"
        "s_mul_i32 s91, s90, 262144\n"
        "s_add_u32 s80, s80, s91\n"
        "s_addc_u32 s81, s81, 0\n"
        "s_lshl_b32 s91, s90, 6\n"
        "s_add_u32 s82, s82, s91\n"
        "s_addc_u32 s83, s83, 0\n"
        "s_add_u32 s74, s80, 0\n"
        "s_addc_u32 s75, s81, 0\n"
        "s_add_u32 m0, s78, s72\n"
        "s_add_u32 m0, m0, 0\n"
        "s_nop 0\n"
        "global_load_lds_dwordx4 v200, s[74:75]\n"
        "s_add_u32 s74, s82, 0\n"
        "s_addc_u32 s75, s83, 0\n"
        "s_add_u32 m0, s78, s73\n"
        "s_add_u32 m0, m0, 0\n"
        "s_nop 0\n"
        "global_load_lds_dwordx4 v201, s[74:75]\n"
        "s_add_u32 s74, s80, 32768\n"
        "s_addc_u32 s75, s81, 0\n"
        "s_add_u32 m0, s78, s72\n"
        "s_add_u32 m0, m0, 1024\n"
        "s_nop 0\n"
        "global_load_lds_dwordx4 v200, s[74:75]\n"
        "s_add_u32 s74, s82, 16384\n"
        "s_addc_u32 s75, s83, 0\n"
        "s_add_u32 m0, s78, s73\n"
        "s_add_u32 m0, m0, 1024\n"
        "s_nop 0\n"
        "global_load_lds_dwordx4 v201, s[74:75]\n"
        "s_add_u32 s78, s78, 32768\n"
        "s_and_b32 s78, s78, 131071\n"
        "s_waitcnt vmcnt(12)\n"
        "s_mov_b32 s77, 0\n"
        "s_branch QKV22_GO_%=\n"
        "QKV22_PRE_%=:\n"
        "s_cmp_eq_u32 s77, 8\n"
        "s_cbranch_scc0 QKV22_PN8_%=\n"
        "s_waitcnt vmcnt(20)\n"
        "s_branch QKV22_GO_%=\n"
        "QKV22_PN8_%=:\n"
        "s_cmp_eq_u32 s77, 16\n"
        "s_cbranch_scc0 QKV22_PN16_%=\n"
        "s_waitcnt vmcnt(28)\n"
        "s_branch QKV22_GO_%=\n"
        "QKV22_PN16_%=:\n"
        "s_cmp_eq_u32 s77, 32\n"
        "s_cbranch_scc0 QKV22_PN32_%=\n"
        "s_waitcnt vmcnt(44)\n"
        "s_branch QKV22_GO_%=\n"
        "QKV22_PN32_%=:\n"
        "s_waitcnt vmcnt(12)\n"
        "QKV22_GO_%=:\n"
        "s_barrier\n"
        "v_add_u32 v192, s76, %12\n"
        "v_add_u32 v196, s76, %13\n"
        "v_xor_b32 v197, 32, v196\n"
        "v_xor_b32 v193, 64, v192\n"
        "ds_read_b64_tr_b16 v[128:129], v192 offset:0\n"
        "ds_read_b64_tr_b16 v[130:131], v192 offset:512\n"
        "ds_read_b64_tr_b16 v[132:133], v193 offset:0\n"
        "ds_read_b64_tr_b16 v[134:135], v193 offset:512\n"
        "ds_read_b128 v[160:163], v196 offset:8192\n"
        "ds_read_b128 v[164:167], v196 offset:10240\n"
        "s_mov_b32 s71, 0\n"
        "s_waitcnt lgkmcnt(0)\n"
        "v_mfma_f32_32x32x16_bf16 %0, v[128:131], v[160:163], 0\n"
        "ds_read_b64_tr_b16 v[144:145], v192 offset:4096\n"
        "ds_read_b64_tr_b16 v[146:147], v192 offset:4608\n"
        "ds_read_b64_tr_b16 v[148:149], v193 offset:4096\n"
        "v_mfma_f32_32x32x16_bf16 %1, v[128:131], v[164:167], 0\n"
        "ds_read_b64_tr_b16 v[150:151], v193 offset:4608\n"
        "ds_read_b128 v[176:179], v197 offset:8192\n"
        "ds_read_b128 v[180:183], v197 offset:10240\n"
        "v_mfma_f32_32x32x16_bf16 %2, v[132:135], v[160:163], 0\n"
        "s_add_u32 s92, s71, 4\n"
        "s_cmp_lt_u32 s92, s70\n"
        "s_cselect_b32 s86, s60, s64\n"
        "s_cselect_b32 s87, s61, s65\n"
        "s_cselect_b32 s88, s62, s66\n"
        "s_cselect_b32 s89, s63, s67\n"
        "s_cselect_b32 s90, 0, s70\n"
        "s_sub_u32 s90, s92, s90\n"
        "s_mul_i32 s91, s90, 262144\n"
        "s_add_u32 s86, s86, s91\n"
        "s_addc_u32 s87, s87, 0\n"
        "s_lshl_b32 s91, s90, 6\n"
        "s_add_u32 s88, s88, s91\n"
        "s_addc_u32 s89, s89, 0\n"
        "v_mfma_f32_32x32x16_bf16 %3, v[132:135], v[164:167], 0\n"
        "s_branch QKV22_MID_%=\n"
        "QKV22_LOOP_%=:\n"
        "s_waitcnt lgkmcnt(0)\n"
        "v_mfma_f32_32x32x16_bf16 %0, v[128:131], v[160:163], %0\n"
        "s_add_u32 s74, s80, 32768\n"
        "s_addc_u32 s75, s81, 0\n"
        "s_add_u32 m0, s78, s72\n"
        "s_add_u32 m0, m0, 1024\n"
        "s_nop 0\n"
        "global_load_lds_dwordx4 v200, s[74:75]\n"
        "ds_read_b64_tr_b16 v[144:145], v192 offset:4096\n"
        "ds_read_b64_tr_b16 v[146:147], v192 offset:4608\n"
        "ds_read_b64_tr_b16 v[148:149], v193 offset:4096\n"
        "v_mfma_f32_32x32x16_bf16 %1, v[128:131], v[164:167], %1\n"
        "ds_read_b64_tr_b16 v[150:151], v193 offset:4608\n"
        "ds_read_b128 v[176:179], v197 offset:8192\n"
        "ds_read_b128 v[180:183], v197 offset:10240\n"
        "v_mfma_f32_32x32x16_bf16 %2, v[132:135], v[160:163], %2\n"
        "s_add_u32 s74, s82, 16384\n"
        "s_addc_u32 s75, s83, 0\n"
        "s_add_u32 m0, s78, s73\n"
        "s_add_u32 m0, m0, 1024\n"
        "s_nop 0\n"
        "global_load_lds_dwordx4 v201, s[74:75]\n"
        "s_add_u32 s92, s71, 4\n"
        "s_cmp_lt_u32 s92, s70\n"
        "s_cselect_b32 s86, s60, s64\n"
        "s_cselect_b32 s87, s61, s65\n"
        "s_cselect_b32 s88, s62, s66\n"
        "s_cselect_b32 s89, s63, s67\n"
        "s_cselect_b32 s90, 0, s70\n"
        "s_sub_u32 s90, s92, s90\n"
        "s_mul_i32 s91, s90, 262144\n"
        "s_add_u32 s86, s86, s91\n"
        "s_addc_u32 s87, s87, 0\n"
        "s_lshl_b32 s91, s90, 6\n"
        "s_add_u32 s88, s88, s91\n"
        "s_addc_u32 s89, s89, 0\n"
        "v_mfma_f32_32x32x16_bf16 %3, v[132:135], v[164:167], %3\n"
        "QKV22_MID_%=:\n"
        "s_cmp_lt_u32 s71, 4\n"
        "s_cbranch_scc1 QKV22_SPECIAL_%=\n"
        "s_waitcnt vmcnt(8) lgkmcnt(0)\n"
        "s_barrier\n"
        "QKV22_WGO_%=:\n"
        "s_mov_b64 s[80:81], s[86:87]\n"
        "s_mov_b64 s[82:83], s[88:89]\n"
        "s_mov_b32 s78, s76\n"
        "v_mfma_f32_32x32x16_bf16 %0, v[144:147], v[176:179], %0\n"
        "s_add_u32 s74, s80, 0\n"
        "s_addc_u32 s75, s81, 0\n"
        "s_add_u32 m0, s78, s72\n"
        "s_add_u32 m0, m0, 0\n"
        "s_nop 0\n"
        "global_load_lds_dwordx4 v200, s[74:75]\n"
        "s_add_u32 s79, s76, 32768\n"
        "s_and_b32 s79, s79, 131071\n"
        "v_add_u32 v192, s79, %12\n"
        "v_add_u32 v196, s79, %13\n"
        "v_xor_b32 v197, 32, v196\n"
        "v_mfma_f32_32x32x16_bf16 %1, v[144:147], v[180:183], %1\n"
        "v_xor_b32 v193, 64, v192\n"
        "ds_read_b64_tr_b16 v[128:129], v192 offset:0\n"
        "ds_read_b64_tr_b16 v[130:131], v192 offset:512\n"
        "ds_read_b64_tr_b16 v[132:133], v193 offset:0\n"
        "ds_read_b64_tr_b16 v[134:135], v193 offset:512\n"
        "v_mfma_f32_32x32x16_bf16 %2, v[148:151], v[176:179], %2\n"
        "s_add_u32 s74, s82, 0\n"
        "s_addc_u32 s75, s83, 0\n"
        "s_add_u32 m0, s78, s73\n"
        "s_add_u32 m0, m0, 0\n"
        "s_nop 0\n"
        "global_load_lds_dwordx4 v201, s[74:75]\n"
        "ds_read_b128 v[160:163], v196 offset:8192\n"
        "ds_read_b128 v[164:167], v196 offset:10240\n"
        "s_add_u32 s76, s76, 32768\n"
        "s_and_b32 s76, s76, 131071\n"
        "s_add_u32 s71, s71, 1\n"
        "v_mfma_f32_32x32x16_bf16 %3, v[148:151], v[180:183], %3\n"
        "s_cmp_lt_u32 s71, s70\n"
        "s_cbranch_scc1 QKV22_LOOP_%=\n"
        "s_branch QKV22_END_%=\n"
        "QKV22_SPECIAL_%=:\n"
        "s_waitcnt lgkmcnt(0)\n"
        "s_cmp_eq_u32 s71, 3\n"
        "s_cbranch_scc1 QKV22_SP3_%=\n"
        "s_cmp_eq_u32 s77, 8\n"
        "s_cbranch_scc0 QKV22_QN8_%=\n"
        "s_waitcnt vmcnt(16)\n"
        "s_barrier\n"
        "s_branch QKV22_WGO_%=\n"
        "QKV22_QN8_%=:\n"
        "s_cmp_eq_u32 s77, 16\n"
        "s_cbranch_scc0 QKV22_QN16_%=\n"
        "s_waitcnt vmcnt(24)\n"
        "s_barrier\n"
        "s_branch QKV22_WGO_%=\n"
        "QKV22_QN16_%=:\n"
        "s_cmp_eq_u32 s77, 32\n"
        "s_cbranch_scc0 QKV22_QN32_%=\n"
        "s_waitcnt vmcnt(40)\n"
        "s_barrier\n"
        "s_branch QKV22_WGO_%=\n"
        "QKV22_QN32_%=:\n"
        "s_waitcnt vmcnt(8)\n"
        "s_barrier\n"
        "s_branch QKV22_WGO_%=\n"
        "QKV22_SP3_%=:\n"
        "s_waitcnt vmcnt(8)\n"
        "s_barrier\n"
        "s_cmp_eq_u64 s[84:85], 0\n"
        "s_cbranch_scc1 QKV22_WGO_%=\n"
        "s_cmp_eq_u32 %9, 0\n"
        "s_cbranch_scc0 QKV22_WGO_%=\n"
        "s_bfe_u32 s90, %8, 0x40018\n"
        "v_mov_b32 v202, 0\n"
        "v_mov_b32 v203, s90\n"
        "s_mov_b64 exec, 1\n"
        "global_atomic_add v202, v203, s[84:85] sc1\n"
        "s_mov_b64 exec, -1\n"
        "s_branch QKV22_WGO_%=\n"
        "QKV22_END_%=:\n"
        "s_add_u32 s74, s80, 32768\n"
        "s_addc_u32 s75, s81, 0\n"
        "s_add_u32 m0, s78, s72\n"
        "s_add_u32 m0, m0, 1024\n"
        "s_nop 0\n"
        "global_load_lds_dwordx4 v200, s[74:75]\n"
        "s_add_u32 s74, s82, 16384\n"
        "s_addc_u32 s75, s83, 0\n"
        "s_add_u32 m0, s78, s73\n"
        "s_add_u32 m0, m0, 1024\n"
        "s_nop 0\n"
        "global_load_lds_dwordx4 v201, s[74:75]\n"
        "s_waitcnt lgkmcnt(0)\n"
        "s_nop 15\n"
        : "=&a"(acc[0][0]), "=&a"(acc[0][1]), "=&a"(acc[1][0]), "=&a"(acc[1][1])
        : "s"(W), "s"(X), "s"(Wn), "s"(Xn), "s"(ctl), "s"(w), "v"(vW), "v"(vX), "v"(aw), "v"(ax), "s"(flag)
        : "v128", "v129", "v130", "v131", "v132", "v133", "v134", "v135", "v136", "v137", "v138", "v139", "v140", "v141", "v142", "v143", "v144", "v145", "v146", "v147", "v148", "v149", "v150", "v151", "v152", "v153", "v154", "v155", "v156", "v157", "v158", "v159", "v160", "v161", "v162", "v163", "v164", "v165", "v166", "v167", "v168", "v169", "v170", "v171", "v172", "v173", "v174", "v175", "v176", "v177", "v178", "v179", "v180", "v181", "v182", "v183", "v184", "v185", "v186", "v187", "v188", "v189", "v190", "v191", "v192", "v193", "v194", "v195", "v196", "v197", "v198", "v199", "v200", "v201", "v202", "v203", "s60", "s61", "s62", "s63", "s64", "s65", "s66", "s67", "s68", "s69", "s70", "s71", "s72", "s73", "s74", "s75", "s76", "s77", "s78", "s79", "s80", "s81", "s82", "s83", "s84", "s85", "s86", "s87", "s88", "s89", "s90", "s91", "s92", "s93", "memory", "scc");
}

__device__ __forceinline__ void kloop_F144(f32x16 (&acc)[4][4], const char* W, const char* X, const char* Wn, const char* Xn, unsigned ctl, int w, unsigned vW, unsigned vX, unsigned aw, unsigned ax, unsigned* flag, ptrdiff_t dW) {
    asm volatile(
        "s_mov_b64 s[60:61], %16\n"
        "s_mov_b64 s[62:63], %17\n"
        "s_mov_b64 s[64:65], %18\n"
        "s_mov_b64 s[66:67], %19\n"
        "s_mov_b64 s[68:69], %27\n"
        "s_mov_b64 s[84:85], %26\n"
        "s_and_b32 s70, %20, 0xff\n"
        "s_bfe_u32 s76, %20, 0x2000a\n"
        "s_lshl_b32 s76, s76, 15\n"
        "s_bfe_u32 s77, %20, 0x80010\n"
        "s_mul_i32 s72, %21, 2048\n"
        "s_mul_i32 s73, %21, 4096\n"
        "s_add_u32 s73, s73, 16384\n"
        "v_mov_b32 v200, %22\n"
        "v_mov_b32 v201, %23\n"
        "s_bitcmp1_b32 %20, 8\n"
        "s_cbranch_scc1 F144_PRE_%=\n"
        "s_waitcnt vmcnt(0)\n"
        "s_barrier\n"
        "s_mov_b32 s78, s76\n"
        "s_mov_b32 s71, 0\n"
        "s_cmp_lt_u32 s71, s70\n"
        "s_cselect_b32 s80, s60, s64\n"
        "s_cselect_b32 s81, s61, s65\n"
        "s_cselect_b32 s82, s62, s66\n"
        "s_cselect_b32 s83, s63, s67\n"
        "s_cselect_b32 s90, 0, s70\n"
        "s_sub_u32 s90, s71, s90\n"
        "s_mul_i32 s91, s90, 114688\n"
        "s_add_u32 s80, s80, s91\n"
        "s_addc_u32 s81, s81, 0\n"
        "s_lshl_b32 s91, s90, 6\n"
        "s_add_u32 s82, s82, s91\n"
        "s_addc_u32 s83, s83, 0\n"
        "s_add_u32 s74, s80, 0\n"
        "s_addc_u32 s75, s81, 0\n"
        "s_add_u32 m0, s78, s72\n"
        "s_add_u32 m0, m0, 0\n"
        "s_nop 0\n"
        "global_load_lds_dwordx4 v200, s[74:75]\n"
        "s_add_u32 s74, s82, 0\n"
        "s_addc_u32 s75, s83, 0\n"
        "s_add_u32 m0, s78, s73\n"
        "s_add_u32 m0, m0, 0\n"
        "s_nop 0\n"
        "global_load_lds_dwordx4 v201, s[74:75]\n"
        "s_add_u32 s74, s80, 14336\n"
        "s_addc_u32 s75, s81, 0\n"
        "s_add_u32 m0, s78, s72\n"
        "s_add_u32 m0, m0, 1024\n"
        "s_nop 0\n"
        "global_load_lds_dwordx4 v200, s[74:75]\n"
        "s_add_u32 s74, s82, 16384\n"
        "s_addc_u32 s75, s83, 0\n"
        "s_add_u32 m0, s78, s73\n"
        "s_add_u32 m0, m0, 1024\n"
        "s_nop 0\n"
        "global_load_lds_dwordx4 v201, s[74:75]\n"
        "s_add_u32 s74, s80, 0\n"
        "s_addc_u32 s75, s81, 0\n"
        "s_add_u32 s74, s74, s68\n"
        "s_addc_u32 s75, s75, s69\n"
        "s_add_u32 m0, s78, s72\n"
        "s_add_u32 m0, m0, 8192\n"
        "s_nop 0\n"
        "global_load_lds_dwordx4 v200, s[74:75]\n"
        "s_add_u32 s74, s82, 32768\n"
        "s_addc_u32 s75, s83, 0\n"
        "s_add_u32 m0, s78, s73\n"
        "s_add_u32 m0, m0, 2048\n"
        "s_nop 0\n"
        "global_load_lds_dwordx4 v201, s[74:75]\n"
        "s_add_u32 s74, s80, 14336\n"
        "s_addc_u32 s75, s81, 0\n"
        "s_add_u32 s74, s74, s68\n"
        "s_addc_u32 s75, s75, s69\n"
        "s_add_u32 m0, s78, s72\n"
        "s_add_u32 m0, m0, 9216\n"
        "s_nop 0\n"
        "global_load_lds_dwordx4 v200, s[74:75]\n"
        "s_add_u32 s74, s82, 49152\n"
        "s_addc_u32 s75, s83, 0\n"
        "s_add_u32 m0, s78, s73\n"
        "s_add_u32 m0, m0, 3072\n"
        "s_nop 0\n"
        "global_load_lds_dwordx4 v201, s[74:75]\n"
        "s_add_u32 s78, s78, 32768\n"
        "s_and_b32 s78, s78, 131071\n"
        "s_mov_b32 s71, 1\n"
        "s_cmp_lt_u32 s71, s70\n"
        "s_cselect_b32 s80, s60, s64\n"
        "s_cselect_b32 s81, s61, s65\n"
        "s_cselect_b32 s82, s62, s66\n"
        "s_cselect_b32 s83, s63, s67\n"
        "s_cselect_b32 s90, 0, s70\n"
        "s_sub_u32 s90, s71, s90\n"
        "s_mul_i32 s91, s90, 114688\n"
        "s_add_u32 s80, s80, s91\n"
        "s_addc_u32 s81, s81, 0\n"
        "s_lshl_b32 s91, s90, 6\n"
        "s_add_u32 s82, s82, s91\n"
        "s_addc_u32 s83, s83, 0\n"
        "s_add_u32 s74, s80, 0\n"
        "s_addc_u32 s75, s81, 0\n"
        "s_add_u32 m0, s78, s72\n"
        "s_add_u32 m0, m0, 0\n"
        "s_nop 0\n"
        "global_load_lds_dwordx4 v200, s[74:75]\n"
        "s_add_u32 s74, s82, 0\n"
        "s_addc_u32 s75, s83, 0\n"
        "s_add_u32 m0, s78, s73\n"
        "s_add_u32 m0, m0, 0\n"
        "s_nop 0\n"
        "global_load_lds_dwordx4 v201, s[74:75]\n"
        "s_add_u32 s74, s80, 14336\n"
        "s_addc_u32 s75, s81, 0\n"
        "s_add_u32 m0, s78, s72\n"
        "s_add_u32 m0, m0, 1024\n"
        "s_nop 0\n"
        "global_load_lds_dwordx4 v200, s[74:75]\n"
        "s_add_u32 s74, s82, 16384\n"
        "s_addc_u32 s75, s83, 0\n"
        "s_add_u32 m0, s78, s73\n"
        "s_add_u32 m0, m0, 1024\n"
        "s_nop 0\n"
        "global_load_lds_dwordx4 v201, s[74:75]\n"
        "s_add_u32 s74, s80, 0\n"
        "s_addc_u32 s75, s81, 0\n"
        "s_add_u32 s74, s74, s68\n"
        "s_addc_u32 s75, s75, s69\n"
        "s_add_u32 m0, s78, s72\n"
        "s_add_u32 m0, m0, 8192\n"
        "s_nop 0\n"
        "global_load_lds_dwordx4 v200, s[74:75]\n"
        "s_add_u32 s74, s82, 32768\n"
        "s_addc_u32 s75, s83, 0\n"
        "s_add_u32 m0, s78, s73\n"
        "s_add_u32 m0, m0, 2048\n"
        "s_nop 0\n"
        "global_load_lds_dwordx4 v201, s[74:75]\n"
        "s_add_u32 s74, s80, 14336\n"
        "s_addc_u32 s75, s81, 0\n"
        "s_add_u32 s74, s74, s68\n"
        "s_addc_u32 s75, s75, s69\n"
        "s_add_u32 m0, s78, s72\n"
        "s_add_u32 m0, m0, 9216\n"
        "s_nop 0\n"
        "global_load_lds_dwordx4 v200, s[74:75]\n"
        "s_add_u32 s74, s82, 49152\n"
        "s_addc_u32 s75, s83, 0\n"
        "s_add_u32 m0, s78, s73\n"
        "s_add_u32 m0, m0, 3072\n"
        "s_nop 0\n"
        "global_load_lds_dwordx4 v201, s[74:75]\n"
        "s_add_u32 s78, s78, 32768\n"
        "s_and_b32 s78, s78, 131071\n"
        "s_mov_b32 s71, 2\n"
        "s_cmp_lt_u32 s71, s70\n"
        "s_cselect_b32 s80, s60, s64\n"
        "s_cselect_b32 s81, s61, s65\n"
        "s_cselect_b32 s82, s62, s66\n"
        "s_cselect_b32 s83, s63, s67\n"
        "s_cselect_b32 s90, 0, s70\n"
        "s_sub_u32 s90, s71, s90\n"
        "s_mul_i32 s91, s90, 114688\n"
        "s_add_u32 s80, s80, s91\n"
        "s_addc_u32 s81, s81, 0\n"
        "s_lshl_b32 s91, s90, 6\n"
        "s_add_u32 s82, s82, s91\n"
        "s_addc_u32 s83, s83, 0\n"
        "s_add_u32 s74, s80, 0\n"
        "s_addc_u32 s75, s81, 0\n"
        "s_add_u32 m0, s78, s72\n"
        "s_add_u32 m0, m0, 0\n"
        "s_nop 0\n"
        "global_load_lds_dwordx4 v200, s[74:75]\n"
        "s_add_u32 s74, s82, 0\n"
        "s_addc_u32 s75, s83, 0\n"
        "s_add_u32 m0, s78, s73\n"
        "s_add_u32 m0, m0, 0\n"
        "s_nop 0\n"
        "global_load_lds_dwordx4 v201, s[74:75]\n"
        "s_add_u32 s74, s80, 14336\n"
        "s_addc_u32 s75, s81, 0\n"
        "s_add_u32 m0, s78, s72\n"
        "s_add_u32 m0, m0, 1024\n"
        "s_nop 0\n"
        "global_load_lds_dwordx4 v200, s[74:75]\n"
        "s_add_u32 s74, s82, 16384\n"
        "s_addc_u32 s75, s83, 0\n"
        "s_add_u32 m0, s78, s73\n"
        "s_add_u32 m0, m0, 1024\n"
        "s_nop 0\n"
        "global_load_lds_dwordx4 v201, s[74:75]\n"
        "s_add_u32 s74, s80, 0\n"
        "s_addc_u32 s75, s81, 0\n"
        "s_add_u32 s74, s74, s68\n"
        "s_addc_u32 s75, s75, s69\n"
        "s_add_u32 m0, s78, s72\n"
        "s_add_u32 m0, m0, 8192\n"
        "s_nop 0\n"
        "global_load_lds_dwordx4 v200, s[74:75]\n"
        "s_add_u32 s74, s82, 32768\n"
        "s_addc_u32 s75, s83, 0\n"
        "s_add_u32 m0, s78, s73\n"
        "s_add_u32 m0, m0, 2048\n"
        "s_nop 0\n"
        "global_load_lds_dwordx4 v201, s[74:75]\n"
        "s_add_u32 s74, s80, 14336\n"
        "s_addc_u32 s75, s81, 0\n"
        "s_add_u32 s74, s74, s68\n"
        "s_addc_u32 s75, s75, s69\n"
        "s_add_u32 m0, s78, s72\n"
        "s_add_u32 m0, m0, 9216\n"
        "s_nop 0\n"
        "global_load_lds_dwordx4 v200, s[74:75]\n"
        "s_add_u32 s74, s82, 49152\n"
        "s_addc_u32 s75, s83, 0\n"
        "s_add_u32 m0, s78, s73\n"
        "s_add_u32 m0, m0, 3072\n"
        "s_nop 0\n"
        "global_load_lds_dwordx4 v201, s[74:75]\n"
        "s_add_u32 s78, s78, 32768\n"
        "s_and_b32 s78, s78, 131071\n"
        "s_mov_b32 s71, 3\n"
        "s_cmp_lt_u32 s71, s70\n"
        "s_cselect_b32 s80, s60, s64\n"
        "s_cselect_b32 s81, s61, s65\n"
        "s_cselect_b32 s82, s62, s66\n"
        "s_cselect_b32 s83, s63, s67\n"
        "s_cselect_b32 s90, 0, s70\n"
        "s_sub_u32 s90, s71, s90\n"
        "s_mul_i32 s91, s90, 114688\n"
        "s_add_u32 s80, s80, s91\n"
        "s_addc_u32 s81, s81, 0\n"
        "s_lshl_b32 s91, s90, 6\n"
        "s_add_u32 s82, s82, s91\n"
        "s_addc_u32 s83, s83, 0\n"
        "s_add_u32 s74, s80, 0\n"
        "s_addc_u32 s75, s81, 0\n"
        "s_add_u32 m0, s78, s72\n"
        "s_add_u32 m0, m0, 0\n"
        "s_nop 0\n"
        "global_load_lds_dwordx4 v200, s[74:75]\n"
        "s_add_u32 s74, s82, 0\n"
        "s_addc_u32 s75, s83, 0\n"
        "s_add_u32 m0, s78, s73\n"
        "s_add_u32 m0, m0, 0\n"
        "s_nop 0\n"
        "global_load_lds_dwordx4 v201, s[74:75]\n"
        "s_add_u32 s74, s80, 14336\n"
        "s_addc_u32 s75, s81, 0\n"
        "s_add_u32 m0, s78, s72\n"
        "s_add_u32 m0, m0, 1024\n"
        "s_nop 0\n"
        "global_load_lds_dwordx4 v200, s[74:75]\n"
        "s_add_u32 s74, s82, 16384\n"
        "s_addc_u32 s75, s83, 0\n"
        "s_add_u32 m0, s78, s73\n"
        "s_add_u32 m0, m0, 1024\n"
        "s_nop 0\n"
        "global_load_lds_dwordx4 v201, s[74:75]\n"
        "s_add_u32 s74, s80, 0\n"
        "s_addc_u32 s75, s81, 0\n"
        "s_add_u32 s74, s74, s68\n"
        "s_addc_u32 s75, s75, s69\n"
        "s_add_u32 m0, s78, s72\n"
        "s_add_u32 m0, m0, 8192\n"
        "s_nop 0\n"
        "global_load_lds_dwordx4 v200, s[74:75]\n"
        "s_add_u32 s74, s82, 32768\n"
        "s_addc_u32 s75, s83, 0\n"
        "s_add_u32 m0, s78, s73\n"
        "s_add_u32 m0, m0, 2048\n"
        "s_nop 0\n"
        "global_load_lds_dwordx4 v201, s[74:75]\n"
        "s_add_u32 s74, s80, 14336\n"
        "s_addc_u32 s75, s81, 0\n"
        "s_add_u32 s74, s74, s68\n"
        "s_addc_u32 s75, s75, s69\n"
        "s_add_u32 m0, s78, s72\n"
        "s_add_u32 m0, m0, 9216\n"
        "s_nop 0\n"
        "global_load_lds_dwordx4 v200, s[74:75]\n"
        "s_add_u32 s74, s82, 49152\n"
        "s_addc_u32 s75, s83, 0\n"
        "s_add_u32 m0, s78, s73\n"
        "s_add_u32 m0, m0, 3072\n"
        "s_nop 0\n"
        "global_load_lds_dwordx4 v201, s[74:75]\n"
        "s_add_u32 s78, s78, 32768\n"
        "s_and_b32 s78, s78, 131071\n"
        "s_waitcnt vmcnt(24)\n"
        "s_mov_b32 s77, 0\n"
        "s_branch F144_GO_%=\n"
        "F144_PRE_%=:\n"
        "s_cmp_eq_u32 s77, 8\n"
        "s_cbranch_scc0 F144_PN8_%=\n"
        "s_waitcnt vmcnt(32)\n"
        "s_branch F144_GO_%=\n"
        "F144_PN8_%=:\n"
        "s_cmp_eq_u32 s77, 16\n"
        "s_cbranch_scc0 F144_PN16_%=\n"
        "s_waitcnt vmcnt(40)\n"
        "s_branch F144_GO_%=\n"
        "F144_PN16_%=:\n"
        "s_cmp_eq_u32 s77, 32\n"
        "s_cbranch_scc0 F144_PN32_%=\n"
        "s_waitcnt vmcnt(56)\n"
        "s_branch F144_GO_%=\n"
        "F144_PN32_%=:\n"
        "s_waitcnt vmcnt(24)\n"
        "F144_GO_%=:\n"
        "s_barrier\n"
        "v_add_u32 v192, s76, %24\n"
        "v_add_u32 v196, s76, %25\n"
        "v_xor_b32 v197, 32, v196\n"
        "v_xor_b32 v193, 64, v192\n"
        "v_xor_b32 v194, 0, v192\n"
        "v_add_u32 v194, 8192, v194\n"
        "v_xor_b32 v195, 64, v192\n"
        "v_add_u32 v195, 8192, v195\n"
        "ds_read_b64_tr_b16 v[128:129], v192 offset:0\n"
        "ds_read_b64_tr_b16 v[130:131], v192 offset:512\n"
        "ds_read_b64_tr_b16 v[132:133], v193 offset:0\n"
        "ds_read_b64_tr_b16 v[134:135], v193 offset:512\n"
        "ds_read_b64_tr_b16 v[136:137], v194 offset:0\n"
        "ds_read_b64_tr_b16 v[138:139], v194 offset:512\n"
        "ds_read_b64_tr_b16 v[140:141], v195 offset:0\n"
        "ds_read_b64_tr_b16 v[142:143], v195 offset:512\n"
        "ds_read_b128 v[160:163], v196 offset:16384\n"
        "ds_read_b128 v[164:167], v196 offset:18432\n"
        "ds_read_b128 v[168:171], v196 offset:20480\n"
        "ds_read_b128 v[172:175], v196 offset:22528\n"
        "s_mov_b32 s71, 0\n"
        "s_waitcnt lgkmcnt(0)\n"
        "v_mfma_f32_32x32x16_bf16 %0, v[128:131], v[160:163], 0\n"
        "ds_read_b64_tr_b16 v[144:145], v192 offset:4096\n"
        "v_mfma_f32_32x32x16_bf16 %1, v[128:131], v[164:167], 0\n"
        "ds_read_b64_tr_b16 v[146:147], v192 offset:4608\n"
        "v_mfma_f32_32x32x16_bf16 %2, v[128:131], v[168:171], 0\n"
        "ds_read_b64_tr_b16 v[148:149], v193 offset:4096\n"
        "v_mfma_f32_32x32x16_bf16 %3, v[128:131], v[172:175], 0\n"
        "ds_read_b64_tr_b16 v[150:151], v193 offset:4608\n"
        "v_mfma_f32_32x32x16_bf16 %4, v[132:135], v[160:163], 0\n"
        "ds_read_b64_tr_b16 v[152:153], v194 offset:4096\n"
        "v_mfma_f32_32x32x16_bf16 %5, v[132:135], v[164:167], 0\n"
        "ds_read_b64_tr_b16 v[154:155], v194 offset:4608\n"
        "v_mfma_f32_32x32x16_bf16 %6, v[132:135], v[168:171], 0\n"
        "ds_read_b64_tr_b16 v[156:157], v195 offset:4096\n"
        "v_mfma_f32_32x32x16_bf16 %7, v[132:135], v[172:175], 0\n"
        "ds_read_b64_tr_b16 v[158:159], v195 offset:4608\n"
        "v_mfma_f32_32x32x16_bf16 %8, v[136:139], v[160:163], 0\n"
        "ds_read_b128 v[176:179], v197 offset:16384\n"
        "v_mfma_f32_32x32x16_bf16 %9, v[136:139], v[164:167], 0\n"
        "ds_read_b128 v[180:183], v197 offset:18432\n"
        "v_mfma_f32_32x32x16_bf16 %10, v[136:139], v[168:171], 0\n"
        "ds_read_b128 v[184:187], v197 offset:20480\n"
        "v_mfma_f32_32x32x16_bf16 %11, v[136:139], v[172:175], 0\n"
        "ds_read_b128 v[188:191], v197 offset:22528\n"
        "v_mfma_f32_32x32x16_bf16 %12, v[140:143], v[160:163], 0\n"
        "s_add_u32 s92, s71, 4\n"
        "s_cmp_lt_u32 s92, s70\n"
        "s_cselect_b32 s86, s60, s64\n"
        "s_cselect_b32 s87, s61, s65\n"
        "s_cselect_b32 s88, s62, s66\n"
        "s_cselect_b32 s89, s63, s67\n"
        "s_cselect_b32 s90, 0, s70\n"
        "s_sub_u32 s90, s92, s90\n"
        "s_mul_i32 s91, s90, 114688\n"
        "s_add_u32 s86, s86, s91\n"
        "s_addc_u32 s87, s87, 0\n"
        "s_lshl_b32 s91, s90, 6\n"
        "s_add_u32 s88, s88, s91\n"
        "s_addc_u32 s89, s89, 0\n"
        "v_mfma_f32_32x32x16_bf16 %13, v[140:143], v[164:167], 0\n"
        "v_mfma_f32_32x32x16_bf16 %14, v[140:143], v[168:171], 0\n"
        "v_mfma_f32_32x32x16_bf16 %15, v[140:143], v[172:175], 0\n"
        "s_branch F144_MID_%=\n"
        "F144_LOOP_%=:\n"
        "s_waitcnt lgkmcnt(0)\n"
        "v_mfma_f32_32x32x16_bf16 %0, v[128:131], v[160:163], %0\n"
        "s_add_u32 s74, s80, 0\n"
        "s_addc_u32 s75, s81, 0\n"
        "s_add_u32 s74, s74, s68\n"
        "s_addc_u32 s75, s75, s69\n"
        "s_add_u32 m0, s78, s72\n"
        "s_add_u32 m0, m0, 8192\n"
        "s_nop 0\n"
        "global_load_lds_dwordx4 v200, s[74:75]\n"
        "ds_read_b64_tr_b16 v[144:145], v192 offset:4096\n"
        "v_mfma_f32_32x32x16_bf16 %1, v[128:131], v[164:167], %1\n"
        "ds_read_b64_tr_b16 v[146:147], v192 offset:4608\n"
        "v_mfma_f32_32x32x16_bf16 %2, v[128:131], v[168:171], %2\n"
        "ds_read_b64_tr_b16 v[148:149], v193 offset:4096\n"
        "v_mfma_f32_32x32x16_bf16 %3, v[128:131], v[172:175], %3\n"
        "ds_read_b64_tr_b16 v[150:151], v193 offset:4608\n"
        "v_mfma_f32_32x32x16_bf16 %4, v[132:135], v[160:163], %4\n"
        "s_add_u32 s74, s82, 32768\n"
        "s_addc_u32 s75, s83, 0\n"
        "s_add_u32 m0, s78, s73\n"
        "s_add_u32 m0, m0, 2048\n"
        "s_nop 0\n"
        "global_load_lds_dwordx4 v201, s[74:75]\n"
        "ds_read_b64_tr_b16 v[152:153], v194 offset:4096\n"
        "v_mfma_f32_32x32x16_bf16 %5, v[132:135], v[164:167], %5\n"
        "ds_read_b64_tr_b16 v[154:155], v194 offset:4608\n"
        "v_mfma_f32_32x32x16_bf16 %6, v[132:135], v[168:171], %6\n"
        "ds_read_b64_tr_b16 v[156:157], v195 offset:4096\n"
        "v_mfma_f32_32x32x16_bf16 %7, v[132:135], v[172:175], %7\n"
        "ds_read_b64_tr_b16 v[158:159], v195 offset:4608\n"
        "v_mfma_f32_32x32x16_bf16 %8, v[136:139], v[160:163], %8\n"
        "s_add_u32 s74, s80, 14336\n"
        "s_addc_u32 s75, s81, 0\n"
        "s_add_u32 s74, s74, s68\n"
        "s_addc_u32 s75, s75, s69\n"
        "s_add_u32 m0, s78, s72\n"
        "s_add_u32 m0, m0, 9216\n"
        "s_nop 0\n"
        "global_load_lds_dwordx4 v200, s[74:75]\n"
        "ds_read_b128 v[176:179], v197 offset:16384\n"
        "v_mfma_f32_32x32x16_bf16 %9, v[136:139], v[164:167], %9\n"
        "ds_read_b128 v[180:183], v197 offset:18432\n"
        "v_mfma_f32_32x32x16_bf16 %10, v[136:139], v[168:171], %10\n"
        "ds_read_b128 v[184:187], v197 offset:20480\n"
        "v_mfma_f32_32x32x16_bf16 %11, v[136:139], v[172:175], %11\n"
        "ds_read_b128 v[188:191], v197 offset:22528\n"
        "v_mfma_f32_32x32x16_bf16 %12, v[140:143], v[160:163], %12\n"
        "s_add_u32 s74, s82, 49152\n"
        "s_addc_u32 s75, s83, 0\n"
        "s_add_u32 m0, s78, s73\n"
        "s_add_u32 m0, m0, 3072\n"
        "s_nop 0\n"
        "global_load_lds_dwordx4 v201, s[74:75]\n"
        "s_add_u32 s92, s71, 4\n"
        "s_cmp_lt_u32 s92, s70\n"
        "s_cselect_b32 s86, s60, s64\n"
        "s_cselect_b32 s87, s61, s65\n"
        "s_cselect_b32 s88, s62, s66\n"
        "s_cselect_b32 s89, s63, s67\n"
        "s_cselect_b32 s90, 0, s70\n"
        "s_sub_u32 s90, s92, s90\n"
        "s_mul_i32 s91, s90, 114688\n"
        "s_add_u32 s86, s86, s91\n"
        "s_addc_u32 s87, s87, 0\n"
        "s_lshl_b32 s91, s90, 6\n"
        "s_add_u32 s88, s88, s91\n"
        "s_addc_u32 s89, s89, 0\n"
        "v_mfma_f32_32x32x16_bf16 %13, v[140:143], v[164:167], %13\n"
        "v_mfma_f32_32x32x16_bf16 %14, v[140:143], v[168:171], %14\n"
        "v_mfma_f32_32x32x16_bf16 %15, v[140:143], v[172:175], %15\n"
        "F144_MID_%=:\n"
        "s_cmp_lt_u32 s71, 4\n"
        "s_cbranch_scc1 F144_SPECIAL_%=\n"
        "s_waitcnt vmcnt(16) lgkmcnt(0)\n"
        "s_barrier\n"
        "F144_WGO_%=:\n"
        "s_mov_b64 s[80:81], s[86:87]\n"
        "s_mov_b64 s[82:83], s[88:89]\n"
        "s_mov_b32 s78, s76\n"
        "v_mfma_f32_32x32x16_bf16 %0, v[144:147], v[176:179], %0\n"
        "s_add_u32 s74, s80, 0\n"
        "s_addc_u32 s75, s81, 0\n"
        "s_add_u32 m0, s78, s72\n"
        "s_add_u32 m0, m0, 0\n"
        "s_nop 0\n"
        "global_load_lds_dwordx4 v200, s[74:75]\n"
        "s_add_u32 s79, s76, 32768\n"
        "s_and_b32 s79, s79, 131071\n"
        "v_mfma_f32_32x32x16_bf16 %1, v[144:147], v[180:183], %1\n"
        "v_add_u32 v192, s79, %24\n"
        "v_add_u32 v196, s79, %25\n"
        "v_mfma_f32_32x32x16_bf16 %2, v[144:147], v[184:187], %2\n"
        "v_xor_b32 v197, 32, v196\n"
        "v_xor_b32 v193, 64, v192\n"
        "v_mfma_f32_32x32x16_bf16 %3, v[144:147], v[188:191], %3\n"
        "v_xor_b32 v194, 0, v192\n"
        "v_add_u32 v194, 8192, v194\n"
        "v_mfma_f32_32x32x16_bf16 %4, v[148:151], v[176:179], %4\n"
        "s_add_u32 s74, s82, 0\n"
        "s_addc_u32 s75, s83, 0\n"
        "s_add_u32 m0, s78, s73\n"
        "s_add_u32 m0, m0, 0\n"
        "s_nop 0\n"
        "global_load_lds_dwordx4 v201, s[74:75]\n"
        "v_xor_b32 v195, 64, v192\n"
        "v_add_u32 v195, 8192, v195\n"
        "v_mfma_f32_32x32x16_bf16 %5, v[148:151], v[180:183], %5\n"
        "ds_read_b64_tr_b16 v[128:129], v192 offset:0\n"
        "ds_read_b64_tr_b16 v[130:131], v192 offset:512\n"
        "v_mfma_f32_32x32x16_bf16 %6, v[148:151], v[184:187], %6\n"
        "ds_read_b64_tr_b16 v[132:133], v193 offset:0\n"
        "ds_read_b64_tr_b16 v[134:135], v193 offset:512\n"
        "v_mfma_f32_32x32x16_bf16 %7, v[148:151], v[188:191], %7\n"
        "ds_read_b64_tr_b16 v[136:137], v194 offset:0\n"
        "ds_read_b64_tr_b16 v[138:139], v194 offset:512\n"
        "v_mfma_f32_32x32x16_bf16 %8, v[152:155], v[176:179], %8\n"
        "s_add_u32 s74, s80, 14336\n"
        "s_addc_u32 s75, s81, 0\n"
        "s_add_u32 m0, s78, s72\n"
        "s_add_u32 m0, m0, 1024\n"
        "s_nop 0\n"
        "global_load_lds_dwordx4 v200, s[74:75]\n"
        "ds_read_b64_tr_b16 v[140:141], v195 offset:0\n"
        "ds_read_b64_tr_b16 v[142:143], v195 offset:512\n"
        "v_mfma_f32_32x32x16_bf16 %9, v[152:155], v[180:183], %9\n"
        "ds_read_b128 v[160:163], v196 offset:16384\n"
        "ds_read_b128 v[164:167], v196 offset:18432\n"
        "v_mfma_f32_32x32x16_bf16 %10, v[152:155], v[184:187], %10\n"
        "ds_read_b128 v[168:171], v196 offset:20480\n"
        "ds_read_b128 v[172:175], v196 offset:22528\n"
        "v_mfma_f32_32x32x16_bf16 %11, v[152:155], v[188:191], %11\n"
        "s_add_u32 s76, s76, 32768\n"
        "s_and_b32 s76, s76, 131071\n"
        "v_mfma_f32_32x32x16_bf16 %12, v[156:159], v[176:179], %12\n"
        "s_add_u32 s74, s82, 16384\n"
        "s_addc_u32 s75, s83, 0\n"
        "s_add_u32 m0, s78, s73\n"
        "s_add_u32 m0, m0, 1024\n"
        "s_nop 0\n"
        "global_load_lds_dwordx4 v201, s[74:75]\n"
        "s_add_u32 s71, s71, 1\n"
        "v_mfma_f32_32x32x16_bf16 %13, v[156:159], v[180:183], %13\n"
        "v_mfma_f32_32x32x16_bf16 %14, v[156:159], v[184:187], %14\n"
        "v_mfma_f32_32x32x16_bf16 %15, v[156:159], v[188:191], %15\n"
        "s_cmp_lt_u32 s71, s70\n"
        "s_cbranch_scc1 F144_LOOP_%=\n"
        "s_branch F144_END_%=\n"
        "F144_SPECIAL_%=:\n"
        "s_waitcnt lgkmcnt(0)\n"
        "s_cmp_eq_u32 s71, 3\n"
        "s_cbranch_scc1 F144_SP3_%=\n"
        "s_cmp_eq_u32 s77, 8\n"
        "s_cbranch_scc0 F144_QN8_%=\n"
        "s_waitcnt vmcnt(24)\n"
        "s_barrier\n"
        "s_branch F144_WGO_%=\n"
        "F144_QN8_%=:\n"
        "s_cmp_eq_u32 s77, 16\n"
        "s_cbranch_scc0 F144_QN16_%=\n"
        "s_waitcnt vmcnt(32)\n"
        "s_barrier\n"
        "s_branch F144_WGO_%=\n"
        "F144_QN16_%=:\n"
        "s_cmp_eq_u32 s77, 32\n"
        "s_cbranch_scc0 F144_QN32_%=\n"
        "s_waitcnt vmcnt(48)\n"
        "s_barrier\n"
        "s_branch F144_WGO_%=\n"
        "F144_QN32_%=:\n"
        "s_waitcnt vmcnt(16)\n"
        "s_barrier\n"
        "s_branch F144_WGO_%=\n"
        "F144_SP3_%=:\n"
        "s_waitcnt vmcnt(16)\n"
        "s_barrier\n"
        "s_cmp_eq_u64 s[84:85], 0\n"
        "s_cbranch_scc1 F144_WGO_%=\n"
        "s_cmp_eq_u32 %21, 0\n"
        "s_cbranch_scc0 F144_WGO_%=\n"
        "s_bfe_u32 s90, %20, 0x40018\n"
        "v_mov_b32 v202, 0\n"
        "v_mov_b32 v203, s90\n"
        "s_mov_b64 exec, 1\n"
        "global_atomic_add v202, v203, s[84:85] sc1\n"
        "s_mov_b64 exec, -1\n"
        "s_branch F144_WGO_%=\n"
        "F144_END_%=:\n"
        "s_add_u32 s74, s80, 0\n"
        "s_addc_u32 s75, s81, 0\n"
        "s_add_u32 s74, s74, s68\n"
        "s_addc_u32 s75, s75, s69\n"
        "s_add_u32 m0, s78, s72\n"
        "s_add_u32 m0, m0, 8192\n"
        "s_nop 0\n"
        "global_load_lds_dwordx4 v200, s[74:75]\n"
        "s_add_u32 s74, s82, 32768\n"
        "s_addc_u32 s75, s83, 0\n"
        "s_add_u32 m0, s78, s73\n"
        "s_add_u32 m0, m0, 2048\n"
        "s_nop 0\n"
        "global_load_lds_dwordx4 v201, s[74:75]\n"
        "s_add_u32 s74, s80, 14336\n"
        "s_addc_u32 s75, s81, 0\n"
        "s_add_u32 s74, s74, s68\n"
        "s_addc_u32 s75, s75, s69\n"
        "s_add_u32 m0, s78, s72\n"
        "s_add_u32 m0, m0, 9216\n"
        "s_nop 0\n"
        "global_load_lds_dwordx4 v200, s[74:75]\n"
        "s_add_u32 s74, s82, 49152\n"
        "s_addc_u32 s75, s83, 0\n"
        "s_add_u32 m0, s78, s73\n"
        "s_add_u32 m0, m0, 3072\n"
        "s_nop 0\n"
        "global_load_lds_dwordx4 v201, s[74:75]\n"
        "s_waitcnt lgkmcnt(0)\n"
        "s_nop 15\n"
        : "=&a"(acc[0][0]), "=&a"(acc[0][1]), "=&a"(acc[0][2]), "=&a"(acc[0][3]), "=&a"(acc[1][0]), "=&a"(acc[1][1]), "=&a"(acc[1][2]), "=&a"(acc[1][3]), "=&a"(acc[2][0]), "=&a"(acc[2][1]), "=&a"(acc[2][2]), "=&a"(acc[2][3]), "=&a"(acc[3][0]), "=&a"(acc[3][1]), "=&a"(acc[3][2]), "=&a"(acc[3][3])
        : "s"(W), "s"(X), "s"(Wn), "s"(Xn), "s"(ctl), "s"(w), "v"(vW), "v"(vX), "v"(aw), "v"(ax), "s"(flag), "s"(dW)
        : "v128", "v129", "v130", "v131", "v132", "v133", "v134", "v135", "v136", "v137", "v138", "v139", "v140", "v141", "v142", "v143", "v144", "v145", "v146", "v147", "v148", "v149", "v150", "v151", "v152", "v153", "v154", "v155", "v156", "v157", "v158", "v159", "v160", "v161", "v162", "v163", "v164", "v165", "v166", "v167", "v168", "v169", "v170", "v171", "v172", "v173", "v174", "v175", "v176", "v177", "v178", "v179", "v180", "v181", "v182", "v183", "v184", "v185", "v186", "v187", "v188", "v189", "v190", "v191", "v192", "v193", "v194", "v195", "v196", "v197", "v198", "v199", "v200", "v201", "v202", "v203", "s60", "s61", "s62", "s63", "s64", "s65", "s66", "s67", "s68", "s69", "s70", "s71", "s72", "s73", "s74", "s75", "s76", "s77", "s78", "s79", "s80", "s81", "s82", "s83", "s84", "s85", "s86", "s87", "s88", "s89", "s90", "s91", "s92", "s93", "memory", "scc");
}

__device__ __forceinline__ void kloop_F222(f32x16 (&acc)[2][2], const char* W, const char* X, const char* Wn, const char* Xn, unsigned ctl, int w, unsigned vW, unsigned vX, unsigned aw, unsigned ax, unsigned* flag, ptrdiff_t dW) {
    asm volatile(
        "s_mov_b64 s[60:61], %4\n"
        "s_mov_b64 s[62:63], %5\n"
        "s_mov_b64 s[64:65], %6\n"
        "s_mov_b64 s[66:67], %7\n"
        "s_mov_b64 s[84:85], %14\n"
        "s_and_b32 s70, %8, 0xff\n"
        "s_bfe_u32 s76, %8, 0x2000a\n"
        "s_lshl_b32 s76, s76, 15\n"
        "s_bfe_u32 s77, %8, 0x80010\n"
        "s_mul_i32 s72, %9, 2048\n"
        "s_mul_i32 s73, %9, 2048\n"
        "s_add_u32 s73, s73, 8192\n"
        "v_mov_b32 v200, %10\n"
        "v_mov_b32 v201, %11\n"
        "s_bitcmp1_b32 %8, 8\n"
        "s_cbranch_scc1 F222_PRE_%=\n"
        "s_waitcnt vmcnt(0)\n"
        "s_barrier\n"
        "s_mov_b32 s78, s76\n"
        "s_mov_b32 s71, 0\n"
        "s_cmp_lt_u32 s71, s70\n"
        "s_cselect_b32 s80, s60, s64\n"
        "s_cselect_b32 s81, s61, s65\n"
        "s_cselect_b32 s82, s62, s66\n"
        "s_cselect_b32 s83, s63, s67\n"
        "s_cselect_b32 s90, 0, s70\n"
        "s_sub_u32 s90, s71, s90\n"
        "s_mul_i32 s91, s90, 32768\n"
        "s_add_u32 s80, s80, s91\n"
        "s_addc_u32 s81, s81, 0\n"
        "s_lshr_b32 s91, s90, 1\n"
        "s_mul_i32 s91, s91, 524288\n"
        "s_add_u32 s82, s82, s91\n"
        "s_addc_u32 s83, s83, 0\n"
        "s_lshr_b32 s91, s82, 21\n"
        "s_lshr_b32 s93, %8, 28\n"
        "s_xor_b32 s91, s91, s93\n"
        "s_and_b32 s91, s91, 1\n"
        "s_lshl_b32 s91, s91, 13\n"
        "s_and_b32 s90, s90, 1\n"
        "s_lshl_b32 s90, s90, 6\n"
        "s_add_u32 s91, s91, s90\n"
        "s_add_u32 s82, s82, s91\n"
        "s_addc_u32 s83, s83, 0\n"
        "s_add_u32 s74, s80, 0\n"
        "s_addc_u32 s75, s81, 0\n"
        "s_add_u32 m0, s78, s72\n"
        "s_add_u32 m0, m0, 0\n"
        "s_nop 0\n"
        "global_load_lds_dwordx4 v200, s[74:75]\n"
        "s_add_u32 s74, s82, 0\n"
        "s_addc_u32 s75, s83, 0\n"
        "s_add_u32 m0, s78, s73\n"
        "s_add_u32 m0, m0, 0\n"
        "s_nop 0\n"
        "global_load_lds_dwordx4 v201, s[74:75]\n"
        "s_add_u32 s74, s80, 4096\n"
        "s_addc_u32 s75, s81, 0\n"
        "s_add_u32 m0, s78, s72\n"
        "s_add_u32 m0, m0, 1024\n"
        "s_nop 0\n"
        "global_load_lds_dwordx4 v200, s[74:75]\n"
        "s_add_u32 s74, s82, 2048\n"
        "s_addc_u32 s75, s83, 0\n"
        "s_add_u32 m0, s78, s73\n"
        "s_add_u32 m0, m0, 1024\n"
        "s_nop 0\n"
        "global_load_lds_dwordx4 v201, s[74:75]\n"
        "s_add_u32 s78, s78, 32768\n"
        "s_and_b32 s78, s78, 131071\n"
        "s_mov_b32 s71, 1\n"
        "s_cmp_lt_u32 s71, s70\n"
        "s_cselect_b32 s80, s60, s64\n"
        "s_cselect_b32 s81, s61, s65\n"
        "s_cselect_b32 s82, s62, s66\n"
        "s_cselect_b32 s83, s63, s67\n"
        "s_cselect_b32 s90, 0, s70\n"
        "s_sub_u32 s90, s71, s90\n"
        "s_mul_i32 s91, s90, 32768\n"
        "s_add_u32 s80, s80, s91\n"
        "s_addc_u32 s81, s81, 0\n"
        "s_lshr_b32 s91, s90, 1\n"
        "s_mul_i32 s91, s91, 524288\n"
        "s_add_u32 s82, s82, s91\n"
        "s_addc_u32 s83, s83, 0\n"
        "s_lshr_b32 s91, s82, 21\n"
        "s_lshr_b32 s93, %8, 28\n"
        "s_xor_b32 s91, s91, s93\n"
        "s_and_b32 s91, s91, 1\n"
        "s_lshl_b32 s91, s91, 13\n"
        "s_and_b32 s90, s90, 1\n"
        "s_lshl_b32 s90, s90, 6\n"
        "s_add_u32 s91, s91, s90\n"
        "s_add_u32 s82, s82, s91\n"
        "s_addc_u32 s83, s83, 0\n"
        "s_add_u32 s74, s80, 0\n"
        "s_addc_u32 s75, s81, 0\n"
        "s_add_u32 m0, s78, s72\n"
        "s_add_u32 m0, m0, 0\n"
        "s_nop 0\n"
        "global_load_lds_dwordx4 v200, s[74:75]\n"
        "s_add_u32 s74, s82, 0\n"
        "s_addc_u32 s75, s83, 0\n"
        "s_add_u32 m0, s78, s73\n"
        "s_add_u32 m0, m0, 0\n"
        "s_nop 0\n"
        "global_load_lds_dwordx4 v201, s[74:75]\n"
        "s_add_u32 s74, s80, 4096\n"
        "s_addc_u32 s75, s81, 0\n"
        "s_add_u32 m0, s78, s72\n"
        "s_add_u32 m0, m0, 1024\n"
        "s_nop 0\n"
        "global_load_lds_dwordx4 v200, s[74:75]\n"
        "s_add_u32 s74, s82, 2048\n"
        "s_addc_u32 s75, s83, 0\n"
        "s_add_u32 m0, s78, s73\n"
        "s_add_u32 m0, m0, 1024\n"
        "s_nop 0\n"
        "global_load_lds_dwordx4 v201, s[74:75]\n"
        "s_add_u32 s78, s78, 32768\n"
        "s_and_b32 s78, s78, 131071\n"
        "s_mov_b32 s71, 2\n"
        "s_cmp_lt_u32 s71, s70\n"
        "s_cselect_b32 s80, s60, s64\n"
        "s_cselect_b32 s81, s61, s65\n"
        "s_cselect_b32 s82, s62, s66\n"
        "s_cselect_b32 s83, s63, s67\n"
        "s_cselect_b32 s90, 0, s70\n"
        "s_sub_u32 s90, s71, s90\n"
        "s_mul_i32 s91, s90, 32768\n"
        "s_add_u32 s80, s80, s91\n"
        "s_addc_u32 s81, s81, 0\n"
        "s_lshr_b32 s91, s90, 1\n"
        "s_mul_i32 s91, s91, 524288\n"
        "s_add_u32 s82, s82, s91\n"
        "s_addc_u32 s83, s83, 0\n"
        "s_lshr_b32 s91, s82, 21\n"
        "s_lshr_b32 s93, %8, 28\n"
        "s_xor_b32 s91, s91, s93\n"
        "s_and_b32 s91, s91, 1\n"
        "s_lshl_b32 s91, s91, 13\n"
        "s_and_b32 s90, s90, 1\n"
        "s_lshl_b32 s90, s90, 6\n"
        "s_add_u32 s91, s91, s90\n"
        "s_add_u32 s82, s82, s91\n"
        "s_addc_u32 s83, s83, 0\n"
        "s_add_u32 s74, s80, 0\n"
        "s_addc_u32 s75, s81, 0\n"
        "s_add_u32 m0, s78, s72\n"
        "s_add_u32 m0, m0, 0\n"
        "s_nop 0\n"
        "global_load_lds_dwordx4 v200, s[74:75]\n"
        "s_add_u32 s74, s82, 0\n"
        "s_addc_u32 s75, s83, 0\n"
        "s_add_u32 m0, s78, s73\n"
        "s_add_u32 m0, m0, 0\n"
        "s_nop 0\n"
        "global_load_lds_dwordx4 v201, s[74:75]\n"
        "s_add_u32 s74, s80, 4096\n"
        "s_addc_u32 s75, s81, 0\n"
        "s_add_u32 m0, s78, s72\n"
        "s_add_u32 m0, m0, 1024\n"
        "s_nop 0\n"
        "global_load_lds_dwordx4 v200, s[74:75]\n"
        "s_add_u32 s74, s82, 2048\n"
        "s_addc_u32 s75, s83, 0\n"
        "s_add_u32 m0, s78, s73\n"
        "s_add_u32 m0, m0, 1024\n"
        "s_nop 0\n"
        "global_load_lds_dwordx4 v201, s[74:75]\n"
        "s_add_u32 s78, s78, 32768\n"
        "s_and_b32 s78, s78, 131071\n"
        "s_mov_b32 s71, 3\n"
        "s_cmp_lt_u32 s71, s70\n"
        "s_cselect_b32 s80, s60, s64\n"
        "s_cselect_b32 s81, s61, s65\n"
        "s_cselect_b32 s82, s62, s66\n"
        "s_cselect_b32 s83, s63, s67\n"
        "s_cselect_b32 s90, 0, s70\n"
        "s_sub_u32 s90, s71, s90\n"
        "s_mul_i32 s91, s90, 32768\n"
        "s_add_u32 s80, s80, s91\n"
        "s_addc_u32 s81, s81, 0\n"
        "s_lshr_b32 s91, s90, 1\n"
        "s_mul_i32 s91, s91, 524288\n"
        "s_add_u32 s82, s82, s91\n"
        "s_addc_u32 s83, s83, 0\n"
        "s_lshr_b32 s91, s82, 21\n"
        "s_lshr_b32 s93, %8, 28\n"
        "s_xor_b32 s91, s91, s93\n"
        "s_and_b32 s91, s91, 1\n"
        "s_lshl_b32 s91, s91, 13\n"
        "s_and_b32 s90, s90, 1\n"
        "s_lshl_b32 s90, s90, 6\n"
        "s_add_u32 s91, s91, s90\n"
        "s_add_u32 s82, s82, s91\n"
        "s_addc_u32 s83, s83, 0\n"
        "s_add_u32 s74, s80, 0\n"
        "s_addc_u32 s75, s81, 0\n"
        "s_add_u32 m0, s78, s72\n"
        "s_add_u32 m0, m0, 0\n"
        "s_nop 0\n"
        "global_load_lds_dwordx4 v200, s[74:75]\n"
        "s_add_u32 s74, s82, 0\n"
        "s_addc_u32 s75, s83, 0\n"
        "s_add_u32 m0, s78, s73\n"
        "s_add_u32 m0, m0, 0\n"
        "s_nop 0\n"
        "global_load_lds_dwordx4 v201, s[74:75]\n"
        "s_add_u32 s74, s80, 4096\n"
        "s_addc_u32 s75, s81, 0\n"
        "s_add_u32 m0, s78, s72\n"
        "s_add_u32 m0, m0, 1024\n"
        "s_nop 0\n"
        "global_load_lds_dwordx4 v200, s[74:75]\n"
        "s_add_u32 s74, s82, 2048\n"
        "s_addc_u32 s75, s83, 0\n"
        "s_add_u32 m0, s78, s73\n"
        "s_add_u32 m0, m0, 1024\n"
        "s_nop 0\n"
        "global_load_lds_dwordx4 v201, s[74:75]\n"
        "s_add_u32 s78, s78, 32768\n"
        "s_and_b32 s78, s78, 131071\n"
        "s_waitcnt vmcnt(12)\n"
        "s_mov_b32 s77, 0\n"
        "s_branch F222_GO_%=\n"
        "F222_PRE_%=:\n"
        "s_cmp_eq_u32 s77, 8\n"
        "s_cbranch_scc0 F222_PN8_%=\n"
        "s_waitcnt vmcnt(20)\n"
        "s_branch F222_GO_%=\n"
        "F222_PN8_%=:\n"
        "s_cmp_eq_u32 s77, 16\n"
        "s_cbranch_scc0 F222_PN16_%=\n"
        "s_waitcnt vmcnt(28)\n"
        "s_branch F222_GO_%=\n"
        "F222_PN16_%=:\n"
        "s_cmp_eq_u32 s77, 32\n"
        "s_cbranch_scc0 F222_PN32_%=\n"
        "s_waitcnt vmcnt(44)\n"
        "s_branch F222_GO_%=\n"
        "F222_PN32_%=:\n"
        "s_waitcnt vmcnt(12)\n"
        "F222_GO_%=:\n"
        "s_barrier\n"
        "v_add_u32 v192, s76, %12\n"
        "v_add_u32 v196, s76, %13\n"
        "v_xor_b32 v197, 32, v196\n"
        "v_xor_b32 v193, 64, v192\n"
        "ds_read_b64_tr_b16 v[128:129], v192 offset:0\n"
        "ds_read_b64_tr_b16 v[130:131], v192 offset:1024\n"
        "ds_read_b64_tr_b16 v[132:133], v193 offset:0\n"
        "ds_read_b64_tr_b16 v[134:135], v193 offset:1024\n"
        "ds_read_b128 v[160:163], v196 offset:8192\n"
        "ds_read_b128 v[164:167], v196 offset:10240\n"
        "s_mov_b32 s71, 0\n"
        "s_waitcnt lgkmcnt(0)\n"
        "v_mfma_f32_32x32x16_bf16 %0, v[128:131], v[160:163], 0\n"
        "ds_read_b64_tr_b16 v[144:145], v192 offset:4096\n"
        "ds_read_b64_tr_b16 v[146:147], v192 offset:5120\n"
        "ds_read_b64_tr_b16 v[148:149], v193 offset:4096\n"
        "v_mfma_f32_32x32x16_bf16 %1, v[128:131], v[164:167], 0\n"
        "ds_read_b64_tr_b16 v[150:151], v193 offset:5120\n"
        "ds_read_b128 v[176:179], v197 offset:8192\n"
        "ds_read_b128 v[180:183], v197 offset:10240\n"
        "v_mfma_f32_32x32x16_bf16 %2, v[132:135], v[160:163], 0\n"
        "s_add_u32 s92, s71, 4\n"
        "s_cmp_lt_u32 s92, s70\n"
        "s_cselect_b32 s86, s60, s64\n"
        "s_cselect_b32 s87, s61, s65\n"
        "s_cselect_b32 s88, s62, s66\n"
        "s_cselect_b32 s89, s63, s67\n"
        "s_cselect_b32 s90, 0, s70\n"
        "s_sub_u32 s90, s92, s90\n"
        "s_mul_i32 s91, s90, 32768\n"
        "s_add_u32 s86, s86, s91\n"
        "s_addc_u32 s87, s87, 0\n"
        "s_lshr_b32 s91, s90, 1\n"
        "s_mul_i32 s91, s91, 524288\n"
        "s_add_u32 s88, s88, s91\n"
        "s_addc_u32 s89, s89, 0\n"
        "s_lshr_b32 s91, s88, 21\n"
        "s_lshr_b32 s93, %8, 28\n"
        "s_xor_b32 s91, s91, s93\n"
        "s_and_b32 s91, s91, 1\n"
        "s_lshl_b32 s91, s91, 13\n"
        "s_and_b32 s90, s90, 1\n"
        "s_lshl_b32 s90, s90, 6\n"
        "s_add_u32 s91, s91, s90\n"
        "s_add_u32 s88, s88, s91\n"
        "s_addc_u32 s89, s89, 0\n"
        "v_mfma_f32_32x32x16_bf16 %3, v[132:135], v[164:167], 0\n"
        "s_branch F222_MID_%=\n"
        "F222_LOOP_%=:\n"
        "s_waitcnt lgkmcnt(0)\n"
        "v_mfma_f32_32x32x16_bf16 %0, v[128:131], v[160:163], %0\n"
        "s_add_u32 s74, s80, 4096\n"
        "s_addc_u32 s75, s81, 0\n"
        "s_add_u32 m0, s78, s72\n"
        "s_add_u32 m0, m0, 1024\n"
        "s_nop 0\n"
        "global_load_lds_dwordx4 v200, s[74:75]\n"
        "ds_read_b64_tr_b16 v[144:145], v192 offset:4096\n"
        "ds_read_b64_tr_b16 v[146:147], v192 offset:5120\n"
        "ds_read_b64_tr_b16 v[148:149], v193 offset:4096\n"
        "v_mfma_f32_32x32x16_bf16 %1, v[128:131], v[164:167], %1\n"
        "ds_read_b64_tr_b16 v[150:151], v193 offset:5120\n"
        "ds_read_b128 v[176:179], v197 offset:8192\n"
        "ds_read_b128 v[180:183], v197 offset:10240\n"
        "v_mfma_f32_32x32x16_bf16 %2, v[132:135], v[160:163], %2\n"
        "s_add_u32 s74, s82, 2048\n"
        "s_addc_u32 s75, s83, 0\n"
        "s_add_u32 m0, s78, s73\n"
        "s_add_u32 m0, m0, 1024\n"
        "s_nop 0\n"
        "global_load_lds_dwordx4 v201, s[74:75]\n"
        "s_add_u32 s92, s71, 4\n"
        "s_cmp_lt_u32 s92, s70\n"
        "s_cselect_b32 s86, s60, s64\n"
        "s_cselect_b32 s87, s61, s65\n"
        "s_cselect_b32 s88, s62, s66\n"
        "s_cselect_b32 s89, s63, s67\n"
        "s_cselect_b32 s90, 0, s70\n"
        "s_sub_u32 s90, s92, s90\n"
        "s_mul_i32 s91, s90, 32768\n"
        "s_add_u32 s86, s86, s91\n"
        "s_addc_u32 s87, s87, 0\n"
        "s_lshr_b32 s91, s90, 1\n"
        "s_mul_i32 s91, s91, 524288\n"
        "s_add_u32 s88, s88, s91\n"
        "s_addc_u32 s89, s89, 0\n"
        "s_lshr_b32 s91, s88, 21\n"
        "s_lshr_b32 s93, %8, 28\n"
        "s_xor_b32 s91, s91, s93\n"
        "s_and_b32 s91, s91, 1\n"
        "s_lshl_b32 s91, s91, 13\n"
        "s_and_b32 s90, s90, 1\n"
        "s_lshl_b32 s90, s90, 6\n"
        "s_add_u32 s91, s91, s90\n"
        "s_add_u32 s88, s88, s91\n"
        "s_addc_u32 s89, s89, 0\n"
        "v_mfma_f32_32x32x16_bf16 %3, v[132:135], v[164:167], %3\n"
        "F222_MID_%=:\n"
        "s_cmp_lt_u32 s71, 4\n"
        "s_cbranch_scc1 F222_SPECIAL_%=\n"
        "s_waitcnt vmcnt(8) lgkmcnt(0)\n"
        "s_barrier\n"
        "F222_WGO_%=:\n"
        "s_mov_b64 s[80:81], s[86:87]\n"
        "s_mov_b64 s[82:83], s[88:89]\n"
        "s_mov_b32 s78, s76\n"
        "v_mfma_f32_32x32x16_bf16 %0, v[144:147], v[176:179], %0\n"
        "s_add_u32 s74, s80, 0\n"
        "s_addc_u32 s75, s81, 0\n"
        "s_add_u32 m0, s78, s72\n"
        "s_add_u32 m0, m0, 0\n"
        "s_nop 0\n"
        "global_load_lds_dwordx4 v200, s[74:75]\n"
        "s_add_u32 s79, s76, 32768\n"
        "s_and_b32 s79, s79, 131071\n"
        "v_add_u32 v192, s79, %12\n"
        "v_add_u32 v196, s79, %13\n"
        "v_xor_b32 v197, 32, v196\n"
        "v_mfma_f32_32x32x16_bf16 %1, v[144:147], v[180:183], %1\n"
        "v_xor_b32 v193, 64, v192\n"
        "ds_read_b64_tr_b16 v[128:129], v192 offset:0\n"
        "ds_read_b64_tr_b16 v[130:131], v192 offset:1024\n"
        "ds_read_b64_tr_b16 v[132:133], v193 offset:0\n"
        "ds_read_b64_tr_b16 v[134:135], v193 offset:1024\n"
        "v_mfma_f32_32x32x16_bf16 %2, v[148:151], v[176:179], %2\n"
        "s_add_u32 s74, s82, 0\n"
        "s_addc_u32 s75, s83, 0\n"
        "s_add_u32 m0, s78, s73\n"
        "s_add_u32 m0, m0, 0\n"
        "s_nop 0\n"
        "global_load_lds_dwordx4 v201, s[74:75]\n"
        "ds_read_b128 v[160:163], v196 offset:8192\n"
        "ds_read_b128 v[164:167], v196 offset:10240\n"
        "s_add_u32 s76, s76, 32768\n"
        "s_and_b32 s76, s76, 131071\n"
        "s_add_u32 s71, s71, 1\n"
        "v_mfma_f32_32x32x16_bf16 %3, v[148:151], v[180:183], %3\n"
        "s_cmp_lt_u32 s71, s70\n"
        "s_cbranch_scc1 F222_LOOP_%=\n"
        "s_branch F222_END_%=\n"
        "F222_SPECIAL_%=:\n"
        "s_waitcnt lgkmcnt(0)\n"
        "s_cmp_eq_u32 s71, 3\n"
        "s_cbranch_scc1 F222_SP3_%=\n"
        "s_cmp_eq_u32 s77, 8\n"
        "s_cbranch_scc0 F222_QN8_%=\n"
        "s_waitcnt vmcnt(16)\n"
        "s_barrier\n"
        "s_branch F222_WGO_%=\n"
        "F222_QN8_%=:\n"
        "s_cmp_eq_u32 s77, 16\n"
        "s_cbranch_scc0 F222_QN16_%=\n"
        "s_waitcnt vmcnt(24)\n"
        "s_barrier\n"
        "s_branch F222_WGO_%=\n"
        "F222_QN16_%=:\n"
        "s_cmp_eq_u32 s77, 32\n"
        "s_cbranch_scc0 F222_QN32_%=\n"
        "s_waitcnt vmcnt(40)\n"
        "s_barrier\n"
        "s_branch F222_WGO_%=\n"
        "F222_QN32_%=:\n"
        "s_waitcnt vmcnt(8)\n"
        "s_barrier\n"
        "s_branch F222_WGO_%=\n"
        "F222_SP3_%=:\n"
        "s_waitcnt vmcnt(8)\n"
        "s_barrier\n"
        "s_cmp_eq_u64 s[84:85], 0\n"
        "s_cbranch_scc1 F222_WGO_%=\n"
        "s_cmp_eq_u32 %9, 0\n"
        "s_cbranch_scc0 F222_WGO_%=\n"
        "s_bfe_u32 s90, %8, 0x40018\n"
        "v_mov_b32 v202, 0\n"
        "v_mov_b32 v203, s90\n"
        "s_mov_b64 exec, 1\n"
        "global_atomic_add v202, v203, s[84:85] sc1\n"
        "s_mov_b64 exec, -1\n"
        "s_branch F222_WGO_%=\n"
        "F222_END_%=:\n"
        "s_add_u32 s74, s80, 4096\n"
        "s_addc_u32 s75, s81, 0\n"
        "s_add_u32 m0, s78, s72\n"
        "s_add_u32 m0, m0, 1024\n"
        "s_nop 0\n"
        "global_load_lds_dwordx4 v200, s[74:75]\n"
        "s_add_u32 s74, s82, 2048\n"
        "s_addc_u32 s75, s83, 0\n"
        "s_add_u32 m0, s78, s73\n"
        "s_add_u32 m0, m0, 1024\n"
        "s_nop 0\n"
        "global_load_lds_dwordx4 v201, s[74:75]\n"
        "s_waitcnt lgkmcnt(0)\n"
        "s_nop 15\n"
        : "=&a"(acc[0][0]), "=&a"(acc[0][1]), "=&a"(acc[1][0]), "=&a"(acc[1][1])
        : "s"(W), "s"(X), "s"(Wn), "s"(Xn), "s"(ctl), "s"(w), "v"(vW), "v"(vX), "v"(aw), "v"(ax), "s"(flag)
        : "v128", "v129", "v130", "v131", "v132", "v133", "v134", "v135", "v136", "v137", "v138", "v139", "v140", "v141", "v142", "v143", "v144", "v145", "v146", "v147", "v148", "v149", "v150", "v151", "v152", "v153", "v154", "v155", "v156", "v157", "v158", "v159", "v160", "v161", "v162", "v163", "v164", "v165", "v166", "v167", "v168", "v169", "v170", "v171", "v172", "v173", "v174", "v175", "v176", "v177", "v178", "v179", "v180", "v181", "v182", "v183", "v184", "v185", "v186", "v187", "v188", "v189", "v190", "v191", "v192", "v193", "v194", "v195", "v196", "v197", "v198", "v199", "v200", "v201", "v202", "v203", "s60", "s61", "s62", "s63", "s64", "s65", "s66", "s67", "s68", "s69", "s70", "s71", "s72", "s73", "s74", "s75", "s76", "s77", "s78", "s79", "s80", "s81", "s82", "s83", "s84", "s85", "s86", "s87", "s88", "s89", "s90", "s91", "s92", "s93", "memory", "scc");
}

template <int TF, int TT, int EPI>
struct Gemm {
    static constexpr bool PERMK = (EPI != EPI_F2);
    static constexpr int NREG = (EPI == EPI_F1) ? 2 : 1;
    static constexpr int TFR = TF / NREG;
    static constexpr int WROW = 128 * TFR;
    static constexpr int RPI = 1024 / WROW;
    static constexpr int NWR = 32 * WROW / 4096;
    static constexpr int NX = 64 * TT * 64 / 4096;
    static constexpr int NSTORE = (EPI == EPI_QKV) ? TF * TT * 2 : (EPI == EPI_F1) ? (TF / 2) * TT * 2 : TF * TT * 2;

    static __device__ __forceinline__ void epilogue(const GTask& t, f32x16 (&acc)[TF][TT], int w, int lane) {
        const int wf = w >> 1, wt = w & 1, hi = lane >> 5, tl = lane & 31;
        if (EPI == EPI_QKV) {
#pragma unroll
            for (int ft = 0; ft < TF; ++ft) {
                const int feat = t.f0 + wf * 32 * TF + ft * 32;
                const int head = feat >> 7, kk = (feat >> 5) & 3;
#pragma unroll
                for (int tt = 0; tt < TT; ++tt) {
                    const int tok = t.tok0 + wt * 32 * TT + tt * 32;
                    char* base = t.out + ((size_t)(tok >> 5) * 32 + head) * 16384 + ((((tok >> 7) ^ t.far) & 1) << 13) + ((tl >> 4) * 4 + kk) * 1024 + (tl & 15) * 16;
                    const f32x16& a = acc[ft][tt];
                    v4u v0 = {pk2(a[0], a[1]), pk2(a[2], a[3]), pk2(a[8], a[9]), pk2(a[10], a[11])};
                    v4u v1 = {pk2(a[4], a[5]), pk2(a[6], a[7]), pk2(a[12], a[13]), pk2(a[14], a[15])};
                    st16_sc1(base + hi * 256, v0);
                    st16_sc1(base + (2 + hi) * 256, v1);
                }
            }
        } else if (EPI == EPI_F1) {
#pragma unroll
            for (int ft = 0; ft < TF / 2; ++ft) {
                const int fcol = t.f0 + wf * (16 * TF) + ft * 32;
#pragma unroll
                for (int tt = 0; tt < TT; ++tt) {
                    const int tok = t.tok0 + wt * 32 * TT + tt * 32 + tl;
                    const f32x16& g = acc[ft][tt]; const f32x16& u = acc[ft + TF / 2][tt];
                    unsigned d[8];
#pragma unroll
                    for (int j = 0; j < 8; ++j) {
                        unsigned gp = pk2(g[2 * j], g[2 * j + 1]), up = pk2(u[2 * j], u[2 * j + 1]);
                        float g0 = __uint_as_float(gp << 16), g1 = __uint_as_float(gp & 0xffff0000u), u0 = __uint_as_float(up << 16), u1 = __uint_as_float(up & 0xffff0000u);
                        g0 = g0 > 0.f ? g0 : g0 * 0.f; g1 = g1 > 0.f ? g1 : g1 * 0.f;
                        d[j] = pk2(g0 * u0, g1 * u1);
                    }
                    const int blk = (fcol >> 6) * 32 + (tok >> 6);
                    char* base = t.out + (size_t)blk * 16384 + ((((blk >> 7) ^ t.far) & 1) << 13) + (tok & 63) * 128 + (fcol & 63) * 2;
#pragma unroll
                    for (int pr = 0; pr < 2; ++pr) {
                        auto s0 = __builtin_amdgcn_permlane32_swap(d[4 * pr], d[4 * pr + 2], false, false);
                        auto s1 = __builtin_amdgcn_permlane32_swap(d[4 * pr + 1], d[4 * pr + 3], false, false);
                        v4u v = {s0[0], s1[0], s0[1], s1[1]};
                        st16_sc1(base + (2 * pr + hi) * 16, v);
                    }
                }
            }
        } else {
#pragma unroll
            for (int ft = 0; ft < TF; ++ft) {
                const int fcol = t.f0 + wf * 32 * TF + ft * 32;
#pragma unroll
                for (int tt = 0; tt < TT; ++tt) {
                    const int tok = t.tok0 + wt * 32 * TT + tt * 32 + tl;
                    const f32x16& a = acc[ft][tt];
                    unsigned d[8];
#pragma unroll
                    for (int j = 0; j < 8; ++j) d[j] = pk2(a[2 * j], a[2 * j + 1]);
                    const int blk = 896 + (tok >> 3);
                    char* base = t.out + (size_t)blk * 16384 + ((((blk >> 7) ^ t.far) & 1) << 13) + (tok & 7) * 1024 + fcol * 2;
#pragma unroll
                    for (int pr = 0; pr < 2; ++pr) {
                        auto s0 = __builtin_amdgcn_permlane32_swap(d[4 * pr], d[4 * pr + 2], false, false);
                        auto s1 = __builtin_amdgcn_permlane32_swap(d[4 * pr + 1], d[4 * pr + 3], false, false);
                        v4u v = {s0[0], s1[0], s0[1], s1[1]};
                        st16_sc1(base + (2 * pr + hi) * 16, v);
                    }
                }
            }
        }
    }

    static __device__ __forceinline__ void run(const GTask& t, bool pre, int pend, bool hasnext, const char* nW, const char* nX, int& par, int w, int lane, unsigned* flag_prev, unsigned inc_prev) {
        constexpr int LPR = 64 / RPI;
        const int rl = lane / LPR, sl = lane % LPR;
        const int f = PERMK ? ((rl & 1) | ((w & 1) << 1)) : rl;
        const unsigned vW = rl * t.ldw + ((sl ^ (4 * f)) * 16);
        const unsigned vX = (lane >> 2) * t.ldx + (((lane & 3) ^ ((lane >> 4) & 3)) * 16);
        const int wf = w >> 1, wt = w & 1;
        const int hi = lane >> 5, blk = (lane >> 4) & 1, q = (lane & 15) >> 2, p = lane & 3;
        const int row0 = PERMK ? (4 * hi + (q & 1) + 8 * (q >> 1)) : (8 * hi + q);
        const unsigned aw = row0 * WROW + (4 * ((TFR * wf) ^ q) + 2 * blk + (p >> 1)) * 16 + 8 * (p & 1);
        const int tl = lane & 31;
        const unsigned ax = (32 * TT * wt + tl) * 64 + ((hi ^ ((tl >> 2) & 3)) * 16);
        const size_t wo = (size_t)w * NWR * RPI * t.ldw, xo = (EPI == EPI_F2) ? (size_t)(w >> 1) * 16384 + (w & 1) * 4096 : (size_t)w * NX * 16 * t.ldx;
        const unsigned cfx = (EPI == EPI_F2) ? (unsigned)((t.far ^ (int)((uintptr_t)t.out >> 21)) & 1) : 0u;
        const unsigned ctl = (2 * t.nk) | (pre ? 0x100u : 0u) | (par << 10) | (pend << 16) | (inc_prev << 24) | (cfx << 28);
        if (!hasnext) { nW = t.W0; nX = t.X; }
        f32x16 acc[TF][TT];
        if constexpr (EPI == EPI_QKV && TF == 4) kloop_QKV44(acc, uni(t.W0 + wo), uni(t.X + xo), uni(nW + wo), uni(nX + xo), __builtin_amdgcn_readfirstlane(ctl), w, vW, vX, aw, ax, uni(flag_prev), 0);
        else if constexpr (EPI == EPI_QKV) kloop_QKV22(acc, uni(t.W0 + wo), uni(t.X + xo), uni(nW + wo), uni(nX + xo), __builtin_amdgcn_readfirstlane(ctl), w, vW, vX, aw, ax, uni(flag_prev), 0);
        else if constexpr (EPI == EPI_F1) kloop_F144(acc, uni(t.W0 + wo), uni(t.X + xo), uni(nW + wo), uni(nX + xo), __builtin_amdgcn_readfirstlane(ctl), w, vW, vX, aw, ax, uni(flag_prev), (ptrdiff_t)uni((const char*)t.dW));
        else kloop_F222(acc, uni(t.W0 + wo), uni(t.X + xo), uni(nW + wo), uni(nX + xo), __builtin_amdgcn_readfirstlane(ctl), w, vW, vX, aw, ax, uni(flag_prev), 0);
        par = (par + 2 * t.nk) & 3;
        epilogue(t, acc, w, lane);
    }
};

__global__ void __launch_bounds__(256) k_prep(Params p) {
    const int b = blockIdx.x, tid = threadIdx.x, lane = tid & 63, w = tid >> 6;
    for (int row = b * 4 + w; row < 2048; row += 1024) {
        v4u v = *(const v4u*)(p.x + row * 1024 + lane * 16);
        float f[8]; f[0] = __uint_as_float(v.x << 16); f[1] = __uint_as_float(v.x & 0xffff0000u); f[2] = __uint_as_float(v.y << 16); f[3] = __uint_as_float(v.y & 0xffff0000u);
        f[4] = __uint_as_float(v.z << 16); f[5] = __uint_as_float(v.z & 0xffff0000u); f[6] = __uint_as_float(v.w << 16); f[7] = __uint_as_float(v.w & 0xffff0000u);
        float s = 0;
#pragma unroll
        for (int j = 0; j < 8; ++j) s += f[j];
#pragma unroll
        for (int m = 1; m < 64; m <<= 1) s += __shfl_xor(s, m);
        const float mean = s * (1.0f / 512);
        float q = 0;
#pragma unroll
        for (int j = 0; j < 8; ++j) { const float d = f[j] - mean; q += d * d; }
#pragma unroll
        for (int m = 1; m < 64; m <<= 1) q += __shfl_xor(q, m);
        const float var = rbf(q * (1.0f / 512)), mb = rbf(mean), den = rbf(sqrtf(rbf(var + 1e-5f)));
        char* dst = p.xn + row * 1024 + (lane >> 1) * 32 + (lane & 1) * 4;
#pragma unroll
        for (int j = 0; j < 4; ++j) { const u16 lo = f2bf(rbf(f[2*j] - mb) / den), hi = f2bf(rbf(f[2*j+1] - mb) / den); *(unsigned*)(dst + j * 8) = lo | ((unsigned)hi << 16); }
    }
    const v4u ones = {~0u, ~0u, ~0u, ~0u};
    for (int i = b * 256 + tid; i < 2 * 64 * 512; i += 65536) { const int s = 1 + (i >> 10), o = i & 1023; *(v4u*)(p.memb + (size_t)s * 16384 + o * 16) = ones; }
    for (int i = b * 256 + tid; i < 2 * 64 * 512; i += 65536) { const int s = i >> 10, o = i & 1023; *(v4u*)(p.part + (size_t)s * 16384 + o * 16) = ones; }
    if (b == 1) for (int i = tid; i < 1024; i += 256) {
        const int t = (i >> 6) & 7, o = i & 63, col = o * 8, cb = col >> 5;
        v4u v = *(const v4u*)(p.mem0 + t * 1024 + col * 2);
        v.x = min(v.x, 0xFFFFFFFEu); v.y = min(v.y, 0xFFFFFFFEu); v.z = min(v.z, 0xFFFFFFFEu); v.w = min(v.w, 0xFFFFFFFEu);
        *(v4u*)(p.memb + (i >> 9) * 8192 + cb * 512 + (t >> 1) * 128 + (t & 1) * 64 + (col & 31) * 2) = v;
    }
    if ((b & 7) == 0) {
        char* a = ((w >> 1) ? p.part : p.memb) + 0x180000 + (w & 1) * 8192 + (b >> 3) * 256;
        unsigned best = 0xffffu;
        for (int i = 0; i < 2; ++i) {
            unsigned v = i; unsigned long long c0, c1;
            asm volatile("global_store_dword %0, %1, off sc1\n\ts_waitcnt vmcnt(0)" :: "v"(a + i * 128), "v"(v) : "memory");
            asm volatile("s_memtime %0\n\ts_waitcnt lgkmcnt(0)\n\tglobal_load_dword %2, %3, off sc1\n\ts_waitcnt vmcnt(0)\n\ts_memtime %1\n\ts_waitcnt lgkmcnt(0)" : "=&s"(c0), "=&s"(c1), "=&v"(v) : "v"(a + i * 128) : "memory");
            best = min(best, (unsigned)(c1 - c0));
        }
        if (lane == 0) p.lat[w * 32 + (b >> 3)] = best;
    }
    for (int i = b * 256 + tid; i < 35840; i += 65536) if (i < 64 || i >= 16384) p.ctl[i] = 0;
}

__device__ __forceinline__ int near_half(const unsigned* lat, int window, int lane) {
    unsigned v0 = lat[lane], v1 = lat[64 + (lane ^ 32)];
    unsigned v = min(v0, v1);
#pragma unroll
    for (int m = 1; m < 32; m <<= 1) v = min(v, (unsigned)__shfl_xor(v, m));
    const unsigned a = __shfl(v, 0), bb = __shfl(v, 32);
    const int h0 = bb < a ? 8192 : 0;
    return __builtin_amdgcn_readfirstlane(window ? 8192 - h0 : h0);
}

__device__ __forceinline__ void wait_ge(const unsigned* word, unsigned target, unsigned* ctl) {
    unsigned spins = 0;
    for (;;) {
        const unsigned v = __builtin_amdgcn_readfirstlane(__hip_atomic_load(word, __ATOMIC_RELAXED, __HIP_MEMORY_SCOPE_AGENT));
        if (v >= target) break;
        if (++spins > 0x80000u) { __hip_atomic_store(ctl, 3u, __ATOMIC_RELAXED, __HIP_MEMORY_SCOPE_AGENT); break; }
        __builtin_amdgcn_s_sleep(4);
    }
}

__device__ __forceinline__ void acc8(float (&a)[8], const v4u& v) {
    a[0] += __uint_as_float(v.x << 16); a[1] += __uint_as_float(v.x & 0xffff0000u); a[2] += __uint_as_float(v.y << 16); a[3] += __uint_as_float(v.y & 0xffff0000u);
    a[4] += __uint_as_float(v.z << 16); a[5] += __uint_as_float(v.z & 0xffff0000u); a[6] += __uint_as_float(v.w << 16); a[7] += __uint_as_float(v.w & 0xffff0000u);
}
__device__ __forceinline__ void reduce_finish(const Params& p, int s, int u, int lane, const float (&a)[8], const v2u (&xb)[2], const v2u (&bb)[2]) {
    const int col = 128 * (u >> 3) + 16 * (u & 7) + 4 * (lane >> 4), t0 = 32 * s + (lane & 15);
#pragma unroll
    for (int qt = 0; qt < 2; ++qt) {
        const float x0 = __uint_as_float(xb[qt].x << 16), x1 = __uint_as_float(xb[qt].x & 0xffff0000u), x2 = __uint_as_float(xb[qt].y << 16), x3 = __uint_as_float(xb[qt].y & 0xffff0000u);
        const float b0 = __uint_as_float(bb[qt].x << 16), b1 = __uint_as_float(bb[qt].x & 0xffff0000u), b2 = __uint_as_float(bb[qt].y << 16), b3 = __uint_as_float(bb[qt].y & 0xffff0000u);
        const float o0 = rbf(x0 + rbf(a[4 * qt])) + b0, o1 = rbf(x1 + rbf(a[4 * qt + 1])) + b1, o2 = rbf(x2 + rbf(a[4 * qt + 2])) + b2, o3 = rbf(x3 + rbf(a[4 * qt + 3])) + b3;
        v2u o = {pk2(o0, o1), pk2(o2, o3)};
        *(v2u*)(p.out + (size_t)(t0 + 16 * qt) * 1024 + col * 2) = o;
    }
}
__device__ __forceinline__ const char* bo_row(const Params& p, int tok, int far) {
    const int blk = 896 + (tok >> 3);
    return p.Bo + (size_t)blk * 16384 + ((((blk >> 7) ^ far) & 1) << 13) + (tok & 7) * 1024;
}
__device__ __forceinline__ void load_xb(const Params& p, int s, int u, int far, int lane, v2u (&xb)[2], v2u (&bb)[2]) {
    const int col = 128 * (u >> 3) + 16 * (u & 7) + 4 * (lane >> 4), t0 = 32 * s + (lane & 15);
    asm volatile("global_load_dwordx2 %0, %4, off\n\tglobal_load_dwordx2 %1, %5, off\n\tglobal_load_dwordx2 %2, %6, off sc1\n\tglobal_load_dwordx2 %3, %7, off sc1"
                 : "=&v"(xb[0]), "=&v"(xb[1]), "=&v"(bb[0]), "=&v"(bb[1])
                 : "v"(p.x + (size_t)t0 * 1024 + col * 2), "v"(p.x + (size_t)(t0 + 16) * 1024 + col * 2), "v"(bo_row(p, t0, far) + col * 2), "v"(bo_row(p, t0 + 16, far) + col * 2) : "memory");
}
__device__ __forceinline__ const char* po_unit(const Params& p, int s, int u, int far) {
    return p.PO + (size_t)s * 0x200000 + (((s ^ far) & 1) << 13) + (size_t)(u >> 3) * 16384 + (u & 7) * 1024;
}
__device__ __forceinline__ void reduce_task(const Params& p, int s, int grp, int far, int w, int lane) {
    const int u = 4 * grp + w;
    const char* src = po_unit(p, s, u, far) + lane * 16;
    v2u xb[2], bb[2];
    load_xb(p, s, u, far, lane, xb, bb);
    v4u v[32];
#pragma unroll
    for (int i = 0; i < 32; ++i) asm volatile("global_load_dwordx4 %0, %1, off sc1" : "=v"(v[i]) : "v"(src + (size_t)i * 65536) : "memory");
    float a[8];
#pragma unroll
    for (int j = 0; j < 8; ++j) a[j] = 0.f;
#pragma unroll
    for (int hb = 0; hb < 4; ++hb) {
        if (hb == 0) asm volatile("s_waitcnt vmcnt(24)" ::: "memory");
        else if (hb == 1) asm volatile("s_waitcnt vmcnt(16)" ::: "memory");
        else if (hb == 2) asm volatile("s_waitcnt vmcnt(8)" ::: "memory");
        else asm volatile("s_waitcnt vmcnt(0)" ::: "memory");
#pragma unroll
        for (int i = 0; i < 8; ++i) { asm volatile("" : "+v"(v[8 * hb + i])); acc8(a, v[8 * hb + i]); }
    }
    asm volatile("" : "+v"(xb[0]), "+v"(xb[1]), "+v"(bb[0]), "+v"(bb[1]));
    reduce_finish(p, s, u, lane, a, xb, bb);
}
__device__ __forceinline__ void reduce_unit(const Params& p, char* lds, int s, int u, int far, int w, int lane) {
    const char* src = po_unit(p, s, u, far) + lane * 16 + (size_t)(8 * w) * 65536;
    v2u xb[2], bb[2];
    load_xb(p, s, u, far, lane, xb, bb);
    v4u v[8];
#pragma unroll
    for (int i = 0; i < 8; ++i) asm volatile("global_load_dwordx4 %0, %1, off sc1" : "=v"(v[i]) : "v"(src + (size_t)i * 65536) : "memory");
    asm volatile("s_waitcnt vmcnt(0)" ::: "memory");
    float a[8];
#pragma unroll
    for (int j = 0; j < 8; ++j) a[j] = 0.f;
#pragma unroll
    for (int i = 0; i < 8; ++i) { asm volatile("" : "+v"(v[i])); acc8(a, v[i]); }
    asm volatile("" : "+v"(xb[0]), "+v"(xb[1]), "+v"(bb[0]), "+v"(bb[1]));
    f32x4* sl = (f32x4*)(lds + w * 2048 + lane * 32);
    sl[0] = (f32x4){a[0], a[1], a[2], a[3]}; sl[1] = (f32x4){a[4], a[5], a[6], a[7]};
    __syncthreads();
    if (w == 0) {
#pragma unroll
        for (int k = 1; k < 4; ++k) {
            const f32x4* o = (const f32x4*)(lds + k * 2048 + lane * 32);
            const f32x4 u0 = o[0], u1 = o[1];
            a[0] += u0[0]; a[1] += u0[1]; a[2] += u0[2]; a[3] += u0[3]; a[4] += u1[0]; a[5] += u1[1]; a[6] += u1[2]; a[7] += u1[3];
        }
        reduce_finish(p, s, u, lane, a, xb, bb);
    }
    __syncthreads();
}

constexpr int NWK = 16;
#define STAG 72
struct WTask { int kind; GTask g; unsigned* flag; unsigned inc; const unsigned* dep; unsigned need; };
__device__ __forceinline__ bool worker_task(const Params& p, int xq, int i, int far, WTask& t) {
    t.dep = nullptr; t.need = 0; t.g.far = far;
    if (i < 16) {
        const int blk = i >> 3, head = 8 * xq + (i & 7);
        t.kind = 0; t.g.W0 = p.Wq + head * 256; t.g.dW = 0; t.g.ldw = 8192;
        t.g.X = p.xn + (size_t)blk * 128 * 1024; t.g.ldx = 1024; t.g.nk = 8;
        t.g.out = p.Qf; t.g.tok0 = blk * 128; t.g.f0 = head * 128;
        t.flag = p.flg + ((2 * 9 + blk) * 16 + (head >> 1)) * 32; t.inc = 1; return true;
    }
    i -= 16;
    if (i < 28) {
        const int tb = 1 + (i >> 2), hp = 4 * xq + (i & 3);
        t.kind = 1; t.g.W0 = p.Wq + hp * 512; t.g.dW = 0; t.g.ldw = 8192;
        t.g.X = p.xn + (size_t)tb * 256 * 1024; t.g.ldx = 1024; t.g.nk = 8;
        t.g.out = p.Qf; t.g.tok0 = tb * 256; t.g.f0 = hp * 256;
        t.flag = p.flg + ((2 * 9 + tb + 1) * 16 + hp) * 32; t.inc = 2; return true;
    }
    i -= 28;
    const int nf = xq < 2 ? 4 : 3;
    if (i < 8 * nf) {
        const int tb = i / nf, fb = xq + 4 * (i % nf);
        t.kind = 2; t.g.W0 = p.Wg + fb * 256; t.g.dW = p.Wu - p.Wg; t.g.ldw = 3584;
        t.g.X = p.xn + (size_t)tb * 256 * 1024; t.g.ldx = 1024; t.g.nk = 8;
        t.g.out = p.h; t.g.tok0 = tb * 256; t.g.f0 = fb * 128;
        t.flag = p.f1c + tb * 32; t.inc = 1; return true;
    }
    i -= 8 * nf;
    if (i < 16) {
        t.kind = 3; t.g.W0 = p.Wd + xq * 256; t.g.dW = 0; t.g.ldw = 1024;
        t.g.X = p.h + (size_t)(2 * i) * 16384; t.g.ldx = 128; t.g.nk = 28;
        t.g.out = p.Bo; t.g.tok0 = i * 128; t.g.f0 = xq * 128;
        t.flag = p.f2f + (i * 4 + xq) * 32; t.inc = 1; t.dep = p.f1c + (i >> 1) * 32; t.need = 14; return true;
    }
    return false;
}

typedef const __attribute__((address_space(4))) Params* KParams;
__device__ __forceinline__ const Params& fresh(KParams kp) {
    asm volatile("" : "+s"(kp));
    return *(const Params*)kp;
}
#define TLS(slot) do {} while (0)
__device__ __forceinline__ void worker_role(KParams kp, int xq, int jj, int w, int lane) {
    const int roles = fresh(kp).roles;
    const int far = near_half(fresh(kp).lat, 0, lane) ? 0 : 1;
    if (roles & 1) {
        int par = 0, pend = 0; bool pre = false;
        unsigned* fprev = nullptr; unsigned iprev = 0;
        int tn = 1;
        TLS(0);
        for (int i = jj;; i += NWK) {
            WTask cur;
            if (!worker_task(fresh(kp), xq, i, far, cur)) break;
            bool more, chain, nextdep; const char* nW; const char* nX;
            { WTask nxt = cur; more = worker_task(fresh(kp), xq, i + NWK, far, nxt);
              nextdep = more && nxt.dep != nullptr; chain = more && nxt.kind == cur.kind && !nextdep; nW = nxt.g.W0; nX = nxt.g.X; }
            if (i >= 16 && i < 16 + NWK) {
                const unsigned long long t0 = __builtin_amdgcn_s_memrealtime();
                while (__builtin_amdgcn_s_memrealtime() - t0 < (unsigned long long)(jj * STAG + xq * (STAG / 4))) __builtin_amdgcn_s_sleep(8);
            }
            if (cur.dep) { if (w == 0) wait_ge(cur.dep, cur.need, fresh(kp).ctl); __builtin_amdgcn_s_barrier(); }
            if (cur.kind == 0) { Gemm<2, 2, EPI_QKV>::run(cur.g, pre, pend, chain, nW, nX, par, w, lane, fprev, iprev); pend = Gemm<2, 2, EPI_QKV>::NSTORE; }
            else if (cur.kind == 1) { Gemm<4, 4, EPI_QKV>::run(cur.g, pre, pend, chain, nW, nX, par, w, lane, fprev, iprev); pend = Gemm<4, 4, EPI_QKV>::NSTORE; }
            else if (cur.kind == 2) { Gemm<4, 4, EPI_F1>::run(cur.g, pre, pend, chain, nW, nX, par, w, lane, fprev, iprev); pend = Gemm<4, 4, EPI_F1>::NSTORE; }
            else { Gemm<2, 2, EPI_F2>::run(cur.g, pre, pend, chain, nW, nX, par, w, lane, fprev, iprev); pend = Gemm<2, 2, EPI_F2>::NSTORE; }
            { WTask c2; worker_task(fresh(kp), xq, i, far, c2); fprev = c2.flag; iprev = c2.inc; }
            if (i < 16 || !more || nextdep) {
                asm volatile("s_waitcnt vmcnt(0)" ::: "memory");
                __builtin_amdgcn_s_barrier();
                if (w == 0 && lane == 0) __hip_atomic_fetch_add(fprev, iprev, __ATOMIC_RELAXED, __HIP_MEMORY_SCOPE_AGENT);
                fprev = nullptr; pend = 0;
            }
            pre = chain;
            if (fresh(kp).halfP > 0) {
                const unsigned long long t0 = __builtin_amdgcn_s_memrealtime(); const unsigned pz = fresh(kp).halfP;
                while (__builtin_amdgcn_s_memrealtime() - t0 < pz) __builtin_amdgcn_s_sleep(16);
            }
            TLS(tn); ++tn;
        }
    }
    if (roles & 8) {
        extern __shared__ __attribute__((aligned(16))) char lds_red[];
        for (int i = jj; i < 120; i += NWK) {
            const int s = i >> 1, grp = 2 * xq + (i & 1);
            const Params& p = fresh(kp);
            if (w == 0) { wait_ge(p.f2f + ((s >> 2) * 4 + xq) * 32, 1, p.ctl); wait_ge(p.cnt + s * 32, 32, p.ctl); }
            __builtin_amdgcn_s_barrier();
            reduce_task(p, s, grp, far, w, lane);
            TLS(16 + (i / NWK < 6 ? i / NWK : 6));
        }
        for (int i = jj; i < 32; i += NWK) {
            const int s = 60 + (i >> 3), u = 8 * xq + (i & 7);
            const Params& p = fresh(kp);
            if (w == 0) { wait_ge(p.f2f + ((s >> 2) * 4 + xq) * 32, 1, p.ctl); wait_ge(p.cnt + s * 32, 32, p.ctl); }
            __builtin_amdgcn_s_barrier();
            reduce_unit(p, lds_red, s, u, far, w, lane);
        }
        TLS(23);
    }
}

__global__ void __launch_bounds__(256, 1) k_main(Params p) {
    extern __shared__ __attribute__((aligned(16))) char lds[];
    const int b = blockIdx.x, tid = threadIdx.x, lane = tid & 63, g = lane >> 4, c = lane & 15;
    const int w = __builtin_amdgcn_readfirstlane(tid >> 6);
    const int j = b >> 3;
    auto ka = __builtin_amdgcn_kernarg_segment_ptr();
    if ((b & 7) < 4) {
        if (!(p.roles & 2)) return;
        const int h = (b & 3) * 8 + (j >> 2), a = j & 3, idx = h * 4 + a;
        const int rl = (idx & 1) ? -1 : (idx >> 1);
        unsigned* cst = (unsigned*)(lds + 0x20000) + tid * 24;
        const int IMG = 0xA000, WOF = 0x20000;
        cst[0] = (c & 1) ? 0xEEEEEEEEu : 0x44444444u;
        cst[1] = (2 * w) * 1024 + (g >> 1) * 512 + (c >> 2) * 128 + ((c >> 1) & 1) * 64 + (g & 1) * 32 + (c & 1) * 16;
        cst[2] = lane * 16;
        cst[3] = IMG + (32 * w + c) * 32 + ((g ^ ((c >> 2) & 3)) * 8);
        cst[4] = IMG + (4 * g + (c >> 2)) * 32 + (((c & 3) ^ g) * 8);
        cst[5] = h * 16384 + (4 * a + w) * 512 + (c >> 2) * 128 + (c & 1) * 64 + ((c & 2) ? 0 : 32) + g * 8;
        cst[6] = WOF + w * 8192 + lane * 16;
        const int rr = rl < 0 ? 0 : rl;
        cst[7] = (lane >> 3) * 16384 + rr * 128 + (lane & 7) * 16;
        cst[8] = rr * 128 + (c & 7) * 16 + (g & 1) * 8;
        cst[9] = 0;
        const int lq = lane >> 4, sl = lane & 15, q = c >> 2, pp = c & 3;
        cst[10] = (4 * w + lq) * 8192 + ((sl ^ ((lq & 1) | (w << 1))) * 16);
        cst[11] = (4 * w + lq) * 8192 + ((sl ^ (2 * ((lq & 1) | (w << 1)))) * 16);
        cst[12] = (4 * w + lq) * 1024 + ((sl ^ (2 * (lq | ((w & 1) << 2)))) * 16);
        cst[13] = (4 * g + q) * 256 + (((4 * w + (pp >> 1)) ^ (2 * (4 * (g & 1) + q))) * 16) + 8 * (pp & 1);
        { const int rowp = 16 * (g >> 1) + 4 * (g & 1) + (q < 2 ? q : 6 + q), fp = 2 * ((q & 1) | (((g & 1) | ((q >> 1) << 1)) << 1));
          cst[14] = rowp * 256 + (((4 * w + (pp >> 1)) ^ fp) * 16) + 8 * (pp & 1); }
        { const int g2 = c >> 2, r2 = c & 3, rowp = 16 * (g2 >> 1) + 4 * (g2 & 1) + (r2 < 2 ? r2 : 6 + r2);
          const int f = (r2 & 1) | (((g2 & 1) | ((r2 >> 1) << 1) | ((g2 >> 1) << 2)) << 1);
          cst[15] = rowp * 256 + (((g >> 1) ^ f) * 16) + 8 * (g & 1); }
#pragma unroll
        for (int d = 0; d < 4; ++d) cst[16 + d] = (c == 2 * d ? 0x3F80u : 0u) | (c == 2 * d + 1 ? 0x3F800000u : 0u);
        __syncthreads();
        const int hs = __builtin_amdgcn_readfirstlane(h), as = __builtin_amdgcn_readfirstlane(a), rs = __builtin_amdgcn_readfirstlane(rl);
        const int hm = near_half(p.lat, 0, lane), hp = near_half(p.lat, 1, lane);
        asm volatile(
            "s_mov_b64 s[36:37], %0\n"
            "s_mov_b32 s72, %1\n"
            "s_mov_b32 s73, %2\n"
            "s_mov_b32 s74, %3\n"
            "s_mov_b32 s75, %4\n"
            "s_load_dwordx8 s[40:47], s[36:37], 0\n"
            "s_load_dwordx8 s[48:55], s[36:37], 32\n"
            "s_load_dwordx2 s[92:93], s[36:37], 64\n"
            "s_load_dwordx4 s[88:91], s[36:37], 72\n"
            "s_load_dwordx2 s[96:97], s[36:37], 88\n"
            "s_load_dwordx2 s[24:25], s[36:37], 232\n"
            "s_load_dwordx2 s[22:23], s[36:37], 152\n"
            "s_waitcnt lgkmcnt(0)\n"
            "s_add_u32 s52, s52, %5\n"
            "s_addc_u32 s53, s53, 0\n"
            "s_add_u32 s54, s54, %6\n"
            "s_addc_u32 s55, s55, 0\n"
            "v_mbcnt_lo_u32_b32 v0, -1, 0\n"
            "v_mbcnt_hi_u32_b32 v0, -1, v0\n"
            "s_mul_i32 s78, s74, 6144\n"
            "v_mul_u32_u24 v0, 96, v0\n"
            "v_add_u32 v0, s78, v0\n"
            "v_add_u32 v0, 131072, v0\n"
            "ds_read_b128 v[108:111], v0\n"
            "ds_read_b128 v[112:115], v0 offset:16\n"
            "ds_read_b128 v[60:63], v0 offset:32\n"
            "ds_read_b128 v[64:67], v0 offset:48\n"
            "ds_read_b128 v[120:123], v0 offset:64\n"
            "s_waitcnt lgkmcnt(0)\n"
            "v_mov_b32 v116, v60\n"
            "v_mov_b32 v117, v61\n"
            "s_mov_b32 s70, s88\n"
            "s_mov_b32 s71, s89\n"
            "s_mov_b32 s77, s90\n"
            "s_mov_b32 s80, s91\n"
            "s_mov_b32 s81, s96\n"
            "s_mov_b32 s82, s97\n"
            "s_mov_b32 s34, s70\n"
            "s_mov_b32 s33, s71\n"
            "s_mov_b32 s35, 0\n"
            "s_mov_b32 s69, 0\n"
            "s_add_u32 s30, s52, 1048576\n"
            "s_addc_u32 s31, s53, 0\n"
            "s_or_b32 s88, s72, s73\n"
            "s_cmp_eq_u32 s88, 0\n"
            "s_cselect_b32 s29, 1, 0\n"
            "s_mov_b32 s83, 0\n"
            "s_mov_b32 s84, 0\n"
            "s_mov_b32 s68, 0\n"
            "s_mov_b32 s76, 0\n"
            "s_mov_b32 s94, 0x3e0293ee\n"
            "s_add_u32 s56, s54, 0x80000\n"
            "s_addc_u32 s57, s55, 0\n"
            "s_lshl_b32 s78, s72, 8\n"
            "s_add_u32 s40, s40, s78\n"
            "s_addc_u32 s41, s41, 0\n"
            "s_add_u32 s42, s42, s78\n"
            "s_addc_u32 s43, s43, 0\n"
            "s_add_u32 s44, s44, s78\n"
            "s_addc_u32 s45, s45, 0\n"
            "s_lshl_b32 s78, s72, 17\n"
            "s_lshl_b32 s79, s73, 8\n"
            "s_add_u32 s78, s78, s79\n"
            "s_add_u32 s46, s46, s78\n"
            "s_addc_u32 s47, s47, 0\n"
            "s_lshl_b32 s78, s72, 7\n"
            "s_add_u32 s24, s24, s78\n"
            "s_addc_u32 s25, s25, 0\n"
            "s_mov_b32 s21, 0\n"
            "s_lshr_b32 s20, %5, 13\n"
            "s_lshl_b32 s78, s72, 14\n"
            "s_add_u32 s48, s48, s78\n"
            "s_addc_u32 s49, s49, 0\n"
            "s_add_u32 s50, s50, s78\n"
            "s_addc_u32 s51, s51, 0\n"
            "v_xor_b32 v68, 128, v62\n"
            "v_xor_b32 v71, 32, v65\n"
            "v_mov_b32 v70, v65\n"
            "s_cmp_eq_u32 s74, 0\n"
            "s_cbranch_scc0 CK0_SKIP_%=\n"
            "s_memrealtime s[26:27]\n"
            "s_waitcnt lgkmcnt(0)\n"
            "s_mov_b32 s14, s26\n"
            "CK0_E_%=:\n"
            "s_memrealtime s[26:27]\n"
            "s_memtime s[78:79]\n"
            "s_waitcnt lgkmcnt(0)\n"
            "s_cmp_eq_u32 s26, s14\n"
            "s_cbranch_scc1 CK0_E_%=\n"
            "s_mov_b32 s15, s78\n"
            "s_mov_b32 s16, s26\n"
            "CK0_SKIP_%=:\n"
            "s_cmp_eq_u32 s73, 1\n"
            "s_cbranch_scc1 PRO_A1_%=\n"
            "s_cmp_eq_u32 s73, 2\n"
            "s_cbranch_scc1 PRO_A2_%=\n"
            "s_cmp_eq_u32 s73, 3\n"
            "s_cbranch_scc1 PRO_A3_%=\n"
            "PRO_A0_%=:\n"
            "s_lshl_b32 s78, s74, 10\n"
            "s_add_u32 m0, s78, 0\n"
            "s_add_u32 s88, s46, 0\n"
            "s_addc_u32 s89, s47, 0\n"
            "global_load_lds_dwordx4 v64, s[88:89]\n"
            "s_lshl_b32 s78, s74, 10\n"
            "s_add_u32 m0, s78, 4096\n"
            "s_add_u32 s88, s46, 16384\n"
            "s_addc_u32 s89, s47, 0\n"
            "global_load_lds_dwordx4 v64, s[88:89]\n"
            "s_lshl_b32 s78, s74, 10\n"
            "s_add_u32 m0, s78, 8192\n"
            "s_add_u32 s88, s46, 32768\n"
            "s_addc_u32 s89, s47, 0\n"
            "global_load_lds_dwordx4 v64, s[88:89]\n"
            "s_lshl_b32 s78, s74, 10\n"
            "s_add_u32 m0, s78, 12288\n"
            "s_add_u32 s88, s46, 49152\n"
            "s_addc_u32 s89, s47, 0\n"
            "global_load_lds_dwordx4 v64, s[88:89]\n"
            "s_lshl_b32 s78, s74, 10\n"
            "s_add_u32 m0, s78, 16384\n"
            "s_add_u32 s88, s46, 65536\n"
            "s_addc_u32 s89, s47, 0\n"
            "global_load_lds_dwordx4 v64, s[88:89]\n"
            "s_lshl_b32 s78, s74, 10\n"
            "s_add_u32 m0, s78, 20480\n"
            "s_add_u32 s88, s46, 81920\n"
            "s_addc_u32 s89, s47, 0\n"
            "global_load_lds_dwordx4 v64, s[88:89]\n"
            "s_lshl_b32 s78, s74, 10\n"
            "s_add_u32 m0, s78, 24576\n"
            "s_add_u32 s88, s46, 98304\n"
            "s_addc_u32 s89, s47, 0\n"
            "global_load_lds_dwordx4 v64, s[88:89]\n"
            "s_lshl_b32 s78, s74, 10\n"
            "s_add_u32 m0, s78, 28672\n"
            "s_add_u32 s88, s46, 114688\n"
            "s_addc_u32 s89, s47, 0\n"
            "global_load_lds_dwordx4 v64, s[88:89]\n"
            "s_lshl_b32 s78, s74, 10\n"
            "s_add_u32 m0, s78, 32768\n"
            "s_add_u32 s88, s44, 0\n"
            "s_addc_u32 s89, s45, 0\n"
            "global_load_lds_dwordx4 v62, s[88:89]\n"
            "s_lshl_b32 s78, s74, 10\n"
            "s_add_u32 m0, s78, 36864\n"
            "s_add_u32 s88, s44, 131072\n"
            "s_addc_u32 s89, s45, 0\n"
            "global_load_lds_dwordx4 v68, s[88:89]\n"
            "s_lshl_b32 s78, s74, 10\n"
            "s_add_u32 m0, s78, 40960\n"
            "s_add_u32 s88, s44, 262144\n"
            "s_addc_u32 s89, s45, 0\n"
            "global_load_lds_dwordx4 v62, s[88:89]\n"
            "s_lshl_b32 s78, s74, 10\n"
            "s_add_u32 m0, s78, 45056\n"
            "s_add_u32 s88, s44, 393216\n"
            "s_addc_u32 s89, s45, 0\n"
            "global_load_lds_dwordx4 v68, s[88:89]\n"
            "s_lshl_b32 s78, s74, 10\n"
            "s_add_u32 m0, s78, 49152\n"
            "s_add_u32 s88, s44, 524288\n"
            "s_addc_u32 s89, s45, 0\n"
            "global_load_lds_dwordx4 v62, s[88:89]\n"
            "s_lshl_b32 s78, s74, 10\n"
            "s_add_u32 m0, s78, 53248\n"
            "s_add_u32 s88, s44, 655360\n"
            "s_addc_u32 s89, s45, 0\n"
            "global_load_lds_dwordx4 v68, s[88:89]\n"
            "s_lshl_b32 s78, s74, 10\n"
            "s_add_u32 m0, s78, 57344\n"
            "s_add_u32 s88, s44, 786432\n"
            "s_addc_u32 s89, s45, 0\n"
            "global_load_lds_dwordx4 v62, s[88:89]\n"
            "s_lshl_b32 s78, s74, 10\n"
            "s_add_u32 m0, s78, 61440\n"
            "s_add_u32 s88, s44, 917504\n"
            "s_addc_u32 s89, s45, 0\n"
            "global_load_lds_dwordx4 v68, s[88:89]\n"
            "s_lshl_b32 s78, s74, 10\n"
            "s_add_u32 m0, s78, 65536\n"
            "s_add_u32 s88, s40, 0\n"
            "s_addc_u32 s89, s41, 0\n"
            "global_load_lds_dwordx4 v63, s[88:89]\n"
            "s_lshl_b32 s78, s74, 10\n"
            "s_add_u32 m0, s78, 69632\n"
            "s_add_u32 s88, s40, 131072\n"
            "s_addc_u32 s89, s41, 0\n"
            "global_load_lds_dwordx4 v63, s[88:89]\n"
            "s_lshl_b32 s78, s74, 10\n"
            "s_add_u32 m0, s78, 73728\n"
            "s_add_u32 s88, s40, 262144\n"
            "s_addc_u32 s89, s41, 0\n"
            "global_load_lds_dwordx4 v63, s[88:89]\n"
            "s_lshl_b32 s78, s74, 10\n"
            "s_add_u32 m0, s78, 77824\n"
            "s_add_u32 s88, s40, 393216\n"
            "s_addc_u32 s89, s41, 0\n"
            "global_load_lds_dwordx4 v63, s[88:89]\n"
            "s_lshl_b32 s78, s74, 10\n"
            "s_add_u32 m0, s78, 81920\n"
            "s_add_u32 s88, s40, 524288\n"
            "s_addc_u32 s89, s41, 0\n"
            "global_load_lds_dwordx4 v63, s[88:89]\n"
            "s_lshl_b32 s78, s74, 10\n"
            "s_add_u32 m0, s78, 86016\n"
            "s_add_u32 s88, s40, 655360\n"
            "s_addc_u32 s89, s41, 0\n"
            "global_load_lds_dwordx4 v63, s[88:89]\n"
            "s_lshl_b32 s78, s74, 10\n"
            "s_add_u32 m0, s78, 90112\n"
            "s_add_u32 s88, s40, 786432\n"
            "s_addc_u32 s89, s41, 0\n"
            "global_load_lds_dwordx4 v63, s[88:89]\n"
            "s_lshl_b32 s78, s74, 10\n"
            "s_add_u32 m0, s78, 94208\n"
            "s_add_u32 s88, s40, 917504\n"
            "s_addc_u32 s89, s41, 0\n"
            "global_load_lds_dwordx4 v63, s[88:89]\n"
            "s_waitcnt vmcnt(16)\n"
            "s_barrier\n"
            "s_lshl_b32 s78, s74, 10\n"
            "s_add_u32 m0, s78, 98304\n"
            "s_add_u32 s88, s42, 0\n"
            "s_addc_u32 s89, s43, 0\n"
            "global_load_lds_dwordx4 v63, s[88:89]\n"
            "ds_read_b64_tr_b16 v[0:1], v70 offset:0\n"
            "s_lshl_b32 s78, s74, 10\n"
            "s_add_u32 m0, s78, 102400\n"
            "s_add_u32 s88, s42, 131072\n"
            "s_addc_u32 s89, s43, 0\n"
            "global_load_lds_dwordx4 v63, s[88:89]\n"
            "ds_read_b64_tr_b16 v[2:3], v70 offset:4096\n"
            "s_lshl_b32 s78, s74, 10\n"
            "s_add_u32 m0, s78, 106496\n"
            "s_add_u32 s88, s42, 262144\n"
            "s_addc_u32 s89, s43, 0\n"
            "global_load_lds_dwordx4 v63, s[88:89]\n"
            "ds_read_b64_tr_b16 v[4:5], v70 offset:8192\n"
            "s_lshl_b32 s78, s74, 10\n"
            "s_add_u32 m0, s78, 110592\n"
            "s_add_u32 s88, s42, 393216\n"
            "s_addc_u32 s89, s43, 0\n"
            "global_load_lds_dwordx4 v63, s[88:89]\n"
            "ds_read_b64_tr_b16 v[6:7], v70 offset:12288\n"
            "s_lshl_b32 s78, s74, 10\n"
            "s_add_u32 m0, s78, 114688\n"
            "s_add_u32 s88, s42, 524288\n"
            "s_addc_u32 s89, s43, 0\n"
            "global_load_lds_dwordx4 v63, s[88:89]\n"
            "ds_read_b64_tr_b16 v[8:9], v70 offset:16384\n"
            "s_lshl_b32 s78, s74, 10\n"
            "s_add_u32 m0, s78, 118784\n"
            "s_add_u32 s88, s42, 655360\n"
            "s_addc_u32 s89, s43, 0\n"
            "global_load_lds_dwordx4 v63, s[88:89]\n"
            "ds_read_b64_tr_b16 v[10:11], v70 offset:20480\n"
            "s_lshl_b32 s78, s74, 10\n"
            "s_add_u32 m0, s78, 122880\n"
            "s_add_u32 s88, s42, 786432\n"
            "s_addc_u32 s89, s43, 0\n"
            "global_load_lds_dwordx4 v63, s[88:89]\n"
            "ds_read_b64_tr_b16 v[12:13], v70 offset:24576\n"
            "s_lshl_b32 s78, s74, 10\n"
            "s_add_u32 m0, s78, 126976\n"
            "s_add_u32 s88, s42, 917504\n"
            "s_addc_u32 s89, s43, 0\n"
            "global_load_lds_dwordx4 v63, s[88:89]\n"
            "ds_read_b64_tr_b16 v[14:15], v70 offset:28672\n"
            "ds_read_b64_tr_b16 v[16:17], v71 offset:0\n"
            "ds_read_b64_tr_b16 v[18:19], v71 offset:4096\n"
            "ds_read_b64_tr_b16 v[20:21], v71 offset:8192\n"
            "ds_read_b64_tr_b16 v[22:23], v71 offset:12288\n"
            "ds_read_b64_tr_b16 v[24:25], v71 offset:16384\n"
            "ds_read_b64_tr_b16 v[26:27], v71 offset:20480\n"
            "ds_read_b64_tr_b16 v[28:29], v71 offset:24576\n"
            "ds_read_b64_tr_b16 v[30:31], v71 offset:28672\n"
            "s_waitcnt lgkmcnt(0)\n"
            "s_waitcnt vmcnt(16)\n"
            "s_barrier\n"
            "s_lshl_b32 s78, s74, 10\n"
            "s_add_u32 m0, s78, 0\n"
            "s_add_u32 s88, s44, 1048576\n"
            "s_addc_u32 s89, s45, 0\n"
            "global_load_lds_dwordx4 v62, s[88:89]\n"
            "v_add_u32 v74, 32768, v67\n"
            "v_xor_b32 v75, 32, v74\n"
            "v_xor_b32 v76, 64, v74\n"
            "v_xor_b32 v77, 96, v74\n"
            "v_xor_b32 v78, 128, v74\n"
            "v_xor_b32 v79, 160, v74\n"
            "v_xor_b32 v80, 192, v74\n"
            "v_xor_b32 v81, 224, v74\n"
            "ds_read_b64 v[32:33], v74 offset:0\n"
            "ds_read_b64 v[34:35], v75 offset:0\n"
            "ds_read_b64 v[36:37], v76 offset:0\n"
            "ds_read_b64 v[38:39], v77 offset:0\n"
            "ds_read_b64 v[40:41], v78 offset:0\n"
            "ds_read_b64 v[42:43], v79 offset:0\n"
            "ds_read_b64 v[44:45], v80 offset:0\n"
            "ds_read_b64 v[46:47], v81 offset:0\n"
            "ds_read_b64 v[84:85], v74 offset:512\n"
            "ds_read_b64 v[86:87], v75 offset:512\n"
            "ds_read_b64 v[88:89], v76 offset:512\n"
            "s_lshl_b32 s78, s74, 10\n"
            "s_add_u32 m0, s78, 4096\n"
            "s_add_u32 s88, s44, 1179648\n"
            "s_addc_u32 s89, s45, 0\n"
            "global_load_lds_dwordx4 v68, s[88:89]\n"
            "ds_read_b64 v[90:91], v77 offset:512\n"
            "ds_read_b64 v[92:93], v78 offset:512\n"
            "ds_read_b64 v[94:95], v79 offset:512\n"
            "ds_read_b64 v[96:97], v80 offset:512\n"
            "ds_read_b64 v[98:99], v81 offset:512\n"
            "s_waitcnt lgkmcnt(8)\n"
            "v_mfma_f32_16x16x32_bf16 v[48:51], v[32:35], v[0:3], 0\n"
            "v_mfma_f32_16x16x32_bf16 v[52:55], v[32:35], v[16:19], 0\n"
            "v_mfma_f32_16x16x32_bf16 v[48:51], v[36:39], v[4:7], v[48:51]\n"
            "v_mfma_f32_16x16x32_bf16 v[52:55], v[36:39], v[20:23], v[52:55]\n"
            "v_mfma_f32_16x16x32_bf16 v[48:51], v[40:43], v[8:11], v[48:51]\n"
            "v_mfma_f32_16x16x32_bf16 v[52:55], v[40:43], v[24:27], v[52:55]\n"
            "v_mfma_f32_16x16x32_bf16 v[48:51], v[44:47], v[12:15], v[48:51]\n"
            "v_mfma_f32_16x16x32_bf16 v[52:55], v[44:47], v[28:31], v[52:55]\n"
            "ds_read_b64 v[32:33], v74 offset:8192\n"
            "ds_read_b64 v[34:35], v75 offset:8192\n"
            "ds_read_b64 v[36:37], v76 offset:8192\n"
            "ds_read_b64 v[38:39], v77 offset:8192\n"
            "ds_read_b64 v[40:41], v78 offset:8192\n"
            "s_lshl_b32 s78, s74, 10\n"
            "s_add_u32 m0, s78, 8192\n"
            "s_add_u32 s88, s44, 1310720\n"
            "s_addc_u32 s89, s45, 0\n"
            "global_load_lds_dwordx4 v62, s[88:89]\n"
            "ds_read_b64 v[42:43], v79 offset:8192\n"
            "ds_read_b64 v[44:45], v80 offset:8192\n"
            "ds_read_b64 v[46:47], v81 offset:8192\n"
            "s_waitcnt lgkmcnt(8)\n"
            "v_mfma_f32_16x16x32_bf16 v[100:103], v[84:87], v[0:3], 0\n"
            "v_mfma_f32_16x16x32_bf16 v[104:107], v[84:87], v[16:19], 0\n"
            "v_mfma_f32_16x16x32_bf16 v[100:103], v[88:91], v[4:7], v[100:103]\n"
            "v_mfma_f32_16x16x32_bf16 v[104:107], v[88:91], v[20:23], v[104:107]\n"
            "v_mfma_f32_16x16x32_bf16 v[100:103], v[92:95], v[8:11], v[100:103]\n"
            "v_mfma_f32_16x16x32_bf16 v[104:107], v[92:95], v[24:27], v[104:107]\n"
            "v_mfma_f32_16x16x32_bf16 v[100:103], v[96:99], v[12:15], v[100:103]\n"
            "v_mfma_f32_16x16x32_bf16 v[104:107], v[96:99], v[28:31], v[104:107]\n"
            "v_cvt_pk_bf16_f32 v128, v48, v49\n"
            "v_cvt_pk_bf16_f32 v129, v50, v51\n"
            "v_cvt_pk_bf16_f32 v192, v52, v53\n"
            "v_cvt_pk_bf16_f32 v193, v54, v55\n"
            "ds_read_b64 v[84:85], v74 offset:8704\n"
            "ds_read_b64 v[86:87], v75 offset:8704\n"
            "ds_read_b64 v[88:89], v76 offset:8704\n"
            "s_lshl_b32 s78, s74, 10\n"
            "s_add_u32 m0, s78, 12288\n"
            "s_add_u32 s88, s44, 1441792\n"
            "s_addc_u32 s89, s45, 0\n"
            "global_load_lds_dwordx4 v68, s[88:89]\n"
            "ds_read_b64 v[90:91], v77 offset:8704\n"
            "ds_read_b64 v[92:93], v78 offset:8704\n"
            "ds_read_b64 v[94:95], v79 offset:8704\n"
            "ds_read_b64 v[96:97], v80 offset:8704\n"
            "ds_read_b64 v[98:99], v81 offset:8704\n"
            "s_waitcnt lgkmcnt(8)\n"
            "v_mfma_f32_16x16x32_bf16 v[48:51], v[32:35], v[0:3], 0\n"
            "v_mfma_f32_16x16x32_bf16 v[52:55], v[32:35], v[16:19], 0\n"
            "v_mfma_f32_16x16x32_bf16 v[48:51], v[36:39], v[4:7], v[48:51]\n"
            "v_mfma_f32_16x16x32_bf16 v[52:55], v[36:39], v[20:23], v[52:55]\n"
            "v_mfma_f32_16x16x32_bf16 v[48:51], v[40:43], v[8:11], v[48:51]\n"
            "v_mfma_f32_16x16x32_bf16 v[52:55], v[40:43], v[24:27], v[52:55]\n"
            "v_mfma_f32_16x16x32_bf16 v[48:51], v[44:47], v[12:15], v[48:51]\n"
            "v_mfma_f32_16x16x32_bf16 v[52:55], v[44:47], v[28:31], v[52:55]\n"
            "v_cvt_pk_bf16_f32 v130, v100, v101\n"
            "v_cvt_pk_bf16_f32 v131, v102, v103\n"
            "v_cvt_pk_bf16_f32 v194, v104, v105\n"
            "v_cvt_pk_bf16_f32 v195, v106, v107\n"
            "ds_read_b64 v[32:33], v74 offset:16384\n"
            "s_lshl_b32 s78, s74, 10\n"
            "s_add_u32 m0, s78, 16384\n"
            "s_add_u32 s88, s44, 1572864\n"
            "s_addc_u32 s89, s45, 0\n"
            "global_load_lds_dwordx4 v62, s[88:89]\n"
            "ds_read_b64 v[34:35], v75 offset:16384\n"
            "ds_read_b64 v[36:37], v76 offset:16384\n"
            "ds_read_b64 v[38:39], v77 offset:16384\n"
            "ds_read_b64 v[40:41], v78 offset:16384\n"
            "ds_read_b64 v[42:43], v79 offset:16384\n"
            "ds_read_b64 v[44:45], v80 offset:16384\n"
            "ds_read_b64 v[46:47], v81 offset:16384\n"
            "s_waitcnt lgkmcnt(8)\n"
            "v_mfma_f32_16x16x32_bf16 v[100:103], v[84:87], v[0:3], 0\n"
            "v_mfma_f32_16x16x32_bf16 v[104:107], v[84:87], v[16:19], 0\n"
            "v_mfma_f32_16x16x32_bf16 v[100:103], v[88:91], v[4:7], v[100:103]\n"
            "v_mfma_f32_16x16x32_bf16 v[104:107], v[88:91], v[20:23], v[104:107]\n"
            "v_mfma_f32_16x16x32_bf16 v[100:103], v[92:95], v[8:11], v[100:103]\n"
            "v_mfma_f32_16x16x32_bf16 v[104:107], v[92:95], v[24:27], v[104:107]\n"
            "v_mfma_f32_16x16x32_bf16 v[100:103], v[96:99], v[12:15], v[100:103]\n"
            "v_mfma_f32_16x16x32_bf16 v[104:107], v[96:99], v[28:31], v[104:107]\n"
            "v_cvt_pk_bf16_f32 v132, v48, v49\n"
            "v_cvt_pk_bf16_f32 v133, v50, v51\n"
            "v_cvt_pk_bf16_f32 v196, v52, v53\n"
            "s_lshl_b32 s78, s74, 10\n"
            "s_add_u32 m0, s78, 20480\n"
            "s_add_u32 s88, s44, 1703936\n"
            "s_addc_u32 s89, s45, 0\n"
            "global_load_lds_dwordx4 v68, s[88:89]\n"
            "v_cvt_pk_bf16_f32 v197, v54, v55\n"
            "ds_read_b64 v[84:85], v74 offset:16896\n"
            "ds_read_b64 v[86:87], v75 offset:16896\n"
            "ds_read_b64 v[88:89], v76 offset:16896\n"
            "ds_read_b64 v[90:91], v77 offset:16896\n"
            "ds_read_b64 v[92:93], v78 offset:16896\n"
            "ds_read_b64 v[94:95], v79 offset:16896\n"
            "ds_read_b64 v[96:97], v80 offset:16896\n"
            "ds_read_b64 v[98:99], v81 offset:16896\n"
            "s_waitcnt lgkmcnt(8)\n"
            "v_mfma_f32_16x16x32_bf16 v[48:51], v[32:35], v[0:3], 0\n"
            "v_mfma_f32_16x16x32_bf16 v[52:55], v[32:35], v[16:19], 0\n"
            "v_mfma_f32_16x16x32_bf16 v[48:51], v[36:39], v[4:7], v[48:51]\n"
            "v_mfma_f32_16x16x32_bf16 v[52:55], v[36:39], v[20:23], v[52:55]\n"
            "v_mfma_f32_16x16x32_bf16 v[48:51], v[40:43], v[8:11], v[48:51]\n"
            "v_mfma_f32_16x16x32_bf16 v[52:55], v[40:43], v[24:27], v[52:55]\n"
            "v_mfma_f32_16x16x32_bf16 v[48:51], v[44:47], v[12:15], v[48:51]\n"
            "v_mfma_f32_16x16x32_bf16 v[52:55], v[44:47], v[28:31], v[52:55]\n"
            "v_cvt_pk_bf16_f32 v134, v100, v101\n"
            "s_lshl_b32 s78, s74, 10\n"
            "s_add_u32 m0, s78, 24576\n"
            "s_add_u32 s88, s44, 1835008\n"
            "s_addc_u32 s89, s45, 0\n"
            "global_load_lds_dwordx4 v62, s[88:89]\n"
            "v_cvt_pk_bf16_f32 v135, v102, v103\n"
            "v_cvt_pk_bf16_f32 v198, v104, v105\n"
            "v_cvt_pk_bf16_f32 v199, v106, v107\n"
            "ds_read_b64 v[32:33], v74 offset:24576\n"
            "ds_read_b64 v[34:35], v75 offset:24576\n"
            "ds_read_b64 v[36:37], v76 offset:24576\n"
            "ds_read_b64 v[38:39], v77 offset:24576\n"
            "ds_read_b64 v[40:41], v78 offset:24576\n"
            "ds_read_b64 v[42:43], v79 offset:24576\n"
            "ds_read_b64 v[44:45], v80 offset:24576\n"
            "ds_read_b64 v[46:47], v81 offset:24576\n"
            "s_waitcnt lgkmcnt(8)\n"
            "v_mfma_f32_16x16x32_bf16 v[100:103], v[84:87], v[0:3], 0\n"
            "v_mfma_f32_16x16x32_bf16 v[104:107], v[84:87], v[16:19], 0\n"
            "v_mfma_f32_16x16x32_bf16 v[100:103], v[88:91], v[4:7], v[100:103]\n"
            "v_mfma_f32_16x16x32_bf16 v[104:107], v[88:91], v[20:23], v[104:107]\n"
            "v_mfma_f32_16x16x32_bf16 v[100:103], v[92:95], v[8:11], v[100:103]\n"
            "v_mfma_f32_16x16x32_bf16 v[104:107], v[92:95], v[24:27], v[104:107]\n"
            "v_mfma_f32_16x16x32_bf16 v[100:103], v[96:99], v[12:15], v[100:103]\n"
            "s_lshl_b32 s78, s74, 10\n"
            "s_add_u32 m0, s78, 28672\n"
            "s_add_u32 s88, s44, 1966080\n"
            "s_addc_u32 s89, s45, 0\n"
            "global_load_lds_dwordx4 v68, s[88:89]\n"
            "v_mfma_f32_16x16x32_bf16 v[104:107], v[96:99], v[28:31], v[104:107]\n"
            "v_cvt_pk_bf16_f32 v136, v48, v49\n"
            "v_cvt_pk_bf16_f32 v137, v50, v51\n"
            "v_cvt_pk_bf16_f32 v200, v52, v53\n"
            "v_cvt_pk_bf16_f32 v201, v54, v55\n"
            "ds_read_b64 v[84:85], v74 offset:25088\n"
            "ds_read_b64 v[86:87], v75 offset:25088\n"
            "ds_read_b64 v[88:89], v76 offset:25088\n"
            "ds_read_b64 v[90:91], v77 offset:25088\n"
            "ds_read_b64 v[92:93], v78 offset:25088\n"
            "ds_read_b64 v[94:95], v79 offset:25088\n"
            "ds_read_b64 v[96:97], v80 offset:25088\n"
            "ds_read_b64 v[98:99], v81 offset:25088\n"
            "s_waitcnt lgkmcnt(8)\n"
            "v_mfma_f32_16x16x32_bf16 v[48:51], v[32:35], v[0:3], 0\n"
            "v_mfma_f32_16x16x32_bf16 v[52:55], v[32:35], v[16:19], 0\n"
            "v_mfma_f32_16x16x32_bf16 v[48:51], v[36:39], v[4:7], v[48:51]\n"
            "v_mfma_f32_16x16x32_bf16 v[52:55], v[36:39], v[20:23], v[52:55]\n"
            "v_mfma_f32_16x16x32_bf16 v[48:51], v[40:43], v[8:11], v[48:51]\n"
            "v_mfma_f32_16x16x32_bf16 v[52:55], v[40:43], v[24:27], v[52:55]\n"
            "v_mfma_f32_16x16x32_bf16 v[48:51], v[44:47], v[12:15], v[48:51]\n"
            "v_mfma_f32_16x16x32_bf16 v[52:55], v[44:47], v[28:31], v[52:55]\n"
            "v_cvt_pk_bf16_f32 v138, v100, v101\n"
            "v_cvt_pk_bf16_f32 v139, v102, v103\n"
            "v_cvt_pk_bf16_f32 v202, v104, v105\n"
            "v_cvt_pk_bf16_f32 v203, v106, v107\n"
            "s_waitcnt lgkmcnt(0)\n"
            "v_mfma_f32_16x16x32_bf16 v[100:103], v[84:87], v[0:3], 0\n"
            "v_mfma_f32_16x16x32_bf16 v[104:107], v[84:87], v[16:19], 0\n"
            "v_mfma_f32_16x16x32_bf16 v[100:103], v[88:91], v[4:7], v[100:103]\n"
            "v_mfma_f32_16x16x32_bf16 v[104:107], v[88:91], v[20:23], v[104:107]\n"
            "v_mfma_f32_16x16x32_bf16 v[100:103], v[92:95], v[8:11], v[100:103]\n"
            "v_mfma_f32_16x16x32_bf16 v[104:107], v[92:95], v[24:27], v[104:107]\n"
            "v_mfma_f32_16x16x32_bf16 v[100:103], v[96:99], v[12:15], v[100:103]\n"
            "v_mfma_f32_16x16x32_bf16 v[104:107], v[96:99], v[28:31], v[104:107]\n"
            "v_cvt_pk_bf16_f32 v140, v48, v49\n"
            "v_cvt_pk_bf16_f32 v141, v50, v51\n"
            "v_cvt_pk_bf16_f32 v204, v52, v53\n"
            "v_cvt_pk_bf16_f32 v205, v54, v55\n"
            "s_nop 2\n"
            "v_cvt_pk_bf16_f32 v142, v100, v101\n"
            "v_cvt_pk_bf16_f32 v143, v102, v103\n"
            "v_cvt_pk_bf16_f32 v206, v104, v105\n"
            "v_cvt_pk_bf16_f32 v207, v106, v107\n"
            "s_waitcnt vmcnt(16)\n"
            "s_barrier\n"
            "s_lshl_b32 s78, s74, 10\n"
            "s_add_u32 m0, s78, 32768\n"
            "s_add_u32 s88, s40, 1048576\n"
            "s_addc_u32 s89, s41, 0\n"
            "global_load_lds_dwordx4 v63, s[88:89]\n"
            "v_add_u32 v72, 65536, v66\n"
            "v_xor_b32 v73, 32, v72\n"
            "s_lshl_b32 s78, s74, 10\n"
            "s_add_u32 m0, s78, 36864\n"
            "s_add_u32 s88, s40, 1179648\n"
            "s_addc_u32 s89, s41, 0\n"
            "global_load_lds_dwordx4 v63, s[88:89]\n"
            "ds_read_b64_tr_b16 a[0:1], v72 offset:0\n"
            "ds_read_b64_tr_b16 a[2:3], v72 offset:512\n"
            "s_lshl_b32 s78, s74, 10\n"
            "s_add_u32 m0, s78, 40960\n"
            "s_add_u32 s88, s40, 1310720\n"
            "s_addc_u32 s89, s41, 0\n"
            "global_load_lds_dwordx4 v63, s[88:89]\n"
            "ds_read_b64_tr_b16 a[4:5], v72 offset:8192\n"
            "ds_read_b64_tr_b16 a[6:7], v72 offset:8704\n"
            "s_lshl_b32 s78, s74, 10\n"
            "s_add_u32 m0, s78, 45056\n"
            "s_add_u32 s88, s40, 1441792\n"
            "s_addc_u32 s89, s41, 0\n"
            "global_load_lds_dwordx4 v63, s[88:89]\n"
            "ds_read_b64_tr_b16 a[64:65], v73 offset:0\n"
            "ds_read_b64_tr_b16 a[66:67], v73 offset:512\n"
            "s_lshl_b32 s78, s74, 10\n"
            "s_add_u32 m0, s78, 49152\n"
            "s_add_u32 s88, s40, 1572864\n"
            "s_addc_u32 s89, s41, 0\n"
            "global_load_lds_dwordx4 v63, s[88:89]\n"
            "ds_read_b64_tr_b16 a[68:69], v73 offset:8192\n"
            "ds_read_b64_tr_b16 a[70:71], v73 offset:8704\n"
            "s_lshl_b32 s78, s74, 10\n"
            "s_add_u32 m0, s78, 53248\n"
            "s_add_u32 s88, s40, 1703936\n"
            "s_addc_u32 s89, s41, 0\n"
            "global_load_lds_dwordx4 v63, s[88:89]\n"
            "s_waitcnt lgkmcnt(0)\n"
            "ds_read_b64_tr_b16 a[8:9], v72 offset:16384\n"
            "s_lshl_b32 s78, s74, 10\n"
            "s_add_u32 m0, s78, 57344\n"
            "s_add_u32 s88, s40, 1835008\n"
            "s_addc_u32 s89, s41, 0\n"
            "global_load_lds_dwordx4 v63, s[88:89]\n"
            "ds_read_b64_tr_b16 a[10:11], v72 offset:16896\n"
            "ds_read_b64_tr_b16 a[12:13], v72 offset:24576\n"
            "s_lshl_b32 s78, s74, 10\n"
            "s_add_u32 m0, s78, 61440\n"
            "s_add_u32 s88, s40, 1966080\n"
            "s_addc_u32 s89, s41, 0\n"
            "global_load_lds_dwordx4 v63, s[88:89]\n"
            "ds_read_b64_tr_b16 a[14:15], v72 offset:25088\n"
            "ds_read_b64_tr_b16 a[72:73], v73 offset:16384\n"
            "ds_read_b64_tr_b16 a[74:75], v73 offset:16896\n"
            "ds_read_b64_tr_b16 a[76:77], v73 offset:24576\n"
            "ds_read_b64_tr_b16 a[78:79], v73 offset:25088\n"
            "s_waitcnt lgkmcnt(0)\n"
            "s_waitcnt vmcnt(16)\n"
            "s_barrier\n"
            "s_lshl_b32 s78, s74, 10\n"
            "s_add_u32 m0, s78, 65536\n"
            "s_add_u32 s88, s42, 1048576\n"
            "s_addc_u32 s89, s43, 0\n"
            "global_load_lds_dwordx4 v63, s[88:89]\n"
            "v_add_u32 v72, 98304, v66\n"
            "v_xor_b32 v73, 32, v72\n"
            "s_lshl_b32 s78, s74, 10\n"
            "s_add_u32 m0, s78, 69632\n"
            "s_add_u32 s88, s42, 1179648\n"
            "s_addc_u32 s89, s43, 0\n"
            "global_load_lds_dwordx4 v63, s[88:89]\n"
            "ds_read_b64_tr_b16 a[128:129], v72 offset:0\n"
            "ds_read_b64_tr_b16 a[130:131], v72 offset:512\n"
            "s_lshl_b32 s78, s74, 10\n"
            "s_add_u32 m0, s78, 73728\n"
            "s_add_u32 s88, s42, 1310720\n"
            "s_addc_u32 s89, s43, 0\n"
            "global_load_lds_dwordx4 v63, s[88:89]\n"
            "ds_read_b64_tr_b16 a[132:133], v72 offset:8192\n"
            "ds_read_b64_tr_b16 a[134:135], v72 offset:8704\n"
            "s_lshl_b32 s78, s74, 10\n"
            "s_add_u32 m0, s78, 77824\n"
            "s_add_u32 s88, s42, 1441792\n"
            "s_addc_u32 s89, s43, 0\n"
            "global_load_lds_dwordx4 v63, s[88:89]\n"
            "ds_read_b64_tr_b16 a[192:193], v73 offset:0\n"
            "ds_read_b64_tr_b16 a[194:195], v73 offset:512\n"
            "s_lshl_b32 s78, s74, 10\n"
            "s_add_u32 m0, s78, 81920\n"
            "s_add_u32 s88, s42, 1572864\n"
            "s_addc_u32 s89, s43, 0\n"
            "global_load_lds_dwordx4 v63, s[88:89]\n"
            "ds_read_b64_tr_b16 a[196:197], v73 offset:8192\n"
            "ds_read_b64_tr_b16 a[198:199], v73 offset:8704\n"
            "s_lshl_b32 s78, s74, 10\n"
            "s_add_u32 m0, s78, 86016\n"
            "s_add_u32 s88, s42, 1703936\n"
            "s_addc_u32 s89, s43, 0\n"
            "global_load_lds_dwordx4 v63, s[88:89]\n"
            "s_waitcnt lgkmcnt(0)\n"
            "ds_read_b64_tr_b16 a[136:137], v72 offset:16384\n"
            "s_lshl_b32 s78, s74, 10\n"
            "s_add_u32 m0, s78, 90112\n"
            "s_add_u32 s88, s42, 1835008\n"
            "s_addc_u32 s89, s43, 0\n"
            "global_load_lds_dwordx4 v63, s[88:89]\n"
            "ds_read_b64_tr_b16 a[138:139], v72 offset:16896\n"
            "ds_read_b64_tr_b16 a[140:141], v72 offset:24576\n"
            "s_lshl_b32 s78, s74, 10\n"
            "s_add_u32 m0, s78, 94208\n"
            "s_add_u32 s88, s42, 1966080\n"
            "s_addc_u32 s89, s43, 0\n"
            "global_load_lds_dwordx4 v63, s[88:89]\n"
            "ds_read_b64_tr_b16 a[142:143], v72 offset:25088\n"
            "ds_read_b64_tr_b16 a[200:201], v73 offset:16384\n"
            "ds_read_b64_tr_b16 a[202:203], v73 offset:16896\n"
            "ds_read_b64_tr_b16 a[204:205], v73 offset:24576\n"
            "ds_read_b64_tr_b16 a[206:207], v73 offset:25088\n"
            "s_waitcnt lgkmcnt(0)\n"
            "s_waitcnt vmcnt(16)\n"
            "s_barrier\n"
            "s_lshl_b32 s78, s74, 10\n"
            "s_add_u32 m0, s78, 98304\n"
            "s_add_u32 s88, s44, 2097152\n"
            "s_addc_u32 s89, s45, 0\n"
            "global_load_lds_dwordx4 v62, s[88:89]\n"
            "v_add_u32 v74, 0, v67\n"
            "v_xor_b32 v75, 32, v74\n"
            "v_xor_b32 v76, 64, v74\n"
            "v_xor_b32 v77, 96, v74\n"
            "v_xor_b32 v78, 128, v74\n"
            "v_xor_b32 v79, 160, v74\n"
            "v_xor_b32 v80, 192, v74\n"
            "v_xor_b32 v81, 224, v74\n"
            "ds_read_b64 v[32:33], v74 offset:0\n"
            "ds_read_b64 v[34:35], v75 offset:0\n"
            "ds_read_b64 v[36:37], v76 offset:0\n"
            "ds_read_b64 v[38:39], v77 offset:0\n"
            "ds_read_b64 v[40:41], v78 offset:0\n"
            "ds_read_b64 v[42:43], v79 offset:0\n"
            "ds_read_b64 v[44:45], v80 offset:0\n"
            "ds_read_b64 v[46:47], v81 offset:0\n"
            "ds_read_b64 v[84:85], v74 offset:512\n"
            "ds_read_b64 v[86:87], v75 offset:512\n"
            "ds_read_b64 v[88:89], v76 offset:512\n"
            "s_lshl_b32 s78, s74, 10\n"
            "s_add_u32 m0, s78, 102400\n"
            "s_add_u32 s88, s44, 2228224\n"
            "s_addc_u32 s89, s45, 0\n"
            "global_load_lds_dwordx4 v68, s[88:89]\n"
            "ds_read_b64 v[90:91], v77 offset:512\n"
            "ds_read_b64 v[92:93], v78 offset:512\n"
            "ds_read_b64 v[94:95], v79 offset:512\n"
            "ds_read_b64 v[96:97], v80 offset:512\n"
            "ds_read_b64 v[98:99], v81 offset:512\n"
            "s_waitcnt lgkmcnt(8)\n"
            "v_mfma_f32_16x16x32_bf16 v[48:51], v[32:35], v[0:3], 0\n"
            "v_mfma_f32_16x16x32_bf16 v[52:55], v[32:35], v[16:19], 0\n"
            "v_mfma_f32_16x16x32_bf16 v[48:51], v[36:39], v[4:7], v[48:51]\n"
            "v_mfma_f32_16x16x32_bf16 v[52:55], v[36:39], v[20:23], v[52:55]\n"
            "v_mfma_f32_16x16x32_bf16 v[48:51], v[40:43], v[8:11], v[48:51]\n"
            "v_mfma_f32_16x16x32_bf16 v[52:55], v[40:43], v[24:27], v[52:55]\n"
            "v_mfma_f32_16x16x32_bf16 v[48:51], v[44:47], v[12:15], v[48:51]\n"
            "v_mfma_f32_16x16x32_bf16 v[52:55], v[44:47], v[28:31], v[52:55]\n"
            "ds_read_b64 v[32:33], v74 offset:8192\n"
            "ds_read_b64 v[34:35], v75 offset:8192\n"
            "ds_read_b64 v[36:37], v76 offset:8192\n"
            "ds_read_b64 v[38:39], v77 offset:8192\n"
            "ds_read_b64 v[40:41], v78 offset:8192\n"
            "s_lshl_b32 s78, s74, 10\n"
            "s_add_u32 m0, s78, 106496\n"
            "s_add_u32 s88, s44, 2359296\n"
            "s_addc_u32 s89, s45, 0\n"
            "global_load_lds_dwordx4 v62, s[88:89]\n"
            "ds_read_b64 v[42:43], v79 offset:8192\n"
            "ds_read_b64 v[44:45], v80 offset:8192\n"
            "ds_read_b64 v[46:47], v81 offset:8192\n"
            "s_waitcnt lgkmcnt(8)\n"
            "v_mfma_f32_16x16x32_bf16 v[100:103], v[84:87], v[0:3], 0\n"
            "v_mfma_f32_16x16x32_bf16 v[104:107], v[84:87], v[16:19], 0\n"
            "v_mfma_f32_16x16x32_bf16 v[100:103], v[88:91], v[4:7], v[100:103]\n"
            "v_mfma_f32_16x16x32_bf16 v[104:107], v[88:91], v[20:23], v[104:107]\n"
            "v_mfma_f32_16x16x32_bf16 v[100:103], v[92:95], v[8:11], v[100:103]\n"
            "v_mfma_f32_16x16x32_bf16 v[104:107], v[92:95], v[24:27], v[104:107]\n"
            "v_mfma_f32_16x16x32_bf16 v[100:103], v[96:99], v[12:15], v[100:103]\n"
            "v_mfma_f32_16x16x32_bf16 v[104:107], v[96:99], v[28:31], v[104:107]\n"
            "v_cvt_pk_bf16_f32 v144, v48, v49\n"
            "v_cvt_pk_bf16_f32 v145, v50, v51\n"
            "v_cvt_pk_bf16_f32 v208, v52, v53\n"
            "v_cvt_pk_bf16_f32 v209, v54, v55\n"
            "ds_read_b64 v[84:85], v74 offset:8704\n"
            "ds_read_b64 v[86:87], v75 offset:8704\n"
            "ds_read_b64 v[88:89], v76 offset:8704\n"
            "s_lshl_b32 s78, s74, 10\n"
            "s_add_u32 m0, s78, 110592\n"
            "s_add_u32 s88, s44, 2490368\n"
            "s_addc_u32 s89, s45, 0\n"
            "global_load_lds_dwordx4 v68, s[88:89]\n"
            "ds_read_b64 v[90:91], v77 offset:8704\n"
            "ds_read_b64 v[92:93], v78 offset:8704\n"
            "ds_read_b64 v[94:95], v79 offset:8704\n"
            "ds_read_b64 v[96:97], v80 offset:8704\n"
            "ds_read_b64 v[98:99], v81 offset:8704\n"
            "s_waitcnt lgkmcnt(8)\n"
            "v_mfma_f32_16x16x32_bf16 v[48:51], v[32:35], v[0:3], 0\n"
            "v_mfma_f32_16x16x32_bf16 v[52:55], v[32:35], v[16:19], 0\n"
            "v_mfma_f32_16x16x32_bf16 v[48:51], v[36:39], v[4:7], v[48:51]\n"
            "v_mfma_f32_16x16x32_bf16 v[52:55], v[36:39], v[20:23], v[52:55]\n"
            "v_mfma_f32_16x16x32_bf16 v[48:51], v[40:43], v[8:11], v[48:51]\n"
            "v_mfma_f32_16x16x32_bf16 v[52:55], v[40:43], v[24:27], v[52:55]\n"
            "v_mfma_f32_16x16x32_bf16 v[48:51], v[44:47], v[12:15], v[48:51]\n"
            "v_mfma_f32_16x16x32_bf16 v[52:55], v[44:47], v[28:31], v[52:55]\n"
            "v_cvt_pk_bf16_f32 v146, v100, v101\n"
            "v_cvt_pk_bf16_f32 v147, v102, v103\n"
            "v_cvt_pk_bf16_f32 v210, v104, v105\n"
            "v_cvt_pk_bf16_f32 v211, v106, v107\n"
            "ds_read_b64 v[32:33], v74 offset:16384\n"
            "s_lshl_b32 s78, s74, 10\n"
            "s_add_u32 m0, s78, 114688\n"
            "s_add_u32 s88, s44, 2621440\n"
            "s_addc_u32 s89, s45, 0\n"
            "global_load_lds_dwordx4 v62, s[88:89]\n"
            "ds_read_b64 v[34:35], v75 offset:16384\n"
            "ds_read_b64 v[36:37], v76 offset:16384\n"
            "ds_read_b64 v[38:39], v77 offset:16384\n"
            "ds_read_b64 v[40:41], v78 offset:16384\n"
            "ds_read_b64 v[42:43], v79 offset:16384\n"
            "ds_read_b64 v[44:45], v80 offset:16384\n"
            "ds_read_b64 v[46:47], v81 offset:16384\n"
            "s_waitcnt lgkmcnt(8)\n"
            "v_mfma_f32_16x16x32_bf16 v[100:103], v[84:87], v[0:3], 0\n"
            "v_mfma_f32_16x16x32_bf16 v[104:107], v[84:87], v[16:19], 0\n"
            "v_mfma_f32_16x16x32_bf16 v[100:103], v[88:91], v[4:7], v[100:103]\n"
            "v_mfma_f32_16x16x32_bf16 v[104:107], v[88:91], v[20:23], v[104:107]\n"
            "v_mfma_f32_16x16x32_bf16 v[100:103], v[92:95], v[8:11], v[100:103]\n"
            "v_mfma_f32_16x16x32_bf16 v[104:107], v[92:95], v[24:27], v[104:107]\n"
            "v_mfma_f32_16x16x32_bf16 v[100:103], v[96:99], v[12:15], v[100:103]\n"
            "v_mfma_f32_16x16x32_bf16 v[104:107], v[96:99], v[28:31], v[104:107]\n"
            "v_cvt_pk_bf16_f32 v148, v48, v49\n"
            "v_cvt_pk_bf16_f32 v149, v50, v51\n"
            "v_cvt_pk_bf16_f32 v212, v52, v53\n"
            "s_lshl_b32 s78, s74, 10\n"
            "s_add_u32 m0, s78, 118784\n"
            "s_add_u32 s88, s44, 2752512\n"
            "s_addc_u32 s89, s45, 0\n"
            "global_load_lds_dwordx4 v68, s[88:89]\n"
            "v_cvt_pk_bf16_f32 v213, v54, v55\n"
            "ds_read_b64 v[84:85], v74 offset:16896\n"
            "ds_read_b64 v[86:87], v75 offset:16896\n"
            "ds_read_b64 v[88:89], v76 offset:16896\n"
            "ds_read_b64 v[90:91], v77 offset:16896\n"
            "ds_read_b64 v[92:93], v78 offset:16896\n"
            "ds_read_b64 v[94:95], v79 offset:16896\n"
            "ds_read_b64 v[96:97], v80 offset:16896\n"
            "ds_read_b64 v[98:99], v81 offset:16896\n"
            "s_waitcnt lgkmcnt(8)\n"
            "v_mfma_f32_16x16x32_bf16 v[48:51], v[32:35], v[0:3], 0\n"
            "v_mfma_f32_16x16x32_bf16 v[52:55], v[32:35], v[16:19], 0\n"
            "v_mfma_f32_16x16x32_bf16 v[48:51], v[36:39], v[4:7], v[48:51]\n"
            "v_mfma_f32_16x16x32_bf16 v[52:55], v[36:39], v[20:23], v[52:55]\n"
            "v_mfma_f32_16x16x32_bf16 v[48:51], v[40:43], v[8:11], v[48:51]\n"
            "v_mfma_f32_16x16x32_bf16 v[52:55], v[40:43], v[24:27], v[52:55]\n"
            "v_mfma_f32_16x16x32_bf16 v[48:51], v[44:47], v[12:15], v[48:51]\n"
            "v_mfma_f32_16x16x32_bf16 v[52:55], v[44:47], v[28:31], v[52:55]\n"
            "v_cvt_pk_bf16_f32 v150, v100, v101\n"
            "s_lshl_b32 s78, s74, 10\n"
            "s_add_u32 m0, s78, 122880\n"
            "s_add_u32 s88, s44, 2883584\n"
            "s_addc_u32 s89, s45, 0\n"
            "global_load_lds_dwordx4 v62, s[88:89]\n"
            "v_cvt_pk_bf16_f32 v151, v102, v103\n"
            "v_cvt_pk_bf16_f32 v214, v104, v105\n"
            "v_cvt_pk_bf16_f32 v215, v106, v107\n"
            "ds_read_b64 v[32:33], v74 offset:24576\n"
            "ds_read_b64 v[34:35], v75 offset:24576\n"
            "ds_read_b64 v[36:37], v76 offset:24576\n"
            "ds_read_b64 v[38:39], v77 offset:24576\n"
            "ds_read_b64 v[40:41], v78 offset:24576\n"
            "ds_read_b64 v[42:43], v79 offset:24576\n"
            "ds_read_b64 v[44:45], v80 offset:24576\n"
            "ds_read_b64 v[46:47], v81 offset:24576\n"
            "s_waitcnt lgkmcnt(8)\n"
            "v_mfma_f32_16x16x32_bf16 v[100:103], v[84:87], v[0:3], 0\n"
            "v_mfma_f32_16x16x32_bf16 v[104:107], v[84:87], v[16:19], 0\n"
            "v_mfma_f32_16x16x32_bf16 v[100:103], v[88:91], v[4:7], v[100:103]\n"
            "v_mfma_f32_16x16x32_bf16 v[104:107], v[88:91], v[20:23], v[104:107]\n"
            "v_mfma_f32_16x16x32_bf16 v[100:103], v[92:95], v[8:11], v[100:103]\n"
            "v_mfma_f32_16x16x32_bf16 v[104:107], v[92:95], v[24:27], v[104:107]\n"
            "v_mfma_f32_16x16x32_bf16 v[100:103], v[96:99], v[12:15], v[100:103]\n"
            "s_lshl_b32 s78, s74, 10\n"
            "s_add_u32 m0, s78, 126976\n"
            "s_add_u32 s88, s44, 3014656\n"
            "s_addc_u32 s89, s45, 0\n"
            "global_load_lds_dwordx4 v68, s[88:89]\n"
            "v_mfma_f32_16x16x32_bf16 v[104:107], v[96:99], v[28:31], v[104:107]\n"
            "v_cvt_pk_bf16_f32 v152, v48, v49\n"
            "v_cvt_pk_bf16_f32 v153, v50, v51\n"
            "v_cvt_pk_bf16_f32 v216, v52, v53\n"
            "v_cvt_pk_bf16_f32 v217, v54, v55\n"
            "ds_read_b64 v[84:85], v74 offset:25088\n"
            "ds_read_b64 v[86:87], v75 offset:25088\n"
            "ds_read_b64 v[88:89], v76 offset:25088\n"
            "ds_read_b64 v[90:91], v77 offset:25088\n"
            "ds_read_b64 v[92:93], v78 offset:25088\n"
            "ds_read_b64 v[94:95], v79 offset:25088\n"
            "ds_read_b64 v[96:97], v80 offset:25088\n"
            "ds_read_b64 v[98:99], v81 offset:25088\n"
            "s_waitcnt lgkmcnt(8)\n"
            "v_mfma_f32_16x16x32_bf16 v[48:51], v[32:35], v[0:3], 0\n"
            "v_mfma_f32_16x16x32_bf16 v[52:55], v[32:35], v[16:19], 0\n"
            "v_mfma_f32_16x16x32_bf16 v[48:51], v[36:39], v[4:7], v[48:51]\n"
            "v_mfma_f32_16x16x32_bf16 v[52:55], v[36:39], v[20:23], v[52:55]\n"
            "v_mfma_f32_16x16x32_bf16 v[48:51], v[40:43], v[8:11], v[48:51]\n"
            "v_mfma_f32_16x16x32_bf16 v[52:55], v[40:43], v[24:27], v[52:55]\n"
            "v_mfma_f32_16x16x32_bf16 v[48:51], v[44:47], v[12:15], v[48:51]\n"
            "v_mfma_f32_16x16x32_bf16 v[52:55], v[44:47], v[28:31], v[52:55]\n"
            "v_cvt_pk_bf16_f32 v154, v100, v101\n"
            "v_cvt_pk_bf16_f32 v155, v102, v103\n"
            "v_cvt_pk_bf16_f32 v218, v104, v105\n"
            "v_cvt_pk_bf16_f32 v219, v106, v107\n"
            "s_waitcnt lgkmcnt(0)\n"
            "v_mfma_f32_16x16x32_bf16 v[100:103], v[84:87], v[0:3], 0\n"
            "v_mfma_f32_16x16x32_bf16 v[104:107], v[84:87], v[16:19], 0\n"
            "v_mfma_f32_16x16x32_bf16 v[100:103], v[88:91], v[4:7], v[100:103]\n"
            "v_mfma_f32_16x16x32_bf16 v[104:107], v[88:91], v[20:23], v[104:107]\n"
            "v_mfma_f32_16x16x32_bf16 v[100:103], v[92:95], v[8:11], v[100:103]\n"
            "v_mfma_f32_16x16x32_bf16 v[104:107], v[92:95], v[24:27], v[104:107]\n"
            "v_mfma_f32_16x16x32_bf16 v[100:103], v[96:99], v[12:15], v[100:103]\n"
            "v_mfma_f32_16x16x32_bf16 v[104:107], v[96:99], v[28:31], v[104:107]\n"
            "v_cvt_pk_bf16_f32 v156, v48, v49\n"
            "v_cvt_pk_bf16_f32 v157, v50, v51\n"
            "v_cvt_pk_bf16_f32 v220, v52, v53\n"
            "v_cvt_pk_bf16_f32 v221, v54, v55\n"
            "s_nop 2\n"
            "v_cvt_pk_bf16_f32 v158, v100, v101\n"
            "v_cvt_pk_bf16_f32 v159, v102, v103\n"
            "v_cvt_pk_bf16_f32 v222, v104, v105\n"
            "v_cvt_pk_bf16_f32 v223, v106, v107\n"
            "s_waitcnt vmcnt(16)\n"
            "s_barrier\n"
            "s_lshl_b32 s78, s74, 10\n"
            "s_add_u32 m0, s78, 0\n"
            "s_add_u32 s88, s40, 2097152\n"
            "s_addc_u32 s89, s41, 0\n"
            "global_load_lds_dwordx4 v63, s[88:89]\n"
            "v_add_u32 v72, 32768, v66\n"
            "v_xor_b32 v73, 32, v72\n"
            "s_lshl_b32 s78, s74, 10\n"
            "s_add_u32 m0, s78, 4096\n"
            "s_add_u32 s88, s40, 2228224\n"
            "s_addc_u32 s89, s41, 0\n"
            "global_load_lds_dwordx4 v63, s[88:89]\n"
            "ds_read_b64_tr_b16 a[16:17], v72 offset:0\n"
            "ds_read_b64_tr_b16 a[18:19], v72 offset:512\n"
            "s_lshl_b32 s78, s74, 10\n"
            "s_add_u32 m0, s78, 8192\n"
            "s_add_u32 s88, s40, 2359296\n"
            "s_addc_u32 s89, s41, 0\n"
            "global_load_lds_dwordx4 v63, s[88:89]\n"
            "ds_read_b64_tr_b16 a[20:21], v72 offset:8192\n"
            "ds_read_b64_tr_b16 a[22:23], v72 offset:8704\n"
            "s_lshl_b32 s78, s74, 10\n"
            "s_add_u32 m0, s78, 12288\n"
            "s_add_u32 s88, s40, 2490368\n"
            "s_addc_u32 s89, s41, 0\n"
            "global_load_lds_dwordx4 v63, s[88:89]\n"
            "ds_read_b64_tr_b16 a[80:81], v73 offset:0\n"
            "ds_read_b64_tr_b16 a[82:83], v73 offset:512\n"
            "s_lshl_b32 s78, s74, 10\n"
            "s_add_u32 m0, s78, 16384\n"
            "s_add_u32 s88, s40, 2621440\n"
            "s_addc_u32 s89, s41, 0\n"
            "global_load_lds_dwordx4 v63, s[88:89]\n"
            "ds_read_b64_tr_b16 a[84:85], v73 offset:8192\n"
            "ds_read_b64_tr_b16 a[86:87], v73 offset:8704\n"
            "s_lshl_b32 s78, s74, 10\n"
            "s_add_u32 m0, s78, 20480\n"
            "s_add_u32 s88, s40, 2752512\n"
            "s_addc_u32 s89, s41, 0\n"
            "global_load_lds_dwordx4 v63, s[88:89]\n"
            "s_waitcnt lgkmcnt(0)\n"
            "ds_read_b64_tr_b16 a[24:25], v72 offset:16384\n"
            "s_lshl_b32 s78, s74, 10\n"
            "s_add_u32 m0, s78, 24576\n"
            "s_add_u32 s88, s40, 2883584\n"
            "s_addc_u32 s89, s41, 0\n"
            "global_load_lds_dwordx4 v63, s[88:89]\n"
            "ds_read_b64_tr_b16 a[26:27], v72 offset:16896\n"
            "ds_read_b64_tr_b16 a[28:29], v72 offset:24576\n"
            "s_lshl_b32 s78, s74, 10\n"
            "s_add_u32 m0, s78, 28672\n"
            "s_add_u32 s88, s40, 3014656\n"
            "s_addc_u32 s89, s41, 0\n"
            "global_load_lds_dwordx4 v63, s[88:89]\n"
            "ds_read_b64_tr_b16 a[30:31], v72 offset:25088\n"
            "ds_read_b64_tr_b16 a[88:89], v73 offset:16384\n"
            "ds_read_b64_tr_b16 a[90:91], v73 offset:16896\n"
            "ds_read_b64_tr_b16 a[92:93], v73 offset:24576\n"
            "ds_read_b64_tr_b16 a[94:95], v73 offset:25088\n"
            "s_waitcnt lgkmcnt(0)\n"
            "s_waitcnt vmcnt(16)\n"
            "s_barrier\n"
            "s_lshl_b32 s78, s74, 10\n"
            "s_add_u32 m0, s78, 32768\n"
            "s_add_u32 s88, s42, 2097152\n"
            "s_addc_u32 s89, s43, 0\n"
            "global_load_lds_dwordx4 v63, s[88:89]\n"
            "v_add_u32 v72, 65536, v66\n"
            "v_xor_b32 v73, 32, v72\n"
            "s_lshl_b32 s78, s74, 10\n"
            "s_add_u32 m0, s78, 36864\n"
            "s_add_u32 s88, s42, 2228224\n"
            "s_addc_u32 s89, s43, 0\n"
            "global_load_lds_dwordx4 v63, s[88:89]\n"
            "ds_read_b64_tr_b16 a[144:145], v72 offset:0\n"
            "ds_read_b64_tr_b16 a[146:147], v72 offset:512\n"
            "s_lshl_b32 s78, s74, 10\n"
            "s_add_u32 m0, s78, 40960\n"
            "s_add_u32 s88, s42, 2359296\n"
            "s_addc_u32 s89, s43, 0\n"
            "global_load_lds_dwordx4 v63, s[88:89]\n"
            "ds_read_b64_tr_b16 a[148:149], v72 offset:8192\n"
            "ds_read_b64_tr_b16 a[150:151], v72 offset:8704\n"
            "s_lshl_b32 s78, s74, 10\n"
            "s_add_u32 m0, s78, 45056\n"
            "s_add_u32 s88, s42, 2490368\n"
            "s_addc_u32 s89, s43, 0\n"
            "global_load_lds_dwordx4 v63, s[88:89]\n"
            "ds_read_b64_tr_b16 a[208:209], v73 offset:0\n"
            "ds_read_b64_tr_b16 a[210:211], v73 offset:512\n"
            "s_lshl_b32 s78, s74, 10\n"
            "s_add_u32 m0, s78, 49152\n"
            "s_add_u32 s88, s42, 2621440\n"
            "s_addc_u32 s89, s43, 0\n"
            "global_load_lds_dwordx4 v63, s[88:89]\n"
            "ds_read_b64_tr_b16 a[212:213], v73 offset:8192\n"
            "ds_read_b64_tr_b16 a[214:215], v73 offset:8704\n"
            "s_lshl_b32 s78, s74, 10\n"
            "s_add_u32 m0, s78, 53248\n"
            "s_add_u32 s88, s42, 2752512\n"
            "s_addc_u32 s89, s43, 0\n"
            "global_load_lds_dwordx4 v63, s[88:89]\n"
            "s_waitcnt lgkmcnt(0)\n"
            "ds_read_b64_tr_b16 a[152:153], v72 offset:16384\n"
            "s_lshl_b32 s78, s74, 10\n"
            "s_add_u32 m0, s78, 57344\n"
            "s_add_u32 s88, s42, 2883584\n"
            "s_addc_u32 s89, s43, 0\n"
            "global_load_lds_dwordx4 v63, s[88:89]\n"
            "ds_read_b64_tr_b16 a[154:155], v72 offset:16896\n"
            "ds_read_b64_tr_b16 a[156:157], v72 offset:24576\n"
            "s_lshl_b32 s78, s74, 10\n"
            "s_add_u32 m0, s78, 61440\n"
            "s_add_u32 s88, s42, 3014656\n"
            "s_addc_u32 s89, s43, 0\n"
            "global_load_lds_dwordx4 v63, s[88:89]\n"
            "ds_read_b64_tr_b16 a[158:159], v72 offset:25088\n"
            "ds_read_b64_tr_b16 a[216:217], v73 offset:16384\n"
            "ds_read_b64_tr_b16 a[218:219], v73 offset:16896\n"
            "ds_read_b64_tr_b16 a[220:221], v73 offset:24576\n"
            "ds_read_b64_tr_b16 a[222:223], v73 offset:25088\n"
            "s_waitcnt lgkmcnt(0)\n"
            "s_waitcnt vmcnt(16)\n"
            "s_barrier\n"
            "s_lshl_b32 s78, s74, 10\n"
            "s_add_u32 m0, s78, 65536\n"
            "s_add_u32 s88, s44, 3145728\n"
            "s_addc_u32 s89, s45, 0\n"
            "global_load_lds_dwordx4 v62, s[88:89]\n"
            "v_add_u32 v74, 98304, v67\n"
            "v_xor_b32 v75, 32, v74\n"
            "v_xor_b32 v76, 64, v74\n"
            "v_xor_b32 v77, 96, v74\n"
            "v_xor_b32 v78, 128, v74\n"
            "v_xor_b32 v79, 160, v74\n"
            "v_xor_b32 v80, 192, v74\n"
            "v_xor_b32 v81, 224, v74\n"
            "ds_read_b64 v[32:33], v74 offset:0\n"
            "ds_read_b64 v[34:35], v75 offset:0\n"
            "ds_read_b64 v[36:37], v76 offset:0\n"
            "ds_read_b64 v[38:39], v77 offset:0\n"
            "ds_read_b64 v[40:41], v78 offset:0\n"
            "ds_read_b64 v[42:43], v79 offset:0\n"
            "ds_read_b64 v[44:45], v80 offset:0\n"
            "ds_read_b64 v[46:47], v81 offset:0\n"
            "ds_read_b64 v[84:85], v74 offset:512\n"
            "ds_read_b64 v[86:87], v75 offset:512\n"
            "ds_read_b64 v[88:89], v76 offset:512\n"
            "s_lshl_b32 s78, s74, 10\n"
            "s_add_u32 m0, s78, 69632\n"
            "s_add_u32 s88, s44, 3276800\n"
            "s_addc_u32 s89, s45, 0\n"
            "global_load_lds_dwordx4 v68, s[88:89]\n"
            "ds_read_b64 v[90:91], v77 offset:512\n"
            "ds_read_b64 v[92:93], v78 offset:512\n"
            "ds_read_b64 v[94:95], v79 offset:512\n"
            "ds_read_b64 v[96:97], v80 offset:512\n"
            "ds_read_b64 v[98:99], v81 offset:512\n"
            "s_waitcnt lgkmcnt(8)\n"
            "v_mfma_f32_16x16x32_bf16 v[48:51], v[32:35], v[0:3], 0\n"
            "v_mfma_f32_16x16x32_bf16 v[52:55], v[32:35], v[16:19], 0\n"
            "v_mfma_f32_16x16x32_bf16 v[48:51], v[36:39], v[4:7], v[48:51]\n"
            "v_mfma_f32_16x16x32_bf16 v[52:55], v[36:39], v[20:23], v[52:55]\n"
            "v_mfma_f32_16x16x32_bf16 v[48:51], v[40:43], v[8:11], v[48:51]\n"
            "v_mfma_f32_16x16x32_bf16 v[52:55], v[40:43], v[24:27], v[52:55]\n"
            "v_mfma_f32_16x16x32_bf16 v[48:51], v[44:47], v[12:15], v[48:51]\n"
            "v_mfma_f32_16x16x32_bf16 v[52:55], v[44:47], v[28:31], v[52:55]\n"
            "ds_read_b64 v[32:33], v74 offset:8192\n"
            "ds_read_b64 v[34:35], v75 offset:8192\n"
            "ds_read_b64 v[36:37], v76 offset:8192\n"
            "ds_read_b64 v[38:39], v77 offset:8192\n"
            "ds_read_b64 v[40:41], v78 offset:8192\n"
            "s_lshl_b32 s78, s74, 10\n"
            "s_add_u32 m0, s78, 73728\n"
            "s_add_u32 s88, s44, 3407872\n"
            "s_addc_u32 s89, s45, 0\n"
            "global_load_lds_dwordx4 v62, s[88:89]\n"
            "ds_read_b64 v[42:43], v79 offset:8192\n"
            "ds_read_b64 v[44:45], v80 offset:8192\n"
            "ds_read_b64 v[46:47], v81 offset:8192\n"
            "s_waitcnt lgkmcnt(8)\n"
            "v_mfma_f32_16x16x32_bf16 v[100:103], v[84:87], v[0:3], 0\n"
            "v_mfma_f32_16x16x32_bf16 v[104:107], v[84:87], v[16:19], 0\n"
            "v_mfma_f32_16x16x32_bf16 v[100:103], v[88:91], v[4:7], v[100:103]\n"
            "v_mfma_f32_16x16x32_bf16 v[104:107], v[88:91], v[20:23], v[104:107]\n"
            "v_mfma_f32_16x16x32_bf16 v[100:103], v[92:95], v[8:11], v[100:103]\n"
            "v_mfma_f32_16x16x32_bf16 v[104:107], v[92:95], v[24:27], v[104:107]\n"
            "v_mfma_f32_16x16x32_bf16 v[100:103], v[96:99], v[12:15], v[100:103]\n"
            "v_mfma_f32_16x16x32_bf16 v[104:107], v[96:99], v[28:31], v[104:107]\n"
            "v_cvt_pk_bf16_f32 v160, v48, v49\n"
            "v_cvt_pk_bf16_f32 v161, v50, v51\n"
            "v_cvt_pk_bf16_f32 v224, v52, v53\n"
            "v_cvt_pk_bf16_f32 v225, v54, v55\n"
            "ds_read_b64 v[84:85], v74 offset:8704\n"
            "ds_read_b64 v[86:87], v75 offset:8704\n"
            "ds_read_b64 v[88:89], v76 offset:8704\n"
            "s_lshl_b32 s78, s74, 10\n"
            "s_add_u32 m0, s78, 77824\n"
            "s_add_u32 s88, s44, 3538944\n"
            "s_addc_u32 s89, s45, 0\n"
            "global_load_lds_dwordx4 v68, s[88:89]\n"
            "ds_read_b64 v[90:91], v77 offset:8704\n"
            "ds_read_b64 v[92:93], v78 offset:8704\n"
            "ds_read_b64 v[94:95], v79 offset:8704\n"
            "ds_read_b64 v[96:97], v80 offset:8704\n"
            "ds_read_b64 v[98:99], v81 offset:8704\n"
            "s_waitcnt lgkmcnt(8)\n"
            "v_mfma_f32_16x16x32_bf16 v[48:51], v[32:35], v[0:3], 0\n"
            "v_mfma_f32_16x16x32_bf16 v[52:55], v[32:35], v[16:19], 0\n"
            "v_mfma_f32_16x16x32_bf16 v[48:51], v[36:39], v[4:7], v[48:51]\n"
            "v_mfma_f32_16x16x32_bf16 v[52:55], v[36:39], v[20:23], v[52:55]\n"
            "v_mfma_f32_16x16x32_bf16 v[48:51], v[40:43], v[8:11], v[48:51]\n"
            "v_mfma_f32_16x16x32_bf16 v[52:55], v[40:43], v[24:27], v[52:55]\n"
            "v_mfma_f32_16x16x32_bf16 v[48:51], v[44:47], v[12:15], v[48:51]\n"
            "v_mfma_f32_16x16x32_bf16 v[52:55], v[44:47], v[28:31], v[52:55]\n"
            "v_cvt_pk_bf16_f32 v162, v100, v101\n"
            "v_cvt_pk_bf16_f32 v163, v102, v103\n"
            "v_cvt_pk_bf16_f32 v226, v104, v105\n"
            "v_cvt_pk_bf16_f32 v227, v106, v107\n"
            "ds_read_b64 v[32:33], v74 offset:16384\n"
            "s_lshl_b32 s78, s74, 10\n"
            "s_add_u32 m0, s78, 81920\n"
            "s_add_u32 s88, s44, 3670016\n"
            "s_addc_u32 s89, s45, 0\n"
            "global_load_lds_dwordx4 v62, s[88:89]\n"
            "ds_read_b64 v[34:35], v75 offset:16384\n"
            "ds_read_b64 v[36:37], v76 offset:16384\n"
            "ds_read_b64 v[38:39], v77 offset:16384\n"
            "ds_read_b64 v[40:41], v78 offset:16384\n"
            "ds_read_b64 v[42:43], v79 offset:16384\n"
            "ds_read_b64 v[44:45], v80 offset:16384\n"
            "ds_read_b64 v[46:47], v81 offset:16384\n"
            "s_waitcnt lgkmcnt(8)\n"
            "v_mfma_f32_16x16x32_bf16 v[100:103], v[84:87], v[0:3], 0\n"
            "v_mfma_f32_16x16x32_bf16 v[104:107], v[84:87], v[16:19], 0\n"
            "v_mfma_f32_16x16x32_bf16 v[100:103], v[88:91], v[4:7], v[100:103]\n"
            "v_mfma_f32_16x16x32_bf16 v[104:107], v[88:91], v[20:23], v[104:107]\n"
            "v_mfma_f32_16x16x32_bf16 v[100:103], v[92:95], v[8:11], v[100:103]\n"
            "v_mfma_f32_16x16x32_bf16 v[104:107], v[92:95], v[24:27], v[104:107]\n"
            "v_mfma_f32_16x16x32_bf16 v[100:103], v[96:99], v[12:15], v[100:103]\n"
            "v_mfma_f32_16x16x32_bf16 v[104:107], v[96:99], v[28:31], v[104:107]\n"
            "v_cvt_pk_bf16_f32 v164, v48, v49\n"
            "v_cvt_pk_bf16_f32 v165, v50, v51\n"
            "v_cvt_pk_bf16_f32 v228, v52, v53\n"
            "s_lshl_b32 s78, s74, 10\n"
            "s_add_u32 m0, s78, 86016\n"
            "s_add_u32 s88, s44, 3801088\n"
            "s_addc_u32 s89, s45, 0\n"
            "global_load_lds_dwordx4 v68, s[88:89]\n"
            "v_cvt_pk_bf16_f32 v229, v54, v55\n"
            "ds_read_b64 v[84:85], v74 offset:16896\n"
            "ds_read_b64 v[86:87], v75 offset:16896\n"
            "ds_read_b64 v[88:89], v76 offset:16896\n"
            "ds_read_b64 v[90:91], v77 offset:16896\n"
            "ds_read_b64 v[92:93], v78 offset:16896\n"
            "ds_read_b64 v[94:95], v79 offset:16896\n"
            "ds_read_b64 v[96:97], v80 offset:16896\n"
            "ds_read_b64 v[98:99], v81 offset:16896\n"
            "s_waitcnt lgkmcnt(8)\n"
            "v_mfma_f32_16x16x32_bf16 v[48:51], v[32:35], v[0:3], 0\n"
            "v_mfma_f32_16x16x32_bf16 v[52:55], v[32:35], v[16:19], 0\n"
            "v_mfma_f32_16x16x32_bf16 v[48:51], v[36:39], v[4:7], v[48:51]\n"
            "v_mfma_f32_16x16x32_bf16 v[52:55], v[36:39], v[20:23], v[52:55]\n"
            "v_mfma_f32_16x16x32_bf16 v[48:51], v[40:43], v[8:11], v[48:51]\n"
            "v_mfma_f32_16x16x32_bf16 v[52:55], v[40:43], v[24:27], v[52:55]\n"
            "v_mfma_f32_16x16x32_bf16 v[48:51], v[44:47], v[12:15], v[48:51]\n"
            "v_mfma_f32_16x16x32_bf16 v[52:55], v[44:47], v[28:31], v[52:55]\n"
            "v_cvt_pk_bf16_f32 v166, v100, v101\n"
            "s_lshl_b32 s78, s74, 10\n"
            "s_add_u32 m0, s78, 90112\n"
            "s_add_u32 s88, s44, 3932160\n"
            "s_addc_u32 s89, s45, 0\n"
            "global_load_lds_dwordx4 v62, s[88:89]\n"
            "v_cvt_pk_bf16_f32 v167, v102, v103\n"
            "v_cvt_pk_bf16_f32 v230, v104, v105\n"
            "v_cvt_pk_bf16_f32 v231, v106, v107\n"
            "ds_read_b64 v[32:33], v74 offset:24576\n"
            "ds_read_b64 v[34:35], v75 offset:24576\n"
            "ds_read_b64 v[36:37], v76 offset:24576\n"
            "ds_read_b64 v[38:39], v77 offset:24576\n"
            "ds_read_b64 v[40:41], v78 offset:24576\n"
            "ds_read_b64 v[42:43], v79 offset:24576\n"
            "ds_read_b64 v[44:45], v80 offset:24576\n"
            "ds_read_b64 v[46:47], v81 offset:24576\n"
            "s_waitcnt lgkmcnt(8)\n"
            "v_mfma_f32_16x16x32_bf16 v[100:103], v[84:87], v[0:3], 0\n"
            "v_mfma_f32_16x16x32_bf16 v[104:107], v[84:87], v[16:19], 0\n"
            "v_mfma_f32_16x16x32_bf16 v[100:103], v[88:91], v[4:7], v[100:103]\n"
            "v_mfma_f32_16x16x32_bf16 v[104:107], v[88:91], v[20:23], v[104:107]\n"
            "v_mfma_f32_16x16x32_bf16 v[100:103], v[92:95], v[8:11], v[100:103]\n"
            "v_mfma_f32_16x16x32_bf16 v[104:107], v[92:95], v[24:27], v[104:107]\n"
            "v_mfma_f32_16x16x32_bf16 v[100:103], v[96:99], v[12:15], v[100:103]\n"
            "s_lshl_b32 s78, s74, 10\n"
            "s_add_u32 m0, s78, 94208\n"
            "s_add_u32 s88, s44, 4063232\n"
            "s_addc_u32 s89, s45, 0\n"
            "global_load_lds_dwordx4 v68, s[88:89]\n"
            "v_mfma_f32_16x16x32_bf16 v[104:107], v[96:99], v[28:31], v[104:107]\n"
            "v_cvt_pk_bf16_f32 v168, v48, v49\n"
            "v_cvt_pk_bf16_f32 v169, v50, v51\n"
            "v_cvt_pk_bf16_f32 v232, v52, v53\n"
            "v_cvt_pk_bf16_f32 v233, v54, v55\n"
            "ds_read_b64 v[84:85], v74 offset:25088\n"
            "ds_read_b64 v[86:87], v75 offset:25088\n"
            "ds_read_b64 v[88:89], v76 offset:25088\n"
            "ds_read_b64 v[90:91], v77 offset:25088\n"
            "ds_read_b64 v[92:93], v78 offset:25088\n"
            "ds_read_b64 v[94:95], v79 offset:25088\n"
            "ds_read_b64 v[96:97], v80 offset:25088\n"
            "ds_read_b64 v[98:99], v81 offset:25088\n"
            "s_waitcnt lgkmcnt(8)\n"
            "v_mfma_f32_16x16x32_bf16 v[48:51], v[32:35], v[0:3], 0\n"
            "v_mfma_f32_16x16x32_bf16 v[52:55], v[32:35], v[16:19], 0\n"
            "v_mfma_f32_16x16x32_bf16 v[48:51], v[36:39], v[4:7], v[48:51]\n"
            "v_mfma_f32_16x16x32_bf16 v[52:55], v[36:39], v[20:23], v[52:55]\n"
            "v_mfma_f32_16x16x32_bf16 v[48:51], v[40:43], v[8:11], v[48:51]\n"
            "v_mfma_f32_16x16x32_bf16 v[52:55], v[40:43], v[24:27], v[52:55]\n"
            "v_mfma_f32_16x16x32_bf16 v[48:51], v[44:47], v[12:15], v[48:51]\n"
            "v_mfma_f32_16x16x32_bf16 v[52:55], v[44:47], v[28:31], v[52:55]\n"
            "v_cvt_pk_bf16_f32 v170, v100, v101\n"
            "v_cvt_pk_bf16_f32 v171, v102, v103\n"
            "v_cvt_pk_bf16_f32 v234, v104, v105\n"
            "v_cvt_pk_bf16_f32 v235, v106, v107\n"
            "s_waitcnt lgkmcnt(0)\n"
            "v_mfma_f32_16x16x32_bf16 v[100:103], v[84:87], v[0:3], 0\n"
            "v_mfma_f32_16x16x32_bf16 v[104:107], v[84:87], v[16:19], 0\n"
            "v_mfma_f32_16x16x32_bf16 v[100:103], v[88:91], v[4:7], v[100:103]\n"
            "v_mfma_f32_16x16x32_bf16 v[104:107], v[88:91], v[20:23], v[104:107]\n"
            "v_mfma_f32_16x16x32_bf16 v[100:103], v[92:95], v[8:11], v[100:103]\n"
            "v_mfma_f32_16x16x32_bf16 v[104:107], v[92:95], v[24:27], v[104:107]\n"
            "v_mfma_f32_16x16x32_bf16 v[100:103], v[96:99], v[12:15], v[100:103]\n"
            "v_mfma_f32_16x16x32_bf16 v[104:107], v[96:99], v[28:31], v[104:107]\n"
            "v_cvt_pk_bf16_f32 v172, v48, v49\n"
            "v_cvt_pk_bf16_f32 v173, v50, v51\n"
            "v_cvt_pk_bf16_f32 v236, v52, v53\n"
            "v_cvt_pk_bf16_f32 v237, v54, v55\n"
            "s_nop 2\n"
            "v_cvt_pk_bf16_f32 v174, v100, v101\n"
            "v_cvt_pk_bf16_f32 v175, v102, v103\n"
            "v_cvt_pk_bf16_f32 v238, v104, v105\n"
            "v_cvt_pk_bf16_f32 v239, v106, v107\n"
            "s_waitcnt vmcnt(16)\n"
            "s_barrier\n"
            "s_lshl_b32 s78, s74, 10\n"
            "s_add_u32 m0, s78, 98304\n"
            "s_add_u32 s88, s40, 3145728\n"
            "s_addc_u32 s89, s41, 0\n"
            "global_load_lds_dwordx4 v63, s[88:89]\n"
            "v_add_u32 v72, 0, v66\n"
            "v_xor_b32 v73, 32, v72\n"
            "s_lshl_b32 s78, s74, 10\n"
            "s_add_u32 m0, s78, 102400\n"
            "s_add_u32 s88, s40, 3276800\n"
            "s_addc_u32 s89, s41, 0\n"
            "global_load_lds_dwordx4 v63, s[88:89]\n"
            "ds_read_b64_tr_b16 a[32:33], v72 offset:0\n"
            "ds_read_b64_tr_b16 a[34:35], v72 offset:512\n"
            "s_lshl_b32 s78, s74, 10\n"
            "s_add_u32 m0, s78, 106496\n"
            "s_add_u32 s88, s40, 3407872\n"
            "s_addc_u32 s89, s41, 0\n"
            "global_load_lds_dwordx4 v63, s[88:89]\n"
            "ds_read_b64_tr_b16 a[36:37], v72 offset:8192\n"
            "ds_read_b64_tr_b16 a[38:39], v72 offset:8704\n"
            "s_lshl_b32 s78, s74, 10\n"
            "s_add_u32 m0, s78, 110592\n"
            "s_add_u32 s88, s40, 3538944\n"
            "s_addc_u32 s89, s41, 0\n"
            "global_load_lds_dwordx4 v63, s[88:89]\n"
            "ds_read_b64_tr_b16 a[96:97], v73 offset:0\n"
            "ds_read_b64_tr_b16 a[98:99], v73 offset:512\n"
            "s_lshl_b32 s78, s74, 10\n"
            "s_add_u32 m0, s78, 114688\n"
            "s_add_u32 s88, s40, 3670016\n"
            "s_addc_u32 s89, s41, 0\n"
            "global_load_lds_dwordx4 v63, s[88:89]\n"
            "ds_read_b64_tr_b16 a[100:101], v73 offset:8192\n"
            "ds_read_b64_tr_b16 a[102:103], v73 offset:8704\n"
            "s_lshl_b32 s78, s74, 10\n"
            "s_add_u32 m0, s78, 118784\n"
            "s_add_u32 s88, s40, 3801088\n"
            "s_addc_u32 s89, s41, 0\n"
            "global_load_lds_dwordx4 v63, s[88:89]\n"
            "s_waitcnt lgkmcnt(0)\n"
            "ds_read_b64_tr_b16 a[40:41], v72 offset:16384\n"
            "s_lshl_b32 s78, s74, 10\n"
            "s_add_u32 m0, s78, 122880\n"
            "s_add_u32 s88, s40, 3932160\n"
            "s_addc_u32 s89, s41, 0\n"
            "global_load_lds_dwordx4 v63, s[88:89]\n"
            "ds_read_b64_tr_b16 a[42:43], v72 offset:16896\n"
            "ds_read_b64_tr_b16 a[44:45], v72 offset:24576\n"
            "s_lshl_b32 s78, s74, 10\n"
            "s_add_u32 m0, s78, 126976\n"
            "s_add_u32 s88, s40, 4063232\n"
            "s_addc_u32 s89, s41, 0\n"
            "global_load_lds_dwordx4 v63, s[88:89]\n"
            "ds_read_b64_tr_b16 a[46:47], v72 offset:25088\n"
            "ds_read_b64_tr_b16 a[104:105], v73 offset:16384\n"
            "ds_read_b64_tr_b16 a[106:107], v73 offset:16896\n"
            "ds_read_b64_tr_b16 a[108:109], v73 offset:24576\n"
            "ds_read_b64_tr_b16 a[110:111], v73 offset:25088\n"
            "s_waitcnt lgkmcnt(0)\n"
            "s_waitcnt vmcnt(16)\n"
            "s_barrier\n"
            "s_lshl_b32 s78, s74, 10\n"
            "s_add_u32 m0, s78, 0\n"
            "s_add_u32 s88, s42, 3145728\n"
            "s_addc_u32 s89, s43, 0\n"
            "global_load_lds_dwordx4 v63, s[88:89]\n"
            "v_add_u32 v72, 32768, v66\n"
            "v_xor_b32 v73, 32, v72\n"
            "s_lshl_b32 s78, s74, 10\n"
            "s_add_u32 m0, s78, 4096\n"
            "s_add_u32 s88, s42, 3276800\n"
            "s_addc_u32 s89, s43, 0\n"
            "global_load_lds_dwordx4 v63, s[88:89]\n"
            "ds_read_b64_tr_b16 a[160:161], v72 offset:0\n"
            "ds_read_b64_tr_b16 a[162:163], v72 offset:512\n"
            "s_lshl_b32 s78, s74, 10\n"
            "s_add_u32 m0, s78, 8192\n"
            "s_add_u32 s88, s42, 3407872\n"
            "s_addc_u32 s89, s43, 0\n"
            "global_load_lds_dwordx4 v63, s[88:89]\n"
            "ds_read_b64_tr_b16 a[164:165], v72 offset:8192\n"
            "ds_read_b64_tr_b16 a[166:167], v72 offset:8704\n"
            "s_lshl_b32 s78, s74, 10\n"
            "s_add_u32 m0, s78, 12288\n"
            "s_add_u32 s88, s42, 3538944\n"
            "s_addc_u32 s89, s43, 0\n"
            "global_load_lds_dwordx4 v63, s[88:89]\n"
            "ds_read_b64_tr_b16 a[224:225], v73 offset:0\n"
            "ds_read_b64_tr_b16 a[226:227], v73 offset:512\n"
            "s_lshl_b32 s78, s74, 10\n"
            "s_add_u32 m0, s78, 16384\n"
            "s_add_u32 s88, s42, 3670016\n"
            "s_addc_u32 s89, s43, 0\n"
            "global_load_lds_dwordx4 v63, s[88:89]\n"
            "ds_read_b64_tr_b16 a[228:229], v73 offset:8192\n"
            "ds_read_b64_tr_b16 a[230:231], v73 offset:8704\n"
            "s_lshl_b32 s78, s74, 10\n"
            "s_add_u32 m0, s78, 20480\n"
            "s_add_u32 s88, s42, 3801088\n"
            "s_addc_u32 s89, s43, 0\n"
            "global_load_lds_dwordx4 v63, s[88:89]\n"
            "s_waitcnt lgkmcnt(0)\n"
            "ds_read_b64_tr_b16 a[168:169], v72 offset:16384\n"
            "s_lshl_b32 s78, s74, 10\n"
            "s_add_u32 m0, s78, 24576\n"
            "s_add_u32 s88, s42, 3932160\n"
            "s_addc_u32 s89, s43, 0\n"
            "global_load_lds_dwordx4 v63, s[88:89]\n"
            "ds_read_b64_tr_b16 a[170:171], v72 offset:16896\n"
            "ds_read_b64_tr_b16 a[172:173], v72 offset:24576\n"
            "s_lshl_b32 s78, s74, 10\n"
            "s_add_u32 m0, s78, 28672\n"
            "s_add_u32 s88, s42, 4063232\n"
            "s_addc_u32 s89, s43, 0\n"
            "global_load_lds_dwordx4 v63, s[88:89]\n"
            "ds_read_b64_tr_b16 a[174:175], v72 offset:25088\n"
            "ds_read_b64_tr_b16 a[232:233], v73 offset:16384\n"
            "ds_read_b64_tr_b16 a[234:235], v73 offset:16896\n"
            "ds_read_b64_tr_b16 a[236:237], v73 offset:24576\n"
            "ds_read_b64_tr_b16 a[238:239], v73 offset:25088\n"
            "s_waitcnt lgkmcnt(0)\n"
            "s_waitcnt vmcnt(16)\n"
            "s_barrier\n"
            "v_add_u32 v74, 65536, v67\n"
            "v_xor_b32 v75, 32, v74\n"
            "v_xor_b32 v76, 64, v74\n"
            "v_xor_b32 v77, 96, v74\n"
            "v_xor_b32 v78, 128, v74\n"
            "v_xor_b32 v79, 160, v74\n"
            "v_xor_b32 v80, 192, v74\n"
            "v_xor_b32 v81, 224, v74\n"
            "ds_read_b64 v[32:33], v74 offset:0\n"
            "ds_read_b64 v[34:35], v75 offset:0\n"
            "ds_read_b64 v[36:37], v76 offset:0\n"
            "ds_read_b64 v[38:39], v77 offset:0\n"
            "ds_read_b64 v[40:41], v78 offset:0\n"
            "ds_read_b64 v[42:43], v79 offset:0\n"
            "ds_read_b64 v[44:45], v80 offset:0\n"
            "ds_read_b64 v[46:47], v81 offset:0\n"
            "ds_read_b64 v[84:85], v74 offset:512\n"
            "ds_read_b64 v[86:87], v75 offset:512\n"
            "ds_read_b64 v[88:89], v76 offset:512\n"
            "ds_read_b64 v[90:91], v77 offset:512\n"
            "ds_read_b64 v[92:93], v78 offset:512\n"
            "ds_read_b64 v[94:95], v79 offset:512\n"
            "ds_read_b64 v[96:97], v80 offset:512\n"
            "ds_read_b64 v[98:99], v81 offset:512\n"
            "s_waitcnt lgkmcnt(8)\n"
            "v_mfma_f32_16x16x32_bf16 v[48:51], v[32:35], v[0:3], 0\n"
            "v_mfma_f32_16x16x32_bf16 v[52:55], v[32:35], v[16:19], 0\n"
            "v_mfma_f32_16x16x32_bf16 v[48:51], v[36:39], v[4:7], v[48:51]\n"
            "v_mfma_f32_16x16x32_bf16 v[52:55], v[36:39], v[20:23], v[52:55]\n"
            "v_mfma_f32_16x16x32_bf16 v[48:51], v[40:43], v[8:11], v[48:51]\n"
            "v_mfma_f32_16x16x32_bf16 v[52:55], v[40:43], v[24:27], v[52:55]\n"
            "v_mfma_f32_16x16x32_bf16 v[48:51], v[44:47], v[12:15], v[48:51]\n"
            "v_mfma_f32_16x16x32_bf16 v[52:55], v[44:47], v[28:31], v[52:55]\n"
            "ds_read_b64 v[32:33], v74 offset:8192\n"
            "ds_read_b64 v[34:35], v75 offset:8192\n"
            "ds_read_b64 v[36:37], v76 offset:8192\n"
            "ds_read_b64 v[38:39], v77 offset:8192\n"
            "ds_read_b64 v[40:41], v78 offset:8192\n"
            "ds_read_b64 v[42:43], v79 offset:8192\n"
            "ds_read_b64 v[44:45], v80 offset:8192\n"
            "ds_read_b64 v[46:47], v81 offset:8192\n"
            "s_waitcnt lgkmcnt(8)\n"
            "v_mfma_f32_16x16x32_bf16 v[100:103], v[84:87], v[0:3], 0\n"
            "v_mfma_f32_16x16x32_bf16 v[104:107], v[84:87], v[16:19], 0\n"
            "v_mfma_f32_16x16x32_bf16 v[100:103], v[88:91], v[4:7], v[100:103]\n"
            "v_mfma_f32_16x16x32_bf16 v[104:107], v[88:91], v[20:23], v[104:107]\n"
            "v_mfma_f32_16x16x32_bf16 v[100:103], v[92:95], v[8:11], v[100:103]\n"
            "v_mfma_f32_16x16x32_bf16 v[104:107], v[92:95], v[24:27], v[104:107]\n"
            "v_mfma_f32_16x16x32_bf16 v[100:103], v[96:99], v[12:15], v[100:103]\n"
            "v_mfma_f32_16x16x32_bf16 v[104:107], v[96:99], v[28:31], v[104:107]\n"
            "v_cvt_pk_bf16_f32 v176, v48, v49\n"
            "v_cvt_pk_bf16_f32 v177, v50, v51\n"
            "v_cvt_pk_bf16_f32 v240, v52, v53\n"
            "v_cvt_pk_bf16_f32 v241, v54, v55\n"
            "ds_read_b64 v[84:85], v74 offset:8704\n"
            "ds_read_b64 v[86:87], v75 offset:8704\n"
            "ds_read_b64 v[88:89], v76 offset:8704\n"
            "ds_read_b64 v[90:91], v77 offset:8704\n"
            "ds_read_b64 v[92:93], v78 offset:8704\n"
            "ds_read_b64 v[94:95], v79 offset:8704\n"
            "ds_read_b64 v[96:97], v80 offset:8704\n"
            "ds_read_b64 v[98:99], v81 offset:8704\n"
            "s_waitcnt lgkmcnt(8)\n"
            "v_mfma_f32_16x16x32_bf16 v[48:51], v[32:35], v[0:3], 0\n"
            "v_mfma_f32_16x16x32_bf16 v[52:55], v[32:35], v[16:19], 0\n"
            "v_mfma_f32_16x16x32_bf16 v[48:51], v[36:39], v[4:7], v[48:51]\n"
            "v_mfma_f32_16x16x32_bf16 v[52:55], v[36:39], v[20:23], v[52:55]\n"
            "v_mfma_f32_16x16x32_bf16 v[48:51], v[40:43], v[8:11], v[48:51]\n"
            "v_mfma_f32_16x16x32_bf16 v[52:55], v[40:43], v[24:27], v[52:55]\n"
            "v_mfma_f32_16x16x32_bf16 v[48:51], v[44:47], v[12:15], v[48:51]\n"
            "v_mfma_f32_16x16x32_bf16 v[52:55], v[44:47], v[28:31], v[52:55]\n"
            "v_cvt_pk_bf16_f32 v178, v100, v101\n"
            "v_cvt_pk_bf16_f32 v179, v102, v103\n"
            "v_cvt_pk_bf16_f32 v242, v104, v105\n"
            "v_cvt_pk_bf16_f32 v243, v106, v107\n"
            "ds_read_b64 v[32:33], v74 offset:16384\n"
            "ds_read_b64 v[34:35], v75 offset:16384\n"
            "ds_read_b64 v[36:37], v76 offset:16384\n"
            "ds_read_b64 v[38:39], v77 offset:16384\n"
            "ds_read_b64 v[40:41], v78 offset:16384\n"
            "ds_read_b64 v[42:43], v79 offset:16384\n"
            "ds_read_b64 v[44:45], v80 offset:16384\n"
            "ds_read_b64 v[46:47], v81 offset:16384\n"
            "s_waitcnt lgkmcnt(8)\n"
            "v_mfma_f32_16x16x32_bf16 v[100:103], v[84:87], v[0:3], 0\n"
            "v_mfma_f32_16x16x32_bf16 v[104:107], v[84:87], v[16:19], 0\n"
            "v_mfma_f32_16x16x32_bf16 v[100:103], v[88:91], v[4:7], v[100:103]\n"
            "v_mfma_f32_16x16x32_bf16 v[104:107], v[88:91], v[20:23], v[104:107]\n"
            "v_mfma_f32_16x16x32_bf16 v[100:103], v[92:95], v[8:11], v[100:103]\n"
            "v_mfma_f32_16x16x32_bf16 v[104:107], v[92:95], v[24:27], v[104:107]\n"
            "v_mfma_f32_16x16x32_bf16 v[100:103], v[96:99], v[12:15], v[100:103]\n"
            "v_mfma_f32_16x16x32_bf16 v[104:107], v[96:99], v[28:31], v[104:107]\n"
            "v_cvt_pk_bf16_f32 v180, v48, v49\n"
            "v_cvt_pk_bf16_f32 v181, v50, v51\n"
            "v_cvt_pk_bf16_f32 v244, v52, v53\n"
            "v_cvt_pk_bf16_f32 v245, v54, v55\n"
            "ds_read_b64 v[84:85], v74 offset:16896\n"
            "ds_read_b64 v[86:87], v75 offset:16896\n"
            "ds_read_b64 v[88:89], v76 offset:16896\n"
            "ds_read_b64 v[90:91], v77 offset:16896\n"
            "ds_read_b64 v[92:93], v78 offset:16896\n"
            "ds_read_b64 v[94:95], v79 offset:16896\n"
            "ds_read_b64 v[96:97], v80 offset:16896\n"
            "ds_read_b64 v[98:99], v81 offset:16896\n"
            "s_waitcnt lgkmcnt(8)\n"
            "v_mfma_f32_16x16x32_bf16 v[48:51], v[32:35], v[0:3], 0\n"
            "v_mfma_f32_16x16x32_bf16 v[52:55], v[32:35], v[16:19], 0\n"
            "v_mfma_f32_16x16x32_bf16 v[48:51], v[36:39], v[4:7], v[48:51]\n"
            "v_mfma_f32_16x16x32_bf16 v[52:55], v[36:39], v[20:23], v[52:55]\n"
            "v_mfma_f32_16x16x32_bf16 v[48:51], v[40:43], v[8:11], v[48:51]\n"
            "v_mfma_f32_16x16x32_bf16 v[52:55], v[40:43], v[24:27], v[52:55]\n"
            "v_mfma_f32_16x16x32_bf16 v[48:51], v[44:47], v[12:15], v[48:51]\n"
            "v_mfma_f32_16x16x32_bf16 v[52:55], v[44:47], v[28:31], v[52:55]\n"
            "v_cvt_pk_bf16_f32 v182, v100, v101\n"
            "v_cvt_pk_bf16_f32 v183, v102, v103\n"
            "v_cvt_pk_bf16_f32 v246, v104, v105\n"
            "v_cvt_pk_bf16_f32 v247, v106, v107\n"
            "ds_read_b64 v[32:33], v74 offset:24576\n"
            "ds_read_b64 v[34:35], v75 offset:24576\n"
            "ds_read_b64 v[36:37], v76 offset:24576\n"
            "ds_read_b64 v[38:39], v77 offset:24576\n"
            "ds_read_b64 v[40:41], v78 offset:24576\n"
            "ds_read_b64 v[42:43], v79 offset:24576\n"
            "ds_read_b64 v[44:45], v80 offset:24576\n"
            "ds_read_b64 v[46:47], v81 offset:24576\n"
            "s_waitcnt lgkmcnt(8)\n"
            "v_mfma_f32_16x16x32_bf16 v[100:103], v[84:87], v[0:3], 0\n"
            "v_mfma_f32_16x16x32_bf16 v[104:107], v[84:87], v[16:19], 0\n"
            "v_mfma_f32_16x16x32_bf16 v[100:103], v[88:91], v[4:7], v[100:103]\n"
            "v_mfma_f32_16x16x32_bf16 v[104:107], v[88:91], v[20:23], v[104:107]\n"
            "v_mfma_f32_16x16x32_bf16 v[100:103], v[92:95], v[8:11], v[100:103]\n"
            "v_mfma_f32_16x16x32_bf16 v[104:107], v[92:95], v[24:27], v[104:107]\n"
            "v_mfma_f32_16x16x32_bf16 v[100:103], v[96:99], v[12:15], v[100:103]\n"
            "v_mfma_f32_16x16x32_bf16 v[104:107], v[96:99], v[28:31], v[104:107]\n"
            "v_cvt_pk_bf16_f32 v184, v48, v49\n"
            "v_cvt_pk_bf16_f32 v185, v50, v51\n"
            "v_cvt_pk_bf16_f32 v248, v52, v53\n"
            "v_cvt_pk_bf16_f32 v249, v54, v55\n"
            "ds_read_b64 v[84:85], v74 offset:25088\n"
            "ds_read_b64 v[86:87], v75 offset:25088\n"
            "ds_read_b64 v[88:89], v76 offset:25088\n"
            "ds_read_b64 v[90:91], v77 offset:25088\n"
            "ds_read_b64 v[92:93], v78 offset:25088\n"
            "ds_read_b64 v[94:95], v79 offset:25088\n"
            "ds_read_b64 v[96:97], v80 offset:25088\n"
            "ds_read_b64 v[98:99], v81 offset:25088\n"
            "s_waitcnt lgkmcnt(8)\n"
            "v_mfma_f32_16x16x32_bf16 v[48:51], v[32:35], v[0:3], 0\n"
            "v_mfma_f32_16x16x32_bf16 v[52:55], v[32:35], v[16:19], 0\n"
            "v_mfma_f32_16x16x32_bf16 v[48:51], v[36:39], v[4:7], v[48:51]\n"
            "v_mfma_f32_16x16x32_bf16 v[52:55], v[36:39], v[20:23], v[52:55]\n"
            "v_mfma_f32_16x16x32_bf16 v[48:51], v[40:43], v[8:11], v[48:51]\n"
            "v_mfma_f32_16x16x32_bf16 v[52:55], v[40:43], v[24:27], v[52:55]\n"
            "v_mfma_f32_16x16x32_bf16 v[48:51], v[44:47], v[12:15], v[48:51]\n"
            "v_mfma_f32_16x16x32_bf16 v[52:55], v[44:47], v[28:31], v[52:55]\n"
            "v_cvt_pk_bf16_f32 v186, v100, v101\n"
            "v_cvt_pk_bf16_f32 v187, v102, v103\n"
            "v_cvt_pk_bf16_f32 v250, v104, v105\n"
            "v_cvt_pk_bf16_f32 v251, v106, v107\n"
            "s_waitcnt lgkmcnt(0)\n"
            "v_mfma_f32_16x16x32_bf16 v[100:103], v[84:87], v[0:3], 0\n"
            "v_mfma_f32_16x16x32_bf16 v[104:107], v[84:87], v[16:19], 0\n"
            "v_mfma_f32_16x16x32_bf16 v[100:103], v[88:91], v[4:7], v[100:103]\n"
            "v_mfma_f32_16x16x32_bf16 v[104:107], v[88:91], v[20:23], v[104:107]\n"
            "v_mfma_f32_16x16x32_bf16 v[100:103], v[92:95], v[8:11], v[100:103]\n"
            "v_mfma_f32_16x16x32_bf16 v[104:107], v[92:95], v[24:27], v[104:107]\n"
            "v_mfma_f32_16x16x32_bf16 v[100:103], v[96:99], v[12:15], v[100:103]\n"
            "v_mfma_f32_16x16x32_bf16 v[104:107], v[96:99], v[28:31], v[104:107]\n"
            "v_cvt_pk_bf16_f32 v188, v48, v49\n"
            "v_cvt_pk_bf16_f32 v189, v50, v51\n"
            "v_cvt_pk_bf16_f32 v252, v52, v53\n"
            "v_cvt_pk_bf16_f32 v253, v54, v55\n"
            "s_nop 2\n"
            "v_cvt_pk_bf16_f32 v190, v100, v101\n"
            "v_cvt_pk_bf16_f32 v191, v102, v103\n"
            "v_cvt_pk_bf16_f32 v254, v104, v105\n"
            "v_cvt_pk_bf16_f32 v255, v106, v107\n"
            "s_waitcnt vmcnt(8)\n"
            "s_barrier\n"
            "v_add_u32 v72, 98304, v66\n"
            "v_xor_b32 v73, 32, v72\n"
            "ds_read_b64_tr_b16 a[48:49], v72 offset:0\n"
            "ds_read_b64_tr_b16 a[50:51], v72 offset:512\n"
            "ds_read_b64_tr_b16 a[52:53], v72 offset:8192\n"
            "ds_read_b64_tr_b16 a[54:55], v72 offset:8704\n"
            "ds_read_b64_tr_b16 a[112:113], v73 offset:0\n"
            "ds_read_b64_tr_b16 a[114:115], v73 offset:512\n"
            "ds_read_b64_tr_b16 a[116:117], v73 offset:8192\n"
            "ds_read_b64_tr_b16 a[118:119], v73 offset:8704\n"
            "s_waitcnt lgkmcnt(0)\n"
            "ds_read_b64_tr_b16 a[56:57], v72 offset:16384\n"
            "ds_read_b64_tr_b16 a[58:59], v72 offset:16896\n"
            "ds_read_b64_tr_b16 a[60:61], v72 offset:24576\n"
            "ds_read_b64_tr_b16 a[62:63], v72 offset:25088\n"
            "ds_read_b64_tr_b16 a[120:121], v73 offset:16384\n"
            "ds_read_b64_tr_b16 a[122:123], v73 offset:16896\n"
            "ds_read_b64_tr_b16 a[124:125], v73 offset:24576\n"
            "ds_read_b64_tr_b16 a[126:127], v73 offset:25088\n"
            "s_waitcnt lgkmcnt(0)\n"
            "s_waitcnt vmcnt(0)\n"
            "s_barrier\n"
            "v_add_u32 v72, 0, v66\n"
            "v_xor_b32 v73, 32, v72\n"
            "ds_read_b64_tr_b16 a[176:177], v72 offset:0\n"
            "ds_read_b64_tr_b16 a[178:179], v72 offset:512\n"
            "ds_read_b64_tr_b16 a[180:181], v72 offset:8192\n"
            "ds_read_b64_tr_b16 a[182:183], v72 offset:8704\n"
            "ds_read_b64_tr_b16 a[240:241], v73 offset:0\n"
            "ds_read_b64_tr_b16 a[242:243], v73 offset:512\n"
            "ds_read_b64_tr_b16 a[244:245], v73 offset:8192\n"
            "ds_read_b64_tr_b16 a[246:247], v73 offset:8704\n"
            "s_waitcnt lgkmcnt(0)\n"
            "ds_read_b64_tr_b16 a[184:185], v72 offset:16384\n"
            "ds_read_b64_tr_b16 a[186:187], v72 offset:16896\n"
            "ds_read_b64_tr_b16 a[188:189], v72 offset:24576\n"
            "ds_read_b64_tr_b16 a[190:191], v72 offset:25088\n"
            "ds_read_b64_tr_b16 a[248:249], v73 offset:16384\n"
            "ds_read_b64_tr_b16 a[250:251], v73 offset:16896\n"
            "ds_read_b64_tr_b16 a[252:253], v73 offset:24576\n"
            "ds_read_b64_tr_b16 a[254:255], v73 offset:25088\n"
            "s_waitcnt lgkmcnt(0)\n"
            "s_branch PRO_WDONE_%=\n"
            "PRO_A1_%=:\n"
            "s_lshl_b32 s78, s74, 10\n"
            "s_add_u32 m0, s78, 0\n"
            "s_add_u32 s88, s46, 0\n"
            "s_addc_u32 s89, s47, 0\n"
            "global_load_lds_dwordx4 v64, s[88:89]\n"
            "s_lshl_b32 s78, s74, 10\n"
            "s_add_u32 m0, s78, 4096\n"
            "s_add_u32 s88, s46, 16384\n"
            "s_addc_u32 s89, s47, 0\n"
            "global_load_lds_dwordx4 v64, s[88:89]\n"
            "s_lshl_b32 s78, s74, 10\n"
            "s_add_u32 m0, s78, 8192\n"
            "s_add_u32 s88, s46, 32768\n"
            "s_addc_u32 s89, s47, 0\n"
            "global_load_lds_dwordx4 v64, s[88:89]\n"
            "s_lshl_b32 s78, s74, 10\n"
            "s_add_u32 m0, s78, 12288\n"
            "s_add_u32 s88, s46, 49152\n"
            "s_addc_u32 s89, s47, 0\n"
            "global_load_lds_dwordx4 v64, s[88:89]\n"
            "s_lshl_b32 s78, s74, 10\n"
            "s_add_u32 m0, s78, 16384\n"
            "s_add_u32 s88, s46, 65536\n"
            "s_addc_u32 s89, s47, 0\n"
            "global_load_lds_dwordx4 v64, s[88:89]\n"
            "s_lshl_b32 s78, s74, 10\n"
            "s_add_u32 m0, s78, 20480\n"
            "s_add_u32 s88, s46, 81920\n"
            "s_addc_u32 s89, s47, 0\n"
            "global_load_lds_dwordx4 v64, s[88:89]\n"
            "s_lshl_b32 s78, s74, 10\n"
            "s_add_u32 m0, s78, 24576\n"
            "s_add_u32 s88, s46, 98304\n"
            "s_addc_u32 s89, s47, 0\n"
            "global_load_lds_dwordx4 v64, s[88:89]\n"
            "s_lshl_b32 s78, s74, 10\n"
            "s_add_u32 m0, s78, 28672\n"
            "s_add_u32 s88, s46, 114688\n"
            "s_addc_u32 s89, s47, 0\n"
            "global_load_lds_dwordx4 v64, s[88:89]\n"
            "s_lshl_b32 s78, s74, 10\n"
            "s_add_u32 m0, s78, 32768\n"
            "s_add_u32 s88, s44, 1048576\n"
            "s_addc_u32 s89, s45, 0\n"
            "global_load_lds_dwordx4 v62, s[88:89]\n"
            "s_lshl_b32 s78, s74, 10\n"
            "s_add_u32 m0, s78, 36864\n"
            "s_add_u32 s88, s44, 1179648\n"
            "s_addc_u32 s89, s45, 0\n"
            "global_load_lds_dwordx4 v68, s[88:89]\n"
            "s_lshl_b32 s78, s74, 10\n"
            "s_add_u32 m0, s78, 40960\n"
            "s_add_u32 s88, s44, 1310720\n"
            "s_addc_u32 s89, s45, 0\n"
            "global_load_lds_dwordx4 v62, s[88:89]\n"
            "s_lshl_b32 s78, s74, 10\n"
            "s_add_u32 m0, s78, 45056\n"
            "s_add_u32 s88, s44, 1441792\n"
            "s_addc_u32 s89, s45, 0\n"
            "global_load_lds_dwordx4 v68, s[88:89]\n"
            "s_lshl_b32 s78, s74, 10\n"
            "s_add_u32 m0, s78, 49152\n"
            "s_add_u32 s88, s44, 1572864\n"
            "s_addc_u32 s89, s45, 0\n"
            "global_load_lds_dwordx4 v62, s[88:89]\n"
            "s_lshl_b32 s78, s74, 10\n"
            "s_add_u32 m0, s78, 53248\n"
            "s_add_u32 s88, s44, 1703936\n"
            "s_addc_u32 s89, s45, 0\n"
            "global_load_lds_dwordx4 v68, s[88:89]\n"
            "s_lshl_b32 s78, s74, 10\n"
            "s_add_u32 m0, s78, 57344\n"
            "s_add_u32 s88, s44, 1835008\n"
            "s_addc_u32 s89, s45, 0\n"
            "global_load_lds_dwordx4 v62, s[88:89]\n"
            "s_lshl_b32 s78, s74, 10\n"
            "s_add_u32 m0, s78, 61440\n"
            "s_add_u32 s88, s44, 1966080\n"
            "s_addc_u32 s89, s45, 0\n"
            "global_load_lds_dwordx4 v68, s[88:89]\n"
            "s_lshl_b32 s78, s74, 10\n"
            "s_add_u32 m0, s78, 65536\n"
            "s_add_u32 s88, s40, 1048576\n"
            "s_addc_u32 s89, s41, 0\n"
            "global_load_lds_dwordx4 v63, s[88:89]\n"
            "s_lshl_b32 s78, s74, 10\n"
            "s_add_u32 m0, s78, 69632\n"
            "s_add_u32 s88, s40, 1179648\n"
            "s_addc_u32 s89, s41, 0\n"
            "global_load_lds_dwordx4 v63, s[88:89]\n"
            "s_lshl_b32 s78, s74, 10\n"
            "s_add_u32 m0, s78, 73728\n"
            "s_add_u32 s88, s40, 1310720\n"
            "s_addc_u32 s89, s41, 0\n"
            "global_load_lds_dwordx4 v63, s[88:89]\n"
            "s_lshl_b32 s78, s74, 10\n"
            "s_add_u32 m0, s78, 77824\n"
            "s_add_u32 s88, s40, 1441792\n"
            "s_addc_u32 s89, s41, 0\n"
            "global_load_lds_dwordx4 v63, s[88:89]\n"
            "s_lshl_b32 s78, s74, 10\n"
            "s_add_u32 m0, s78, 81920\n"
            "s_add_u32 s88, s40, 1572864\n"
            "s_addc_u32 s89, s41, 0\n"
            "global_load_lds_dwordx4 v63, s[88:89]\n"
            "s_lshl_b32 s78, s74, 10\n"
            "s_add_u32 m0, s78, 86016\n"
            "s_add_u32 s88, s40, 1703936\n"
            "s_addc_u32 s89, s41, 0\n"
            "global_load_lds_dwordx4 v63, s[88:89]\n"
            "s_lshl_b32 s78, s74, 10\n"
            "s_add_u32 m0, s78, 90112\n"
            "s_add_u32 s88, s40, 1835008\n"
            "s_addc_u32 s89, s41, 0\n"
            "global_load_lds_dwordx4 v63, s[88:89]\n"
            "s_lshl_b32 s78, s74, 10\n"
            "s_add_u32 m0, s78, 94208\n"
            "s_add_u32 s88, s40, 1966080\n"
            "s_addc_u32 s89, s41, 0\n"
            "global_load_lds_dwordx4 v63, s[88:89]\n"
            "s_waitcnt vmcnt(16)\n"
            "s_barrier\n"
            "s_lshl_b32 s78, s74, 10\n"
            "s_add_u32 m0, s78, 98304\n"
            "s_add_u32 s88, s42, 1048576\n"
            "s_addc_u32 s89, s43, 0\n"
            "global_load_lds_dwordx4 v63, s[88:89]\n"
            "ds_read_b64_tr_b16 v[0:1], v70 offset:0\n"
            "s_lshl_b32 s78, s74, 10\n"
            "s_add_u32 m0, s78, 102400\n"
            "s_add_u32 s88, s42, 1179648\n"
            "s_addc_u32 s89, s43, 0\n"
            "global_load_lds_dwordx4 v63, s[88:89]\n"
            "ds_read_b64_tr_b16 v[2:3], v70 offset:4096\n"
            "s_lshl_b32 s78, s74, 10\n"
            "s_add_u32 m0, s78, 106496\n"
            "s_add_u32 s88, s42, 1310720\n"
            "s_addc_u32 s89, s43, 0\n"
            "global_load_lds_dwordx4 v63, s[88:89]\n"
            "ds_read_b64_tr_b16 v[4:5], v70 offset:8192\n"
            "s_lshl_b32 s78, s74, 10\n"
            "s_add_u32 m0, s78, 110592\n"
            "s_add_u32 s88, s42, 1441792\n"
            "s_addc_u32 s89, s43, 0\n"
            "global_load_lds_dwordx4 v63, s[88:89]\n"
            "ds_read_b64_tr_b16 v[6:7], v70 offset:12288\n"
            "s_lshl_b32 s78, s74, 10\n"
            "s_add_u32 m0, s78, 114688\n"
            "s_add_u32 s88, s42, 1572864\n"
            "s_addc_u32 s89, s43, 0\n"
            "global_load_lds_dwordx4 v63, s[88:89]\n"
            "ds_read_b64_tr_b16 v[8:9], v70 offset:16384\n"
            "s_lshl_b32 s78, s74, 10\n"
            "s_add_u32 m0, s78, 118784\n"
            "s_add_u32 s88, s42, 1703936\n"
            "s_addc_u32 s89, s43, 0\n"
            "global_load_lds_dwordx4 v63, s[88:89]\n"
            "ds_read_b64_tr_b16 v[10:11], v70 offset:20480\n"
            "s_lshl_b32 s78, s74, 10\n"
            "s_add_u32 m0, s78, 122880\n"
            "s_add_u32 s88, s42, 1835008\n"
            "s_addc_u32 s89, s43, 0\n"
            "global_load_lds_dwordx4 v63, s[88:89]\n"
            "ds_read_b64_tr_b16 v[12:13], v70 offset:24576\n"
            "s_lshl_b32 s78, s74, 10\n"
            "s_add_u32 m0, s78, 126976\n"
            "s_add_u32 s88, s42, 1966080\n"
            "s_addc_u32 s89, s43, 0\n"
            "global_load_lds_dwordx4 v63, s[88:89]\n"
            "ds_read_b64_tr_b16 v[14:15], v70 offset:28672\n"
            "ds_read_b64_tr_b16 v[16:17], v71 offset:0\n"
            "ds_read_b64_tr_b16 v[18:19], v71 offset:4096\n"
            "ds_read_b64_tr_b16 v[20:21], v71 offset:8192\n"
            "ds_read_b64_tr_b16 v[22:23], v71 offset:12288\n"
            "ds_read_b64_tr_b16 v[24:25], v71 offset:16384\n"
            "ds_read_b64_tr_b16 v[26:27], v71 offset:20480\n"
            "ds_read_b64_tr_b16 v[28:29], v71 offset:24576\n"
            "ds_read_b64_tr_b16 v[30:31], v71 offset:28672\n"
            "s_waitcnt lgkmcnt(0)\n"
            "s_waitcnt vmcnt(16)\n"
            "s_barrier\n"
            "s_lshl_b32 s78, s74, 10\n"
            "s_add_u32 m0, s78, 0\n"
            "s_add_u32 s88, s44, 2097152\n"
            "s_addc_u32 s89, s45, 0\n"
            "global_load_lds_dwordx4 v62, s[88:89]\n"
            "v_add_u32 v74, 32768, v67\n"
            "v_xor_b32 v75, 32, v74\n"
            "v_xor_b32 v76, 64, v74\n"
            "v_xor_b32 v77, 96, v74\n"
            "v_xor_b32 v78, 128, v74\n"
            "v_xor_b32 v79, 160, v74\n"
            "v_xor_b32 v80, 192, v74\n"
            "v_xor_b32 v81, 224, v74\n"
            "ds_read_b64 v[32:33], v74 offset:0\n"
            "ds_read_b64 v[34:35], v75 offset:0\n"
            "ds_read_b64 v[36:37], v76 offset:0\n"
            "ds_read_b64 v[38:39], v77 offset:0\n"
            "ds_read_b64 v[40:41], v78 offset:0\n"
            "ds_read_b64 v[42:43], v79 offset:0\n"
            "ds_read_b64 v[44:45], v80 offset:0\n"
            "ds_read_b64 v[46:47], v81 offset:0\n"
            "ds_read_b64 v[84:85], v74 offset:512\n"
            "ds_read_b64 v[86:87], v75 offset:512\n"
            "ds_read_b64 v[88:89], v76 offset:512\n"
            "s_lshl_b32 s78, s74, 10\n"
            "s_add_u32 m0, s78, 4096\n"
            "s_add_u32 s88, s44, 2228224\n"
            "s_addc_u32 s89, s45, 0\n"
            "global_load_lds_dwordx4 v68, s[88:89]\n"
            "ds_read_b64 v[90:91], v77 offset:512\n"
            "ds_read_b64 v[92:93], v78 offset:512\n"
            "ds_read_b64 v[94:95], v79 offset:512\n"
            "ds_read_b64 v[96:97], v80 offset:512\n"
            "ds_read_b64 v[98:99], v81 offset:512\n"
            "s_waitcnt lgkmcnt(8)\n"
            "v_mfma_f32_16x16x32_bf16 v[48:51], v[32:35], v[0:3], 0\n"
            "v_mfma_f32_16x16x32_bf16 v[52:55], v[32:35], v[16:19], 0\n"
            "v_mfma_f32_16x16x32_bf16 v[48:51], v[36:39], v[4:7], v[48:51]\n"
            "v_mfma_f32_16x16x32_bf16 v[52:55], v[36:39], v[20:23], v[52:55]\n"
            "v_mfma_f32_16x16x32_bf16 v[48:51], v[40:43], v[8:11], v[48:51]\n"
            "v_mfma_f32_16x16x32_bf16 v[52:55], v[40:43], v[24:27], v[52:55]\n"
            "v_mfma_f32_16x16x32_bf16 v[48:51], v[44:47], v[12:15], v[48:51]\n"
            "v_mfma_f32_16x16x32_bf16 v[52:55], v[44:47], v[28:31], v[52:55]\n"
            "ds_read_b64 v[32:33], v74 offset:8192\n"
            "ds_read_b64 v[34:35], v75 offset:8192\n"
            "ds_read_b64 v[36:37], v76 offset:8192\n"
            "ds_read_b64 v[38:39], v77 offset:8192\n"
            "ds_read_b64 v[40:41], v78 offset:8192\n"
            "s_lshl_b32 s78, s74, 10\n"
            "s_add_u32 m0, s78, 8192\n"
            "s_add_u32 s88, s44, 2359296\n"
            "s_addc_u32 s89, s45, 0\n"
            "global_load_lds_dwordx4 v62, s[88:89]\n"
            "ds_read_b64 v[42:43], v79 offset:8192\n"
            "ds_read_b64 v[44:45], v80 offset:8192\n"
            "ds_read_b64 v[46:47], v81 offset:8192\n"
            "s_waitcnt lgkmcnt(8)\n"
            "v_mfma_f32_16x16x32_bf16 v[100:103], v[84:87], v[0:3], 0\n"
            "v_mfma_f32_16x16x32_bf16 v[104:107], v[84:87], v[16:19], 0\n"
            "v_mfma_f32_16x16x32_bf16 v[100:103], v[88:91], v[4:7], v[100:103]\n"
            "v_mfma_f32_16x16x32_bf16 v[104:107], v[88:91], v[20:23], v[104:107]\n"
            "v_mfma_f32_16x16x32_bf16 v[100:103], v[92:95], v[8:11], v[100:103]\n"
            "v_mfma_f32_16x16x32_bf16 v[104:107], v[92:95], v[24:27], v[104:107]\n"
            "v_mfma_f32_16x16x32_bf16 v[100:103], v[96:99], v[12:15], v[100:103]\n"
            "v_mfma_f32_16x16x32_bf16 v[104:107], v[96:99], v[28:31], v[104:107]\n"
            "v_cvt_pk_bf16_f32 v144, v48, v49\n"
            "v_cvt_pk_bf16_f32 v145, v50, v51\n"
            "v_cvt_pk_bf16_f32 v208, v52, v53\n"
            "v_cvt_pk_bf16_f32 v209, v54, v55\n"
            "ds_read_b64 v[84:85], v74 offset:8704\n"
            "ds_read_b64 v[86:87], v75 offset:8704\n"
            "ds_read_b64 v[88:89], v76 offset:8704\n"
            "s_lshl_b32 s78, s74, 10\n"
            "s_add_u32 m0, s78, 12288\n"
            "s_add_u32 s88, s44, 2490368\n"
            "s_addc_u32 s89, s45, 0\n"
            "global_load_lds_dwordx4 v68, s[88:89]\n"
            "ds_read_b64 v[90:91], v77 offset:8704\n"
            "ds_read_b64 v[92:93], v78 offset:8704\n"
            "ds_read_b64 v[94:95], v79 offset:8704\n"
            "ds_read_b64 v[96:97], v80 offset:8704\n"
            "ds_read_b64 v[98:99], v81 offset:8704\n"
            "s_waitcnt lgkmcnt(8)\n"
            "v_mfma_f32_16x16x32_bf16 v[48:51], v[32:35], v[0:3], 0\n"
            "v_mfma_f32_16x16x32_bf16 v[52:55], v[32:35], v[16:19], 0\n"
            "v_mfma_f32_16x16x32_bf16 v[48:51], v[36:39], v[4:7], v[48:51]\n"
            "v_mfma_f32_16x16x32_bf16 v[52:55], v[36:39], v[20:23], v[52:55]\n"
            "v_mfma_f32_16x16x32_bf16 v[48:51], v[40:43], v[8:11], v[48:51]\n"
            "v_mfma_f32_16x16x32_bf16 v[52:55], v[40:43], v[24:27], v[52:55]\n"
            "v_mfma_f32_16x16x32_bf16 v[48:51], v[44:47], v[12:15], v[48:51]\n"
            "v_mfma_f32_16x16x32_bf16 v[52:55], v[44:47], v[28:31], v[52:55]\n"
            "v_cvt_pk_bf16_f32 v146, v100, v101\n"
            "v_cvt_pk_bf16_f32 v147, v102, v103\n"
            "v_cvt_pk_bf16_f32 v210, v104, v105\n"
            "v_cvt_pk_bf16_f32 v211, v106, v107\n"
            "ds_read_b64 v[32:33], v74 offset:16384\n"
            "s_lshl_b32 s78, s74, 10\n"
            "s_add_u32 m0, s78, 16384\n"
            "s_add_u32 s88, s44, 2621440\n"
            "s_addc_u32 s89, s45, 0\n"
            "global_load_lds_dwordx4 v62, s[88:89]\n"
            "ds_read_b64 v[34:35], v75 offset:16384\n"
            "ds_read_b64 v[36:37], v76 offset:16384\n"
            "ds_read_b64 v[38:39], v77 offset:16384\n"
            "ds_read_b64 v[40:41], v78 offset:16384\n"
            "ds_read_b64 v[42:43], v79 offset:16384\n"
            "ds_read_b64 v[44:45], v80 offset:16384\n"
            "ds_read_b64 v[46:47], v81 offset:16384\n"
            "s_waitcnt lgkmcnt(8)\n"
            "v_mfma_f32_16x16x32_bf16 v[100:103], v[84:87], v[0:3], 0\n"
            "v_mfma_f32_16x16x32_bf16 v[104:107], v[84:87], v[16:19], 0\n"
            "v_mfma_f32_16x16x32_bf16 v[100:103], v[88:91], v[4:7], v[100:103]\n"
            "v_mfma_f32_16x16x32_bf16 v[104:107], v[88:91], v[20:23], v[104:107]\n"
            "v_mfma_f32_16x16x32_bf16 v[100:103], v[92:95], v[8:11], v[100:103]\n"
            "v_mfma_f32_16x16x32_bf16 v[104:107], v[92:95], v[24:27], v[104:107]\n"
            "v_mfma_f32_16x16x32_bf16 v[100:103], v[96:99], v[12:15], v[100:103]\n"
            "v_mfma_f32_16x16x32_bf16 v[104:107], v[96:99], v[28:31], v[104:107]\n"
            "v_cvt_pk_bf16_f32 v148, v48, v49\n"
            "v_cvt_pk_bf16_f32 v149, v50, v51\n"
            "v_cvt_pk_bf16_f32 v212, v52, v53\n"
            "s_lshl_b32 s78, s74, 10\n"
            "s_add_u32 m0, s78, 20480\n"
            "s_add_u32 s88, s44, 2752512\n"
            "s_addc_u32 s89, s45, 0\n"
            "global_load_lds_dwordx4 v68, s[88:89]\n"
            "v_cvt_pk_bf16_f32 v213, v54, v55\n"
            "ds_read_b64 v[84:85], v74 offset:16896\n"
            "ds_read_b64 v[86:87], v75 offset:16896\n"
            "ds_read_b64 v[88:89], v76 offset:16896\n"
            "ds_read_b64 v[90:91], v77 offset:16896\n"
            "ds_read_b64 v[92:93], v78 offset:16896\n"
            "ds_read_b64 v[94:95], v79 offset:16896\n"
            "ds_read_b64 v[96:97], v80 offset:16896\n"
            "ds_read_b64 v[98:99], v81 offset:16896\n"
            "s_waitcnt lgkmcnt(8)\n"
            "v_mfma_f32_16x16x32_bf16 v[48:51], v[32:35], v[0:3], 0\n"
            "v_mfma_f32_16x16x32_bf16 v[52:55], v[32:35], v[16:19], 0\n"
            "v_mfma_f32_16x16x32_bf16 v[48:51], v[36:39], v[4:7], v[48:51]\n"
            "v_mfma_f32_16x16x32_bf16 v[52:55], v[36:39], v[20:23], v[52:55]\n"
            "v_mfma_f32_16x16x32_bf16 v[48:51], v[40:43], v[8:11], v[48:51]\n"
            "v_mfma_f32_16x16x32_bf16 v[52:55], v[40:43], v[24:27], v[52:55]\n"
            "v_mfma_f32_16x16x32_bf16 v[48:51], v[44:47], v[12:15], v[48:51]\n"
            "v_mfma_f32_16x16x32_bf16 v[52:55], v[44:47], v[28:31], v[52:55]\n"
            "v_cvt_pk_bf16_f32 v150, v100, v101\n"
            "s_lshl_b32 s78, s74, 10\n"
            "s_add_u32 m0, s78, 24576\n"
            "s_add_u32 s88, s44, 2883584\n"
            "s_addc_u32 s89, s45, 0\n"
            "global_load_lds_dwordx4 v62, s[88:89]\n"
            "v_cvt_pk_bf16_f32 v151, v102, v103\n"
            "v_cvt_pk_bf16_f32 v214, v104, v105\n"
            "v_cvt_pk_bf16_f32 v215, v106, v107\n"
            "ds_read_b64 v[32:33], v74 offset:24576\n"
            "ds_read_b64 v[34:35], v75 offset:24576\n"
            "ds_read_b64 v[36:37], v76 offset:24576\n"
            "ds_read_b64 v[38:39], v77 offset:24576\n"
            "ds_read_b64 v[40:41], v78 offset:24576\n"
            "ds_read_b64 v[42:43], v79 offset:24576\n"
            "ds_read_b64 v[44:45], v80 offset:24576\n"
            "ds_read_b64 v[46:47], v81 offset:24576\n"
            "s_waitcnt lgkmcnt(8)\n"
            "v_mfma_f32_16x16x32_bf16 v[100:103], v[84:87], v[0:3], 0\n"
            "v_mfma_f32_16x16x32_bf16 v[104:107], v[84:87], v[16:19], 0\n"
            "v_mfma_f32_16x16x32_bf16 v[100:103], v[88:91], v[4:7], v[100:103]\n"
            "v_mfma_f32_16x16x32_bf16 v[104:107], v[88:91], v[20:23], v[104:107]\n"
            "v_mfma_f32_16x16x32_bf16 v[100:103], v[92:95], v[8:11], v[100:103]\n"
            "v_mfma_f32_16x16x32_bf16 v[104:107], v[92:95], v[24:27], v[104:107]\n"
            "v_mfma_f32_16x16x32_bf16 v[100:103], v[96:99], v[12:15], v[100:103]\n"
            "s_lshl_b32 s78, s74, 10\n"
            "s_add_u32 m0, s78, 28672\n"
            "s_add_u32 s88, s44, 3014656\n"
            "s_addc_u32 s89, s45, 0\n"
            "global_load_lds_dwordx4 v68, s[88:89]\n"
            "v_mfma_f32_16x16x32_bf16 v[104:107], v[96:99], v[28:31], v[104:107]\n"
            "v_cvt_pk_bf16_f32 v152, v48, v49\n"
            "v_cvt_pk_bf16_f32 v153, v50, v51\n"
            "v_cvt_pk_bf16_f32 v216, v52, v53\n"
            "v_cvt_pk_bf16_f32 v217, v54, v55\n"
            "ds_read_b64 v[84:85], v74 offset:25088\n"
            "ds_read_b64 v[86:87], v75 offset:25088\n"
            "ds_read_b64 v[88:89], v76 offset:25088\n"
            "ds_read_b64 v[90:91], v77 offset:25088\n"
            "ds_read_b64 v[92:93], v78 offset:25088\n"
            "ds_read_b64 v[94:95], v79 offset:25088\n"
            "ds_read_b64 v[96:97], v80 offset:25088\n"
            "ds_read_b64 v[98:99], v81 offset:25088\n"
            "s_waitcnt lgkmcnt(8)\n"
            "v_mfma_f32_16x16x32_bf16 v[48:51], v[32:35], v[0:3], 0\n"
            "v_mfma_f32_16x16x32_bf16 v[52:55], v[32:35], v[16:19], 0\n"
            "v_mfma_f32_16x16x32_bf16 v[48:51], v[36:39], v[4:7], v[48:51]\n"
            "v_mfma_f32_16x16x32_bf16 v[52:55], v[36:39], v[20:23], v[52:55]\n"
            "v_mfma_f32_16x16x32_bf16 v[48:51], v[40:43], v[8:11], v[48:51]\n"
            "v_mfma_f32_16x16x32_bf16 v[52:55], v[40:43], v[24:27], v[52:55]\n"
            "v_mfma_f32_16x16x32_bf16 v[48:51], v[44:47], v[12:15], v[48:51]\n"
            "v_mfma_f32_16x16x32_bf16 v[52:55], v[44:47], v[28:31], v[52:55]\n"
            "v_cvt_pk_bf16_f32 v154, v100, v101\n"
            "v_cvt_pk_bf16_f32 v155, v102, v103\n"
            "v_cvt_pk_bf16_f32 v218, v104, v105\n"
            "v_cvt_pk_bf16_f32 v219, v106, v107\n"
            "s_waitcnt lgkmcnt(0)\n"
            "v_mfma_f32_16x16x32_bf16 v[100:103], v[84:87], v[0:3], 0\n"
            "v_mfma_f32_16x16x32_bf16 v[104:107], v[84:87], v[16:19], 0\n"
            "v_mfma_f32_16x16x32_bf16 v[100:103], v[88:91], v[4:7], v[100:103]\n"
            "v_mfma_f32_16x16x32_bf16 v[104:107], v[88:91], v[20:23], v[104:107]\n"
            "v_mfma_f32_16x16x32_bf16 v[100:103], v[92:95], v[8:11], v[100:103]\n"
            "v_mfma_f32_16x16x32_bf16 v[104:107], v[92:95], v[24:27], v[104:107]\n"
            "v_mfma_f32_16x16x32_bf16 v[100:103], v[96:99], v[12:15], v[100:103]\n"
            "v_mfma_f32_16x16x32_bf16 v[104:107], v[96:99], v[28:31], v[104:107]\n"
            "v_cvt_pk_bf16_f32 v156, v48, v49\n"
            "v_cvt_pk_bf16_f32 v157, v50, v51\n"
            "v_cvt_pk_bf16_f32 v220, v52, v53\n"
            "v_cvt_pk_bf16_f32 v221, v54, v55\n"
            "s_nop 2\n"
            "v_cvt_pk_bf16_f32 v158, v100, v101\n"
            "v_cvt_pk_bf16_f32 v159, v102, v103\n"
            "v_cvt_pk_bf16_f32 v222, v104, v105\n"
            "v_cvt_pk_bf16_f32 v223, v106, v107\n"
            "s_waitcnt vmcnt(16)\n"
            "s_barrier\n"
            "s_lshl_b32 s78, s74, 10\n"
            "s_add_u32 m0, s78, 32768\n"
            "s_add_u32 s88, s40, 2097152\n"
            "s_addc_u32 s89, s41, 0\n"
            "global_load_lds_dwordx4 v63, s[88:89]\n"
            "v_add_u32 v72, 65536, v66\n"
            "v_xor_b32 v73, 32, v72\n"
            "s_lshl_b32 s78, s74, 10\n"
            "s_add_u32 m0, s78, 36864\n"
            "s_add_u32 s88, s40, 2228224\n"
            "s_addc_u32 s89, s41, 0\n"
            "global_load_lds_dwordx4 v63, s[88:89]\n"
            "ds_read_b64_tr_b16 a[16:17], v72 offset:0\n"
            "ds_read_b64_tr_b16 a[18:19], v72 offset:512\n"
            "s_lshl_b32 s78, s74, 10\n"
            "s_add_u32 m0, s78, 40960\n"
            "s_add_u32 s88, s40, 2359296\n"
            "s_addc_u32 s89, s41, 0\n"
            "global_load_lds_dwordx4 v63, s[88:89]\n"
            "ds_read_b64_tr_b16 a[20:21], v72 offset:8192\n"
            "ds_read_b64_tr_b16 a[22:23], v72 offset:8704\n"
            "s_lshl_b32 s78, s74, 10\n"
            "s_add_u32 m0, s78, 45056\n"
            "s_add_u32 s88, s40, 2490368\n"
            "s_addc_u32 s89, s41, 0\n"
            "global_load_lds_dwordx4 v63, s[88:89]\n"
            "ds_read_b64_tr_b16 a[80:81], v73 offset:0\n"
            "ds_read_b64_tr_b16 a[82:83], v73 offset:512\n"
            "s_lshl_b32 s78, s74, 10\n"
            "s_add_u32 m0, s78, 49152\n"
            "s_add_u32 s88, s40, 2621440\n"
            "s_addc_u32 s89, s41, 0\n"
            "global_load_lds_dwordx4 v63, s[88:89]\n"
            "ds_read_b64_tr_b16 a[84:85], v73 offset:8192\n"
            "ds_read_b64_tr_b16 a[86:87], v73 offset:8704\n"
            "s_lshl_b32 s78, s74, 10\n"
            "s_add_u32 m0, s78, 53248\n"
            "s_add_u32 s88, s40, 2752512\n"
            "s_addc_u32 s89, s41, 0\n"
            "global_load_lds_dwordx4 v63, s[88:89]\n"
            "s_waitcnt lgkmcnt(0)\n"
            "ds_read_b64_tr_b16 a[24:25], v72 offset:16384\n"
            "s_lshl_b32 s78, s74, 10\n"
            "s_add_u32 m0, s78, 57344\n"
            "s_add_u32 s88, s40, 2883584\n"
            "s_addc_u32 s89, s41, 0\n"
            "global_load_lds_dwordx4 v63, s[88:89]\n"
            "ds_read_b64_tr_b16 a[26:27], v72 offset:16896\n"
            "ds_read_b64_tr_b16 a[28:29], v72 offset:24576\n"
            "s_lshl_b32 s78, s74, 10\n"
            "s_add_u32 m0, s78, 61440\n"
            "s_add_u32 s88, s40, 3014656\n"
            "s_addc_u32 s89, s41, 0\n"
            "global_load_lds_dwordx4 v63, s[88:89]\n"
            "ds_read_b64_tr_b16 a[30:31], v72 offset:25088\n"
            "ds_read_b64_tr_b16 a[88:89], v73 offset:16384\n"
            "ds_read_b64_tr_b16 a[90:91], v73 offset:16896\n"
            "ds_read_b64_tr_b16 a[92:93], v73 offset:24576\n"
            "ds_read_b64_tr_b16 a[94:95], v73 offset:25088\n"
            "s_waitcnt lgkmcnt(0)\n"
            "s_waitcnt vmcnt(16)\n"
            "s_barrier\n"
            "s_lshl_b32 s78, s74, 10\n"
            "s_add_u32 m0, s78, 65536\n"
            "s_add_u32 s88, s42, 2097152\n"
            "s_addc_u32 s89, s43, 0\n"
            "global_load_lds_dwordx4 v63, s[88:89]\n"
            "v_add_u32 v72, 98304, v66\n"
            "v_xor_b32 v73, 32, v72\n"
            "s_lshl_b32 s78, s74, 10\n"
            "s_add_u32 m0, s78, 69632\n"
            "s_add_u32 s88, s42, 2228224\n"
            "s_addc_u32 s89, s43, 0\n"
            "global_load_lds_dwordx4 v63, s[88:89]\n"
            "ds_read_b64_tr_b16 a[144:145], v72 offset:0\n"
            "ds_read_b64_tr_b16 a[146:147], v72 offset:512\n"
            "s_lshl_b32 s78, s74, 10\n"
            "s_add_u32 m0, s78, 73728\n"
            "s_add_u32 s88, s42, 2359296\n"
            "s_addc_u32 s89, s43, 0\n"
            "global_load_lds_dwordx4 v63, s[88:89]\n"
            "ds_read_b64_tr_b16 a[148:149], v72 offset:8192\n"
            "ds_read_b64_tr_b16 a[150:151], v72 offset:8704\n"
            "s_lshl_b32 s78, s74, 10\n"
            "s_add_u32 m0, s78, 77824\n"
            "s_add_u32 s88, s42, 2490368\n"
            "s_addc_u32 s89, s43, 0\n"
            "global_load_lds_dwordx4 v63, s[88:89]\n"
            "ds_read_b64_tr_b16 a[208:209], v73 offset:0\n"
            "ds_read_b64_tr_b16 a[210:211], v73 offset:512\n"
            "s_lshl_b32 s78, s74, 10\n"
            "s_add_u32 m0, s78, 81920\n"
            "s_add_u32 s88, s42, 2621440\n"
            "s_addc_u32 s89, s43, 0\n"
            "global_load_lds_dwordx4 v63, s[88:89]\n"
            "ds_read_b64_tr_b16 a[212:213], v73 offset:8192\n"
            "ds_read_b64_tr_b16 a[214:215], v73 offset:8704\n"
            "s_lshl_b32 s78, s74, 10\n"
            "s_add_u32 m0, s78, 86016\n"
            "s_add_u32 s88, s42, 2752512\n"
            "s_addc_u32 s89, s43, 0\n"
            "global_load_lds_dwordx4 v63, s[88:89]\n"
            "s_waitcnt lgkmcnt(0)\n"
            "ds_read_b64_tr_b16 a[152:153], v72 offset:16384\n"
            "s_lshl_b32 s78, s74, 10\n"
            "s_add_u32 m0, s78, 90112\n"
            "s_add_u32 s88, s42, 2883584\n"
            "s_addc_u32 s89, s43, 0\n"
            "global_load_lds_dwordx4 v63, s[88:89]\n"
            "ds_read_b64_tr_b16 a[154:155], v72 offset:16896\n"
            "ds_read_b64_tr_b16 a[156:157], v72 offset:24576\n"
            "s_lshl_b32 s78, s74, 10\n"
            "s_add_u32 m0, s78, 94208\n"
            "s_add_u32 s88, s42, 3014656\n"
            "s_addc_u32 s89, s43, 0\n"
            "global_load_lds_dwordx4 v63, s[88:89]\n"
            "ds_read_b64_tr_b16 a[158:159], v72 offset:25088\n"
            "ds_read_b64_tr_b16 a[216:217], v73 offset:16384\n"
            "ds_read_b64_tr_b16 a[218:219], v73 offset:16896\n"
            "ds_read_b64_tr_b16 a[220:221], v73 offset:24576\n"
            "ds_read_b64_tr_b16 a[222:223], v73 offset:25088\n"
            "s_waitcnt lgkmcnt(0)\n"
            "s_waitcnt vmcnt(16)\n"
            "s_barrier\n"
            "s_lshl_b32 s78, s74, 10\n"
            "s_add_u32 m0, s78, 98304\n"
            "s_add_u32 s88, s44, 3145728\n"
            "s_addc_u32 s89, s45, 0\n"
            "global_load_lds_dwordx4 v62, s[88:89]\n"
            "v_add_u32 v74, 0, v67\n"
            "v_xor_b32 v75, 32, v74\n"
            "v_xor_b32 v76, 64, v74\n"
            "v_xor_b32 v77, 96, v74\n"
            "v_xor_b32 v78, 128, v74\n"
            "v_xor_b32 v79, 160, v74\n"
            "v_xor_b32 v80, 192, v74\n"
            "v_xor_b32 v81, 224, v74\n"
            "ds_read_b64 v[32:33], v74 offset:0\n"
            "ds_read_b64 v[34:35], v75 offset:0\n"
            "ds_read_b64 v[36:37], v76 offset:0\n"
            "ds_read_b64 v[38:39], v77 offset:0\n"
            "ds_read_b64 v[40:41], v78 offset:0\n"
            "ds_read_b64 v[42:43], v79 offset:0\n"
            "ds_read_b64 v[44:45], v80 offset:0\n"
            "ds_read_b64 v[46:47], v81 offset:0\n"
            "ds_read_b64 v[84:85], v74 offset:512\n"
            "ds_read_b64 v[86:87], v75 offset:512\n"
            "ds_read_b64 v[88:89], v76 offset:512\n"
            "s_lshl_b32 s78, s74, 10\n"
            "s_add_u32 m0, s78, 102400\n"
            "s_add_u32 s88, s44, 3276800\n"
            "s_addc_u32 s89, s45, 0\n"
            "global_load_lds_dwordx4 v68, s[88:89]\n"
            "ds_read_b64 v[90:91], v77 offset:512\n"
            "ds_read_b64 v[92:93], v78 offset:512\n"
            "ds_read_b64 v[94:95], v79 offset:512\n"
            "ds_read_b64 v[96:97], v80 offset:512\n"
            "ds_read_b64 v[98:99], v81 offset:512\n"
            "s_waitcnt lgkmcnt(8)\n"
            "v_mfma_f32_16x16x32_bf16 v[48:51], v[32:35], v[0:3], 0\n"
            "v_mfma_f32_16x16x32_bf16 v[52:55], v[32:35], v[16:19], 0\n"
            "v_mfma_f32_16x16x32_bf16 v[48:51], v[36:39], v[4:7], v[48:51]\n"
            "v_mfma_f32_16x16x32_bf16 v[52:55], v[36:39], v[20:23], v[52:55]\n"
            "v_mfma_f32_16x16x32_bf16 v[48:51], v[40:43], v[8:11], v[48:51]\n"
            "v_mfma_f32_16x16x32_bf16 v[52:55], v[40:43], v[24:27], v[52:55]\n"
            "v_mfma_f32_16x16x32_bf16 v[48:51], v[44:47], v[12:15], v[48:51]\n"
            "v_mfma_f32_16x16x32_bf16 v[52:55], v[44:47], v[28:31], v[52:55]\n"
            "ds_read_b64 v[32:33], v74 offset:8192\n"
            "ds_read_b64 v[34:35], v75 offset:8192\n"
            "ds_read_b64 v[36:37], v76 offset:8192\n"
            "ds_read_b64 v[38:39], v77 offset:8192\n"
            "ds_read_b64 v[40:41], v78 offset:8192\n"
            "s_lshl_b32 s78, s74, 10\n"
            "s_add_u32 m0, s78, 106496\n"
            "s_add_u32 s88, s44, 3407872\n"
            "s_addc_u32 s89, s45, 0\n"
            "global_load_lds_dwordx4 v62, s[88:89]\n"
            "ds_read_b64 v[42:43], v79 offset:8192\n"
            "ds_read_b64 v[44:45], v80 offset:8192\n"
            "ds_read_b64 v[46:47], v81 offset:8192\n"
            "s_waitcnt lgkmcnt(8)\n"
            "v_mfma_f32_16x16x32_bf16 v[100:103], v[84:87], v[0:3], 0\n"
            "v_mfma_f32_16x16x32_bf16 v[104:107], v[84:87], v[16:19], 0\n"
            "v_mfma_f32_16x16x32_bf16 v[100:103], v[88:91], v[4:7], v[100:103]\n"
            "v_mfma_f32_16x16x32_bf16 v[104:107], v[88:91], v[20:23], v[104:107]\n"
            "v_mfma_f32_16x16x32_bf16 v[100:103], v[92:95], v[8:11], v[100:103]\n"
            "v_mfma_f32_16x16x32_bf16 v[104:107], v[92:95], v[24:27], v[104:107]\n"
            "v_mfma_f32_16x16x32_bf16 v[100:103], v[96:99], v[12:15], v[100:103]\n"
            "v_mfma_f32_16x16x32_bf16 v[104:107], v[96:99], v[28:31], v[104:107]\n"
            "v_cvt_pk_bf16_f32 v160, v48, v49\n"
            "v_cvt_pk_bf16_f32 v161, v50, v51\n"
            "v_cvt_pk_bf16_f32 v224, v52, v53\n"
            "v_cvt_pk_bf16_f32 v225, v54, v55\n"
            "ds_read_b64 v[84:85], v74 offset:8704\n"
            "ds_read_b64 v[86:87], v75 offset:8704\n"
            "ds_read_b64 v[88:89], v76 offset:8704\n"
            "s_lshl_b32 s78, s74, 10\n"
            "s_add_u32 m0, s78, 110592\n"
            "s_add_u32 s88, s44, 3538944\n"
            "s_addc_u32 s89, s45, 0\n"
            "global_load_lds_dwordx4 v68, s[88:89]\n"
            "ds_read_b64 v[90:91], v77 offset:8704\n"
            "ds_read_b64 v[92:93], v78 offset:8704\n"
            "ds_read_b64 v[94:95], v79 offset:8704\n"
            "ds_read_b64 v[96:97], v80 offset:8704\n"
            "ds_read_b64 v[98:99], v81 offset:8704\n"
            "s_waitcnt lgkmcnt(8)\n"
            "v_mfma_f32_16x16x32_bf16 v[48:51], v[32:35], v[0:3], 0\n"
            "v_mfma_f32_16x16x32_bf16 v[52:55], v[32:35], v[16:19], 0\n"
            "v_mfma_f32_16x16x32_bf16 v[48:51], v[36:39], v[4:7], v[48:51]\n"
            "v_mfma_f32_16x16x32_bf16 v[52:55], v[36:39], v[20:23], v[52:55]\n"
            "v_mfma_f32_16x16x32_bf16 v[48:51], v[40:43], v[8:11], v[48:51]\n"
            "v_mfma_f32_16x16x32_bf16 v[52:55], v[40:43], v[24:27], v[52:55]\n"
            "v_mfma_f32_16x16x32_bf16 v[48:51], v[44:47], v[12:15], v[48:51]\n"
            "v_mfma_f32_16x16x32_bf16 v[52:55], v[44:47], v[28:31], v[52:55]\n"
            "v_cvt_pk_bf16_f32 v162, v100, v101\n"
            "v_cvt_pk_bf16_f32 v163, v102, v103\n"
            "v_cvt_pk_bf16_f32 v226, v104, v105\n"
            "v_cvt_pk_bf16_f32 v227, v106, v107\n"
            "ds_read_b64 v[32:33], v74 offset:16384\n"
            "s_lshl_b32 s78, s74, 10\n"
            "s_add_u32 m0, s78, 114688\n"
            "s_add_u32 s88, s44, 3670016\n"
            "s_addc_u32 s89, s45, 0\n"
            "global_load_lds_dwordx4 v62, s[88:89]\n"
            "ds_read_b64 v[34:35], v75 offset:16384\n"
            "ds_read_b64 v[36:37], v76 offset:16384\n"
            "ds_read_b64 v[38:39], v77 offset:16384\n"
            "ds_read_b64 v[40:41], v78 offset:16384\n"
            "ds_read_b64 v[42:43], v79 offset:16384\n"
            "ds_read_b64 v[44:45], v80 offset:16384\n"
            "ds_read_b64 v[46:47], v81 offset:16384\n"
            "s_waitcnt lgkmcnt(8)\n"
            "v_mfma_f32_16x16x32_bf16 v[100:103], v[84:87], v[0:3], 0\n"
            "v_mfma_f32_16x16x32_bf16 v[104:107], v[84:87], v[16:19], 0\n"
            "v_mfma_f32_16x16x32_bf16 v[100:103], v[88:91], v[4:7], v[100:103]\n"
            "v_mfma_f32_16x16x32_bf16 v[104:107], v[88:91], v[20:23], v[104:107]\n"
            "v_mfma_f32_16x16x32_bf16 v[100:103], v[92:95], v[8:11], v[100:103]\n"
            "v_mfma_f32_16x16x32_bf16 v[104:107], v[92:95], v[24:27], v[104:107]\n"
            "v_mfma_f32_16x16x32_bf16 v[100:103], v[96:99], v[12:15], v[100:103]\n"
            "v_mfma_f32_16x16x32_bf16 v[104:107], v[96:99], v[28:31], v[104:107]\n"
            "v_cvt_pk_bf16_f32 v164, v48, v49\n"
            "v_cvt_pk_bf16_f32 v165, v50, v51\n"
            "v_cvt_pk_bf16_f32 v228, v52, v53\n"
            "s_lshl_b32 s78, s74, 10\n"
            "s_add_u32 m0, s78, 118784\n"
            "s_add_u32 s88, s44, 3801088\n"
            "s_addc_u32 s89, s45, 0\n"
            "global_load_lds_dwordx4 v68, s[88:89]\n"
            "v_cvt_pk_bf16_f32 v229, v54, v55\n"
            "ds_read_b64 v[84:85], v74 offset:16896\n"
            "ds_read_b64 v[86:87], v75 offset:16896\n"
            "ds_read_b64 v[88:89], v76 offset:16896\n"
            "ds_read_b64 v[90:91], v77 offset:16896\n"
            "ds_read_b64 v[92:93], v78 offset:16896\n"
            "ds_read_b64 v[94:95], v79 offset:16896\n"
            "ds_read_b64 v[96:97], v80 offset:16896\n"
            "ds_read_b64 v[98:99], v81 offset:16896\n"
            "s_waitcnt lgkmcnt(8)\n"
            "v_mfma_f32_16x16x32_bf16 v[48:51], v[32:35], v[0:3], 0\n"
            "v_mfma_f32_16x16x32_bf16 v[52:55], v[32:35], v[16:19], 0\n"
            "v_mfma_f32_16x16x32_bf16 v[48:51], v[36:39], v[4:7], v[48:51]\n"
            "v_mfma_f32_16x16x32_bf16 v[52:55], v[36:39], v[20:23], v[52:55]\n"
            "v_mfma_f32_16x16x32_bf16 v[48:51], v[40:43], v[8:11], v[48:51]\n"
            "v_mfma_f32_16x16x32_bf16 v[52:55], v[40:43], v[24:27], v[52:55]\n"
            "v_mfma_f32_16x16x32_bf16 v[48:51], v[44:47], v[12:15], v[48:51]\n"
            "v_mfma_f32_16x16x32_bf16 v[52:55], v[44:47], v[28:31], v[52:55]\n"
            "v_cvt_pk_bf16_f32 v166, v100, v101\n"
            "s_lshl_b32 s78, s74, 10\n"
            "s_add_u32 m0, s78, 122880\n"
            "s_add_u32 s88, s44, 3932160\n"
            "s_addc_u32 s89, s45, 0\n"
            "global_load_lds_dwordx4 v62, s[88:89]\n"
            "v_cvt_pk_bf16_f32 v167, v102, v103\n"
            "v_cvt_pk_bf16_f32 v230, v104, v105\n"
            "v_cvt_pk_bf16_f32 v231, v106, v107\n"
            "ds_read_b64 v[32:33], v74 offset:24576\n"
            "ds_read_b64 v[34:35], v75 offset:24576\n"
            "ds_read_b64 v[36:37], v76 offset:24576\n"
            "ds_read_b64 v[38:39], v77 offset:24576\n"
            "ds_read_b64 v[40:41], v78 offset:24576\n"
            "ds_read_b64 v[42:43], v79 offset:24576\n"
            "ds_read_b64 v[44:45], v80 offset:24576\n"
            "ds_read_b64 v[46:47], v81 offset:24576\n"
            "s_waitcnt lgkmcnt(8)\n"
            "v_mfma_f32_16x16x32_bf16 v[100:103], v[84:87], v[0:3], 0\n"
            "v_mfma_f32_16x16x32_bf16 v[104:107], v[84:87], v[16:19], 0\n"
            "v_mfma_f32_16x16x32_bf16 v[100:103], v[88:91], v[4:7], v[100:103]\n"
            "v_mfma_f32_16x16x32_bf16 v[104:107], v[88:91], v[20:23], v[104:107]\n"
            "v_mfma_f32_16x16x32_bf16 v[100:103], v[92:95], v[8:11], v[100:103]\n"
            "v_mfma_f32_16x16x32_bf16 v[104:107], v[92:95], v[24:27], v[104:107]\n"
            "v_mfma_f32_16x16x32_bf16 v[100:103], v[96:99], v[12:15], v[100:103]\n"
            "s_lshl_b32 s78, s74, 10\n"
            "s_add_u32 m0, s78, 126976\n"
            "s_add_u32 s88, s44, 4063232\n"
            "s_addc_u32 s89, s45, 0\n"
            "global_load_lds_dwordx4 v68, s[88:89]\n"
            "v_mfma_f32_16x16x32_bf16 v[104:107], v[96:99], v[28:31], v[104:107]\n"
            "v_cvt_pk_bf16_f32 v168, v48, v49\n"
            "v_cvt_pk_bf16_f32 v169, v50, v51\n"
            "v_cvt_pk_bf16_f32 v232, v52, v53\n"
            "v_cvt_pk_bf16_f32 v233, v54, v55\n"
            "ds_read_b64 v[84:85], v74 offset:25088\n"
            "ds_read_b64 v[86:87], v75 offset:25088\n"
            "ds_read_b64 v[88:89], v76 offset:25088\n"
            "ds_read_b64 v[90:91], v77 offset:25088\n"
            "ds_read_b64 v[92:93], v78 offset:25088\n"
            "ds_read_b64 v[94:95], v79 offset:25088\n"
            "ds_read_b64 v[96:97], v80 offset:25088\n"
            "ds_read_b64 v[98:99], v81 offset:25088\n"
            "s_waitcnt lgkmcnt(8)\n"
            "v_mfma_f32_16x16x32_bf16 v[48:51], v[32:35], v[0:3], 0\n"
            "v_mfma_f32_16x16x32_bf16 v[52:55], v[32:35], v[16:19], 0\n"
            "v_mfma_f32_16x16x32_bf16 v[48:51], v[36:39], v[4:7], v[48:51]\n"
            "v_mfma_f32_16x16x32_bf16 v[52:55], v[36:39], v[20:23], v[52:55]\n"
            "v_mfma_f32_16x16x32_bf16 v[48:51], v[40:43], v[8:11], v[48:51]\n"
            "v_mfma_f32_16x16x32_bf16 v[52:55], v[40:43], v[24:27], v[52:55]\n"
            "v_mfma_f32_16x16x32_bf16 v[48:51], v[44:47], v[12:15], v[48:51]\n"
            "v_mfma_f32_16x16x32_bf16 v[52:55], v[44:47], v[28:31], v[52:55]\n"
            "v_cvt_pk_bf16_f32 v170, v100, v101\n"
            "v_cvt_pk_bf16_f32 v171, v102, v103\n"
            "v_cvt_pk_bf16_f32 v234, v104, v105\n"
            "v_cvt_pk_bf16_f32 v235, v106, v107\n"
            "s_waitcnt lgkmcnt(0)\n"
            "v_mfma_f32_16x16x32_bf16 v[100:103], v[84:87], v[0:3], 0\n"
            "v_mfma_f32_16x16x32_bf16 v[104:107], v[84:87], v[16:19], 0\n"
            "v_mfma_f32_16x16x32_bf16 v[100:103], v[88:91], v[4:7], v[100:103]\n"
            "v_mfma_f32_16x16x32_bf16 v[104:107], v[88:91], v[20:23], v[104:107]\n"
            "v_mfma_f32_16x16x32_bf16 v[100:103], v[92:95], v[8:11], v[100:103]\n"
            "v_mfma_f32_16x16x32_bf16 v[104:107], v[92:95], v[24:27], v[104:107]\n"
            "v_mfma_f32_16x16x32_bf16 v[100:103], v[96:99], v[12:15], v[100:103]\n"
            "v_mfma_f32_16x16x32_bf16 v[104:107], v[96:99], v[28:31], v[104:107]\n"
            "v_cvt_pk_bf16_f32 v172, v48, v49\n"
            "v_cvt_pk_bf16_f32 v173, v50, v51\n"
            "v_cvt_pk_bf16_f32 v236, v52, v53\n"
            "v_cvt_pk_bf16_f32 v237, v54, v55\n"
            "s_nop 2\n"
            "v_cvt_pk_bf16_f32 v174, v100, v101\n"
            "v_cvt_pk_bf16_f32 v175, v102, v103\n"
            "v_cvt_pk_bf16_f32 v238, v104, v105\n"
            "v_cvt_pk_bf16_f32 v239, v106, v107\n"
            "s_waitcnt vmcnt(16)\n"
            "s_barrier\n"
            "s_lshl_b32 s78, s74, 10\n"
            "s_add_u32 m0, s78, 0\n"
            "s_add_u32 s88, s40, 3145728\n"
            "s_addc_u32 s89, s41, 0\n"
            "global_load_lds_dwordx4 v63, s[88:89]\n"
            "v_add_u32 v72, 32768, v66\n"
            "v_xor_b32 v73, 32, v72\n"
            "s_lshl_b32 s78, s74, 10\n"
            "s_add_u32 m0, s78, 4096\n"
            "s_add_u32 s88, s40, 3276800\n"
            "s_addc_u32 s89, s41, 0\n"
            "global_load_lds_dwordx4 v63, s[88:89]\n"
            "ds_read_b64_tr_b16 a[32:33], v72 offset:0\n"
            "ds_read_b64_tr_b16 a[34:35], v72 offset:512\n"
            "s_lshl_b32 s78, s74, 10\n"
            "s_add_u32 m0, s78, 8192\n"
            "s_add_u32 s88, s40, 3407872\n"
            "s_addc_u32 s89, s41, 0\n"
            "global_load_lds_dwordx4 v63, s[88:89]\n"
            "ds_read_b64_tr_b16 a[36:37], v72 offset:8192\n"
            "ds_read_b64_tr_b16 a[38:39], v72 offset:8704\n"
            "s_lshl_b32 s78, s74, 10\n"
            "s_add_u32 m0, s78, 12288\n"
            "s_add_u32 s88, s40, 3538944\n"
            "s_addc_u32 s89, s41, 0\n"
            "global_load_lds_dwordx4 v63, s[88:89]\n"
            "ds_read_b64_tr_b16 a[96:97], v73 offset:0\n"
            "ds_read_b64_tr_b16 a[98:99], v73 offset:512\n"
            "s_lshl_b32 s78, s74, 10\n"
            "s_add_u32 m0, s78, 16384\n"
            "s_add_u32 s88, s40, 3670016\n"
            "s_addc_u32 s89, s41, 0\n"
            "global_load_lds_dwordx4 v63, s[88:89]\n"
            "ds_read_b64_tr_b16 a[100:101], v73 offset:8192\n"
            "ds_read_b64_tr_b16 a[102:103], v73 offset:8704\n"
            "s_lshl_b32 s78, s74, 10\n"
            "s_add_u32 m0, s78, 20480\n"
            "s_add_u32 s88, s40, 3801088\n"
            "s_addc_u32 s89, s41, 0\n"
            "global_load_lds_dwordx4 v63, s[88:89]\n"
            "s_waitcnt lgkmcnt(0)\n"
            "ds_read_b64_tr_b16 a[40:41], v72 offset:16384\n"
            "s_lshl_b32 s78, s74, 10\n"
            "s_add_u32 m0, s78, 24576\n"
            "s_add_u32 s88, s40, 3932160\n"
            "s_addc_u32 s89, s41, 0\n"
            "global_load_lds_dwordx4 v63, s[88:89]\n"
            "ds_read_b64_tr_b16 a[42:43], v72 offset:16896\n"
            "ds_read_b64_tr_b16 a[44:45], v72 offset:24576\n"
            "s_lshl_b32 s78, s74, 10\n"
            "s_add_u32 m0, s78, 28672\n"
            "s_add_u32 s88, s40, 4063232\n"
            "s_addc_u32 s89, s41, 0\n"
            "global_load_lds_dwordx4 v63, s[88:89]\n"
            "ds_read_b64_tr_b16 a[46:47], v72 offset:25088\n"
            "ds_read_b64_tr_b16 a[104:105], v73 offset:16384\n"
            "ds_read_b64_tr_b16 a[106:107], v73 offset:16896\n"
            "ds_read_b64_tr_b16 a[108:109], v73 offset:24576\n"
            "ds_read_b64_tr_b16 a[110:111], v73 offset:25088\n"
            "s_waitcnt lgkmcnt(0)\n"
            "s_waitcnt vmcnt(16)\n"
            "s_barrier\n"
            "s_lshl_b32 s78, s74, 10\n"
            "s_add_u32 m0, s78, 32768\n"
            "s_add_u32 s88, s42, 3145728\n"
            "s_addc_u32 s89, s43, 0\n"
            "global_load_lds_dwordx4 v63, s[88:89]\n"
            "v_add_u32 v72, 65536, v66\n"
            "v_xor_b32 v73, 32, v72\n"
            "s_lshl_b32 s78, s74, 10\n"
            "s_add_u32 m0, s78, 36864\n"
            "s_add_u32 s88, s42, 3276800\n"
            "s_addc_u32 s89, s43, 0\n"
            "global_load_lds_dwordx4 v63, s[88:89]\n"
            "ds_read_b64_tr_b16 a[160:161], v72 offset:0\n"
            "ds_read_b64_tr_b16 a[162:163], v72 offset:512\n"
            "s_lshl_b32 s78, s74, 10\n"
            "s_add_u32 m0, s78, 40960\n"
            "s_add_u32 s88, s42, 3407872\n"
            "s_addc_u32 s89, s43, 0\n"
            "global_load_lds_dwordx4 v63, s[88:89]\n"
            "ds_read_b64_tr_b16 a[164:165], v72 offset:8192\n"
            "ds_read_b64_tr_b16 a[166:167], v72 offset:8704\n"
            "s_lshl_b32 s78, s74, 10\n"
            "s_add_u32 m0, s78, 45056\n"
            "s_add_u32 s88, s42, 3538944\n"
            "s_addc_u32 s89, s43, 0\n"
            "global_load_lds_dwordx4 v63, s[88:89]\n"
            "ds_read_b64_tr_b16 a[224:225], v73 offset:0\n"
            "ds_read_b64_tr_b16 a[226:227], v73 offset:512\n"
            "s_lshl_b32 s78, s74, 10\n"
            "s_add_u32 m0, s78, 49152\n"
            "s_add_u32 s88, s42, 3670016\n"
            "s_addc_u32 s89, s43, 0\n"
            "global_load_lds_dwordx4 v63, s[88:89]\n"
            "ds_read_b64_tr_b16 a[228:229], v73 offset:8192\n"
            "ds_read_b64_tr_b16 a[230:231], v73 offset:8704\n"
            "s_lshl_b32 s78, s74, 10\n"
            "s_add_u32 m0, s78, 53248\n"
            "s_add_u32 s88, s42, 3801088\n"
            "s_addc_u32 s89, s43, 0\n"
            "global_load_lds_dwordx4 v63, s[88:89]\n"
            "s_waitcnt lgkmcnt(0)\n"
            "ds_read_b64_tr_b16 a[168:169], v72 offset:16384\n"
            "s_lshl_b32 s78, s74, 10\n"
            "s_add_u32 m0, s78, 57344\n"
            "s_add_u32 s88, s42, 3932160\n"
            "s_addc_u32 s89, s43, 0\n"
            "global_load_lds_dwordx4 v63, s[88:89]\n"
            "ds_read_b64_tr_b16 a[170:171], v72 offset:16896\n"
            "ds_read_b64_tr_b16 a[172:173], v72 offset:24576\n"
            "s_lshl_b32 s78, s74, 10\n"
            "s_add_u32 m0, s78, 61440\n"
            "s_add_u32 s88, s42, 4063232\n"
            "s_addc_u32 s89, s43, 0\n"
            "global_load_lds_dwordx4 v63, s[88:89]\n"
            "ds_read_b64_tr_b16 a[174:175], v72 offset:25088\n"
            "ds_read_b64_tr_b16 a[232:233], v73 offset:16384\n"
            "ds_read_b64_tr_b16 a[234:235], v73 offset:16896\n"
            "ds_read_b64_tr_b16 a[236:237], v73 offset:24576\n"
            "ds_read_b64_tr_b16 a[238:239], v73 offset:25088\n"
            "s_waitcnt lgkmcnt(0)\n"
            "s_waitcnt vmcnt(16)\n"
            "s_barrier\n"
            "s_lshl_b32 s78, s74, 10\n"
            "s_add_u32 m0, s78, 65536\n"
            "s_add_u32 s88, s44, 0\n"
            "s_addc_u32 s89, s45, 0\n"
            "global_load_lds_dwordx4 v62, s[88:89]\n"
            "v_add_u32 v74, 98304, v67\n"
            "v_xor_b32 v75, 32, v74\n"
            "v_xor_b32 v76, 64, v74\n"
            "v_xor_b32 v77, 96, v74\n"
            "v_xor_b32 v78, 128, v74\n"
            "v_xor_b32 v79, 160, v74\n"
            "v_xor_b32 v80, 192, v74\n"
            "v_xor_b32 v81, 224, v74\n"
            "ds_read_b64 v[32:33], v74 offset:0\n"
            "ds_read_b64 v[34:35], v75 offset:0\n"
            "ds_read_b64 v[36:37], v76 offset:0\n"
            "ds_read_b64 v[38:39], v77 offset:0\n"
            "ds_read_b64 v[40:41], v78 offset:0\n"
            "ds_read_b64 v[42:43], v79 offset:0\n"
            "ds_read_b64 v[44:45], v80 offset:0\n"
            "ds_read_b64 v[46:47], v81 offset:0\n"
            "ds_read_b64 v[84:85], v74 offset:512\n"
            "ds_read_b64 v[86:87], v75 offset:512\n"
            "ds_read_b64 v[88:89], v76 offset:512\n"
            "s_lshl_b32 s78, s74, 10\n"
            "s_add_u32 m0, s78, 69632\n"
            "s_add_u32 s88, s44, 131072\n"
            "s_addc_u32 s89, s45, 0\n"
            "global_load_lds_dwordx4 v68, s[88:89]\n"
            "ds_read_b64 v[90:91], v77 offset:512\n"
            "ds_read_b64 v[92:93], v78 offset:512\n"
            "ds_read_b64 v[94:95], v79 offset:512\n"
            "ds_read_b64 v[96:97], v80 offset:512\n"
            "ds_read_b64 v[98:99], v81 offset:512\n"
            "s_waitcnt lgkmcnt(8)\n"
            "v_mfma_f32_16x16x32_bf16 v[48:51], v[32:35], v[0:3], 0\n"
            "v_mfma_f32_16x16x32_bf16 v[52:55], v[32:35], v[16:19], 0\n"
            "v_mfma_f32_16x16x32_bf16 v[48:51], v[36:39], v[4:7], v[48:51]\n"
            "v_mfma_f32_16x16x32_bf16 v[52:55], v[36:39], v[20:23], v[52:55]\n"
            "v_mfma_f32_16x16x32_bf16 v[48:51], v[40:43], v[8:11], v[48:51]\n"
            "v_mfma_f32_16x16x32_bf16 v[52:55], v[40:43], v[24:27], v[52:55]\n"
            "v_mfma_f32_16x16x32_bf16 v[48:51], v[44:47], v[12:15], v[48:51]\n"
            "v_mfma_f32_16x16x32_bf16 v[52:55], v[44:47], v[28:31], v[52:55]\n"
            "ds_read_b64 v[32:33], v74 offset:8192\n"
            "ds_read_b64 v[34:35], v75 offset:8192\n"
            "ds_read_b64 v[36:37], v76 offset:8192\n"
            "ds_read_b64 v[38:39], v77 offset:8192\n"
            "ds_read_b64 v[40:41], v78 offset:8192\n"
            "s_lshl_b32 s78, s74, 10\n"
            "s_add_u32 m0, s78, 73728\n"
            "s_add_u32 s88, s44, 262144\n"
            "s_addc_u32 s89, s45, 0\n"
            "global_load_lds_dwordx4 v62, s[88:89]\n"
            "ds_read_b64 v[42:43], v79 offset:8192\n"
            "ds_read_b64 v[44:45], v80 offset:8192\n"
            "ds_read_b64 v[46:47], v81 offset:8192\n"
            "s_waitcnt lgkmcnt(8)\n"
            "v_mfma_f32_16x16x32_bf16 v[100:103], v[84:87], v[0:3], 0\n"
            "v_mfma_f32_16x16x32_bf16 v[104:107], v[84:87], v[16:19], 0\n"
            "v_mfma_f32_16x16x32_bf16 v[100:103], v[88:91], v[4:7], v[100:103]\n"
            "v_mfma_f32_16x16x32_bf16 v[104:107], v[88:91], v[20:23], v[104:107]\n"
            "v_mfma_f32_16x16x32_bf16 v[100:103], v[92:95], v[8:11], v[100:103]\n"
            "v_mfma_f32_16x16x32_bf16 v[104:107], v[92:95], v[24:27], v[104:107]\n"
            "v_mfma_f32_16x16x32_bf16 v[100:103], v[96:99], v[12:15], v[100:103]\n"
            "v_mfma_f32_16x16x32_bf16 v[104:107], v[96:99], v[28:31], v[104:107]\n"
            "v_cvt_pk_bf16_f32 v176, v48, v49\n"
            "v_cvt_pk_bf16_f32 v177, v50, v51\n"
            "v_cvt_pk_bf16_f32 v240, v52, v53\n"
            "v_cvt_pk_bf16_f32 v241, v54, v55\n"
            "ds_read_b64 v[84:85], v74 offset:8704\n"
            "ds_read_b64 v[86:87], v75 offset:8704\n"
            "ds_read_b64 v[88:89], v76 offset:8704\n"
            "s_lshl_b32 s78, s74, 10\n"
            "s_add_u32 m0, s78, 77824\n"
            "s_add_u32 s88, s44, 393216\n"
            "s_addc_u32 s89, s45, 0\n"
            "global_load_lds_dwordx4 v68, s[88:89]\n"
            "ds_read_b64 v[90:91], v77 offset:8704\n"
            "ds_read_b64 v[92:93], v78 offset:8704\n"
            "ds_read_b64 v[94:95], v79 offset:8704\n"
            "ds_read_b64 v[96:97], v80 offset:8704\n"
            "ds_read_b64 v[98:99], v81 offset:8704\n"
            "s_waitcnt lgkmcnt(8)\n"
            "v_mfma_f32_16x16x32_bf16 v[48:51], v[32:35], v[0:3], 0\n"
            "v_mfma_f32_16x16x32_bf16 v[52:55], v[32:35], v[16:19], 0\n"
            "v_mfma_f32_16x16x32_bf16 v[48:51], v[36:39], v[4:7], v[48:51]\n"
            "v_mfma_f32_16x16x32_bf16 v[52:55], v[36:39], v[20:23], v[52:55]\n"
            "v_mfma_f32_16x16x32_bf16 v[48:51], v[40:43], v[8:11], v[48:51]\n"
            "v_mfma_f32_16x16x32_bf16 v[52:55], v[40:43], v[24:27], v[52:55]\n"
            "v_mfma_f32_16x16x32_bf16 v[48:51], v[44:47], v[12:15], v[48:51]\n"
            "v_mfma_f32_16x16x32_bf16 v[52:55], v[44:47], v[28:31], v[52:55]\n"
            "v_cvt_pk_bf16_f32 v178, v100, v101\n"
            "v_cvt_pk_bf16_f32 v179, v102, v103\n"
            "v_cvt_pk_bf16_f32 v242, v104, v105\n"
            "v_cvt_pk_bf16_f32 v243, v106, v107\n"
            "ds_read_b64 v[32:33], v74 offset:16384\n"
            "s_lshl_b32 s78, s74, 10\n"
            "s_add_u32 m0, s78, 81920\n"
            "s_add_u32 s88, s44, 524288\n"
            "s_addc_u32 s89, s45, 0\n"
            "global_load_lds_dwordx4 v62, s[88:89]\n"
            "ds_read_b64 v[34:35], v75 offset:16384\n"
            "ds_read_b64 v[36:37], v76 offset:16384\n"
            "ds_read_b64 v[38:39], v77 offset:16384\n"
            "ds_read_b64 v[40:41], v78 offset:16384\n"
            "ds_read_b64 v[42:43], v79 offset:16384\n"
            "ds_read_b64 v[44:45], v80 offset:16384\n"
            "ds_read_b64 v[46:47], v81 offset:16384\n"
            "s_waitcnt lgkmcnt(8)\n"
            "v_mfma_f32_16x16x32_bf16 v[100:103], v[84:87], v[0:3], 0\n"
            "v_mfma_f32_16x16x32_bf16 v[104:107], v[84:87], v[16:19], 0\n"
            "v_mfma_f32_16x16x32_bf16 v[100:103], v[88:91], v[4:7], v[100:103]\n"
            "v_mfma_f32_16x16x32_bf16 v[104:107], v[88:91], v[20:23], v[104:107]\n"
            "v_mfma_f32_16x16x32_bf16 v[100:103], v[92:95], v[8:11], v[100:103]\n"
            "v_mfma_f32_16x16x32_bf16 v[104:107], v[92:95], v[24:27], v[104:107]\n"
            "v_mfma_f32_16x16x32_bf16 v[100:103], v[96:99], v[12:15], v[100:103]\n"
            "v_mfma_f32_16x16x32_bf16 v[104:107], v[96:99], v[28:31], v[104:107]\n"
            "v_cvt_pk_bf16_f32 v180, v48, v49\n"
            "v_cvt_pk_bf16_f32 v181, v50, v51\n"
            "v_cvt_pk_bf16_f32 v244, v52, v53\n"
            "s_lshl_b32 s78, s74, 10\n"
            "s_add_u32 m0, s78, 86016\n"
            "s_add_u32 s88, s44, 655360\n"
            "s_addc_u32 s89, s45, 0\n"
            "global_load_lds_dwordx4 v68, s[88:89]\n"
            "v_cvt_pk_bf16_f32 v245, v54, v55\n"
            "ds_read_b64 v[84:85], v74 offset:16896\n"
            "ds_read_b64 v[86:87], v75 offset:16896\n"
            "ds_read_b64 v[88:89], v76 offset:16896\n"
            "ds_read_b64 v[90:91], v77 offset:16896\n"
            "ds_read_b64 v[92:93], v78 offset:16896\n"
            "ds_read_b64 v[94:95], v79 offset:16896\n"
            "ds_read_b64 v[96:97], v80 offset:16896\n"
            "ds_read_b64 v[98:99], v81 offset:16896\n"
            "s_waitcnt lgkmcnt(8)\n"
            "v_mfma_f32_16x16x32_bf16 v[48:51], v[32:35], v[0:3], 0\n"
            "v_mfma_f32_16x16x32_bf16 v[52:55], v[32:35], v[16:19], 0\n"
            "v_mfma_f32_16x16x32_bf16 v[48:51], v[36:39], v[4:7], v[48:51]\n"
            "v_mfma_f32_16x16x32_bf16 v[52:55], v[36:39], v[20:23], v[52:55]\n"
            "v_mfma_f32_16x16x32_bf16 v[48:51], v[40:43], v[8:11], v[48:51]\n"
            "v_mfma_f32_16x16x32_bf16 v[52:55], v[40:43], v[24:27], v[52:55]\n"
            "v_mfma_f32_16x16x32_bf16 v[48:51], v[44:47], v[12:15], v[48:51]\n"
            "v_mfma_f32_16x16x32_bf16 v[52:55], v[44:47], v[28:31], v[52:55]\n"
            "v_cvt_pk_bf16_f32 v182, v100, v101\n"
            "s_lshl_b32 s78, s74, 10\n"
            "s_add_u32 m0, s78, 90112\n"
            "s_add_u32 s88, s44, 786432\n"
            "s_addc_u32 s89, s45, 0\n"
            "global_load_lds_dwordx4 v62, s[88:89]\n"
            "v_cvt_pk_bf16_f32 v183, v102, v103\n"
            "v_cvt_pk_bf16_f32 v246, v104, v105\n"
            "v_cvt_pk_bf16_f32 v247, v106, v107\n"
            "ds_read_b64 v[32:33], v74 offset:24576\n"
            "ds_read_b64 v[34:35], v75 offset:24576\n"
            "ds_read_b64 v[36:37], v76 offset:24576\n"
            "ds_read_b64 v[38:39], v77 offset:24576\n"
            "ds_read_b64 v[40:41], v78 offset:24576\n"
            "ds_read_b64 v[42:43], v79 offset:24576\n"
            "ds_read_b64 v[44:45], v80 offset:24576\n"
            "ds_read_b64 v[46:47], v81 offset:24576\n"
            "s_waitcnt lgkmcnt(8)\n"
            "v_mfma_f32_16x16x32_bf16 v[100:103], v[84:87], v[0:3], 0\n"
            "v_mfma_f32_16x16x32_bf16 v[104:107], v[84:87], v[16:19], 0\n"
            "v_mfma_f32_16x16x32_bf16 v[100:103], v[88:91], v[4:7], v[100:103]\n"
            "v_mfma_f32_16x16x32_bf16 v[104:107], v[88:91], v[20:23], v[104:107]\n"
            "v_mfma_f32_16x16x32_bf16 v[100:103], v[92:95], v[8:11], v[100:103]\n"
            "v_mfma_f32_16x16x32_bf16 v[104:107], v[92:95], v[24:27], v[104:107]\n"
            "v_mfma_f32_16x16x32_bf16 v[100:103], v[96:99], v[12:15], v[100:103]\n"
            "s_lshl_b32 s78, s74, 10\n"
            "s_add_u32 m0, s78, 94208\n"
            "s_add_u32 s88, s44, 917504\n"
            "s_addc_u32 s89, s45, 0\n"
            "global_load_lds_dwordx4 v68, s[88:89]\n"
            "v_mfma_f32_16x16x32_bf16 v[104:107], v[96:99], v[28:31], v[104:107]\n"
            "v_cvt_pk_bf16_f32 v184, v48, v49\n"
            "v_cvt_pk_bf16_f32 v185, v50, v51\n"
            "v_cvt_pk_bf16_f32 v248, v52, v53\n"
            "v_cvt_pk_bf16_f32 v249, v54, v55\n"
            "ds_read_b64 v[84:85], v74 offset:25088\n"
            "ds_read_b64 v[86:87], v75 offset:25088\n"
            "ds_read_b64 v[88:89], v76 offset:25088\n"
            "ds_read_b64 v[90:91], v77 offset:25088\n"
            "ds_read_b64 v[92:93], v78 offset:25088\n"
            "ds_read_b64 v[94:95], v79 offset:25088\n"
            "ds_read_b64 v[96:97], v80 offset:25088\n"
            "ds_read_b64 v[98:99], v81 offset:25088\n"
            "s_waitcnt lgkmcnt(8)\n"
            "v_mfma_f32_16x16x32_bf16 v[48:51], v[32:35], v[0:3], 0\n"
            "v_mfma_f32_16x16x32_bf16 v[52:55], v[32:35], v[16:19], 0\n"
            "v_mfma_f32_16x16x32_bf16 v[48:51], v[36:39], v[4:7], v[48:51]\n"
            "v_mfma_f32_16x16x32_bf16 v[52:55], v[36:39], v[20:23], v[52:55]\n"
            "v_mfma_f32_16x16x32_bf16 v[48:51], v[40:43], v[8:11], v[48:51]\n"
            "v_mfma_f32_16x16x32_bf16 v[52:55], v[40:43], v[24:27], v[52:55]\n"
            "v_mfma_f32_16x16x32_bf16 v[48:51], v[44:47], v[12:15], v[48:51]\n"
            "v_mfma_f32_16x16x32_bf16 v[52:55], v[44:47], v[28:31], v[52:55]\n"
            "v_cvt_pk_bf16_f32 v186, v100, v101\n"
            "v_cvt_pk_bf16_f32 v187, v102, v103\n"
            "v_cvt_pk_bf16_f32 v250, v104, v105\n"
            "v_cvt_pk_bf16_f32 v251, v106, v107\n"
            "s_waitcnt lgkmcnt(0)\n"
            "v_mfma_f32_16x16x32_bf16 v[100:103], v[84:87], v[0:3], 0\n"
            "v_mfma_f32_16x16x32_bf16 v[104:107], v[84:87], v[16:19], 0\n"
            "v_mfma_f32_16x16x32_bf16 v[100:103], v[88:91], v[4:7], v[100:103]\n"
            "v_mfma_f32_16x16x32_bf16 v[104:107], v[88:91], v[20:23], v[104:107]\n"
            "v_mfma_f32_16x16x32_bf16 v[100:103], v[92:95], v[8:11], v[100:103]\n"
            "v_mfma_f32_16x16x32_bf16 v[104:107], v[92:95], v[24:27], v[104:107]\n"
            "v_mfma_f32_16x16x32_bf16 v[100:103], v[96:99], v[12:15], v[100:103]\n"
            "v_mfma_f32_16x16x32_bf16 v[104:107], v[96:99], v[28:31], v[104:107]\n"
            "v_cvt_pk_bf16_f32 v188, v48, v49\n"
            "v_cvt_pk_bf16_f32 v189, v50, v51\n"
            "v_cvt_pk_bf16_f32 v252, v52, v53\n"
            "v_cvt_pk_bf16_f32 v253, v54, v55\n"
            "s_nop 2\n"
            "v_cvt_pk_bf16_f32 v190, v100, v101\n"
            "v_cvt_pk_bf16_f32 v191, v102, v103\n"
            "v_cvt_pk_bf16_f32 v254, v104, v105\n"
            "v_cvt_pk_bf16_f32 v255, v106, v107\n"
            "s_waitcnt vmcnt(16)\n"
            "s_barrier\n"
            "s_lshl_b32 s78, s74, 10\n"
            "s_add_u32 m0, s78, 98304\n"
            "s_add_u32 s88, s40, 0\n"
            "s_addc_u32 s89, s41, 0\n"
            "global_load_lds_dwordx4 v63, s[88:89]\n"
            "v_add_u32 v72, 0, v66\n"
            "v_xor_b32 v73, 32, v72\n"
            "s_lshl_b32 s78, s74, 10\n"
            "s_add_u32 m0, s78, 102400\n"
            "s_add_u32 s88, s40, 131072\n"
            "s_addc_u32 s89, s41, 0\n"
            "global_load_lds_dwordx4 v63, s[88:89]\n"
            "ds_read_b64_tr_b16 a[48:49], v72 offset:0\n"
            "ds_read_b64_tr_b16 a[50:51], v72 offset:512\n"
            "s_lshl_b32 s78, s74, 10\n"
            "s_add_u32 m0, s78, 106496\n"
            "s_add_u32 s88, s40, 262144\n"
            "s_addc_u32 s89, s41, 0\n"
            "global_load_lds_dwordx4 v63, s[88:89]\n"
            "ds_read_b64_tr_b16 a[52:53], v72 offset:8192\n"
            "ds_read_b64_tr_b16 a[54:55], v72 offset:8704\n"
            "s_lshl_b32 s78, s74, 10\n"
            "s_add_u32 m0, s78, 110592\n"
            "s_add_u32 s88, s40, 393216\n"
            "s_addc_u32 s89, s41, 0\n"
            "global_load_lds_dwordx4 v63, s[88:89]\n"
            "ds_read_b64_tr_b16 a[112:113], v73 offset:0\n"
            "ds_read_b64_tr_b16 a[114:115], v73 offset:512\n"
            "s_lshl_b32 s78, s74, 10\n"
            "s_add_u32 m0, s78, 114688\n"
            "s_add_u32 s88, s40, 524288\n"
            "s_addc_u32 s89, s41, 0\n"
            "global_load_lds_dwordx4 v63, s[88:89]\n"
            "ds_read_b64_tr_b16 a[116:117], v73 offset:8192\n"
            "ds_read_b64_tr_b16 a[118:119], v73 offset:8704\n"
            "s_lshl_b32 s78, s74, 10\n"
            "s_add_u32 m0, s78, 118784\n"
            "s_add_u32 s88, s40, 655360\n"
            "s_addc_u32 s89, s41, 0\n"
            "global_load_lds_dwordx4 v63, s[88:89]\n"
            "s_waitcnt lgkmcnt(0)\n"
            "ds_read_b64_tr_b16 a[56:57], v72 offset:16384\n"
            "s_lshl_b32 s78, s74, 10\n"
            "s_add_u32 m0, s78, 122880\n"
            "s_add_u32 s88, s40, 786432\n"
            "s_addc_u32 s89, s41, 0\n"
            "global_load_lds_dwordx4 v63, s[88:89]\n"
            "ds_read_b64_tr_b16 a[58:59], v72 offset:16896\n"
            "ds_read_b64_tr_b16 a[60:61], v72 offset:24576\n"
            "s_lshl_b32 s78, s74, 10\n"
            "s_add_u32 m0, s78, 126976\n"
            "s_add_u32 s88, s40, 917504\n"
            "s_addc_u32 s89, s41, 0\n"
            "global_load_lds_dwordx4 v63, s[88:89]\n"
            "ds_read_b64_tr_b16 a[62:63], v72 offset:25088\n"
            "ds_read_b64_tr_b16 a[120:121], v73 offset:16384\n"
            "ds_read_b64_tr_b16 a[122:123], v73 offset:16896\n"
            "ds_read_b64_tr_b16 a[124:125], v73 offset:24576\n"
            "ds_read_b64_tr_b16 a[126:127], v73 offset:25088\n"
            "s_waitcnt lgkmcnt(0)\n"
            "s_waitcnt vmcnt(16)\n"
            "s_barrier\n"
            "s_lshl_b32 s78, s74, 10\n"
            "s_add_u32 m0, s78, 0\n"
            "s_add_u32 s88, s42, 0\n"
            "s_addc_u32 s89, s43, 0\n"
            "global_load_lds_dwordx4 v63, s[88:89]\n"
            "v_add_u32 v72, 32768, v66\n"
            "v_xor_b32 v73, 32, v72\n"
            "s_lshl_b32 s78, s74, 10\n"
            "s_add_u32 m0, s78, 4096\n"
            "s_add_u32 s88, s42, 131072\n"
            "s_addc_u32 s89, s43, 0\n"
            "global_load_lds_dwordx4 v63, s[88:89]\n"
            "ds_read_b64_tr_b16 a[176:177], v72 offset:0\n"
            "ds_read_b64_tr_b16 a[178:179], v72 offset:512\n"
            "s_lshl_b32 s78, s74, 10\n"
            "s_add_u32 m0, s78, 8192\n"
            "s_add_u32 s88, s42, 262144\n"
            "s_addc_u32 s89, s43, 0\n"
            "global_load_lds_dwordx4 v63, s[88:89]\n"
            "ds_read_b64_tr_b16 a[180:181], v72 offset:8192\n"
            "ds_read_b64_tr_b16 a[182:183], v72 offset:8704\n"
            "s_lshl_b32 s78, s74, 10\n"
            "s_add_u32 m0, s78, 12288\n"
            "s_add_u32 s88, s42, 393216\n"
            "s_addc_u32 s89, s43, 0\n"
            "global_load_lds_dwordx4 v63, s[88:89]\n"
            "ds_read_b64_tr_b16 a[240:241], v73 offset:0\n"
            "ds_read_b64_tr_b16 a[242:243], v73 offset:512\n"
            "s_lshl_b32 s78, s74, 10\n"
            "s_add_u32 m0, s78, 16384\n"
            "s_add_u32 s88, s42, 524288\n"
            "s_addc_u32 s89, s43, 0\n"
            "global_load_lds_dwordx4 v63, s[88:89]\n"
            "ds_read_b64_tr_b16 a[244:245], v73 offset:8192\n"
            "ds_read_b64_tr_b16 a[246:247], v73 offset:8704\n"
            "s_lshl_b32 s78, s74, 10\n"
            "s_add_u32 m0, s78, 20480\n"
            "s_add_u32 s88, s42, 655360\n"
            "s_addc_u32 s89, s43, 0\n"
            "global_load_lds_dwordx4 v63, s[88:89]\n"
            "s_waitcnt lgkmcnt(0)\n"
            "ds_read_b64_tr_b16 a[184:185], v72 offset:16384\n"
            "s_lshl_b32 s78, s74, 10\n"
            "s_add_u32 m0, s78, 24576\n"
            "s_add_u32 s88, s42, 786432\n"
            "s_addc_u32 s89, s43, 0\n"
            "global_load_lds_dwordx4 v63, s[88:89]\n"
            "ds_read_b64_tr_b16 a[186:187], v72 offset:16896\n"
            "ds_read_b64_tr_b16 a[188:189], v72 offset:24576\n"
            "s_lshl_b32 s78, s74, 10\n"
            "s_add_u32 m0, s78, 28672\n"
            "s_add_u32 s88, s42, 917504\n"
            "s_addc_u32 s89, s43, 0\n"
            "global_load_lds_dwordx4 v63, s[88:89]\n"
            "ds_read_b64_tr_b16 a[190:191], v72 offset:25088\n"
            "ds_read_b64_tr_b16 a[248:249], v73 offset:16384\n"
            "ds_read_b64_tr_b16 a[250:251], v73 offset:16896\n"
            "ds_read_b64_tr_b16 a[252:253], v73 offset:24576\n"
            "ds_read_b64_tr_b16 a[254:255], v73 offset:25088\n"
            "s_waitcnt lgkmcnt(0)\n"
            "s_waitcnt vmcnt(16)\n"
            "s_barrier\n"
            "v_add_u32 v74, 65536, v67\n"
            "v_xor_b32 v75, 32, v74\n"
            "v_xor_b32 v76, 64, v74\n"
            "v_xor_b32 v77, 96, v74\n"
            "v_xor_b32 v78, 128, v74\n"
            "v_xor_b32 v79, 160, v74\n"
            "v_xor_b32 v80, 192, v74\n"
            "v_xor_b32 v81, 224, v74\n"
            "ds_read_b64 v[32:33], v74 offset:0\n"
            "ds_read_b64 v[34:35], v75 offset:0\n"
            "ds_read_b64 v[36:37], v76 offset:0\n"
            "ds_read_b64 v[38:39], v77 offset:0\n"
            "ds_read_b64 v[40:41], v78 offset:0\n"
            "ds_read_b64 v[42:43], v79 offset:0\n"
            "ds_read_b64 v[44:45], v80 offset:0\n"
            "ds_read_b64 v[46:47], v81 offset:0\n"
            "ds_read_b64 v[84:85], v74 offset:512\n"
            "ds_read_b64 v[86:87], v75 offset:512\n"
            "ds_read_b64 v[88:89], v76 offset:512\n"
            "ds_read_b64 v[90:91], v77 offset:512\n"
            "ds_read_b64 v[92:93], v78 offset:512\n"
            "ds_read_b64 v[94:95], v79 offset:512\n"
            "ds_read_b64 v[96:97], v80 offset:512\n"
            "ds_read_b64 v[98:99], v81 offset:512\n"
            "s_waitcnt lgkmcnt(8)\n"
            "v_mfma_f32_16x16x32_bf16 v[48:51], v[32:35], v[0:3], 0\n"
            "v_mfma_f32_16x16x32_bf16 v[52:55], v[32:35], v[16:19], 0\n"
            "v_mfma_f32_16x16x32_bf16 v[48:51], v[36:39], v[4:7], v[48:51]\n"
            "v_mfma_f32_16x16x32_bf16 v[52:55], v[36:39], v[20:23], v[52:55]\n"
            "v_mfma_f32_16x16x32_bf16 v[48:51], v[40:43], v[8:11], v[48:51]\n"
            "v_mfma_f32_16x16x32_bf16 v[52:55], v[40:43], v[24:27], v[52:55]\n"
            "v_mfma_f32_16x16x32_bf16 v[48:51], v[44:47], v[12:15], v[48:51]\n"
            "v_mfma_f32_16x16x32_bf16 v[52:55], v[44:47], v[28:31], v[52:55]\n"
            "ds_read_b64 v[32:33], v74 offset:8192\n"
            "ds_read_b64 v[34:35], v75 offset:8192\n"
            "ds_read_b64 v[36:37], v76 offset:8192\n"
            "ds_read_b64 v[38:39], v77 offset:8192\n"
            "ds_read_b64 v[40:41], v78 offset:8192\n"
            "ds_read_b64 v[42:43], v79 offset:8192\n"
            "ds_read_b64 v[44:45], v80 offset:8192\n"
            "ds_read_b64 v[46:47], v81 offset:8192\n"
            "s_waitcnt lgkmcnt(8)\n"
            "v_mfma_f32_16x16x32_bf16 v[100:103], v[84:87], v[0:3], 0\n"
            "v_mfma_f32_16x16x32_bf16 v[104:107], v[84:87], v[16:19], 0\n"
            "v_mfma_f32_16x16x32_bf16 v[100:103], v[88:91], v[4:7], v[100:103]\n"
            "v_mfma_f32_16x16x32_bf16 v[104:107], v[88:91], v[20:23], v[104:107]\n"
            "v_mfma_f32_16x16x32_bf16 v[100:103], v[92:95], v[8:11], v[100:103]\n"
            "v_mfma_f32_16x16x32_bf16 v[104:107], v[92:95], v[24:27], v[104:107]\n"
            "v_mfma_f32_16x16x32_bf16 v[100:103], v[96:99], v[12:15], v[100:103]\n"
            "v_mfma_f32_16x16x32_bf16 v[104:107], v[96:99], v[28:31], v[104:107]\n"
            "v_cvt_pk_bf16_f32 v128, v48, v49\n"
            "v_cvt_pk_bf16_f32 v129, v50, v51\n"
            "v_cvt_pk_bf16_f32 v192, v52, v53\n"
            "v_cvt_pk_bf16_f32 v193, v54, v55\n"
            "ds_read_b64 v[84:85], v74 offset:8704\n"
            "ds_read_b64 v[86:87], v75 offset:8704\n"
            "ds_read_b64 v[88:89], v76 offset:8704\n"
            "ds_read_b64 v[90:91], v77 offset:8704\n"
            "ds_read_b64 v[92:93], v78 offset:8704\n"
            "ds_read_b64 v[94:95], v79 offset:8704\n"
            "ds_read_b64 v[96:97], v80 offset:8704\n"
            "ds_read_b64 v[98:99], v81 offset:8704\n"
            "s_waitcnt lgkmcnt(8)\n"
            "v_mfma_f32_16x16x32_bf16 v[48:51], v[32:35], v[0:3], 0\n"
            "v_mfma_f32_16x16x32_bf16 v[52:55], v[32:35], v[16:19], 0\n"
            "v_mfma_f32_16x16x32_bf16 v[48:51], v[36:39], v[4:7], v[48:51]\n"
            "v_mfma_f32_16x16x32_bf16 v[52:55], v[36:39], v[20:23], v[52:55]\n"
            "v_mfma_f32_16x16x32_bf16 v[48:51], v[40:43], v[8:11], v[48:51]\n"
            "v_mfma_f32_16x16x32_bf16 v[52:55], v[40:43], v[24:27], v[52:55]\n"
            "v_mfma_f32_16x16x32_bf16 v[48:51], v[44:47], v[12:15], v[48:51]\n"
            "v_mfma_f32_16x16x32_bf16 v[52:55], v[44:47], v[28:31], v[52:55]\n"
            "v_cvt_pk_bf16_f32 v130, v100, v101\n"
            "v_cvt_pk_bf16_f32 v131, v102, v103\n"
            "v_cvt_pk_bf16_f32 v194, v104, v105\n"
            "v_cvt_pk_bf16_f32 v195, v106, v107\n"
            "ds_read_b64 v[32:33], v74 offset:16384\n"
            "ds_read_b64 v[34:35], v75 offset:16384\n"
            "ds_read_b64 v[36:37], v76 offset:16384\n"
            "ds_read_b64 v[38:39], v77 offset:16384\n"
            "ds_read_b64 v[40:41], v78 offset:16384\n"
            "ds_read_b64 v[42:43], v79 offset:16384\n"
            "ds_read_b64 v[44:45], v80 offset:16384\n"
            "ds_read_b64 v[46:47], v81 offset:16384\n"
            "s_waitcnt lgkmcnt(8)\n"
            "v_mfma_f32_16x16x32_bf16 v[100:103], v[84:87], v[0:3], 0\n"
            "v_mfma_f32_16x16x32_bf16 v[104:107], v[84:87], v[16:19], 0\n"
            "v_mfma_f32_16x16x32_bf16 v[100:103], v[88:91], v[4:7], v[100:103]\n"
            "v_mfma_f32_16x16x32_bf16 v[104:107], v[88:91], v[20:23], v[104:107]\n"
            "v_mfma_f32_16x16x32_bf16 v[100:103], v[92:95], v[8:11], v[100:103]\n"
            "v_mfma_f32_16x16x32_bf16 v[104:107], v[92:95], v[24:27], v[104:107]\n"
            "v_mfma_f32_16x16x32_bf16 v[100:103], v[96:99], v[12:15], v[100:103]\n"
            "v_mfma_f32_16x16x32_bf16 v[104:107], v[96:99], v[28:31], v[104:107]\n"
            "v_cvt_pk_bf16_f32 v132, v48, v49\n"
            "v_cvt_pk_bf16_f32 v133, v50, v51\n"
            "v_cvt_pk_bf16_f32 v196, v52, v53\n"
            "v_cvt_pk_bf16_f32 v197, v54, v55\n"
            "ds_read_b64 v[84:85], v74 offset:16896\n"
            "ds_read_b64 v[86:87], v75 offset:16896\n"
            "ds_read_b64 v[88:89], v76 offset:16896\n"
            "ds_read_b64 v[90:91], v77 offset:16896\n"
            "ds_read_b64 v[92:93], v78 offset:16896\n"
            "ds_read_b64 v[94:95], v79 offset:16896\n"
            "ds_read_b64 v[96:97], v80 offset:16896\n"
            "ds_read_b64 v[98:99], v81 offset:16896\n"
            "s_waitcnt lgkmcnt(8)\n"
            "v_mfma_f32_16x16x32_bf16 v[48:51], v[32:35], v[0:3], 0\n"
            "v_mfma_f32_16x16x32_bf16 v[52:55], v[32:35], v[16:19], 0\n"
            "v_mfma_f32_16x16x32_bf16 v[48:51], v[36:39], v[4:7], v[48:51]\n"
            "v_mfma_f32_16x16x32_bf16 v[52:55], v[36:39], v[20:23], v[52:55]\n"
            "v_mfma_f32_16x16x32_bf16 v[48:51], v[40:43], v[8:11], v[48:51]\n"
            "v_mfma_f32_16x16x32_bf16 v[52:55], v[40:43], v[24:27], v[52:55]\n"
            "v_mfma_f32_16x16x32_bf16 v[48:51], v[44:47], v[12:15], v[48:51]\n"
            "v_mfma_f32_16x16x32_bf16 v[52:55], v[44:47], v[28:31], v[52:55]\n"
            "v_cvt_pk_bf16_f32 v134, v100, v101\n"
            "v_cvt_pk_bf16_f32 v135, v102, v103\n"
            "v_cvt_pk_bf16_f32 v198, v104, v105\n"
            "v_cvt_pk_bf16_f32 v199, v106, v107\n"
            "ds_read_b64 v[32:33], v74 offset:24576\n"
            "ds_read_b64 v[34:35], v75 offset:24576\n"
            "ds_read_b64 v[36:37], v76 offset:24576\n"
            "ds_read_b64 v[38:39], v77 offset:24576\n"
            "ds_read_b64 v[40:41], v78 offset:24576\n"
            "ds_read_b64 v[42:43], v79 offset:24576\n"
            "ds_read_b64 v[44:45], v80 offset:24576\n"
            "ds_read_b64 v[46:47], v81 offset:24576\n"
            "s_waitcnt lgkmcnt(8)\n"
            "v_mfma_f32_16x16x32_bf16 v[100:103], v[84:87], v[0:3], 0\n"
            "v_mfma_f32_16x16x32_bf16 v[104:107], v[84:87], v[16:19], 0\n"
            "v_mfma_f32_16x16x32_bf16 v[100:103], v[88:91], v[4:7], v[100:103]\n"
            "v_mfma_f32_16x16x32_bf16 v[104:107], v[88:91], v[20:23], v[104:107]\n"
            "v_mfma_f32_16x16x32_bf16 v[100:103], v[92:95], v[8:11], v[100:103]\n"
            "v_mfma_f32_16x16x32_bf16 v[104:107], v[92:95], v[24:27], v[104:107]\n"
            "v_mfma_f32_16x16x32_bf16 v[100:103], v[96:99], v[12:15], v[100:103]\n"
            "v_mfma_f32_16x16x32_bf16 v[104:107], v[96:99], v[28:31], v[104:107]\n"
            "v_cvt_pk_bf16_f32 v136, v48, v49\n"
            "v_cvt_pk_bf16_f32 v137, v50, v51\n"
            "v_cvt_pk_bf16_f32 v200, v52, v53\n"
            "v_cvt_pk_bf16_f32 v201, v54, v55\n"
            "ds_read_b64 v[84:85], v74 offset:25088\n"
            "ds_read_b64 v[86:87], v75 offset:25088\n"
            "ds_read_b64 v[88:89], v76 offset:25088\n"
            "ds_read_b64 v[90:91], v77 offset:25088\n"
            "ds_read_b64 v[92:93], v78 offset:25088\n"
            "ds_read_b64 v[94:95], v79 offset:25088\n"
            "ds_read_b64 v[96:97], v80 offset:25088\n"
            "ds_read_b64 v[98:99], v81 offset:25088\n"
            "s_waitcnt lgkmcnt(8)\n"
            "v_mfma_f32_16x16x32_bf16 v[48:51], v[32:35], v[0:3], 0\n"
            "v_mfma_f32_16x16x32_bf16 v[52:55], v[32:35], v[16:19], 0\n"
            "v_mfma_f32_16x16x32_bf16 v[48:51], v[36:39], v[4:7], v[48:51]\n"
            "v_mfma_f32_16x16x32_bf16 v[52:55], v[36:39], v[20:23], v[52:55]\n"
            "v_mfma_f32_16x16x32_bf16 v[48:51], v[40:43], v[8:11], v[48:51]\n"
            "v_mfma_f32_16x16x32_bf16 v[52:55], v[40:43], v[24:27], v[52:55]\n"
            "v_mfma_f32_16x16x32_bf16 v[48:51], v[44:47], v[12:15], v[48:51]\n"
            "v_mfma_f32_16x16x32_bf16 v[52:55], v[44:47], v[28:31], v[52:55]\n"
            "v_cvt_pk_bf16_f32 v138, v100, v101\n"
            "v_cvt_pk_bf16_f32 v139, v102, v103\n"
            "v_cvt_pk_bf16_f32 v202, v104, v105\n"
            "v_cvt_pk_bf16_f32 v203, v106, v107\n"
            "s_waitcnt lgkmcnt(0)\n"
            "v_mfma_f32_16x16x32_bf16 v[100:103], v[84:87], v[0:3], 0\n"
            "v_mfma_f32_16x16x32_bf16 v[104:107], v[84:87], v[16:19], 0\n"
            "v_mfma_f32_16x16x32_bf16 v[100:103], v[88:91], v[4:7], v[100:103]\n"
            "v_mfma_f32_16x16x32_bf16 v[104:107], v[88:91], v[20:23], v[104:107]\n"
            "v_mfma_f32_16x16x32_bf16 v[100:103], v[92:95], v[8:11], v[100:103]\n"
            "v_mfma_f32_16x16x32_bf16 v[104:107], v[92:95], v[24:27], v[104:107]\n"
            "v_mfma_f32_16x16x32_bf16 v[100:103], v[96:99], v[12:15], v[100:103]\n"
            "v_mfma_f32_16x16x32_bf16 v[104:107], v[96:99], v[28:31], v[104:107]\n"
            "v_cvt_pk_bf16_f32 v140, v48, v49\n"
            "v_cvt_pk_bf16_f32 v141, v50, v51\n"
            "v_cvt_pk_bf16_f32 v204, v52, v53\n"
            "v_cvt_pk_bf16_f32 v205, v54, v55\n"
            "s_nop 2\n"
            "v_cvt_pk_bf16_f32 v142, v100, v101\n"
            "v_cvt_pk_bf16_f32 v143, v102, v103\n"
            "v_cvt_pk_bf16_f32 v206, v104, v105\n"
            "v_cvt_pk_bf16_f32 v207, v106, v107\n"
            "s_waitcnt vmcnt(8)\n"
            "s_barrier\n"
            "v_add_u32 v72, 98304, v66\n"
            "v_xor_b32 v73, 32, v72\n"
            "ds_read_b64_tr_b16 a[0:1], v72 offset:0\n"
            "ds_read_b64_tr_b16 a[2:3], v72 offset:512\n"
            "ds_read_b64_tr_b16 a[4:5], v72 offset:8192\n"
            "ds_read_b64_tr_b16 a[6:7], v72 offset:8704\n"
            "ds_read_b64_tr_b16 a[64:65], v73 offset:0\n"
            "ds_read_b64_tr_b16 a[66:67], v73 offset:512\n"
            "ds_read_b64_tr_b16 a[68:69], v73 offset:8192\n"
            "ds_read_b64_tr_b16 a[70:71], v73 offset:8704\n"
            "s_waitcnt lgkmcnt(0)\n"
            "ds_read_b64_tr_b16 a[8:9], v72 offset:16384\n"
            "ds_read_b64_tr_b16 a[10:11], v72 offset:16896\n"
            "ds_read_b64_tr_b16 a[12:13], v72 offset:24576\n"
            "ds_read_b64_tr_b16 a[14:15], v72 offset:25088\n"
            "ds_read_b64_tr_b16 a[72:73], v73 offset:16384\n"
            "ds_read_b64_tr_b16 a[74:75], v73 offset:16896\n"
            "ds_read_b64_tr_b16 a[76:77], v73 offset:24576\n"
            "ds_read_b64_tr_b16 a[78:79], v73 offset:25088\n"
            "s_waitcnt lgkmcnt(0)\n"
            "s_waitcnt vmcnt(0)\n"
            "s_barrier\n"
            "v_add_u32 v72, 0, v66\n"
            "v_xor_b32 v73, 32, v72\n"
            "ds_read_b64_tr_b16 a[128:129], v72 offset:0\n"
            "ds_read_b64_tr_b16 a[130:131], v72 offset:512\n"
            "ds_read_b64_tr_b16 a[132:133], v72 offset:8192\n"
            "ds_read_b64_tr_b16 a[134:135], v72 offset:8704\n"
            "ds_read_b64_tr_b16 a[192:193], v73 offset:0\n"
            "ds_read_b64_tr_b16 a[194:195], v73 offset:512\n"
            "ds_read_b64_tr_b16 a[196:197], v73 offset:8192\n"
            "ds_read_b64_tr_b16 a[198:199], v73 offset:8704\n"
            "s_waitcnt lgkmcnt(0)\n"
            "ds_read_b64_tr_b16 a[136:137], v72 offset:16384\n"
            "ds_read_b64_tr_b16 a[138:139], v72 offset:16896\n"
            "ds_read_b64_tr_b16 a[140:141], v72 offset:24576\n"
            "ds_read_b64_tr_b16 a[142:143], v72 offset:25088\n"
            "ds_read_b64_tr_b16 a[200:201], v73 offset:16384\n"
            "ds_read_b64_tr_b16 a[202:203], v73 offset:16896\n"
            "ds_read_b64_tr_b16 a[204:205], v73 offset:24576\n"
            "ds_read_b64_tr_b16 a[206:207], v73 offset:25088\n"
            "s_waitcnt lgkmcnt(0)\n"
            "s_branch PRO_WDONE_%=\n"
            "PRO_A2_%=:\n"
            "s_lshl_b32 s78, s74, 10\n"
            "s_add_u32 m0, s78, 0\n"
            "s_add_u32 s88, s46, 0\n"
            "s_addc_u32 s89, s47, 0\n"
            "global_load_lds_dwordx4 v64, s[88:89]\n"
            "s_lshl_b32 s78, s74, 10\n"
            "s_add_u32 m0, s78, 4096\n"
            "s_add_u32 s88, s46, 16384\n"
            "s_addc_u32 s89, s47, 0\n"
            "global_load_lds_dwordx4 v64, s[88:89]\n"
            "s_lshl_b32 s78, s74, 10\n"
            "s_add_u32 m0, s78, 8192\n"
            "s_add_u32 s88, s46, 32768\n"
            "s_addc_u32 s89, s47, 0\n"
            "global_load_lds_dwordx4 v64, s[88:89]\n"
            "s_lshl_b32 s78, s74, 10\n"
            "s_add_u32 m0, s78, 12288\n"
            "s_add_u32 s88, s46, 49152\n"
            "s_addc_u32 s89, s47, 0\n"
            "global_load_lds_dwordx4 v64, s[88:89]\n"
            "s_lshl_b32 s78, s74, 10\n"
            "s_add_u32 m0, s78, 16384\n"
            "s_add_u32 s88, s46, 65536\n"
            "s_addc_u32 s89, s47, 0\n"
            "global_load_lds_dwordx4 v64, s[88:89]\n"
            "s_lshl_b32 s78, s74, 10\n"
            "s_add_u32 m0, s78, 20480\n"
            "s_add_u32 s88, s46, 81920\n"
            "s_addc_u32 s89, s47, 0\n"
            "global_load_lds_dwordx4 v64, s[88:89]\n"
            "s_lshl_b32 s78, s74, 10\n"
            "s_add_u32 m0, s78, 24576\n"
            "s_add_u32 s88, s46, 98304\n"
            "s_addc_u32 s89, s47, 0\n"
            "global_load_lds_dwordx4 v64, s[88:89]\n"
            "s_lshl_b32 s78, s74, 10\n"
            "s_add_u32 m0, s78, 28672\n"
            "s_add_u32 s88, s46, 114688\n"
            "s_addc_u32 s89, s47, 0\n"
            "global_load_lds_dwordx4 v64, s[88:89]\n"
            "s_lshl_b32 s78, s74, 10\n"
            "s_add_u32 m0, s78, 32768\n"
            "s_add_u32 s88, s44, 2097152\n"
            "s_addc_u32 s89, s45, 0\n"
            "global_load_lds_dwordx4 v62, s[88:89]\n"
            "s_lshl_b32 s78, s74, 10\n"
            "s_add_u32 m0, s78, 36864\n"
            "s_add_u32 s88, s44, 2228224\n"
            "s_addc_u32 s89, s45, 0\n"
            "global_load_lds_dwordx4 v68, s[88:89]\n"
            "s_lshl_b32 s78, s74, 10\n"
            "s_add_u32 m0, s78, 40960\n"
            "s_add_u32 s88, s44, 2359296\n"
            "s_addc_u32 s89, s45, 0\n"
            "global_load_lds_dwordx4 v62, s[88:89]\n"
            "s_lshl_b32 s78, s74, 10\n"
            "s_add_u32 m0, s78, 45056\n"
            "s_add_u32 s88, s44, 2490368\n"
            "s_addc_u32 s89, s45, 0\n"
            "global_load_lds_dwordx4 v68, s[88:89]\n"
            "s_lshl_b32 s78, s74, 10\n"
            "s_add_u32 m0, s78, 49152\n"
            "s_add_u32 s88, s44, 2621440\n"
            "s_addc_u32 s89, s45, 0\n"
            "global_load_lds_dwordx4 v62, s[88:89]\n"
            "s_lshl_b32 s78, s74, 10\n"
            "s_add_u32 m0, s78, 53248\n"
            "s_add_u32 s88, s44, 2752512\n"
            "s_addc_u32 s89, s45, 0\n"
            "global_load_lds_dwordx4 v68, s[88:89]\n"
            "s_lshl_b32 s78, s74, 10\n"
            "s_add_u32 m0, s78, 57344\n"
            "s_add_u32 s88, s44, 2883584\n"
            "s_addc_u32 s89, s45, 0\n"
            "global_load_lds_dwordx4 v62, s[88:89]\n"
            "s_lshl_b32 s78, s74, 10\n"
            "s_add_u32 m0, s78, 61440\n"
            "s_add_u32 s88, s44, 3014656\n"
            "s_addc_u32 s89, s45, 0\n"
            "global_load_lds_dwordx4 v68, s[88:89]\n"
            "s_lshl_b32 s78, s74, 10\n"
            "s_add_u32 m0, s78, 65536\n"
            "s_add_u32 s88, s40, 2097152\n"
            "s_addc_u32 s89, s41, 0\n"
            "global_load_lds_dwordx4 v63, s[88:89]\n"
            "s_lshl_b32 s78, s74, 10\n"
            "s_add_u32 m0, s78, 69632\n"
            "s_add_u32 s88, s40, 2228224\n"
            "s_addc_u32 s89, s41, 0\n"
            "global_load_lds_dwordx4 v63, s[88:89]\n"
            "s_lshl_b32 s78, s74, 10\n"
            "s_add_u32 m0, s78, 73728\n"
            "s_add_u32 s88, s40, 2359296\n"
            "s_addc_u32 s89, s41, 0\n"
            "global_load_lds_dwordx4 v63, s[88:89]\n"
            "s_lshl_b32 s78, s74, 10\n"
            "s_add_u32 m0, s78, 77824\n"
            "s_add_u32 s88, s40, 2490368\n"
            "s_addc_u32 s89, s41, 0\n"
            "global_load_lds_dwordx4 v63, s[88:89]\n"
            "s_lshl_b32 s78, s74, 10\n"
            "s_add_u32 m0, s78, 81920\n"
            "s_add_u32 s88, s40, 2621440\n"
            "s_addc_u32 s89, s41, 0\n"
            "global_load_lds_dwordx4 v63, s[88:89]\n"
            "s_lshl_b32 s78, s74, 10\n"
            "s_add_u32 m0, s78, 86016\n"
            "s_add_u32 s88, s40, 2752512\n"
            "s_addc_u32 s89, s41, 0\n"
            "global_load_lds_dwordx4 v63, s[88:89]\n"
            "s_lshl_b32 s78, s74, 10\n"
            "s_add_u32 m0, s78, 90112\n"
            "s_add_u32 s88, s40, 2883584\n"
            "s_addc_u32 s89, s41, 0\n"
            "global_load_lds_dwordx4 v63, s[88:89]\n"
            "s_lshl_b32 s78, s74, 10\n"
            "s_add_u32 m0, s78, 94208\n"
            "s_add_u32 s88, s40, 3014656\n"
            "s_addc_u32 s89, s41, 0\n"
            "global_load_lds_dwordx4 v63, s[88:89]\n"
            "s_waitcnt vmcnt(16)\n"
            "s_barrier\n"
            "s_lshl_b32 s78, s74, 10\n"
            "s_add_u32 m0, s78, 98304\n"
            "s_add_u32 s88, s42, 2097152\n"
            "s_addc_u32 s89, s43, 0\n"
            "global_load_lds_dwordx4 v63, s[88:89]\n"
            "ds_read_b64_tr_b16 v[0:1], v70 offset:0\n"
            "s_lshl_b32 s78, s74, 10\n"
            "s_add_u32 m0, s78, 102400\n"
            "s_add_u32 s88, s42, 2228224\n"
            "s_addc_u32 s89, s43, 0\n"
            "global_load_lds_dwordx4 v63, s[88:89]\n"
            "ds_read_b64_tr_b16 v[2:3], v70 offset:4096\n"
            "s_lshl_b32 s78, s74, 10\n"
            "s_add_u32 m0, s78, 106496\n"
            "s_add_u32 s88, s42, 2359296\n"
            "s_addc_u32 s89, s43, 0\n"
            "global_load_lds_dwordx4 v63, s[88:89]\n"
            "ds_read_b64_tr_b16 v[4:5], v70 offset:8192\n"
            "s_lshl_b32 s78, s74, 10\n"
            "s_add_u32 m0, s78, 110592\n"
            "s_add_u32 s88, s42, 2490368\n"
            "s_addc_u32 s89, s43, 0\n"
            "global_load_lds_dwordx4 v63, s[88:89]\n"
            "ds_read_b64_tr_b16 v[6:7], v70 offset:12288\n"
            "s_lshl_b32 s78, s74, 10\n"
            "s_add_u32 m0, s78, 114688\n"
            "s_add_u32 s88, s42, 2621440\n"
            "s_addc_u32 s89, s43, 0\n"
            "global_load_lds_dwordx4 v63, s[88:89]\n"
            "ds_read_b64_tr_b16 v[8:9], v70 offset:16384\n"
            "s_lshl_b32 s78, s74, 10\n"
            "s_add_u32 m0, s78, 118784\n"
            "s_add_u32 s88, s42, 2752512\n"
            "s_addc_u32 s89, s43, 0\n"
            "global_load_lds_dwordx4 v63, s[88:89]\n"
            "ds_read_b64_tr_b16 v[10:11], v70 offset:20480\n"
            "s_lshl_b32 s78, s74, 10\n"
            "s_add_u32 m0, s78, 122880\n"
            "s_add_u32 s88, s42, 2883584\n"
            "s_addc_u32 s89, s43, 0\n"
            "global_load_lds_dwordx4 v63, s[88:89]\n"
            "ds_read_b64_tr_b16 v[12:13], v70 offset:24576\n"
            "s_lshl_b32 s78, s74, 10\n"
            "s_add_u32 m0, s78, 126976\n"
            "s_add_u32 s88, s42, 3014656\n"
            "s_addc_u32 s89, s43, 0\n"
            "global_load_lds_dwordx4 v63, s[88:89]\n"
            "ds_read_b64_tr_b16 v[14:15], v70 offset:28672\n"
            "ds_read_b64_tr_b16 v[16:17], v71 offset:0\n"
            "ds_read_b64_tr_b16 v[18:19], v71 offset:4096\n"
            "ds_read_b64_tr_b16 v[20:21], v71 offset:8192\n"
            "ds_read_b64_tr_b16 v[22:23], v71 offset:12288\n"
            "ds_read_b64_tr_b16 v[24:25], v71 offset:16384\n"
            "ds_read_b64_tr_b16 v[26:27], v71 offset:20480\n"
            "ds_read_b64_tr_b16 v[28:29], v71 offset:24576\n"
            "ds_read_b64_tr_b16 v[30:31], v71 offset:28672\n"
            "s_waitcnt lgkmcnt(0)\n"
            "s_waitcnt vmcnt(16)\n"
            "s_barrier\n"
            "s_lshl_b32 s78, s74, 10\n"
            "s_add_u32 m0, s78, 0\n"
            "s_add_u32 s88, s44, 3145728\n"
            "s_addc_u32 s89, s45, 0\n"
            "global_load_lds_dwordx4 v62, s[88:89]\n"
            "v_add_u32 v74, 32768, v67\n"
            "v_xor_b32 v75, 32, v74\n"
            "v_xor_b32 v76, 64, v74\n"
            "v_xor_b32 v77, 96, v74\n"
            "v_xor_b32 v78, 128, v74\n"
            "v_xor_b32 v79, 160, v74\n"
            "v_xor_b32 v80, 192, v74\n"
            "v_xor_b32 v81, 224, v74\n"
            "ds_read_b64 v[32:33], v74 offset:0\n"
            "ds_read_b64 v[34:35], v75 offset:0\n"
            "ds_read_b64 v[36:37], v76 offset:0\n"
            "ds_read_b64 v[38:39], v77 offset:0\n"
            "ds_read_b64 v[40:41], v78 offset:0\n"
            "ds_read_b64 v[42:43], v79 offset:0\n"
            "ds_read_b64 v[44:45], v80 offset:0\n"
            "ds_read_b64 v[46:47], v81 offset:0\n"
            "ds_read_b64 v[84:85], v74 offset:512\n"
            "ds_read_b64 v[86:87], v75 offset:512\n"
            "ds_read_b64 v[88:89], v76 offset:512\n"
            "s_lshl_b32 s78, s74, 10\n"
            "s_add_u32 m0, s78, 4096\n"
            "s_add_u32 s88, s44, 3276800\n"
            "s_addc_u32 s89, s45, 0\n"
            "global_load_lds_dwordx4 v68, s[88:89]\n"
            "ds_read_b64 v[90:91], v77 offset:512\n"
            "ds_read_b64 v[92:93], v78 offset:512\n"
            "ds_read_b64 v[94:95], v79 offset:512\n"
            "ds_read_b64 v[96:97], v80 offset:512\n"
            "ds_read_b64 v[98:99], v81 offset:512\n"
            "s_waitcnt lgkmcnt(8)\n"
            "v_mfma_f32_16x16x32_bf16 v[48:51], v[32:35], v[0:3], 0\n"
            "v_mfma_f32_16x16x32_bf16 v[52:55], v[32:35], v[16:19], 0\n"
            "v_mfma_f32_16x16x32_bf16 v[48:51], v[36:39], v[4:7], v[48:51]\n"
            "v_mfma_f32_16x16x32_bf16 v[52:55], v[36:39], v[20:23], v[52:55]\n"
            "v_mfma_f32_16x16x32_bf16 v[48:51], v[40:43], v[8:11], v[48:51]\n"
            "v_mfma_f32_16x16x32_bf16 v[52:55], v[40:43], v[24:27], v[52:55]\n"
            "v_mfma_f32_16x16x32_bf16 v[48:51], v[44:47], v[12:15], v[48:51]\n"
            "v_mfma_f32_16x16x32_bf16 v[52:55], v[44:47], v[28:31], v[52:55]\n"
            "ds_read_b64 v[32:33], v74 offset:8192\n"
            "ds_read_b64 v[34:35], v75 offset:8192\n"
            "ds_read_b64 v[36:37], v76 offset:8192\n"
            "ds_read_b64 v[38:39], v77 offset:8192\n"
            "ds_read_b64 v[40:41], v78 offset:8192\n"
            "s_lshl_b32 s78, s74, 10\n"
            "s_add_u32 m0, s78, 8192\n"
            "s_add_u32 s88, s44, 3407872\n"
            "s_addc_u32 s89, s45, 0\n"
            "global_load_lds_dwordx4 v62, s[88:89]\n"
            "ds_read_b64 v[42:43], v79 offset:8192\n"
            "ds_read_b64 v[44:45], v80 offset:8192\n"
            "ds_read_b64 v[46:47], v81 offset:8192\n"
            "s_waitcnt lgkmcnt(8)\n"
            "v_mfma_f32_16x16x32_bf16 v[100:103], v[84:87], v[0:3], 0\n"
            "v_mfma_f32_16x16x32_bf16 v[104:107], v[84:87], v[16:19], 0\n"
            "v_mfma_f32_16x16x32_bf16 v[100:103], v[88:91], v[4:7], v[100:103]\n"
            "v_mfma_f32_16x16x32_bf16 v[104:107], v[88:91], v[20:23], v[104:107]\n"
            "v_mfma_f32_16x16x32_bf16 v[100:103], v[92:95], v[8:11], v[100:103]\n"
            "v_mfma_f32_16x16x32_bf16 v[104:107], v[92:95], v[24:27], v[104:107]\n"
            "v_mfma_f32_16x16x32_bf16 v[100:103], v[96:99], v[12:15], v[100:103]\n"
            "v_mfma_f32_16x16x32_bf16 v[104:107], v[96:99], v[28:31], v[104:107]\n"
            "v_cvt_pk_bf16_f32 v160, v48, v49\n"
            "v_cvt_pk_bf16_f32 v161, v50, v51\n"
            "v_cvt_pk_bf16_f32 v224, v52, v53\n"
            "v_cvt_pk_bf16_f32 v225, v54, v55\n"
            "ds_read_b64 v[84:85], v74 offset:8704\n"
            "ds_read_b64 v[86:87], v75 offset:8704\n"
            "ds_read_b64 v[88:89], v76 offset:8704\n"
            "s_lshl_b32 s78, s74, 10\n"
            "s_add_u32 m0, s78, 12288\n"
            "s_add_u32 s88, s44, 3538944\n"
            "s_addc_u32 s89, s45, 0\n"
            "global_load_lds_dwordx4 v68, s[88:89]\n"
            "ds_read_b64 v[90:91], v77 offset:8704\n"
            "ds_read_b64 v[92:93], v78 offset:8704\n"
            "ds_read_b64 v[94:95], v79 offset:8704\n"
            "ds_read_b64 v[96:97], v80 offset:8704\n"
            "ds_read_b64 v[98:99], v81 offset:8704\n"
            "s_waitcnt lgkmcnt(8)\n"
            "v_mfma_f32_16x16x32_bf16 v[48:51], v[32:35], v[0:3], 0\n"
            "v_mfma_f32_16x16x32_bf16 v[52:55], v[32:35], v[16:19], 0\n"
            "v_mfma_f32_16x16x32_bf16 v[48:51], v[36:39], v[4:7], v[48:51]\n"
            "v_mfma_f32_16x16x32_bf16 v[52:55], v[36:39], v[20:23], v[52:55]\n"
            "v_mfma_f32_16x16x32_bf16 v[48:51], v[40:43], v[8:11], v[48:51]\n"
            "v_mfma_f32_16x16x32_bf16 v[52:55], v[40:43], v[24:27], v[52:55]\n"
            "v_mfma_f32_16x16x32_bf16 v[48:51], v[44:47], v[12:15], v[48:51]\n"
            "v_mfma_f32_16x16x32_bf16 v[52:55], v[44:47], v[28:31], v[52:55]\n"
            "v_cvt_pk_bf16_f32 v162, v100, v101\n"
            "v_cvt_pk_bf16_f32 v163, v102, v103\n"
            "v_cvt_pk_bf16_f32 v226, v104, v105\n"
            "v_cvt_pk_bf16_f32 v227, v106, v107\n"
            "ds_read_b64 v[32:33], v74 offset:16384\n"
            "s_lshl_b32 s78, s74, 10\n"
            "s_add_u32 m0, s78, 16384\n"
            "s_add_u32 s88, s44, 3670016\n"
            "s_addc_u32 s89, s45, 0\n"
            "global_load_lds_dwordx4 v62, s[88:89]\n"
            "ds_read_b64 v[34:35], v75 offset:16384\n"
            "ds_read_b64 v[36:37], v76 offset:16384\n"
            "ds_read_b64 v[38:39], v77 offset:16384\n"
            "ds_read_b64 v[40:41], v78 offset:16384\n"
            "ds_read_b64 v[42:43], v79 offset:16384\n"
            "ds_read_b64 v[44:45], v80 offset:16384\n"
            "ds_read_b64 v[46:47], v81 offset:16384\n"
            "s_waitcnt lgkmcnt(8)\n"
            "v_mfma_f32_16x16x32_bf16 v[100:103], v[84:87], v[0:3], 0\n"
            "v_mfma_f32_16x16x32_bf16 v[104:107], v[84:87], v[16:19], 0\n"
            "v_mfma_f32_16x16x32_bf16 v[100:103], v[88:91], v[4:7], v[100:103]\n"
            "v_mfma_f32_16x16x32_bf16 v[104:107], v[88:91], v[20:23], v[104:107]\n"
            "v_mfma_f32_16x16x32_bf16 v[100:103], v[92:95], v[8:11], v[100:103]\n"
            "v_mfma_f32_16x16x32_bf16 v[104:107], v[92:95], v[24:27], v[104:107]\n"
            "v_mfma_f32_16x16x32_bf16 v[100:103], v[96:99], v[12:15], v[100:103]\n"
            "v_mfma_f32_16x16x32_bf16 v[104:107], v[96:99], v[28:31], v[104:107]\n"
            "v_cvt_pk_bf16_f32 v164, v48, v49\n"
            "v_cvt_pk_bf16_f32 v165, v50, v51\n"
            "v_cvt_pk_bf16_f32 v228, v52, v53\n"
            "s_lshl_b32 s78, s74, 10\n"
            "s_add_u32 m0, s78, 20480\n"
            "s_add_u32 s88, s44, 3801088\n"
            "s_addc_u32 s89, s45, 0\n"
            "global_load_lds_dwordx4 v68, s[88:89]\n"
            "v_cvt_pk_bf16_f32 v229, v54, v55\n"
            "ds_read_b64 v[84:85], v74 offset:16896\n"
            "ds_read_b64 v[86:87], v75 offset:16896\n"
            "ds_read_b64 v[88:89], v76 offset:16896\n"
            "ds_read_b64 v[90:91], v77 offset:16896\n"
            "ds_read_b64 v[92:93], v78 offset:16896\n"
            "ds_read_b64 v[94:95], v79 offset:16896\n"
            "ds_read_b64 v[96:97], v80 offset:16896\n"
            "ds_read_b64 v[98:99], v81 offset:16896\n"
            "s_waitcnt lgkmcnt(8)\n"
            "v_mfma_f32_16x16x32_bf16 v[48:51], v[32:35], v[0:3], 0\n"
            "v_mfma_f32_16x16x32_bf16 v[52:55], v[32:35], v[16:19], 0\n"
            "v_mfma_f32_16x16x32_bf16 v[48:51], v[36:39], v[4:7], v[48:51]\n"
            "v_mfma_f32_16x16x32_bf16 v[52:55], v[36:39], v[20:23], v[52:55]\n"
            "v_mfma_f32_16x16x32_bf16 v[48:51], v[40:43], v[8:11], v[48:51]\n"
            "v_mfma_f32_16x16x32_bf16 v[52:55], v[40:43], v[24:27], v[52:55]\n"
            "v_mfma_f32_16x16x32_bf16 v[48:51], v[44:47], v[12:15], v[48:51]\n"
            "v_mfma_f32_16x16x32_bf16 v[52:55], v[44:47], v[28:31], v[52:55]\n"
            "v_cvt_pk_bf16_f32 v166, v100, v101\n"
            "s_lshl_b32 s78, s74, 10\n"
            "s_add_u32 m0, s78, 24576\n"
            "s_add_u32 s88, s44, 3932160\n"
            "s_addc_u32 s89, s45, 0\n"
            "global_load_lds_dwordx4 v62, s[88:89]\n"
            "v_cvt_pk_bf16_f32 v167, v102, v103\n"
            "v_cvt_pk_bf16_f32 v230, v104, v105\n"
            "v_cvt_pk_bf16_f32 v231, v106, v107\n"
            "ds_read_b64 v[32:33], v74 offset:24576\n"
            "ds_read_b64 v[34:35], v75 offset:24576\n"
            "ds_read_b64 v[36:37], v76 offset:24576\n"
            "ds_read_b64 v[38:39], v77 offset:24576\n"
            "ds_read_b64 v[40:41], v78 offset:24576\n"
            "ds_read_b64 v[42:43], v79 offset:24576\n"
            "ds_read_b64 v[44:45], v80 offset:24576\n"
            "ds_read_b64 v[46:47], v81 offset:24576\n"
            "s_waitcnt lgkmcnt(8)\n"
            "v_mfma_f32_16x16x32_bf16 v[100:103], v[84:87], v[0:3], 0\n"
            "v_mfma_f32_16x16x32_bf16 v[104:107], v[84:87], v[16:19], 0\n"
            "v_mfma_f32_16x16x32_bf16 v[100:103], v[88:91], v[4:7], v[100:103]\n"
            "v_mfma_f32_16x16x32_bf16 v[104:107], v[88:91], v[20:23], v[104:107]\n"
            "v_mfma_f32_16x16x32_bf16 v[100:103], v[92:95], v[8:11], v[100:103]\n"
            "v_mfma_f32_16x16x32_bf16 v[104:107], v[92:95], v[24:27], v[104:107]\n"
            "v_mfma_f32_16x16x32_bf16 v[100:103], v[96:99], v[12:15], v[100:103]\n"
            "s_lshl_b32 s78, s74, 10\n"
            "s_add_u32 m0, s78, 28672\n"
            "s_add_u32 s88, s44, 4063232\n"
            "s_addc_u32 s89, s45, 0\n"
            "global_load_lds_dwordx4 v68, s[88:89]\n"
            "v_mfma_f32_16x16x32_bf16 v[104:107], v[96:99], v[28:31], v[104:107]\n"
            "v_cvt_pk_bf16_f32 v168, v48, v49\n"
            "v_cvt_pk_bf16_f32 v169, v50, v51\n"
            "v_cvt_pk_bf16_f32 v232, v52, v53\n"
            "v_cvt_pk_bf16_f32 v233, v54, v55\n"
            "ds_read_b64 v[84:85], v74 offset:25088\n"
            "ds_read_b64 v[86:87], v75 offset:25088\n"
            "ds_read_b64 v[88:89], v76 offset:25088\n"
            "ds_read_b64 v[90:91], v77 offset:25088\n"
            "ds_read_b64 v[92:93], v78 offset:25088\n"
            "ds_read_b64 v[94:95], v79 offset:25088\n"
            "ds_read_b64 v[96:97], v80 offset:25088\n"
            "ds_read_b64 v[98:99], v81 offset:25088\n"
            "s_waitcnt lgkmcnt(8)\n"
            "v_mfma_f32_16x16x32_bf16 v[48:51], v[32:35], v[0:3], 0\n"
            "v_mfma_f32_16x16x32_bf16 v[52:55], v[32:35], v[16:19], 0\n"
            "v_mfma_f32_16x16x32_bf16 v[48:51], v[36:39], v[4:7], v[48:51]\n"
            "v_mfma_f32_16x16x32_bf16 v[52:55], v[36:39], v[20:23], v[52:55]\n"
            "v_mfma_f32_16x16x32_bf16 v[48:51], v[40:43], v[8:11], v[48:51]\n"
            "v_mfma_f32_16x16x32_bf16 v[52:55], v[40:43], v[24:27], v[52:55]\n"
            "v_mfma_f32_16x16x32_bf16 v[48:51], v[44:47], v[12:15], v[48:51]\n"
            "v_mfma_f32_16x16x32_bf16 v[52:55], v[44:47], v[28:31], v[52:55]\n"
            "v_cvt_pk_bf16_f32 v170, v100, v101\n"
            "v_cvt_pk_bf16_f32 v171, v102, v103\n"
            "v_cvt_pk_bf16_f32 v234, v104, v105\n"
            "v_cvt_pk_bf16_f32 v235, v106, v107\n"
            "s_waitcnt lgkmcnt(0)\n"
            "v_mfma_f32_16x16x32_bf16 v[100:103], v[84:87], v[0:3], 0\n"
            "v_mfma_f32_16x16x32_bf16 v[104:107], v[84:87], v[16:19], 0\n"
            "v_mfma_f32_16x16x32_bf16 v[100:103], v[88:91], v[4:7], v[100:103]\n"
            "v_mfma_f32_16x16x32_bf16 v[104:107], v[88:91], v[20:23], v[104:107]\n"
            "v_mfma_f32_16x16x32_bf16 v[100:103], v[92:95], v[8:11], v[100:103]\n"
            "v_mfma_f32_16x16x32_bf16 v[104:107], v[92:95], v[24:27], v[104:107]\n"
            "v_mfma_f32_16x16x32_bf16 v[100:103], v[96:99], v[12:15], v[100:103]\n"
            "v_mfma_f32_16x16x32_bf16 v[104:107], v[96:99], v[28:31], v[104:107]\n"
            "v_cvt_pk_bf16_f32 v172, v48, v49\n"
            "v_cvt_pk_bf16_f32 v173, v50, v51\n"
            "v_cvt_pk_bf16_f32 v236, v52, v53\n"
            "v_cvt_pk_bf16_f32 v237, v54, v55\n"
            "s_nop 2\n"
            "v_cvt_pk_bf16_f32 v174, v100, v101\n"
            "v_cvt_pk_bf16_f32 v175, v102, v103\n"
            "v_cvt_pk_bf16_f32 v238, v104, v105\n"
            "v_cvt_pk_bf16_f32 v239, v106, v107\n"
            "s_waitcnt vmcnt(16)\n"
            "s_barrier\n"
            "s_lshl_b32 s78, s74, 10\n"
            "s_add_u32 m0, s78, 32768\n"
            "s_add_u32 s88, s40, 3145728\n"
            "s_addc_u32 s89, s41, 0\n"
            "global_load_lds_dwordx4 v63, s[88:89]\n"
            "v_add_u32 v72, 65536, v66\n"
            "v_xor_b32 v73, 32, v72\n"
            "s_lshl_b32 s78, s74, 10\n"
            "s_add_u32 m0, s78, 36864\n"
            "s_add_u32 s88, s40, 3276800\n"
            "s_addc_u32 s89, s41, 0\n"
            "global_load_lds_dwordx4 v63, s[88:89]\n"
            "ds_read_b64_tr_b16 a[32:33], v72 offset:0\n"
            "ds_read_b64_tr_b16 a[34:35], v72 offset:512\n"
            "s_lshl_b32 s78, s74, 10\n"
            "s_add_u32 m0, s78, 40960\n"
            "s_add_u32 s88, s40, 3407872\n"
            "s_addc_u32 s89, s41, 0\n"
            "global_load_lds_dwordx4 v63, s[88:89]\n"
            "ds_read_b64_tr_b16 a[36:37], v72 offset:8192\n"
            "ds_read_b64_tr_b16 a[38:39], v72 offset:8704\n"
            "s_lshl_b32 s78, s74, 10\n"
            "s_add_u32 m0, s78, 45056\n"
            "s_add_u32 s88, s40, 3538944\n"
            "s_addc_u32 s89, s41, 0\n"
            "global_load_lds_dwordx4 v63, s[88:89]\n"
            "ds_read_b64_tr_b16 a[96:97], v73 offset:0\n"
            "ds_read_b64_tr_b16 a[98:99], v73 offset:512\n"
            "s_lshl_b32 s78, s74, 10\n"
            "s_add_u32 m0, s78, 49152\n"
            "s_add_u32 s88, s40, 3670016\n"
            "s_addc_u32 s89, s41, 0\n"
            "global_load_lds_dwordx4 v63, s[88:89]\n"
            "ds_read_b64_tr_b16 a[100:101], v73 offset:8192\n"
            "ds_read_b64_tr_b16 a[102:103], v73 offset:8704\n"
            "s_lshl_b32 s78, s74, 10\n"
            "s_add_u32 m0, s78, 53248\n"
            "s_add_u32 s88, s40, 3801088\n"
            "s_addc_u32 s89, s41, 0\n"
            "global_load_lds_dwordx4 v63, s[88:89]\n"
            "s_waitcnt lgkmcnt(0)\n"
            "ds_read_b64_tr_b16 a[40:41], v72 offset:16384\n"
            "s_lshl_b32 s78, s74, 10\n"
            "s_add_u32 m0, s78, 57344\n"
            "s_add_u32 s88, s40, 3932160\n"
            "s_addc_u32 s89, s41, 0\n"
            "global_load_lds_dwordx4 v63, s[88:89]\n"
            "ds_read_b64_tr_b16 a[42:43], v72 offset:16896\n"
            "ds_read_b64_tr_b16 a[44:45], v72 offset:24576\n"
            "s_lshl_b32 s78, s74, 10\n"
            "s_add_u32 m0, s78, 61440\n"
            "s_add_u32 s88, s40, 4063232\n"
            "s_addc_u32 s89, s41, 0\n"
            "global_load_lds_dwordx4 v63, s[88:89]\n"
            "ds_read_b64_tr_b16 a[46:47], v72 offset:25088\n"
            "ds_read_b64_tr_b16 a[104:105], v73 offset:16384\n"
            "ds_read_b64_tr_b16 a[106:107], v73 offset:16896\n"
            "ds_read_b64_tr_b16 a[108:109], v73 offset:24576\n"
            "ds_read_b64_tr_b16 a[110:111], v73 offset:25088\n"
            "s_waitcnt lgkmcnt(0)\n"
            "s_waitcnt vmcnt(16)\n"
            "s_barrier\n"
            "s_lshl_b32 s78, s74, 10\n"
            "s_add_u32 m0, s78, 65536\n"
            "s_add_u32 s88, s42, 3145728\n"
            "s_addc_u32 s89, s43, 0\n"
            "global_load_lds_dwordx4 v63, s[88:89]\n"
            "v_add_u32 v72, 98304, v66\n"
            "v_xor_b32 v73, 32, v72\n"
            "s_lshl_b32 s78, s74, 10\n"
            "s_add_u32 m0, s78, 69632\n"
            "s_add_u32 s88, s42, 3276800\n"
            "s_addc_u32 s89, s43, 0\n"
            "global_load_lds_dwordx4 v63, s[88:89]\n"
            "ds_read_b64_tr_b16 a[160:161], v72 offset:0\n"
            "ds_read_b64_tr_b16 a[162:163], v72 offset:512\n"
            "s_lshl_b32 s78, s74, 10\n"
            "s_add_u32 m0, s78, 73728\n"
            "s_add_u32 s88, s42, 3407872\n"
            "s_addc_u32 s89, s43, 0\n"
            "global_load_lds_dwordx4 v63, s[88:89]\n"
            "ds_read_b64_tr_b16 a[164:165], v72 offset:8192\n"
            "ds_read_b64_tr_b16 a[166:167], v72 offset:8704\n"
            "s_lshl_b32 s78, s74, 10\n"
            "s_add_u32 m0, s78, 77824\n"
            "s_add_u32 s88, s42, 3538944\n"
            "s_addc_u32 s89, s43, 0\n"
            "global_load_lds_dwordx4 v63, s[88:89]\n"
            "ds_read_b64_tr_b16 a[224:225], v73 offset:0\n"
            "ds_read_b64_tr_b16 a[226:227], v73 offset:512\n"
            "s_lshl_b32 s78, s74, 10\n"
            "s_add_u32 m0, s78, 81920\n"
            "s_add_u32 s88, s42, 3670016\n"
            "s_addc_u32 s89, s43, 0\n"
            "global_load_lds_dwordx4 v63, s[88:89]\n"
            "ds_read_b64_tr_b16 a[228:229], v73 offset:8192\n"
            "ds_read_b64_tr_b16 a[230:231], v73 offset:8704\n"
            "s_lshl_b32 s78, s74, 10\n"
            "s_add_u32 m0, s78, 86016\n"
            "s_add_u32 s88, s42, 3801088\n"
            "s_addc_u32 s89, s43, 0\n"
            "global_load_lds_dwordx4 v63, s[88:89]\n"
            "s_waitcnt lgkmcnt(0)\n"
            "ds_read_b64_tr_b16 a[168:169], v72 offset:16384\n"
            "s_lshl_b32 s78, s74, 10\n"
            "s_add_u32 m0, s78, 90112\n"
            "s_add_u32 s88, s42, 3932160\n"
            "s_addc_u32 s89, s43, 0\n"
            "global_load_lds_dwordx4 v63, s[88:89]\n"
            "ds_read_b64_tr_b16 a[170:171], v72 offset:16896\n"
            "ds_read_b64_tr_b16 a[172:173], v72 offset:24576\n"
            "s_lshl_b32 s78, s74, 10\n"
            "s_add_u32 m0, s78, 94208\n"
            "s_add_u32 s88, s42, 4063232\n"
            "s_addc_u32 s89, s43, 0\n"
            "global_load_lds_dwordx4 v63, s[88:89]\n"
            "ds_read_b64_tr_b16 a[174:175], v72 offset:25088\n"
            "ds_read_b64_tr_b16 a[232:233], v73 offset:16384\n"
            "ds_read_b64_tr_b16 a[234:235], v73 offset:16896\n"
            "ds_read_b64_tr_b16 a[236:237], v73 offset:24576\n"
            "ds_read_b64_tr_b16 a[238:239], v73 offset:25088\n"
            "s_waitcnt lgkmcnt(0)\n"
            "s_waitcnt vmcnt(16)\n"
            "s_barrier\n"
            "s_lshl_b32 s78, s74, 10\n"
            "s_add_u32 m0, s78, 98304\n"
            "s_add_u32 s88, s44, 0\n"
            "s_addc_u32 s89, s45, 0\n"
            "global_load_lds_dwordx4 v62, s[88:89]\n"
            "v_add_u32 v74, 0, v67\n"
            "v_xor_b32 v75, 32, v74\n"
            "v_xor_b32 v76, 64, v74\n"
            "v_xor_b32 v77, 96, v74\n"
            "v_xor_b32 v78, 128, v74\n"
            "v_xor_b32 v79, 160, v74\n"
            "v_xor_b32 v80, 192, v74\n"
            "v_xor_b32 v81, 224, v74\n"
            "ds_read_b64 v[32:33], v74 offset:0\n"
            "ds_read_b64 v[34:35], v75 offset:0\n"
            "ds_read_b64 v[36:37], v76 offset:0\n"
            "ds_read_b64 v[38:39], v77 offset:0\n"
            "ds_read_b64 v[40:41], v78 offset:0\n"
            "ds_read_b64 v[42:43], v79 offset:0\n"
            "ds_read_b64 v[44:45], v80 offset:0\n"
            "ds_read_b64 v[46:47], v81 offset:0\n"
            "ds_read_b64 v[84:85], v74 offset:512\n"
            "ds_read_b64 v[86:87], v75 offset:512\n"
            "ds_read_b64 v[88:89], v76 offset:512\n"
            "s_lshl_b32 s78, s74, 10\n"
            "s_add_u32 m0, s78, 102400\n"
            "s_add_u32 s88, s44, 131072\n"
            "s_addc_u32 s89, s45, 0\n"
            "global_load_lds_dwordx4 v68, s[88:89]\n"
            "ds_read_b64 v[90:91], v77 offset:512\n"
            "ds_read_b64 v[92:93], v78 offset:512\n"
            "ds_read_b64 v[94:95], v79 offset:512\n"
            "ds_read_b64 v[96:97], v80 offset:512\n"
            "ds_read_b64 v[98:99], v81 offset:512\n"
            "s_waitcnt lgkmcnt(8)\n"
            "v_mfma_f32_16x16x32_bf16 v[48:51], v[32:35], v[0:3], 0\n"
            "v_mfma_f32_16x16x32_bf16 v[52:55], v[32:35], v[16:19], 0\n"
            "v_mfma_f32_16x16x32_bf16 v[48:51], v[36:39], v[4:7], v[48:51]\n"
            "v_mfma_f32_16x16x32_bf16 v[52:55], v[36:39], v[20:23], v[52:55]\n"
            "v_mfma_f32_16x16x32_bf16 v[48:51], v[40:43], v[8:11], v[48:51]\n"
            "v_mfma_f32_16x16x32_bf16 v[52:55], v[40:43], v[24:27], v[52:55]\n"
            "v_mfma_f32_16x16x32_bf16 v[48:51], v[44:47], v[12:15], v[48:51]\n"
            "v_mfma_f32_16x16x32_bf16 v[52:55], v[44:47], v[28:31], v[52:55]\n"
            "ds_read_b64 v[32:33], v74 offset:8192\n"
            "ds_read_b64 v[34:35], v75 offset:8192\n"
            "ds_read_b64 v[36:37], v76 offset:8192\n"
            "ds_read_b64 v[38:39], v77 offset:8192\n"
            "ds_read_b64 v[40:41], v78 offset:8192\n"
            "s_lshl_b32 s78, s74, 10\n"
            "s_add_u32 m0, s78, 106496\n"
            "s_add_u32 s88, s44, 262144\n"
            "s_addc_u32 s89, s45, 0\n"
            "global_load_lds_dwordx4 v62, s[88:89]\n"
            "ds_read_b64 v[42:43], v79 offset:8192\n"
            "ds_read_b64 v[44:45], v80 offset:8192\n"
            "ds_read_b64 v[46:47], v81 offset:8192\n"
            "s_waitcnt lgkmcnt(8)\n"
            "v_mfma_f32_16x16x32_bf16 v[100:103], v[84:87], v[0:3], 0\n"
            "v_mfma_f32_16x16x32_bf16 v[104:107], v[84:87], v[16:19], 0\n"
            "v_mfma_f32_16x16x32_bf16 v[100:103], v[88:91], v[4:7], v[100:103]\n"
            "v_mfma_f32_16x16x32_bf16 v[104:107], v[88:91], v[20:23], v[104:107]\n"
            "v_mfma_f32_16x16x32_bf16 v[100:103], v[92:95], v[8:11], v[100:103]\n"
            "v_mfma_f32_16x16x32_bf16 v[104:107], v[92:95], v[24:27], v[104:107]\n"
            "v_mfma_f32_16x16x32_bf16 v[100:103], v[96:99], v[12:15], v[100:103]\n"
            "v_mfma_f32_16x16x32_bf16 v[104:107], v[96:99], v[28:31], v[104:107]\n"
            "v_cvt_pk_bf16_f32 v176, v48, v49\n"
            "v_cvt_pk_bf16_f32 v177, v50, v51\n"
            "v_cvt_pk_bf16_f32 v240, v52, v53\n"
            "v_cvt_pk_bf16_f32 v241, v54, v55\n"
            "ds_read_b64 v[84:85], v74 offset:8704\n"
            "ds_read_b64 v[86:87], v75 offset:8704\n"
            "ds_read_b64 v[88:89], v76 offset:8704\n"
            "s_lshl_b32 s78, s74, 10\n"
            "s_add_u32 m0, s78, 110592\n"
            "s_add_u32 s88, s44, 393216\n"
            "s_addc_u32 s89, s45, 0\n"
            "global_load_lds_dwordx4 v68, s[88:89]\n"
            "ds_read_b64 v[90:91], v77 offset:8704\n"
            "ds_read_b64 v[92:93], v78 offset:8704\n"
            "ds_read_b64 v[94:95], v79 offset:8704\n"
            "ds_read_b64 v[96:97], v80 offset:8704\n"
            "ds_read_b64 v[98:99], v81 offset:8704\n"
            "s_waitcnt lgkmcnt(8)\n"
            "v_mfma_f32_16x16x32_bf16 v[48:51], v[32:35], v[0:3], 0\n"
            "v_mfma_f32_16x16x32_bf16 v[52:55], v[32:35], v[16:19], 0\n"
            "v_mfma_f32_16x16x32_bf16 v[48:51], v[36:39], v[4:7], v[48:51]\n"
            "v_mfma_f32_16x16x32_bf16 v[52:55], v[36:39], v[20:23], v[52:55]\n"
            "v_mfma_f32_16x16x32_bf16 v[48:51], v[40:43], v[8:11], v[48:51]\n"
            "v_mfma_f32_16x16x32_bf16 v[52:55], v[40:43], v[24:27], v[52:55]\n"
            "v_mfma_f32_16x16x32_bf16 v[48:51], v[44:47], v[12:15], v[48:51]\n"
            "v_mfma_f32_16x16x32_bf16 v[52:55], v[44:47], v[28:31], v[52:55]\n"
            "v_cvt_pk_bf16_f32 v178, v100, v101\n"
            "v_cvt_pk_bf16_f32 v179, v102, v103\n"
            "v_cvt_pk_bf16_f32 v242, v104, v105\n"
            "v_cvt_pk_bf16_f32 v243, v106, v107\n"
            "ds_read_b64 v[32:33], v74 offset:16384\n"
            "s_lshl_b32 s78, s74, 10\n"
            "s_add_u32 m0, s78, 114688\n"
            "s_add_u32 s88, s44, 524288\n"
            "s_addc_u32 s89, s45, 0\n"
            "global_load_lds_dwordx4 v62, s[88:89]\n"
            "ds_read_b64 v[34:35], v75 offset:16384\n"
            "ds_read_b64 v[36:37], v76 offset:16384\n"
            "ds_read_b64 v[38:39], v77 offset:16384\n"
            "ds_read_b64 v[40:41], v78 offset:16384\n"
            "ds_read_b64 v[42:43], v79 offset:16384\n"
            "ds_read_b64 v[44:45], v80 offset:16384\n"
            "ds_read_b64 v[46:47], v81 offset:16384\n"
            "s_waitcnt lgkmcnt(8)\n"
            "v_mfma_f32_16x16x32_bf16 v[100:103], v[84:87], v[0:3], 0\n"
            "v_mfma_f32_16x16x32_bf16 v[104:107], v[84:87], v[16:19], 0\n"
            "v_mfma_f32_16x16x32_bf16 v[100:103], v[88:91], v[4:7], v[100:103]\n"
            "v_mfma_f32_16x16x32_bf16 v[104:107], v[88:91], v[20:23], v[104:107]\n"
            "v_mfma_f32_16x16x32_bf16 v[100:103], v[92:95], v[8:11], v[100:103]\n"
            "v_mfma_f32_16x16x32_bf16 v[104:107], v[92:95], v[24:27], v[104:107]\n"
            "v_mfma_f32_16x16x32_bf16 v[100:103], v[96:99], v[12:15], v[100:103]\n"
            "v_mfma_f32_16x16x32_bf16 v[104:107], v[96:99], v[28:31], v[104:107]\n"
            "v_cvt_pk_bf16_f32 v180, v48, v49\n"
            "v_cvt_pk_bf16_f32 v181, v50, v51\n"
            "v_cvt_pk_bf16_f32 v244, v52, v53\n"
            "s_lshl_b32 s78, s74, 10\n"
            "s_add_u32 m0, s78, 118784\n"
            "s_add_u32 s88, s44, 655360\n"
            "s_addc_u32 s89, s45, 0\n"
            "global_load_lds_dwordx4 v68, s[88:89]\n"
            "v_cvt_pk_bf16_f32 v245, v54, v55\n"
            "ds_read_b64 v[84:85], v74 offset:16896\n"
            "ds_read_b64 v[86:87], v75 offset:16896\n"
            "ds_read_b64 v[88:89], v76 offset:16896\n"
            "ds_read_b64 v[90:91], v77 offset:16896\n"
            "ds_read_b64 v[92:93], v78 offset:16896\n"
            "ds_read_b64 v[94:95], v79 offset:16896\n"
            "ds_read_b64 v[96:97], v80 offset:16896\n"
            "ds_read_b64 v[98:99], v81 offset:16896\n"
            "s_waitcnt lgkmcnt(8)\n"
            "v_mfma_f32_16x16x32_bf16 v[48:51], v[32:35], v[0:3], 0\n"
            "v_mfma_f32_16x16x32_bf16 v[52:55], v[32:35], v[16:19], 0\n"
            "v_mfma_f32_16x16x32_bf16 v[48:51], v[36:39], v[4:7], v[48:51]\n"
            "v_mfma_f32_16x16x32_bf16 v[52:55], v[36:39], v[20:23], v[52:55]\n"
            "v_mfma_f32_16x16x32_bf16 v[48:51], v[40:43], v[8:11], v[48:51]\n"
            "v_mfma_f32_16x16x32_bf16 v[52:55], v[40:43], v[24:27], v[52:55]\n"
            "v_mfma_f32_16x16x32_bf16 v[48:51], v[44:47], v[12:15], v[48:51]\n"
            "v_mfma_f32_16x16x32_bf16 v[52:55], v[44:47], v[28:31], v[52:55]\n"
            "v_cvt_pk_bf16_f32 v182, v100, v101\n"
            "s_lshl_b32 s78, s74, 10\n"
            "s_add_u32 m0, s78, 122880\n"
            "s_add_u32 s88, s44, 786432\n"
            "s_addc_u32 s89, s45, 0\n"
            "global_load_lds_dwordx4 v62, s[88:89]\n"
            "v_cvt_pk_bf16_f32 v183, v102, v103\n"
            "v_cvt_pk_bf16_f32 v246, v104, v105\n"
            "v_cvt_pk_bf16_f32 v247, v106, v107\n"
            "ds_read_b64 v[32:33], v74 offset:24576\n"
            "ds_read_b64 v[34:35], v75 offset:24576\n"
            "ds_read_b64 v[36:37], v76 offset:24576\n"
            "ds_read_b64 v[38:39], v77 offset:24576\n"
            "ds_read_b64 v[40:41], v78 offset:24576\n"
            "ds_read_b64 v[42:43], v79 offset:24576\n"
            "ds_read_b64 v[44:45], v80 offset:24576\n"
            "ds_read_b64 v[46:47], v81 offset:24576\n"
            "s_waitcnt lgkmcnt(8)\n"
            "v_mfma_f32_16x16x32_bf16 v[100:103], v[84:87], v[0:3], 0\n"
            "v_mfma_f32_16x16x32_bf16 v[104:107], v[84:87], v[16:19], 0\n"
            "v_mfma_f32_16x16x32_bf16 v[100:103], v[88:91], v[4:7], v[100:103]\n"
            "v_mfma_f32_16x16x32_bf16 v[104:107], v[88:91], v[20:23], v[104:107]\n"
            "v_mfma_f32_16x16x32_bf16 v[100:103], v[92:95], v[8:11], v[100:103]\n"
            "v_mfma_f32_16x16x32_bf16 v[104:107], v[92:95], v[24:27], v[104:107]\n"
            "v_mfma_f32_16x16x32_bf16 v[100:103], v[96:99], v[12:15], v[100:103]\n"
            "s_lshl_b32 s78, s74, 10\n"
            "s_add_u32 m0, s78, 126976\n"
            "s_add_u32 s88, s44, 917504\n"
            "s_addc_u32 s89, s45, 0\n"
            "global_load_lds_dwordx4 v68, s[88:89]\n"
            "v_mfma_f32_16x16x32_bf16 v[104:107], v[96:99], v[28:31], v[104:107]\n"
            "v_cvt_pk_bf16_f32 v184, v48, v49\n"
            "v_cvt_pk_bf16_f32 v185, v50, v51\n"
            "v_cvt_pk_bf16_f32 v248, v52, v53\n"
            "v_cvt_pk_bf16_f32 v249, v54, v55\n"
            "ds_read_b64 v[84:85], v74 offset:25088\n"
            "ds_read_b64 v[86:87], v75 offset:25088\n"
            "ds_read_b64 v[88:89], v76 offset:25088\n"
            "ds_read_b64 v[90:91], v77 offset:25088\n"
            "ds_read_b64 v[92:93], v78 offset:25088\n"
            "ds_read_b64 v[94:95], v79 offset:25088\n"
            "ds_read_b64 v[96:97], v80 offset:25088\n"
            "ds_read_b64 v[98:99], v81 offset:25088\n"
            "s_waitcnt lgkmcnt(8)\n"
            "v_mfma_f32_16x16x32_bf16 v[48:51], v[32:35], v[0:3], 0\n"
            "v_mfma_f32_16x16x32_bf16 v[52:55], v[32:35], v[16:19], 0\n"
            "v_mfma_f32_16x16x32_bf16 v[48:51], v[36:39], v[4:7], v[48:51]\n"
            "v_mfma_f32_16x16x32_bf16 v[52:55], v[36:39], v[20:23], v[52:55]\n"
            "v_mfma_f32_16x16x32_bf16 v[48:51], v[40:43], v[8:11], v[48:51]\n"
            "v_mfma_f32_16x16x32_bf16 v[52:55], v[40:43], v[24:27], v[52:55]\n"
            "v_mfma_f32_16x16x32_bf16 v[48:51], v[44:47], v[12:15], v[48:51]\n"
            "v_mfma_f32_16x16x32_bf16 v[52:55], v[44:47], v[28:31], v[52:55]\n"
            "v_cvt_pk_bf16_f32 v186, v100, v101\n"
            "v_cvt_pk_bf16_f32 v187, v102, v103\n"
            "v_cvt_pk_bf16_f32 v250, v104, v105\n"
            "v_cvt_pk_bf16_f32 v251, v106, v107\n"
            "s_waitcnt lgkmcnt(0)\n"
            "v_mfma_f32_16x16x32_bf16 v[100:103], v[84:87], v[0:3], 0\n"
            "v_mfma_f32_16x16x32_bf16 v[104:107], v[84:87], v[16:19], 0\n"
            "v_mfma_f32_16x16x32_bf16 v[100:103], v[88:91], v[4:7], v[100:103]\n"
            "v_mfma_f32_16x16x32_bf16 v[104:107], v[88:91], v[20:23], v[104:107]\n"
            "v_mfma_f32_16x16x32_bf16 v[100:103], v[92:95], v[8:11], v[100:103]\n"
            "v_mfma_f32_16x16x32_bf16 v[104:107], v[92:95], v[24:27], v[104:107]\n"
            "v_mfma_f32_16x16x32_bf16 v[100:103], v[96:99], v[12:15], v[100:103]\n"
            "v_mfma_f32_16x16x32_bf16 v[104:107], v[96:99], v[28:31], v[104:107]\n"
            "v_cvt_pk_bf16_f32 v188, v48, v49\n"
            "v_cvt_pk_bf16_f32 v189, v50, v51\n"
            "v_cvt_pk_bf16_f32 v252, v52, v53\n"
            "v_cvt_pk_bf16_f32 v253, v54, v55\n"
            "s_nop 2\n"
            "v_cvt_pk_bf16_f32 v190, v100, v101\n"
            "v_cvt_pk_bf16_f32 v191, v102, v103\n"
            "v_cvt_pk_bf16_f32 v254, v104, v105\n"
            "v_cvt_pk_bf16_f32 v255, v106, v107\n"
            "s_waitcnt vmcnt(16)\n"
            "s_barrier\n"
            "s_lshl_b32 s78, s74, 10\n"
            "s_add_u32 m0, s78, 0\n"
            "s_add_u32 s88, s40, 0\n"
            "s_addc_u32 s89, s41, 0\n"
            "global_load_lds_dwordx4 v63, s[88:89]\n"
            "v_add_u32 v72, 32768, v66\n"
            "v_xor_b32 v73, 32, v72\n"
            "s_lshl_b32 s78, s74, 10\n"
            "s_add_u32 m0, s78, 4096\n"
            "s_add_u32 s88, s40, 131072\n"
            "s_addc_u32 s89, s41, 0\n"
            "global_load_lds_dwordx4 v63, s[88:89]\n"
            "ds_read_b64_tr_b16 a[48:49], v72 offset:0\n"
            "ds_read_b64_tr_b16 a[50:51], v72 offset:512\n"
            "s_lshl_b32 s78, s74, 10\n"
            "s_add_u32 m0, s78, 8192\n"
            "s_add_u32 s88, s40, 262144\n"
            "s_addc_u32 s89, s41, 0\n"
            "global_load_lds_dwordx4 v63, s[88:89]\n"
            "ds_read_b64_tr_b16 a[52:53], v72 offset:8192\n"
            "ds_read_b64_tr_b16 a[54:55], v72 offset:8704\n"
            "s_lshl_b32 s78, s74, 10\n"
            "s_add_u32 m0, s78, 12288\n"
            "s_add_u32 s88, s40, 393216\n"
            "s_addc_u32 s89, s41, 0\n"
            "global_load_lds_dwordx4 v63, s[88:89]\n"
            "ds_read_b64_tr_b16 a[112:113], v73 offset:0\n"
            "ds_read_b64_tr_b16 a[114:115], v73 offset:512\n"
            "s_lshl_b32 s78, s74, 10\n"
            "s_add_u32 m0, s78, 16384\n"
            "s_add_u32 s88, s40, 524288\n"
            "s_addc_u32 s89, s41, 0\n"
            "global_load_lds_dwordx4 v63, s[88:89]\n"
            "ds_read_b64_tr_b16 a[116:117], v73 offset:8192\n"
            "ds_read_b64_tr_b16 a[118:119], v73 offset:8704\n"
            "s_lshl_b32 s78, s74, 10\n"
            "s_add_u32 m0, s78, 20480\n"
            "s_add_u32 s88, s40, 655360\n"
            "s_addc_u32 s89, s41, 0\n"
            "global_load_lds_dwordx4 v63, s[88:89]\n"
            "s_waitcnt lgkmcnt(0)\n"
            "ds_read_b64_tr_b16 a[56:57], v72 offset:16384\n"
            "s_lshl_b32 s78, s74, 10\n"
            "s_add_u32 m0, s78, 24576\n"
            "s_add_u32 s88, s40, 786432\n"
            "s_addc_u32 s89, s41, 0\n"
            "global_load_lds_dwordx4 v63, s[88:89]\n"
            "ds_read_b64_tr_b16 a[58:59], v72 offset:16896\n"
            "ds_read_b64_tr_b16 a[60:61], v72 offset:24576\n"
            "s_lshl_b32 s78, s74, 10\n"
            "s_add_u32 m0, s78, 28672\n"
            "s_add_u32 s88, s40, 917504\n"
            "s_addc_u32 s89, s41, 0\n"
            "global_load_lds_dwordx4 v63, s[88:89]\n"
            "ds_read_b64_tr_b16 a[62:63], v72 offset:25088\n"
            "ds_read_b64_tr_b16 a[120:121], v73 offset:16384\n"
            "ds_read_b64_tr_b16 a[122:123], v73 offset:16896\n"
            "ds_read_b64_tr_b16 a[124:125], v73 offset:24576\n"
            "ds_read_b64_tr_b16 a[126:127], v73 offset:25088\n"
            "s_waitcnt lgkmcnt(0)\n"
            "s_waitcnt vmcnt(16)\n"
            "s_barrier\n"
            "s_lshl_b32 s78, s74, 10\n"
            "s_add_u32 m0, s78, 32768\n"
            "s_add_u32 s88, s42, 0\n"
            "s_addc_u32 s89, s43, 0\n"
            "global_load_lds_dwordx4 v63, s[88:89]\n"
            "v_add_u32 v72, 65536, v66\n"
            "v_xor_b32 v73, 32, v72\n"
            "s_lshl_b32 s78, s74, 10\n"
            "s_add_u32 m0, s78, 36864\n"
            "s_add_u32 s88, s42, 131072\n"
            "s_addc_u32 s89, s43, 0\n"
            "global_load_lds_dwordx4 v63, s[88:89]\n"
            "ds_read_b64_tr_b16 a[176:177], v72 offset:0\n"
            "ds_read_b64_tr_b16 a[178:179], v72 offset:512\n"
            "s_lshl_b32 s78, s74, 10\n"
            "s_add_u32 m0, s78, 40960\n"
            "s_add_u32 s88, s42, 262144\n"
            "s_addc_u32 s89, s43, 0\n"
            "global_load_lds_dwordx4 v63, s[88:89]\n"
            "ds_read_b64_tr_b16 a[180:181], v72 offset:8192\n"
            "ds_read_b64_tr_b16 a[182:183], v72 offset:8704\n"
            "s_lshl_b32 s78, s74, 10\n"
            "s_add_u32 m0, s78, 45056\n"
            "s_add_u32 s88, s42, 393216\n"
            "s_addc_u32 s89, s43, 0\n"
            "global_load_lds_dwordx4 v63, s[88:89]\n"
            "ds_read_b64_tr_b16 a[240:241], v73 offset:0\n"
            "ds_read_b64_tr_b16 a[242:243], v73 offset:512\n"
            "s_lshl_b32 s78, s74, 10\n"
            "s_add_u32 m0, s78, 49152\n"
            "s_add_u32 s88, s42, 524288\n"
            "s_addc_u32 s89, s43, 0\n"
            "global_load_lds_dwordx4 v63, s[88:89]\n"
            "ds_read_b64_tr_b16 a[244:245], v73 offset:8192\n"
            "ds_read_b64_tr_b16 a[246:247], v73 offset:8704\n"
            "s_lshl_b32 s78, s74, 10\n"
            "s_add_u32 m0, s78, 53248\n"
            "s_add_u32 s88, s42, 655360\n"
            "s_addc_u32 s89, s43, 0\n"
            "global_load_lds_dwordx4 v63, s[88:89]\n"
            "s_waitcnt lgkmcnt(0)\n"
            "ds_read_b64_tr_b16 a[184:185], v72 offset:16384\n"
            "s_lshl_b32 s78, s74, 10\n"
            "s_add_u32 m0, s78, 57344\n"
            "s_add_u32 s88, s42, 786432\n"
            "s_addc_u32 s89, s43, 0\n"
            "global_load_lds_dwordx4 v63, s[88:89]\n"
            "ds_read_b64_tr_b16 a[186:187], v72 offset:16896\n"
            "ds_read_b64_tr_b16 a[188:189], v72 offset:24576\n"
            "s_lshl_b32 s78, s74, 10\n"
            "s_add_u32 m0, s78, 61440\n"
            "s_add_u32 s88, s42, 917504\n"
            "s_addc_u32 s89, s43, 0\n"
            "global_load_lds_dwordx4 v63, s[88:89]\n"
            "ds_read_b64_tr_b16 a[190:191], v72 offset:25088\n"
            "ds_read_b64_tr_b16 a[248:249], v73 offset:16384\n"
            "ds_read_b64_tr_b16 a[250:251], v73 offset:16896\n"
            "ds_read_b64_tr_b16 a[252:253], v73 offset:24576\n"
            "ds_read_b64_tr_b16 a[254:255], v73 offset:25088\n"
            "s_waitcnt lgkmcnt(0)\n"
            "s_waitcnt vmcnt(16)\n"
            "s_barrier\n"
            "s_lshl_b32 s78, s74, 10\n"
            "s_add_u32 m0, s78, 65536\n"
            "s_add_u32 s88, s44, 1048576\n"
            "s_addc_u32 s89, s45, 0\n"
            "global_load_lds_dwordx4 v62, s[88:89]\n"
            "v_add_u32 v74, 98304, v67\n"
            "v_xor_b32 v75, 32, v74\n"
            "v_xor_b32 v76, 64, v74\n"
            "v_xor_b32 v77, 96, v74\n"
            "v_xor_b32 v78, 128, v74\n"
            "v_xor_b32 v79, 160, v74\n"
            "v_xor_b32 v80, 192, v74\n"
            "v_xor_b32 v81, 224, v74\n"
            "ds_read_b64 v[32:33], v74 offset:0\n"
            "ds_read_b64 v[34:35], v75 offset:0\n"
            "ds_read_b64 v[36:37], v76 offset:0\n"
            "ds_read_b64 v[38:39], v77 offset:0\n"
            "ds_read_b64 v[40:41], v78 offset:0\n"
            "ds_read_b64 v[42:43], v79 offset:0\n"
            "ds_read_b64 v[44:45], v80 offset:0\n"
            "ds_read_b64 v[46:47], v81 offset:0\n"
            "ds_read_b64 v[84:85], v74 offset:512\n"
            "ds_read_b64 v[86:87], v75 offset:512\n"
            "ds_read_b64 v[88:89], v76 offset:512\n"
            "s_lshl_b32 s78, s74, 10\n"
            "s_add_u32 m0, s78, 69632\n"
            "s_add_u32 s88, s44, 1179648\n"
            "s_addc_u32 s89, s45, 0\n"
            "global_load_lds_dwordx4 v68, s[88:89]\n"
            "ds_read_b64 v[90:91], v77 offset:512\n"
            "ds_read_b64 v[92:93], v78 offset:512\n"
            "ds_read_b64 v[94:95], v79 offset:512\n"
            "ds_read_b64 v[96:97], v80 offset:512\n"
            "ds_read_b64 v[98:99], v81 offset:512\n"
            "s_waitcnt lgkmcnt(8)\n"
            "v_mfma_f32_16x16x32_bf16 v[48:51], v[32:35], v[0:3], 0\n"
            "v_mfma_f32_16x16x32_bf16 v[52:55], v[32:35], v[16:19], 0\n"
            "v_mfma_f32_16x16x32_bf16 v[48:51], v[36:39], v[4:7], v[48:51]\n"
            "v_mfma_f32_16x16x32_bf16 v[52:55], v[36:39], v[20:23], v[52:55]\n"
            "v_mfma_f32_16x16x32_bf16 v[48:51], v[40:43], v[8:11], v[48:51]\n"
            "v_mfma_f32_16x16x32_bf16 v[52:55], v[40:43], v[24:27], v[52:55]\n"
            "v_mfma_f32_16x16x32_bf16 v[48:51], v[44:47], v[12:15], v[48:51]\n"
            "v_mfma_f32_16x16x32_bf16 v[52:55], v[44:47], v[28:31], v[52:55]\n"
            "ds_read_b64 v[32:33], v74 offset:8192\n"
            "ds_read_b64 v[34:35], v75 offset:8192\n"
            "ds_read_b64 v[36:37], v76 offset:8192\n"
            "ds_read_b64 v[38:39], v77 offset:8192\n"
            "ds_read_b64 v[40:41], v78 offset:8192\n"
            "s_lshl_b32 s78, s74, 10\n"
            "s_add_u32 m0, s78, 73728\n"
            "s_add_u32 s88, s44, 1310720\n"
            "s_addc_u32 s89, s45, 0\n"
            "global_load_lds_dwordx4 v62, s[88:89]\n"
            "ds_read_b64 v[42:43], v79 offset:8192\n"
            "ds_read_b64 v[44:45], v80 offset:8192\n"
            "ds_read_b64 v[46:47], v81 offset:8192\n"
            "s_waitcnt lgkmcnt(8)\n"
            "v_mfma_f32_16x16x32_bf16 v[100:103], v[84:87], v[0:3], 0\n"
            "v_mfma_f32_16x16x32_bf16 v[104:107], v[84:87], v[16:19], 0\n"
            "v_mfma_f32_16x16x32_bf16 v[100:103], v[88:91], v[4:7], v[100:103]\n"
            "v_mfma_f32_16x16x32_bf16 v[104:107], v[88:91], v[20:23], v[104:107]\n"
            "v_mfma_f32_16x16x32_bf16 v[100:103], v[92:95], v[8:11], v[100:103]\n"
            "v_mfma_f32_16x16x32_bf16 v[104:107], v[92:95], v[24:27], v[104:107]\n"
            "v_mfma_f32_16x16x32_bf16 v[100:103], v[96:99], v[12:15], v[100:103]\n"
            "v_mfma_f32_16x16x32_bf16 v[104:107], v[96:99], v[28:31], v[104:107]\n"
            "v_cvt_pk_bf16_f32 v128, v48, v49\n"
            "v_cvt_pk_bf16_f32 v129, v50, v51\n"
            "v_cvt_pk_bf16_f32 v192, v52, v53\n"
            "v_cvt_pk_bf16_f32 v193, v54, v55\n"
            "ds_read_b64 v[84:85], v74 offset:8704\n"
            "ds_read_b64 v[86:87], v75 offset:8704\n"
            "ds_read_b64 v[88:89], v76 offset:8704\n"
            "s_lshl_b32 s78, s74, 10\n"
            "s_add_u32 m0, s78, 77824\n"
            "s_add_u32 s88, s44, 1441792\n"
            "s_addc_u32 s89, s45, 0\n"
            "global_load_lds_dwordx4 v68, s[88:89]\n"
            "ds_read_b64 v[90:91], v77 offset:8704\n"
            "ds_read_b64 v[92:93], v78 offset:8704\n"
            "ds_read_b64 v[94:95], v79 offset:8704\n"
            "ds_read_b64 v[96:97], v80 offset:8704\n"
            "ds_read_b64 v[98:99], v81 offset:8704\n"
            "s_waitcnt lgkmcnt(8)\n"
            "v_mfma_f32_16x16x32_bf16 v[48:51], v[32:35], v[0:3], 0\n"
            "v_mfma_f32_16x16x32_bf16 v[52:55], v[32:35], v[16:19], 0\n"
            "v_mfma_f32_16x16x32_bf16 v[48:51], v[36:39], v[4:7], v[48:51]\n"
            "v_mfma_f32_16x16x32_bf16 v[52:55], v[36:39], v[20:23], v[52:55]\n"
            "v_mfma_f32_16x16x32_bf16 v[48:51], v[40:43], v[8:11], v[48:51]\n"
            "v_mfma_f32_16x16x32_bf16 v[52:55], v[40:43], v[24:27], v[52:55]\n"
            "v_mfma_f32_16x16x32_bf16 v[48:51], v[44:47], v[12:15], v[48:51]\n"
            "v_mfma_f32_16x16x32_bf16 v[52:55], v[44:47], v[28:31], v[52:55]\n"
            "v_cvt_pk_bf16_f32 v130, v100, v101\n"
            "v_cvt_pk_bf16_f32 v131, v102, v103\n"
            "v_cvt_pk_bf16_f32 v194, v104, v105\n"
            "v_cvt_pk_bf16_f32 v195, v106, v107\n"
            "ds_read_b64 v[32:33], v74 offset:16384\n"
            "s_lshl_b32 s78, s74, 10\n"
            "s_add_u32 m0, s78, 81920\n"
            "s_add_u32 s88, s44, 1572864\n"
            "s_addc_u32 s89, s45, 0\n"
            "global_load_lds_dwordx4 v62, s[88:89]\n"
            "ds_read_b64 v[34:35], v75 offset:16384\n"
            "ds_read_b64 v[36:37], v76 offset:16384\n"
            "ds_read_b64 v[38:39], v77 offset:16384\n"
            "ds_read_b64 v[40:41], v78 offset:16384\n"
            "ds_read_b64 v[42:43], v79 offset:16384\n"
            "ds_read_b64 v[44:45], v80 offset:16384\n"
            "ds_read_b64 v[46:47], v81 offset:16384\n"
            "s_waitcnt lgkmcnt(8)\n"
            "v_mfma_f32_16x16x32_bf16 v[100:103], v[84:87], v[0:3], 0\n"
            "v_mfma_f32_16x16x32_bf16 v[104:107], v[84:87], v[16:19], 0\n"
            "v_mfma_f32_16x16x32_bf16 v[100:103], v[88:91], v[4:7], v[100:103]\n"
            "v_mfma_f32_16x16x32_bf16 v[104:107], v[88:91], v[20:23], v[104:107]\n"
            "v_mfma_f32_16x16x32_bf16 v[100:103], v[92:95], v[8:11], v[100:103]\n"
            "v_mfma_f32_16x16x32_bf16 v[104:107], v[92:95], v[24:27], v[104:107]\n"
            "v_mfma_f32_16x16x32_bf16 v[100:103], v[96:99], v[12:15], v[100:103]\n"
            "v_mfma_f32_16x16x32_bf16 v[104:107], v[96:99], v[28:31], v[104:107]\n"
            "v_cvt_pk_bf16_f32 v132, v48, v49\n"
            "v_cvt_pk_bf16_f32 v133, v50, v51\n"
            "v_cvt_pk_bf16_f32 v196, v52, v53\n"
            "s_lshl_b32 s78, s74, 10\n"
            "s_add_u32 m0, s78, 86016\n"
            "s_add_u32 s88, s44, 1703936\n"
            "s_addc_u32 s89, s45, 0\n"
            "global_load_lds_dwordx4 v68, s[88:89]\n"
            "v_cvt_pk_bf16_f32 v197, v54, v55\n"
            "ds_read_b64 v[84:85], v74 offset:16896\n"
            "ds_read_b64 v[86:87], v75 offset:16896\n"
            "ds_read_b64 v[88:89], v76 offset:16896\n"
            "ds_read_b64 v[90:91], v77 offset:16896\n"
            "ds_read_b64 v[92:93], v78 offset:16896\n"
            "ds_read_b64 v[94:95], v79 offset:16896\n"
            "ds_read_b64 v[96:97], v80 offset:16896\n"
            "ds_read_b64 v[98:99], v81 offset:16896\n"
            "s_waitcnt lgkmcnt(8)\n"
            "v_mfma_f32_16x16x32_bf16 v[48:51], v[32:35], v[0:3], 0\n"
            "v_mfma_f32_16x16x32_bf16 v[52:55], v[32:35], v[16:19], 0\n"
            "v_mfma_f32_16x16x32_bf16 v[48:51], v[36:39], v[4:7], v[48:51]\n"
            "v_mfma_f32_16x16x32_bf16 v[52:55], v[36:39], v[20:23], v[52:55]\n"
            "v_mfma_f32_16x16x32_bf16 v[48:51], v[40:43], v[8:11], v[48:51]\n"
            "v_mfma_f32_16x16x32_bf16 v[52:55], v[40:43], v[24:27], v[52:55]\n"
            "v_mfma_f32_16x16x32_bf16 v[48:51], v[44:47], v[12:15], v[48:51]\n"
            "v_mfma_f32_16x16x32_bf16 v[52:55], v[44:47], v[28:31], v[52:55]\n"
            "v_cvt_pk_bf16_f32 v134, v100, v101\n"
            "s_lshl_b32 s78, s74, 10\n"
            "s_add_u32 m0, s78, 90112\n"
            "s_add_u32 s88, s44, 1835008\n"
            "s_addc_u32 s89, s45, 0\n"
            "global_load_lds_dwordx4 v62, s[88:89]\n"
            "v_cvt_pk_bf16_f32 v135, v102, v103\n"
            "v_cvt_pk_bf16_f32 v198, v104, v105\n"
            "v_cvt_pk_bf16_f32 v199, v106, v107\n"
            "ds_read_b64 v[32:33], v74 offset:24576\n"
            "ds_read_b64 v[34:35], v75 offset:24576\n"
            "ds_read_b64 v[36:37], v76 offset:24576\n"
            "ds_read_b64 v[38:39], v77 offset:24576\n"
            "ds_read_b64 v[40:41], v78 offset:24576\n"
            "ds_read_b64 v[42:43], v79 offset:24576\n"
            "ds_read_b64 v[44:45], v80 offset:24576\n"
            "ds_read_b64 v[46:47], v81 offset:24576\n"
            "s_waitcnt lgkmcnt(8)\n"
            "v_mfma_f32_16x16x32_bf16 v[100:103], v[84:87], v[0:3], 0\n"
            "v_mfma_f32_16x16x32_bf16 v[104:107], v[84:87], v[16:19], 0\n"
            "v_mfma_f32_16x16x32_bf16 v[100:103], v[88:91], v[4:7], v[100:103]\n"
            "v_mfma_f32_16x16x32_bf16 v[104:107], v[88:91], v[20:23], v[104:107]\n"
            "v_mfma_f32_16x16x32_bf16 v[100:103], v[92:95], v[8:11], v[100:103]\n"
            "v_mfma_f32_16x16x32_bf16 v[104:107], v[92:95], v[24:27], v[104:107]\n"
            "v_mfma_f32_16x16x32_bf16 v[100:103], v[96:99], v[12:15], v[100:103]\n"
            "s_lshl_b32 s78, s74, 10\n"
            "s_add_u32 m0, s78, 94208\n"
            "s_add_u32 s88, s44, 1966080\n"
            "s_addc_u32 s89, s45, 0\n"
            "global_load_lds_dwordx4 v68, s[88:89]\n"
            "v_mfma_f32_16x16x32_bf16 v[104:107], v[96:99], v[28:31], v[104:107]\n"
            "v_cvt_pk_bf16_f32 v136, v48, v49\n"
            "v_cvt_pk_bf16_f32 v137, v50, v51\n"
            "v_cvt_pk_bf16_f32 v200, v52, v53\n"
            "v_cvt_pk_bf16_f32 v201, v54, v55\n"
            "ds_read_b64 v[84:85], v74 offset:25088\n"
            "ds_read_b64 v[86:87], v75 offset:25088\n"
            "ds_read_b64 v[88:89], v76 offset:25088\n"
            "ds_read_b64 v[90:91], v77 offset:25088\n"
            "ds_read_b64 v[92:93], v78 offset:25088\n"
            "ds_read_b64 v[94:95], v79 offset:25088\n"
            "ds_read_b64 v[96:97], v80 offset:25088\n"
            "ds_read_b64 v[98:99], v81 offset:25088\n"
            "s_waitcnt lgkmcnt(8)\n"
            "v_mfma_f32_16x16x32_bf16 v[48:51], v[32:35], v[0:3], 0\n"
            "v_mfma_f32_16x16x32_bf16 v[52:55], v[32:35], v[16:19], 0\n"
            "v_mfma_f32_16x16x32_bf16 v[48:51], v[36:39], v[4:7], v[48:51]\n"
            "v_mfma_f32_16x16x32_bf16 v[52:55], v[36:39], v[20:23], v[52:55]\n"
            "v_mfma_f32_16x16x32_bf16 v[48:51], v[40:43], v[8:11], v[48:51]\n"
            "v_mfma_f32_16x16x32_bf16 v[52:55], v[40:43], v[24:27], v[52:55]\n"
            "v_mfma_f32_16x16x32_bf16 v[48:51], v[44:47], v[12:15], v[48:51]\n"
            "v_mfma_f32_16x16x32_bf16 v[52:55], v[44:47], v[28:31], v[52:55]\n"
            "v_cvt_pk_bf16_f32 v138, v100, v101\n"
            "v_cvt_pk_bf16_f32 v139, v102, v103\n"
            "v_cvt_pk_bf16_f32 v202, v104, v105\n"
            "v_cvt_pk_bf16_f32 v203, v106, v107\n"
            "s_waitcnt lgkmcnt(0)\n"
            "v_mfma_f32_16x16x32_bf16 v[100:103], v[84:87], v[0:3], 0\n"
            "v_mfma_f32_16x16x32_bf16 v[104:107], v[84:87], v[16:19], 0\n"
            "v_mfma_f32_16x16x32_bf16 v[100:103], v[88:91], v[4:7], v[100:103]\n"
            "v_mfma_f32_16x16x32_bf16 v[104:107], v[88:91], v[20:23], v[104:107]\n"
            "v_mfma_f32_16x16x32_bf16 v[100:103], v[92:95], v[8:11], v[100:103]\n"
            "v_mfma_f32_16x16x32_bf16 v[104:107], v[92:95], v[24:27], v[104:107]\n"
            "v_mfma_f32_16x16x32_bf16 v[100:103], v[96:99], v[12:15], v[100:103]\n"
            "v_mfma_f32_16x16x32_bf16 v[104:107], v[96:99], v[28:31], v[104:107]\n"
            "v_cvt_pk_bf16_f32 v140, v48, v49\n"
            "v_cvt_pk_bf16_f32 v141, v50, v51\n"
            "v_cvt_pk_bf16_f32 v204, v52, v53\n"
            "v_cvt_pk_bf16_f32 v205, v54, v55\n"
            "s_nop 2\n"
            "v_cvt_pk_bf16_f32 v142, v100, v101\n"
            "v_cvt_pk_bf16_f32 v143, v102, v103\n"
            "v_cvt_pk_bf16_f32 v206, v104, v105\n"
            "v_cvt_pk_bf16_f32 v207, v106, v107\n"
            "s_waitcnt vmcnt(16)\n"
            "s_barrier\n"
            "s_lshl_b32 s78, s74, 10\n"
            "s_add_u32 m0, s78, 98304\n"
            "s_add_u32 s88, s40, 1048576\n"
            "s_addc_u32 s89, s41, 0\n"
            "global_load_lds_dwordx4 v63, s[88:89]\n"
            "v_add_u32 v72, 0, v66\n"
            "v_xor_b32 v73, 32, v72\n"
            "s_lshl_b32 s78, s74, 10\n"
            "s_add_u32 m0, s78, 102400\n"
            "s_add_u32 s88, s40, 1179648\n"
            "s_addc_u32 s89, s41, 0\n"
            "global_load_lds_dwordx4 v63, s[88:89]\n"
            "ds_read_b64_tr_b16 a[0:1], v72 offset:0\n"
            "ds_read_b64_tr_b16 a[2:3], v72 offset:512\n"
            "s_lshl_b32 s78, s74, 10\n"
            "s_add_u32 m0, s78, 106496\n"
            "s_add_u32 s88, s40, 1310720\n"
            "s_addc_u32 s89, s41, 0\n"
            "global_load_lds_dwordx4 v63, s[88:89]\n"
            "ds_read_b64_tr_b16 a[4:5], v72 offset:8192\n"
            "ds_read_b64_tr_b16 a[6:7], v72 offset:8704\n"
            "s_lshl_b32 s78, s74, 10\n"
            "s_add_u32 m0, s78, 110592\n"
            "s_add_u32 s88, s40, 1441792\n"
            "s_addc_u32 s89, s41, 0\n"
            "global_load_lds_dwordx4 v63, s[88:89]\n"
            "ds_read_b64_tr_b16 a[64:65], v73 offset:0\n"
            "ds_read_b64_tr_b16 a[66:67], v73 offset:512\n"
            "s_lshl_b32 s78, s74, 10\n"
            "s_add_u32 m0, s78, 114688\n"
            "s_add_u32 s88, s40, 1572864\n"
            "s_addc_u32 s89, s41, 0\n"
            "global_load_lds_dwordx4 v63, s[88:89]\n"
            "ds_read_b64_tr_b16 a[68:69], v73 offset:8192\n"
            "ds_read_b64_tr_b16 a[70:71], v73 offset:8704\n"
            "s_lshl_b32 s78, s74, 10\n"
            "s_add_u32 m0, s78, 118784\n"
            "s_add_u32 s88, s40, 1703936\n"
            "s_addc_u32 s89, s41, 0\n"
            "global_load_lds_dwordx4 v63, s[88:89]\n"
            "s_waitcnt lgkmcnt(0)\n"
            "ds_read_b64_tr_b16 a[8:9], v72 offset:16384\n"
            "s_lshl_b32 s78, s74, 10\n"
            "s_add_u32 m0, s78, 122880\n"
            "s_add_u32 s88, s40, 1835008\n"
            "s_addc_u32 s89, s41, 0\n"
            "global_load_lds_dwordx4 v63, s[88:89]\n"
            "ds_read_b64_tr_b16 a[10:11], v72 offset:16896\n"
            "ds_read_b64_tr_b16 a[12:13], v72 offset:24576\n"
            "s_lshl_b32 s78, s74, 10\n"
            "s_add_u32 m0, s78, 126976\n"
            "s_add_u32 s88, s40, 1966080\n"
            "s_addc_u32 s89, s41, 0\n"
            "global_load_lds_dwordx4 v63, s[88:89]\n"
            "ds_read_b64_tr_b16 a[14:15], v72 offset:25088\n"
            "ds_read_b64_tr_b16 a[72:73], v73 offset:16384\n"
            "ds_read_b64_tr_b16 a[74:75], v73 offset:16896\n"
            "ds_read_b64_tr_b16 a[76:77], v73 offset:24576\n"
            "ds_read_b64_tr_b16 a[78:79], v73 offset:25088\n"
            "s_waitcnt lgkmcnt(0)\n"
            "s_waitcnt vmcnt(16)\n"
            "s_barrier\n"
            "s_lshl_b32 s78, s74, 10\n"
            "s_add_u32 m0, s78, 0\n"
            "s_add_u32 s88, s42, 1048576\n"
            "s_addc_u32 s89, s43, 0\n"
            "global_load_lds_dwordx4 v63, s[88:89]\n"
            "v_add_u32 v72, 32768, v66\n"
            "v_xor_b32 v73, 32, v72\n"
            "s_lshl_b32 s78, s74, 10\n"
            "s_add_u32 m0, s78, 4096\n"
            "s_add_u32 s88, s42, 1179648\n"
            "s_addc_u32 s89, s43, 0\n"
            "global_load_lds_dwordx4 v63, s[88:89]\n"
            "ds_read_b64_tr_b16 a[128:129], v72 offset:0\n"
            "ds_read_b64_tr_b16 a[130:131], v72 offset:512\n"
            "s_lshl_b32 s78, s74, 10\n"
            "s_add_u32 m0, s78, 8192\n"
            "s_add_u32 s88, s42, 1310720\n"
            "s_addc_u32 s89, s43, 0\n"
            "global_load_lds_dwordx4 v63, s[88:89]\n"
            "ds_read_b64_tr_b16 a[132:133], v72 offset:8192\n"
            "ds_read_b64_tr_b16 a[134:135], v72 offset:8704\n"
            "s_lshl_b32 s78, s74, 10\n"
            "s_add_u32 m0, s78, 12288\n"
            "s_add_u32 s88, s42, 1441792\n"
            "s_addc_u32 s89, s43, 0\n"
            "global_load_lds_dwordx4 v63, s[88:89]\n"
            "ds_read_b64_tr_b16 a[192:193], v73 offset:0\n"
            "ds_read_b64_tr_b16 a[194:195], v73 offset:512\n"
            "s_lshl_b32 s78, s74, 10\n"
            "s_add_u32 m0, s78, 16384\n"
            "s_add_u32 s88, s42, 1572864\n"
            "s_addc_u32 s89, s43, 0\n"
            "global_load_lds_dwordx4 v63, s[88:89]\n"
            "ds_read_b64_tr_b16 a[196:197], v73 offset:8192\n"
            "ds_read_b64_tr_b16 a[198:199], v73 offset:8704\n"
            "s_lshl_b32 s78, s74, 10\n"
            "s_add_u32 m0, s78, 20480\n"
            "s_add_u32 s88, s42, 1703936\n"
            "s_addc_u32 s89, s43, 0\n"
            "global_load_lds_dwordx4 v63, s[88:89]\n"
            "s_waitcnt lgkmcnt(0)\n"
            "ds_read_b64_tr_b16 a[136:137], v72 offset:16384\n"
            "s_lshl_b32 s78, s74, 10\n"
            "s_add_u32 m0, s78, 24576\n"
            "s_add_u32 s88, s42, 1835008\n"
            "s_addc_u32 s89, s43, 0\n"
            "global_load_lds_dwordx4 v63, s[88:89]\n"
            "ds_read_b64_tr_b16 a[138:139], v72 offset:16896\n"
            "ds_read_b64_tr_b16 a[140:141], v72 offset:24576\n"
            "s_lshl_b32 s78, s74, 10\n"
            "s_add_u32 m0, s78, 28672\n"
            "s_add_u32 s88, s42, 1966080\n"
            "s_addc_u32 s89, s43, 0\n"
            "global_load_lds_dwordx4 v63, s[88:89]\n"
            "ds_read_b64_tr_b16 a[142:143], v72 offset:25088\n"
            "ds_read_b64_tr_b16 a[200:201], v73 offset:16384\n"
            "ds_read_b64_tr_b16 a[202:203], v73 offset:16896\n"
            "ds_read_b64_tr_b16 a[204:205], v73 offset:24576\n"
            "ds_read_b64_tr_b16 a[206:207], v73 offset:25088\n"
            "s_waitcnt lgkmcnt(0)\n"
            "s_waitcnt vmcnt(16)\n"
            "s_barrier\n"
            "v_add_u32 v74, 65536, v67\n"
            "v_xor_b32 v75, 32, v74\n"
            "v_xor_b32 v76, 64, v74\n"
            "v_xor_b32 v77, 96, v74\n"
            "v_xor_b32 v78, 128, v74\n"
            "v_xor_b32 v79, 160, v74\n"
            "v_xor_b32 v80, 192, v74\n"
            "v_xor_b32 v81, 224, v74\n"
            "ds_read_b64 v[32:33], v74 offset:0\n"
            "ds_read_b64 v[34:35], v75 offset:0\n"
            "ds_read_b64 v[36:37], v76 offset:0\n"
            "ds_read_b64 v[38:39], v77 offset:0\n"
            "ds_read_b64 v[40:41], v78 offset:0\n"
            "ds_read_b64 v[42:43], v79 offset:0\n"
            "ds_read_b64 v[44:45], v80 offset:0\n"
            "ds_read_b64 v[46:47], v81 offset:0\n"
            "ds_read_b64 v[84:85], v74 offset:512\n"
            "ds_read_b64 v[86:87], v75 offset:512\n"
            "ds_read_b64 v[88:89], v76 offset:512\n"
            "ds_read_b64 v[90:91], v77 offset:512\n"
            "ds_read_b64 v[92:93], v78 offset:512\n"
            "ds_read_b64 v[94:95], v79 offset:512\n"
            "ds_read_b64 v[96:97], v80 offset:512\n"
            "ds_read_b64 v[98:99], v81 offset:512\n"
            "s_waitcnt lgkmcnt(8)\n"
            "v_mfma_f32_16x16x32_bf16 v[48:51], v[32:35], v[0:3], 0\n"
            "v_mfma_f32_16x16x32_bf16 v[52:55], v[32:35], v[16:19], 0\n"
            "v_mfma_f32_16x16x32_bf16 v[48:51], v[36:39], v[4:7], v[48:51]\n"
            "v_mfma_f32_16x16x32_bf16 v[52:55], v[36:39], v[20:23], v[52:55]\n"
            "v_mfma_f32_16x16x32_bf16 v[48:51], v[40:43], v[8:11], v[48:51]\n"
            "v_mfma_f32_16x16x32_bf16 v[52:55], v[40:43], v[24:27], v[52:55]\n"
            "v_mfma_f32_16x16x32_bf16 v[48:51], v[44:47], v[12:15], v[48:51]\n"
            "v_mfma_f32_16x16x32_bf16 v[52:55], v[44:47], v[28:31], v[52:55]\n"
            "ds_read_b64 v[32:33], v74 offset:8192\n"
            "ds_read_b64 v[34:35], v75 offset:8192\n"
            "ds_read_b64 v[36:37], v76 offset:8192\n"
            "ds_read_b64 v[38:39], v77 offset:8192\n"
            "ds_read_b64 v[40:41], v78 offset:8192\n"
            "ds_read_b64 v[42:43], v79 offset:8192\n"
            "ds_read_b64 v[44:45], v80 offset:8192\n"
            "ds_read_b64 v[46:47], v81 offset:8192\n"
            "s_waitcnt lgkmcnt(8)\n"
            "v_mfma_f32_16x16x32_bf16 v[100:103], v[84:87], v[0:3], 0\n"
            "v_mfma_f32_16x16x32_bf16 v[104:107], v[84:87], v[16:19], 0\n"
            "v_mfma_f32_16x16x32_bf16 v[100:103], v[88:91], v[4:7], v[100:103]\n"
            "v_mfma_f32_16x16x32_bf16 v[104:107], v[88:91], v[20:23], v[104:107]\n"
            "v_mfma_f32_16x16x32_bf16 v[100:103], v[92:95], v[8:11], v[100:103]\n"
            "v_mfma_f32_16x16x32_bf16 v[104:107], v[92:95], v[24:27], v[104:107]\n"
            "v_mfma_f32_16x16x32_bf16 v[100:103], v[96:99], v[12:15], v[100:103]\n"
            "v_mfma_f32_16x16x32_bf16 v[104:107], v[96:99], v[28:31], v[104:107]\n"
            "v_cvt_pk_bf16_f32 v144, v48, v49\n"
            "v_cvt_pk_bf16_f32 v145, v50, v51\n"
            "v_cvt_pk_bf16_f32 v208, v52, v53\n"
            "v_cvt_pk_bf16_f32 v209, v54, v55\n"
            "ds_read_b64 v[84:85], v74 offset:8704\n"
            "ds_read_b64 v[86:87], v75 offset:8704\n"
            "ds_read_b64 v[88:89], v76 offset:8704\n"
            "ds_read_b64 v[90:91], v77 offset:8704\n"
            "ds_read_b64 v[92:93], v78 offset:8704\n"
            "ds_read_b64 v[94:95], v79 offset:8704\n"
            "ds_read_b64 v[96:97], v80 offset:8704\n"
            "ds_read_b64 v[98:99], v81 offset:8704\n"
            "s_waitcnt lgkmcnt(8)\n"
            "v_mfma_f32_16x16x32_bf16 v[48:51], v[32:35], v[0:3], 0\n"
            "v_mfma_f32_16x16x32_bf16 v[52:55], v[32:35], v[16:19], 0\n"
            "v_mfma_f32_16x16x32_bf16 v[48:51], v[36:39], v[4:7], v[48:51]\n"
            "v_mfma_f32_16x16x32_bf16 v[52:55], v[36:39], v[20:23], v[52:55]\n"
            "v_mfma_f32_16x16x32_bf16 v[48:51], v[40:43], v[8:11], v[48:51]\n"
            "v_mfma_f32_16x16x32_bf16 v[52:55], v[40:43], v[24:27], v[52:55]\n"
            "v_mfma_f32_16x16x32_bf16 v[48:51], v[44:47], v[12:15], v[48:51]\n"
            "v_mfma_f32_16x16x32_bf16 v[52:55], v[44:47], v[28:31], v[52:55]\n"
            "v_cvt_pk_bf16_f32 v146, v100, v101\n"
            "v_cvt_pk_bf16_f32 v147, v102, v103\n"
            "v_cvt_pk_bf16_f32 v210, v104, v105\n"
            "v_cvt_pk_bf16_f32 v211, v106, v107\n"
            "ds_read_b64 v[32:33], v74 offset:16384\n"
            "ds_read_b64 v[34:35], v75 offset:16384\n"
            "ds_read_b64 v[36:37], v76 offset:16384\n"
            "ds_read_b64 v[38:39], v77 offset:16384\n"
            "ds_read_b64 v[40:41], v78 offset:16384\n"
            "ds_read_b64 v[42:43], v79 offset:16384\n"
            "ds_read_b64 v[44:45], v80 offset:16384\n"
            "ds_read_b64 v[46:47], v81 offset:16384\n"
            "s_waitcnt lgkmcnt(8)\n"
            "v_mfma_f32_16x16x32_bf16 v[100:103], v[84:87], v[0:3], 0\n"
            "v_mfma_f32_16x16x32_bf16 v[104:107], v[84:87], v[16:19], 0\n"
            "v_mfma_f32_16x16x32_bf16 v[100:103], v[88:91], v[4:7], v[100:103]\n"
            "v_mfma_f32_16x16x32_bf16 v[104:107], v[88:91], v[20:23], v[104:107]\n"
            "v_mfma_f32_16x16x32_bf16 v[100:103], v[92:95], v[8:11], v[100:103]\n"
            "v_mfma_f32_16x16x32_bf16 v[104:107], v[92:95], v[24:27], v[104:107]\n"
            "v_mfma_f32_16x16x32_bf16 v[100:103], v[96:99], v[12:15], v[100:103]\n"
            "v_mfma_f32_16x16x32_bf16 v[104:107], v[96:99], v[28:31], v[104:107]\n"
            "v_cvt_pk_bf16_f32 v148, v48, v49\n"
            "v_cvt_pk_bf16_f32 v149, v50, v51\n"
            "v_cvt_pk_bf16_f32 v212, v52, v53\n"
            "v_cvt_pk_bf16_f32 v213, v54, v55\n"
            "ds_read_b64 v[84:85], v74 offset:16896\n"
            "ds_read_b64 v[86:87], v75 offset:16896\n"
            "ds_read_b64 v[88:89], v76 offset:16896\n"
            "ds_read_b64 v[90:91], v77 offset:16896\n"
            "ds_read_b64 v[92:93], v78 offset:16896\n"
            "ds_read_b64 v[94:95], v79 offset:16896\n"
            "ds_read_b64 v[96:97], v80 offset:16896\n"
            "ds_read_b64 v[98:99], v81 offset:16896\n"
            "s_waitcnt lgkmcnt(8)\n"
            "v_mfma_f32_16x16x32_bf16 v[48:51], v[32:35], v[0:3], 0\n"
            "v_mfma_f32_16x16x32_bf16 v[52:55], v[32:35], v[16:19], 0\n"
            "v_mfma_f32_16x16x32_bf16 v[48:51], v[36:39], v[4:7], v[48:51]\n"
            "v_mfma_f32_16x16x32_bf16 v[52:55], v[36:39], v[20:23], v[52:55]\n"
            "v_mfma_f32_16x16x32_bf16 v[48:51], v[40:43], v[8:11], v[48:51]\n"
            "v_mfma_f32_16x16x32_bf16 v[52:55], v[40:43], v[24:27], v[52:55]\n"
            "v_mfma_f32_16x16x32_bf16 v[48:51], v[44:47], v[12:15], v[48:51]\n"
            "v_mfma_f32_16x16x32_bf16 v[52:55], v[44:47], v[28:31], v[52:55]\n"
            "v_cvt_pk_bf16_f32 v150, v100, v101\n"
            "v_cvt_pk_bf16_f32 v151, v102, v103\n"
            "v_cvt_pk_bf16_f32 v214, v104, v105\n"
            "v_cvt_pk_bf16_f32 v215, v106, v107\n"
            "ds_read_b64 v[32:33], v74 offset:24576\n"
            "ds_read_b64 v[34:35], v75 offset:24576\n"
            "ds_read_b64 v[36:37], v76 offset:24576\n"
            "ds_read_b64 v[38:39], v77 offset:24576\n"
            "ds_read_b64 v[40:41], v78 offset:24576\n"
            "ds_read_b64 v[42:43], v79 offset:24576\n"
            "ds_read_b64 v[44:45], v80 offset:24576\n"
            "ds_read_b64 v[46:47], v81 offset:24576\n"
            "s_waitcnt lgkmcnt(8)\n"
            "v_mfma_f32_16x16x32_bf16 v[100:103], v[84:87], v[0:3], 0\n"
            "v_mfma_f32_16x16x32_bf16 v[104:107], v[84:87], v[16:19], 0\n"
            "v_mfma_f32_16x16x32_bf16 v[100:103], v[88:91], v[4:7], v[100:103]\n"
            "v_mfma_f32_16x16x32_bf16 v[104:107], v[88:91], v[20:23], v[104:107]\n"
            "v_mfma_f32_16x16x32_bf16 v[100:103], v[92:95], v[8:11], v[100:103]\n"
            "v_mfma_f32_16x16x32_bf16 v[104:107], v[92:95], v[24:27], v[104:107]\n"
            "v_mfma_f32_16x16x32_bf16 v[100:103], v[96:99], v[12:15], v[100:103]\n"
            "v_mfma_f32_16x16x32_bf16 v[104:107], v[96:99], v[28:31], v[104:107]\n"
            "v_cvt_pk_bf16_f32 v152, v48, v49\n"
            "v_cvt_pk_bf16_f32 v153, v50, v51\n"
            "v_cvt_pk_bf16_f32 v216, v52, v53\n"
            "v_cvt_pk_bf16_f32 v217, v54, v55\n"
            "ds_read_b64 v[84:85], v74 offset:25088\n"
            "ds_read_b64 v[86:87], v75 offset:25088\n"
            "ds_read_b64 v[88:89], v76 offset:25088\n"
            "ds_read_b64 v[90:91], v77 offset:25088\n"
            "ds_read_b64 v[92:93], v78 offset:25088\n"
            "ds_read_b64 v[94:95], v79 offset:25088\n"
            "ds_read_b64 v[96:97], v80 offset:25088\n"
            "ds_read_b64 v[98:99], v81 offset:25088\n"
            "s_waitcnt lgkmcnt(8)\n"
            "v_mfma_f32_16x16x32_bf16 v[48:51], v[32:35], v[0:3], 0\n"
            "v_mfma_f32_16x16x32_bf16 v[52:55], v[32:35], v[16:19], 0\n"
            "v_mfma_f32_16x16x32_bf16 v[48:51], v[36:39], v[4:7], v[48:51]\n"
            "v_mfma_f32_16x16x32_bf16 v[52:55], v[36:39], v[20:23], v[52:55]\n"
            "v_mfma_f32_16x16x32_bf16 v[48:51], v[40:43], v[8:11], v[48:51]\n"
            "v_mfma_f32_16x16x32_bf16 v[52:55], v[40:43], v[24:27], v[52:55]\n"
            "v_mfma_f32_16x16x32_bf16 v[48:51], v[44:47], v[12:15], v[48:51]\n"
            "v_mfma_f32_16x16x32_bf16 v[52:55], v[44:47], v[28:31], v[52:55]\n"
            "v_cvt_pk_bf16_f32 v154, v100, v101\n"
            "v_cvt_pk_bf16_f32 v155, v102, v103\n"
            "v_cvt_pk_bf16_f32 v218, v104, v105\n"
            "v_cvt_pk_bf16_f32 v219, v106, v107\n"
            "s_waitcnt lgkmcnt(0)\n"
            "v_mfma_f32_16x16x32_bf16 v[100:103], v[84:87], v[0:3], 0\n"
            "v_mfma_f32_16x16x32_bf16 v[104:107], v[84:87], v[16:19], 0\n"
            "v_mfma_f32_16x16x32_bf16 v[100:103], v[88:91], v[4:7], v[100:103]\n"
            "v_mfma_f32_16x16x32_bf16 v[104:107], v[88:91], v[20:23], v[104:107]\n"
            "v_mfma_f32_16x16x32_bf16 v[100:103], v[92:95], v[8:11], v[100:103]\n"
            "v_mfma_f32_16x16x32_bf16 v[104:107], v[92:95], v[24:27], v[104:107]\n"
            "v_mfma_f32_16x16x32_bf16 v[100:103], v[96:99], v[12:15], v[100:103]\n"
            "v_mfma_f32_16x16x32_bf16 v[104:107], v[96:99], v[28:31], v[104:107]\n"
            "v_cvt_pk_bf16_f32 v156, v48, v49\n"
            "v_cvt_pk_bf16_f32 v157, v50, v51\n"
            "v_cvt_pk_bf16_f32 v220, v52, v53\n"
            "v_cvt_pk_bf16_f32 v221, v54, v55\n"
            "s_nop 2\n"
            "v_cvt_pk_bf16_f32 v158, v100, v101\n"
            "v_cvt_pk_bf16_f32 v159, v102, v103\n"
            "v_cvt_pk_bf16_f32 v222, v104, v105\n"
            "v_cvt_pk_bf16_f32 v223, v106, v107\n"
            "s_waitcnt vmcnt(8)\n"
            "s_barrier\n"
            "v_add_u32 v72, 98304, v66\n"
            "v_xor_b32 v73, 32, v72\n"
            "ds_read_b64_tr_b16 a[16:17], v72 offset:0\n"
            "ds_read_b64_tr_b16 a[18:19], v72 offset:512\n"
            "ds_read_b64_tr_b16 a[20:21], v72 offset:8192\n"
            "ds_read_b64_tr_b16 a[22:23], v72 offset:8704\n"
            "ds_read_b64_tr_b16 a[80:81], v73 offset:0\n"
            "ds_read_b64_tr_b16 a[82:83], v73 offset:512\n"
            "ds_read_b64_tr_b16 a[84:85], v73 offset:8192\n"
            "ds_read_b64_tr_b16 a[86:87], v73 offset:8704\n"
            "s_waitcnt lgkmcnt(0)\n"
            "ds_read_b64_tr_b16 a[24:25], v72 offset:16384\n"
            "ds_read_b64_tr_b16 a[26:27], v72 offset:16896\n"
            "ds_read_b64_tr_b16 a[28:29], v72 offset:24576\n"
            "ds_read_b64_tr_b16 a[30:31], v72 offset:25088\n"
            "ds_read_b64_tr_b16 a[88:89], v73 offset:16384\n"
            "ds_read_b64_tr_b16 a[90:91], v73 offset:16896\n"
            "ds_read_b64_tr_b16 a[92:93], v73 offset:24576\n"
            "ds_read_b64_tr_b16 a[94:95], v73 offset:25088\n"
            "s_waitcnt lgkmcnt(0)\n"
            "s_waitcnt vmcnt(0)\n"
            "s_barrier\n"
            "v_add_u32 v72, 0, v66\n"
            "v_xor_b32 v73, 32, v72\n"
            "ds_read_b64_tr_b16 a[144:145], v72 offset:0\n"
            "ds_read_b64_tr_b16 a[146:147], v72 offset:512\n"
            "ds_read_b64_tr_b16 a[148:149], v72 offset:8192\n"
            "ds_read_b64_tr_b16 a[150:151], v72 offset:8704\n"
            "ds_read_b64_tr_b16 a[208:209], v73 offset:0\n"
            "ds_read_b64_tr_b16 a[210:211], v73 offset:512\n"
            "ds_read_b64_tr_b16 a[212:213], v73 offset:8192\n"
            "ds_read_b64_tr_b16 a[214:215], v73 offset:8704\n"
            "s_waitcnt lgkmcnt(0)\n"
            "ds_read_b64_tr_b16 a[152:153], v72 offset:16384\n"
            "ds_read_b64_tr_b16 a[154:155], v72 offset:16896\n"
            "ds_read_b64_tr_b16 a[156:157], v72 offset:24576\n"
            "ds_read_b64_tr_b16 a[158:159], v72 offset:25088\n"
            "ds_read_b64_tr_b16 a[216:217], v73 offset:16384\n"
            "ds_read_b64_tr_b16 a[218:219], v73 offset:16896\n"
            "ds_read_b64_tr_b16 a[220:221], v73 offset:24576\n"
            "ds_read_b64_tr_b16 a[222:223], v73 offset:25088\n"
            "s_waitcnt lgkmcnt(0)\n"
            "s_branch PRO_WDONE_%=\n"
            "PRO_A3_%=:\n"
            "s_lshl_b32 s78, s74, 10\n"
            "s_add_u32 m0, s78, 0\n"
            "s_add_u32 s88, s46, 0\n"
            "s_addc_u32 s89, s47, 0\n"
            "global_load_lds_dwordx4 v64, s[88:89]\n"
            "s_lshl_b32 s78, s74, 10\n"
            "s_add_u32 m0, s78, 4096\n"
            "s_add_u32 s88, s46, 16384\n"
            "s_addc_u32 s89, s47, 0\n"
            "global_load_lds_dwordx4 v64, s[88:89]\n"
            "s_lshl_b32 s78, s74, 10\n"
            "s_add_u32 m0, s78, 8192\n"
            "s_add_u32 s88, s46, 32768\n"
            "s_addc_u32 s89, s47, 0\n"
            "global_load_lds_dwordx4 v64, s[88:89]\n"
            "s_lshl_b32 s78, s74, 10\n"
            "s_add_u32 m0, s78, 12288\n"
            "s_add_u32 s88, s46, 49152\n"
            "s_addc_u32 s89, s47, 0\n"
            "global_load_lds_dwordx4 v64, s[88:89]\n"
            "s_lshl_b32 s78, s74, 10\n"
            "s_add_u32 m0, s78, 16384\n"
            "s_add_u32 s88, s46, 65536\n"
            "s_addc_u32 s89, s47, 0\n"
            "global_load_lds_dwordx4 v64, s[88:89]\n"
            "s_lshl_b32 s78, s74, 10\n"
            "s_add_u32 m0, s78, 20480\n"
            "s_add_u32 s88, s46, 81920\n"
            "s_addc_u32 s89, s47, 0\n"
            "global_load_lds_dwordx4 v64, s[88:89]\n"
            "s_lshl_b32 s78, s74, 10\n"
            "s_add_u32 m0, s78, 24576\n"
            "s_add_u32 s88, s46, 98304\n"
            "s_addc_u32 s89, s47, 0\n"
            "global_load_lds_dwordx4 v64, s[88:89]\n"
            "s_lshl_b32 s78, s74, 10\n"
            "s_add_u32 m0, s78, 28672\n"
            "s_add_u32 s88, s46, 114688\n"
            "s_addc_u32 s89, s47, 0\n"
            "global_load_lds_dwordx4 v64, s[88:89]\n"
            "s_lshl_b32 s78, s74, 10\n"
            "s_add_u32 m0, s78, 32768\n"
            "s_add_u32 s88, s44, 3145728\n"
            "s_addc_u32 s89, s45, 0\n"
            "global_load_lds_dwordx4 v62, s[88:89]\n"
            "s_lshl_b32 s78, s74, 10\n"
            "s_add_u32 m0, s78, 36864\n"
            "s_add_u32 s88, s44, 3276800\n"
            "s_addc_u32 s89, s45, 0\n"
            "global_load_lds_dwordx4 v68, s[88:89]\n"
            "s_lshl_b32 s78, s74, 10\n"
            "s_add_u32 m0, s78, 40960\n"
            "s_add_u32 s88, s44, 3407872\n"
            "s_addc_u32 s89, s45, 0\n"
            "global_load_lds_dwordx4 v62, s[88:89]\n"
            "s_lshl_b32 s78, s74, 10\n"
            "s_add_u32 m0, s78, 45056\n"
            "s_add_u32 s88, s44, 3538944\n"
            "s_addc_u32 s89, s45, 0\n"
            "global_load_lds_dwordx4 v68, s[88:89]\n"
            "s_lshl_b32 s78, s74, 10\n"
            "s_add_u32 m0, s78, 49152\n"
            "s_add_u32 s88, s44, 3670016\n"
            "s_addc_u32 s89, s45, 0\n"
            "global_load_lds_dwordx4 v62, s[88:89]\n"
            "s_lshl_b32 s78, s74, 10\n"
            "s_add_u32 m0, s78, 53248\n"
            "s_add_u32 s88, s44, 3801088\n"
            "s_addc_u32 s89, s45, 0\n"
            "global_load_lds_dwordx4 v68, s[88:89]\n"
            "s_lshl_b32 s78, s74, 10\n"
            "s_add_u32 m0, s78, 57344\n"
            "s_add_u32 s88, s44, 3932160\n"
            "s_addc_u32 s89, s45, 0\n"
            "global_load_lds_dwordx4 v62, s[88:89]\n"
            "s_lshl_b32 s78, s74, 10\n"
            "s_add_u32 m0, s78, 61440\n"
            "s_add_u32 s88, s44, 4063232\n"
            "s_addc_u32 s89, s45, 0\n"
            "global_load_lds_dwordx4 v68, s[88:89]\n"
            "s_lshl_b32 s78, s74, 10\n"
            "s_add_u32 m0, s78, 65536\n"
            "s_add_u32 s88, s40, 3145728\n"
            "s_addc_u32 s89, s41, 0\n"
            "global_load_lds_dwordx4 v63, s[88:89]\n"
            "s_lshl_b32 s78, s74, 10\n"
            "s_add_u32 m0, s78, 69632\n"
            "s_add_u32 s88, s40, 3276800\n"
            "s_addc_u32 s89, s41, 0\n"
            "global_load_lds_dwordx4 v63, s[88:89]\n"
            "s_lshl_b32 s78, s74, 10\n"
            "s_add_u32 m0, s78, 73728\n"
            "s_add_u32 s88, s40, 3407872\n"
            "s_addc_u32 s89, s41, 0\n"
            "global_load_lds_dwordx4 v63, s[88:89]\n"
            "s_lshl_b32 s78, s74, 10\n"
            "s_add_u32 m0, s78, 77824\n"
            "s_add_u32 s88, s40, 3538944\n"
            "s_addc_u32 s89, s41, 0\n"
            "global_load_lds_dwordx4 v63, s[88:89]\n"
            "s_lshl_b32 s78, s74, 10\n"
            "s_add_u32 m0, s78, 81920\n"
            "s_add_u32 s88, s40, 3670016\n"
            "s_addc_u32 s89, s41, 0\n"
            "global_load_lds_dwordx4 v63, s[88:89]\n"
            "s_lshl_b32 s78, s74, 10\n"
            "s_add_u32 m0, s78, 86016\n"
            "s_add_u32 s88, s40, 3801088\n"
            "s_addc_u32 s89, s41, 0\n"
            "global_load_lds_dwordx4 v63, s[88:89]\n"
            "s_lshl_b32 s78, s74, 10\n"
            "s_add_u32 m0, s78, 90112\n"
            "s_add_u32 s88, s40, 3932160\n"
            "s_addc_u32 s89, s41, 0\n"
            "global_load_lds_dwordx4 v63, s[88:89]\n"
            "s_lshl_b32 s78, s74, 10\n"
            "s_add_u32 m0, s78, 94208\n"
            "s_add_u32 s88, s40, 4063232\n"
            "s_addc_u32 s89, s41, 0\n"
            "global_load_lds_dwordx4 v63, s[88:89]\n"
            "s_waitcnt vmcnt(16)\n"
            "s_barrier\n"
            "s_lshl_b32 s78, s74, 10\n"
            "s_add_u32 m0, s78, 98304\n"
            "s_add_u32 s88, s42, 3145728\n"
            "s_addc_u32 s89, s43, 0\n"
            "global_load_lds_dwordx4 v63, s[88:89]\n"
            "ds_read_b64_tr_b16 v[0:1], v70 offset:0\n"
            "s_lshl_b32 s78, s74, 10\n"
            "s_add_u32 m0, s78, 102400\n"
            "s_add_u32 s88, s42, 3276800\n"
            "s_addc_u32 s89, s43, 0\n"
            "global_load_lds_dwordx4 v63, s[88:89]\n"
            "ds_read_b64_tr_b16 v[2:3], v70 offset:4096\n"
            "s_lshl_b32 s78, s74, 10\n"
            "s_add_u32 m0, s78, 106496\n"
            "s_add_u32 s88, s42, 3407872\n"
            "s_addc_u32 s89, s43, 0\n"
            "global_load_lds_dwordx4 v63, s[88:89]\n"
            "ds_read_b64_tr_b16 v[4:5], v70 offset:8192\n"
            "s_lshl_b32 s78, s74, 10\n"
            "s_add_u32 m0, s78, 110592\n"
            "s_add_u32 s88, s42, 3538944\n"
            "s_addc_u32 s89, s43, 0\n"
            "global_load_lds_dwordx4 v63, s[88:89]\n"
            "ds_read_b64_tr_b16 v[6:7], v70 offset:12288\n"
            "s_lshl_b32 s78, s74, 10\n"
            "s_add_u32 m0, s78, 114688\n"
            "s_add_u32 s88, s42, 3670016\n"
            "s_addc_u32 s89, s43, 0\n"
            "global_load_lds_dwordx4 v63, s[88:89]\n"
            "ds_read_b64_tr_b16 v[8:9], v70 offset:16384\n"
            "s_lshl_b32 s78, s74, 10\n"
            "s_add_u32 m0, s78, 118784\n"
            "s_add_u32 s88, s42, 3801088\n"
            "s_addc_u32 s89, s43, 0\n"
            "global_load_lds_dwordx4 v63, s[88:89]\n"
            "ds_read_b64_tr_b16 v[10:11], v70 offset:20480\n"
            "s_lshl_b32 s78, s74, 10\n"
            "s_add_u32 m0, s78, 122880\n"
            "s_add_u32 s88, s42, 3932160\n"
            "s_addc_u32 s89, s43, 0\n"
            "global_load_lds_dwordx4 v63, s[88:89]\n"
            "ds_read_b64_tr_b16 v[12:13], v70 offset:24576\n"
            "s_lshl_b32 s78, s74, 10\n"
            "s_add_u32 m0, s78, 126976\n"
            "s_add_u32 s88, s42, 4063232\n"
            "s_addc_u32 s89, s43, 0\n"
            "global_load_lds_dwordx4 v63, s[88:89]\n"
            "ds_read_b64_tr_b16 v[14:15], v70 offset:28672\n"
            "ds_read_b64_tr_b16 v[16:17], v71 offset:0\n"
            "ds_read_b64_tr_b16 v[18:19], v71 offset:4096\n"
            "ds_read_b64_tr_b16 v[20:21], v71 offset:8192\n"
            "ds_read_b64_tr_b16 v[22:23], v71 offset:12288\n"
            "ds_read_b64_tr_b16 v[24:25], v71 offset:16384\n"
            "ds_read_b64_tr_b16 v[26:27], v71 offset:20480\n"
            "ds_read_b64_tr_b16 v[28:29], v71 offset:24576\n"
            "ds_read_b64_tr_b16 v[30:31], v71 offset:28672\n"
            "s_waitcnt lgkmcnt(0)\n"
            "s_waitcnt vmcnt(16)\n"
            "s_barrier\n"
            "s_lshl_b32 s78, s74, 10\n"
            "s_add_u32 m0, s78, 0\n"
            "s_add_u32 s88, s44, 0\n"
            "s_addc_u32 s89, s45, 0\n"
            "global_load_lds_dwordx4 v62, s[88:89]\n"
            "v_add_u32 v74, 32768, v67\n"
            "v_xor_b32 v75, 32, v74\n"
            "v_xor_b32 v76, 64, v74\n"
            "v_xor_b32 v77, 96, v74\n"
            "v_xor_b32 v78, 128, v74\n"
            "v_xor_b32 v79, 160, v74\n"
            "v_xor_b32 v80, 192, v74\n"
            "v_xor_b32 v81, 224, v74\n"
            "ds_read_b64 v[32:33], v74 offset:0\n"
            "ds_read_b64 v[34:35], v75 offset:0\n"
            "ds_read_b64 v[36:37], v76 offset:0\n"
            "ds_read_b64 v[38:39], v77 offset:0\n"
            "ds_read_b64 v[40:41], v78 offset:0\n"
            "ds_read_b64 v[42:43], v79 offset:0\n"
            "ds_read_b64 v[44:45], v80 offset:0\n"
            "ds_read_b64 v[46:47], v81 offset:0\n"
            "ds_read_b64 v[84:85], v74 offset:512\n"
            "ds_read_b64 v[86:87], v75 offset:512\n"
            "ds_read_b64 v[88:89], v76 offset:512\n"
            "s_lshl_b32 s78, s74, 10\n"
            "s_add_u32 m0, s78, 4096\n"
            "s_add_u32 s88, s44, 131072\n"
            "s_addc_u32 s89, s45, 0\n"
            "global_load_lds_dwordx4 v68, s[88:89]\n"
            "ds_read_b64 v[90:91], v77 offset:512\n"
            "ds_read_b64 v[92:93], v78 offset:512\n"
            "ds_read_b64 v[94:95], v79 offset:512\n"
            "ds_read_b64 v[96:97], v80 offset:512\n"
            "ds_read_b64 v[98:99], v81 offset:512\n"
            "s_waitcnt lgkmcnt(8)\n"
            "v_mfma_f32_16x16x32_bf16 v[48:51], v[32:35], v[0:3], 0\n"
            "v_mfma_f32_16x16x32_bf16 v[52:55], v[32:35], v[16:19], 0\n"
            "v_mfma_f32_16x16x32_bf16 v[48:51], v[36:39], v[4:7], v[48:51]\n"
            "v_mfma_f32_16x16x32_bf16 v[52:55], v[36:39], v[20:23], v[52:55]\n"
            "v_mfma_f32_16x16x32_bf16 v[48:51], v[40:43], v[8:11], v[48:51]\n"
            "v_mfma_f32_16x16x32_bf16 v[52:55], v[40:43], v[24:27], v[52:55]\n"
            "v_mfma_f32_16x16x32_bf16 v[48:51], v[44:47], v[12:15], v[48:51]\n"
            "v_mfma_f32_16x16x32_bf16 v[52:55], v[44:47], v[28:31], v[52:55]\n"
            "ds_read_b64 v[32:33], v74 offset:8192\n"
            "ds_read_b64 v[34:35], v75 offset:8192\n"
            "ds_read_b64 v[36:37], v76 offset:8192\n"
            "ds_read_b64 v[38:39], v77 offset:8192\n"
            "ds_read_b64 v[40:41], v78 offset:8192\n"
            "s_lshl_b32 s78, s74, 10\n"
            "s_add_u32 m0, s78, 8192\n"
            "s_add_u32 s88, s44, 262144\n"
            "s_addc_u32 s89, s45, 0\n"
            "global_load_lds_dwordx4 v62, s[88:89]\n"
            "ds_read_b64 v[42:43], v79 offset:8192\n"
            "ds_read_b64 v[44:45], v80 offset:8192\n"
            "ds_read_b64 v[46:47], v81 offset:8192\n"
            "s_waitcnt lgkmcnt(8)\n"
            "v_mfma_f32_16x16x32_bf16 v[100:103], v[84:87], v[0:3], 0\n"
            "v_mfma_f32_16x16x32_bf16 v[104:107], v[84:87], v[16:19], 0\n"
            "v_mfma_f32_16x16x32_bf16 v[100:103], v[88:91], v[4:7], v[100:103]\n"
            "v_mfma_f32_16x16x32_bf16 v[104:107], v[88:91], v[20:23], v[104:107]\n"
            "v_mfma_f32_16x16x32_bf16 v[100:103], v[92:95], v[8:11], v[100:103]\n"
            "v_mfma_f32_16x16x32_bf16 v[104:107], v[92:95], v[24:27], v[104:107]\n"
            "v_mfma_f32_16x16x32_bf16 v[100:103], v[96:99], v[12:15], v[100:103]\n"
            "v_mfma_f32_16x16x32_bf16 v[104:107], v[96:99], v[28:31], v[104:107]\n"
            "v_cvt_pk_bf16_f32 v176, v48, v49\n"
            "v_cvt_pk_bf16_f32 v177, v50, v51\n"
            "v_cvt_pk_bf16_f32 v240, v52, v53\n"
            "v_cvt_pk_bf16_f32 v241, v54, v55\n"
            "ds_read_b64 v[84:85], v74 offset:8704\n"
            "ds_read_b64 v[86:87], v75 offset:8704\n"
            "ds_read_b64 v[88:89], v76 offset:8704\n"
            "s_lshl_b32 s78, s74, 10\n"
            "s_add_u32 m0, s78, 12288\n"
            "s_add_u32 s88, s44, 393216\n"
            "s_addc_u32 s89, s45, 0\n"
            "global_load_lds_dwordx4 v68, s[88:89]\n"
            "ds_read_b64 v[90:91], v77 offset:8704\n"
            "ds_read_b64 v[92:93], v78 offset:8704\n"
            "ds_read_b64 v[94:95], v79 offset:8704\n"
            "ds_read_b64 v[96:97], v80 offset:8704\n"
            "ds_read_b64 v[98:99], v81 offset:8704\n"
            "s_waitcnt lgkmcnt(8)\n"
            "v_mfma_f32_16x16x32_bf16 v[48:51], v[32:35], v[0:3], 0\n"
            "v_mfma_f32_16x16x32_bf16 v[52:55], v[32:35], v[16:19], 0\n"
            "v_mfma_f32_16x16x32_bf16 v[48:51], v[36:39], v[4:7], v[48:51]\n"
            "v_mfma_f32_16x16x32_bf16 v[52:55], v[36:39], v[20:23], v[52:55]\n"
            "v_mfma_f32_16x16x32_bf16 v[48:51], v[40:43], v[8:11], v[48:51]\n"
            "v_mfma_f32_16x16x32_bf16 v[52:55], v[40:43], v[24:27], v[52:55]\n"
            "v_mfma_f32_16x16x32_bf16 v[48:51], v[44:47], v[12:15], v[48:51]\n"
            "v_mfma_f32_16x16x32_bf16 v[52:55], v[44:47], v[28:31], v[52:55]\n"
            "v_cvt_pk_bf16_f32 v178, v100, v101\n"
            "v_cvt_pk_bf16_f32 v179, v102, v103\n"
            "v_cvt_pk_bf16_f32 v242, v104, v105\n"
            "v_cvt_pk_bf16_f32 v243, v106, v107\n"
            "ds_read_b64 v[32:33], v74 offset:16384\n"
            "s_lshl_b32 s78, s74, 10\n"
            "s_add_u32 m0, s78, 16384\n"
            "s_add_u32 s88, s44, 524288\n"
            "s_addc_u32 s89, s45, 0\n"
            "global_load_lds_dwordx4 v62, s[88:89]\n"
            "ds_read_b64 v[34:35], v75 offset:16384\n"
            "ds_read_b64 v[36:37], v76 offset:16384\n"
            "ds_read_b64 v[38:39], v77 offset:16384\n"
            "ds_read_b64 v[40:41], v78 offset:16384\n"
            "ds_read_b64 v[42:43], v79 offset:16384\n"
            "ds_read_b64 v[44:45], v80 offset:16384\n"
            "ds_read_b64 v[46:47], v81 offset:16384\n"
            "s_waitcnt lgkmcnt(8)\n"
            "v_mfma_f32_16x16x32_bf16 v[100:103], v[84:87], v[0:3], 0\n"
            "v_mfma_f32_16x16x32_bf16 v[104:107], v[84:87], v[16:19], 0\n"
            "v_mfma_f32_16x16x32_bf16 v[100:103], v[88:91], v[4:7], v[100:103]\n"
            "v_mfma_f32_16x16x32_bf16 v[104:107], v[88:91], v[20:23], v[104:107]\n"
            "v_mfma_f32_16x16x32_bf16 v[100:103], v[92:95], v[8:11], v[100:103]\n"
            "v_mfma_f32_16x16x32_bf16 v[104:107], v[92:95], v[24:27], v[104:107]\n"
            "v_mfma_f32_16x16x32_bf16 v[100:103], v[96:99], v[12:15], v[100:103]\n"
            "v_mfma_f32_16x16x32_bf16 v[104:107], v[96:99], v[28:31], v[104:107]\n"
            "v_cvt_pk_bf16_f32 v180, v48, v49\n"
            "v_cvt_pk_bf16_f32 v181, v50, v51\n"
            "v_cvt_pk_bf16_f32 v244, v52, v53\n"
            "s_lshl_b32 s78, s74, 10\n"
            "s_add_u32 m0, s78, 20480\n"
            "s_add_u32 s88, s44, 655360\n"
            "s_addc_u32 s89, s45, 0\n"
            "global_load_lds_dwordx4 v68, s[88:89]\n"
            "v_cvt_pk_bf16_f32 v245, v54, v55\n"
            "ds_read_b64 v[84:85], v74 offset:16896\n"
            "ds_read_b64 v[86:87], v75 offset:16896\n"
            "ds_read_b64 v[88:89], v76 offset:16896\n"
            "ds_read_b64 v[90:91], v77 offset:16896\n"
            "ds_read_b64 v[92:93], v78 offset:16896\n"
            "ds_read_b64 v[94:95], v79 offset:16896\n"
            "ds_read_b64 v[96:97], v80 offset:16896\n"
            "ds_read_b64 v[98:99], v81 offset:16896\n"
            "s_waitcnt lgkmcnt(8)\n"
            "v_mfma_f32_16x16x32_bf16 v[48:51], v[32:35], v[0:3], 0\n"
            "v_mfma_f32_16x16x32_bf16 v[52:55], v[32:35], v[16:19], 0\n"
            "v_mfma_f32_16x16x32_bf16 v[48:51], v[36:39], v[4:7], v[48:51]\n"
            "v_mfma_f32_16x16x32_bf16 v[52:55], v[36:39], v[20:23], v[52:55]\n"
            "v_mfma_f32_16x16x32_bf16 v[48:51], v[40:43], v[8:11], v[48:51]\n"
            "v_mfma_f32_16x16x32_bf16 v[52:55], v[40:43], v[24:27], v[52:55]\n"
            "v_mfma_f32_16x16x32_bf16 v[48:51], v[44:47], v[12:15], v[48:51]\n"
            "v_mfma_f32_16x16x32_bf16 v[52:55], v[44:47], v[28:31], v[52:55]\n"
            "v_cvt_pk_bf16_f32 v182, v100, v101\n"
            "s_lshl_b32 s78, s74, 10\n"
            "s_add_u32 m0, s78, 24576\n"
            "s_add_u32 s88, s44, 786432\n"
            "s_addc_u32 s89, s45, 0\n"
            "global_load_lds_dwordx4 v62, s[88:89]\n"
            "v_cvt_pk_bf16_f32 v183, v102, v103\n"
            "v_cvt_pk_bf16_f32 v246, v104, v105\n"
            "v_cvt_pk_bf16_f32 v247, v106, v107\n"
            "ds_read_b64 v[32:33], v74 offset:24576\n"
            "ds_read_b64 v[34:35], v75 offset:24576\n"
            "ds_read_b64 v[36:37], v76 offset:24576\n"
            "ds_read_b64 v[38:39], v77 offset:24576\n"
            "ds_read_b64 v[40:41], v78 offset:24576\n"
            "ds_read_b64 v[42:43], v79 offset:24576\n"
            "ds_read_b64 v[44:45], v80 offset:24576\n"
            "ds_read_b64 v[46:47], v81 offset:24576\n"
            "s_waitcnt lgkmcnt(8)\n"
            "v_mfma_f32_16x16x32_bf16 v[100:103], v[84:87], v[0:3], 0\n"
            "v_mfma_f32_16x16x32_bf16 v[104:107], v[84:87], v[16:19], 0\n"
            "v_mfma_f32_16x16x32_bf16 v[100:103], v[88:91], v[4:7], v[100:103]\n"
            "v_mfma_f32_16x16x32_bf16 v[104:107], v[88:91], v[20:23], v[104:107]\n"
            "v_mfma_f32_16x16x32_bf16 v[100:103], v[92:95], v[8:11], v[100:103]\n"
            "v_mfma_f32_16x16x32_bf16 v[104:107], v[92:95], v[24:27], v[104:107]\n"
            "v_mfma_f32_16x16x32_bf16 v[100:103], v[96:99], v[12:15], v[100:103]\n"
            "s_lshl_b32 s78, s74, 10\n"
            "s_add_u32 m0, s78, 28672\n"
            "s_add_u32 s88, s44, 917504\n"
            "s_addc_u32 s89, s45, 0\n"
            "global_load_lds_dwordx4 v68, s[88:89]\n"
            "v_mfma_f32_16x16x32_bf16 v[104:107], v[96:99], v[28:31], v[104:107]\n"
            "v_cvt_pk_bf16_f32 v184, v48, v49\n"
            "v_cvt_pk_bf16_f32 v185, v50, v51\n"
            "v_cvt_pk_bf16_f32 v248, v52, v53\n"
            "v_cvt_pk_bf16_f32 v249, v54, v55\n"
            "ds_read_b64 v[84:85], v74 offset:25088\n"
            "ds_read_b64 v[86:87], v75 offset:25088\n"
            "ds_read_b64 v[88:89], v76 offset:25088\n"
            "ds_read_b64 v[90:91], v77 offset:25088\n"
            "ds_read_b64 v[92:93], v78 offset:25088\n"
            "ds_read_b64 v[94:95], v79 offset:25088\n"
            "ds_read_b64 v[96:97], v80 offset:25088\n"
            "ds_read_b64 v[98:99], v81 offset:25088\n"
            "s_waitcnt lgkmcnt(8)\n"
            "v_mfma_f32_16x16x32_bf16 v[48:51], v[32:35], v[0:3], 0\n"
            "v_mfma_f32_16x16x32_bf16 v[52:55], v[32:35], v[16:19], 0\n"
            "v_mfma_f32_16x16x32_bf16 v[48:51], v[36:39], v[4:7], v[48:51]\n"
            "v_mfma_f32_16x16x32_bf16 v[52:55], v[36:39], v[20:23], v[52:55]\n"
            "v_mfma_f32_16x16x32_bf16 v[48:51], v[40:43], v[8:11], v[48:51]\n"
            "v_mfma_f32_16x16x32_bf16 v[52:55], v[40:43], v[24:27], v[52:55]\n"
            "v_mfma_f32_16x16x32_bf16 v[48:51], v[44:47], v[12:15], v[48:51]\n"
            "v_mfma_f32_16x16x32_bf16 v[52:55], v[44:47], v[28:31], v[52:55]\n"
            "v_cvt_pk_bf16_f32 v186, v100, v101\n"
            "v_cvt_pk_bf16_f32 v187, v102, v103\n"
            "v_cvt_pk_bf16_f32 v250, v104, v105\n"
            "v_cvt_pk_bf16_f32 v251, v106, v107\n"
            "s_waitcnt lgkmcnt(0)\n"
            "v_mfma_f32_16x16x32_bf16 v[100:103], v[84:87], v[0:3], 0\n"
            "v_mfma_f32_16x16x32_bf16 v[104:107], v[84:87], v[16:19], 0\n"
            "v_mfma_f32_16x16x32_bf16 v[100:103], v[88:91], v[4:7], v[100:103]\n"
            "v_mfma_f32_16x16x32_bf16 v[104:107], v[88:91], v[20:23], v[104:107]\n"
            "v_mfma_f32_16x16x32_bf16 v[100:103], v[92:95], v[8:11], v[100:103]\n"
            "v_mfma_f32_16x16x32_bf16 v[104:107], v[92:95], v[24:27], v[104:107]\n"
            "v_mfma_f32_16x16x32_bf16 v[100:103], v[96:99], v[12:15], v[100:103]\n"
            "v_mfma_f32_16x16x32_bf16 v[104:107], v[96:99], v[28:31], v[104:107]\n"
            "v_cvt_pk_bf16_f32 v188, v48, v49\n"
            "v_cvt_pk_bf16_f32 v189, v50, v51\n"
            "v_cvt_pk_bf16_f32 v252, v52, v53\n"
            "v_cvt_pk_bf16_f32 v253, v54, v55\n"
            "s_nop 2\n"
            "v_cvt_pk_bf16_f32 v190, v100, v101\n"
            "v_cvt_pk_bf16_f32 v191, v102, v103\n"
            "v_cvt_pk_bf16_f32 v254, v104, v105\n"
            "v_cvt_pk_bf16_f32 v255, v106, v107\n"
            "s_waitcnt vmcnt(16)\n"
            "s_barrier\n"
            "s_lshl_b32 s78, s74, 10\n"
            "s_add_u32 m0, s78, 32768\n"
            "s_add_u32 s88, s40, 0\n"
            "s_addc_u32 s89, s41, 0\n"
            "global_load_lds_dwordx4 v63, s[88:89]\n"
            "v_add_u32 v72, 65536, v66\n"
            "v_xor_b32 v73, 32, v72\n"
            "s_lshl_b32 s78, s74, 10\n"
            "s_add_u32 m0, s78, 36864\n"
            "s_add_u32 s88, s40, 131072\n"
            "s_addc_u32 s89, s41, 0\n"
            "global_load_lds_dwordx4 v63, s[88:89]\n"
            "ds_read_b64_tr_b16 a[48:49], v72 offset:0\n"
            "ds_read_b64_tr_b16 a[50:51], v72 offset:512\n"
            "s_lshl_b32 s78, s74, 10\n"
            "s_add_u32 m0, s78, 40960\n"
            "s_add_u32 s88, s40, 262144\n"
            "s_addc_u32 s89, s41, 0\n"
            "global_load_lds_dwordx4 v63, s[88:89]\n"
            "ds_read_b64_tr_b16 a[52:53], v72 offset:8192\n"
            "ds_read_b64_tr_b16 a[54:55], v72 offset:8704\n"
            "s_lshl_b32 s78, s74, 10\n"
            "s_add_u32 m0, s78, 45056\n"
            "s_add_u32 s88, s40, 393216\n"
            "s_addc_u32 s89, s41, 0\n"
            "global_load_lds_dwordx4 v63, s[88:89]\n"
            "ds_read_b64_tr_b16 a[112:113], v73 offset:0\n"
            "ds_read_b64_tr_b16 a[114:115], v73 offset:512\n"
            "s_lshl_b32 s78, s74, 10\n"
            "s_add_u32 m0, s78, 49152\n"
            "s_add_u32 s88, s40, 524288\n"
            "s_addc_u32 s89, s41, 0\n"
            "global_load_lds_dwordx4 v63, s[88:89]\n"
            "ds_read_b64_tr_b16 a[116:117], v73 offset:8192\n"
            "ds_read_b64_tr_b16 a[118:119], v73 offset:8704\n"
            "s_lshl_b32 s78, s74, 10\n"
            "s_add_u32 m0, s78, 53248\n"
            "s_add_u32 s88, s40, 655360\n"
            "s_addc_u32 s89, s41, 0\n"
            "global_load_lds_dwordx4 v63, s[88:89]\n"
            "s_waitcnt lgkmcnt(0)\n"
            "ds_read_b64_tr_b16 a[56:57], v72 offset:16384\n"
            "s_lshl_b32 s78, s74, 10\n"
            "s_add_u32 m0, s78, 57344\n"
            "s_add_u32 s88, s40, 786432\n"
            "s_addc_u32 s89, s41, 0\n"
            "global_load_lds_dwordx4 v63, s[88:89]\n"
            "ds_read_b64_tr_b16 a[58:59], v72 offset:16896\n"
            "ds_read_b64_tr_b16 a[60:61], v72 offset:24576\n"
            "s_lshl_b32 s78, s74, 10\n"
            "s_add_u32 m0, s78, 61440\n"
            "s_add_u32 s88, s40, 917504\n"
            "s_addc_u32 s89, s41, 0\n"
            "global_load_lds_dwordx4 v63, s[88:89]\n"
            "ds_read_b64_tr_b16 a[62:63], v72 offset:25088\n"
            "ds_read_b64_tr_b16 a[120:121], v73 offset:16384\n"
            "ds_read_b64_tr_b16 a[122:123], v73 offset:16896\n"
            "ds_read_b64_tr_b16 a[124:125], v73 offset:24576\n"
            "ds_read_b64_tr_b16 a[126:127], v73 offset:25088\n"
            "s_waitcnt lgkmcnt(0)\n"
            "s_waitcnt vmcnt(16)\n"
            "s_barrier\n"
            "s_lshl_b32 s78, s74, 10\n"
            "s_add_u32 m0, s78, 65536\n"
            "s_add_u32 s88, s42, 0\n"
            "s_addc_u32 s89, s43, 0\n"
            "global_load_lds_dwordx4 v63, s[88:89]\n"
            "v_add_u32 v72, 98304, v66\n"
            "v_xor_b32 v73, 32, v72\n"
            "s_lshl_b32 s78, s74, 10\n"
            "s_add_u32 m0, s78, 69632\n"
            "s_add_u32 s88, s42, 131072\n"
            "s_addc_u32 s89, s43, 0\n"
            "global_load_lds_dwordx4 v63, s[88:89]\n"
            "ds_read_b64_tr_b16 a[176:177], v72 offset:0\n"
            "ds_read_b64_tr_b16 a[178:179], v72 offset:512\n"
            "s_lshl_b32 s78, s74, 10\n"
            "s_add_u32 m0, s78, 73728\n"
            "s_add_u32 s88, s42, 262144\n"
            "s_addc_u32 s89, s43, 0\n"
            "global_load_lds_dwordx4 v63, s[88:89]\n"
            "ds_read_b64_tr_b16 a[180:181], v72 offset:8192\n"
            "ds_read_b64_tr_b16 a[182:183], v72 offset:8704\n"
            "s_lshl_b32 s78, s74, 10\n"
            "s_add_u32 m0, s78, 77824\n"
            "s_add_u32 s88, s42, 393216\n"
            "s_addc_u32 s89, s43, 0\n"
            "global_load_lds_dwordx4 v63, s[88:89]\n"
            "ds_read_b64_tr_b16 a[240:241], v73 offset:0\n"
            "ds_read_b64_tr_b16 a[242:243], v73 offset:512\n"
            "s_lshl_b32 s78, s74, 10\n"
            "s_add_u32 m0, s78, 81920\n"
            "s_add_u32 s88, s42, 524288\n"
            "s_addc_u32 s89, s43, 0\n"
            "global_load_lds_dwordx4 v63, s[88:89]\n"
            "ds_read_b64_tr_b16 a[244:245], v73 offset:8192\n"
            "ds_read_b64_tr_b16 a[246:247], v73 offset:8704\n"
            "s_lshl_b32 s78, s74, 10\n"
            "s_add_u32 m0, s78, 86016\n"
            "s_add_u32 s88, s42, 655360\n"
            "s_addc_u32 s89, s43, 0\n"
            "global_load_lds_dwordx4 v63, s[88:89]\n"
            "s_waitcnt lgkmcnt(0)\n"
            "ds_read_b64_tr_b16 a[184:185], v72 offset:16384\n"
            "s_lshl_b32 s78, s74, 10\n"
            "s_add_u32 m0, s78, 90112\n"
            "s_add_u32 s88, s42, 786432\n"
            "s_addc_u32 s89, s43, 0\n"
            "global_load_lds_dwordx4 v63, s[88:89]\n"
            "ds_read_b64_tr_b16 a[186:187], v72 offset:16896\n"
            "ds_read_b64_tr_b16 a[188:189], v72 offset:24576\n"
            "s_lshl_b32 s78, s74, 10\n"
            "s_add_u32 m0, s78, 94208\n"
            "s_add_u32 s88, s42, 917504\n"
            "s_addc_u32 s89, s43, 0\n"
            "global_load_lds_dwordx4 v63, s[88:89]\n"
            "ds_read_b64_tr_b16 a[190:191], v72 offset:25088\n"
            "ds_read_b64_tr_b16 a[248:249], v73 offset:16384\n"
            "ds_read_b64_tr_b16 a[250:251], v73 offset:16896\n"
            "ds_read_b64_tr_b16 a[252:253], v73 offset:24576\n"
            "ds_read_b64_tr_b16 a[254:255], v73 offset:25088\n"
            "s_waitcnt lgkmcnt(0)\n"
            "s_waitcnt vmcnt(16)\n"
            "s_barrier\n"
            "s_lshl_b32 s78, s74, 10\n"
            "s_add_u32 m0, s78, 98304\n"
            "s_add_u32 s88, s44, 1048576\n"
            "s_addc_u32 s89, s45, 0\n"
            "global_load_lds_dwordx4 v62, s[88:89]\n"
            "v_add_u32 v74, 0, v67\n"
            "v_xor_b32 v75, 32, v74\n"
            "v_xor_b32 v76, 64, v74\n"
            "v_xor_b32 v77, 96, v74\n"
            "v_xor_b32 v78, 128, v74\n"
            "v_xor_b32 v79, 160, v74\n"
            "v_xor_b32 v80, 192, v74\n"
            "v_xor_b32 v81, 224, v74\n"
            "ds_read_b64 v[32:33], v74 offset:0\n"
            "ds_read_b64 v[34:35], v75 offset:0\n"
            "ds_read_b64 v[36:37], v76 offset:0\n"
            "ds_read_b64 v[38:39], v77 offset:0\n"
            "ds_read_b64 v[40:41], v78 offset:0\n"
            "ds_read_b64 v[42:43], v79 offset:0\n"
            "ds_read_b64 v[44:45], v80 offset:0\n"
            "ds_read_b64 v[46:47], v81 offset:0\n"
            "ds_read_b64 v[84:85], v74 offset:512\n"
            "ds_read_b64 v[86:87], v75 offset:512\n"
            "ds_read_b64 v[88:89], v76 offset:512\n"
            "s_lshl_b32 s78, s74, 10\n"
            "s_add_u32 m0, s78, 102400\n"
            "s_add_u32 s88, s44, 1179648\n"
            "s_addc_u32 s89, s45, 0\n"
            "global_load_lds_dwordx4 v68, s[88:89]\n"
            "ds_read_b64 v[90:91], v77 offset:512\n"
            "ds_read_b64 v[92:93], v78 offset:512\n"
            "ds_read_b64 v[94:95], v79 offset:512\n"
            "ds_read_b64 v[96:97], v80 offset:512\n"
            "ds_read_b64 v[98:99], v81 offset:512\n"
            "s_waitcnt lgkmcnt(8)\n"
            "v_mfma_f32_16x16x32_bf16 v[48:51], v[32:35], v[0:3], 0\n"
            "v_mfma_f32_16x16x32_bf16 v[52:55], v[32:35], v[16:19], 0\n"
            "v_mfma_f32_16x16x32_bf16 v[48:51], v[36:39], v[4:7], v[48:51]\n"
            "v_mfma_f32_16x16x32_bf16 v[52:55], v[36:39], v[20:23], v[52:55]\n"
            "v_mfma_f32_16x16x32_bf16 v[48:51], v[40:43], v[8:11], v[48:51]\n"
            "v_mfma_f32_16x16x32_bf16 v[52:55], v[40:43], v[24:27], v[52:55]\n"
            "v_mfma_f32_16x16x32_bf16 v[48:51], v[44:47], v[12:15], v[48:51]\n"
            "v_mfma_f32_16x16x32_bf16 v[52:55], v[44:47], v[28:31], v[52:55]\n"
            "ds_read_b64 v[32:33], v74 offset:8192\n"
            "ds_read_b64 v[34:35], v75 offset:8192\n"
            "ds_read_b64 v[36:37], v76 offset:8192\n"
            "ds_read_b64 v[38:39], v77 offset:8192\n"
            "ds_read_b64 v[40:41], v78 offset:8192\n"
            "s_lshl_b32 s78, s74, 10\n"
            "s_add_u32 m0, s78, 106496\n"
            "s_add_u32 s88, s44, 1310720\n"
            "s_addc_u32 s89, s45, 0\n"
            "global_load_lds_dwordx4 v62, s[88:89]\n"
            "ds_read_b64 v[42:43], v79 offset:8192\n"
            "ds_read_b64 v[44:45], v80 offset:8192\n"
            "ds_read_b64 v[46:47], v81 offset:8192\n"
            "s_waitcnt lgkmcnt(8)\n"
            "v_mfma_f32_16x16x32_bf16 v[100:103], v[84:87], v[0:3], 0\n"
            "v_mfma_f32_16x16x32_bf16 v[104:107], v[84:87], v[16:19], 0\n"
            "v_mfma_f32_16x16x32_bf16 v[100:103], v[88:91], v[4:7], v[100:103]\n"
            "v_mfma_f32_16x16x32_bf16 v[104:107], v[88:91], v[20:23], v[104:107]\n"
            "v_mfma_f32_16x16x32_bf16 v[100:103], v[92:95], v[8:11], v[100:103]\n"
            "v_mfma_f32_16x16x32_bf16 v[104:107], v[92:95], v[24:27], v[104:107]\n"
            "v_mfma_f32_16x16x32_bf16 v[100:103], v[96:99], v[12:15], v[100:103]\n"
            "v_mfma_f32_16x16x32_bf16 v[104:107], v[96:99], v[28:31], v[104:107]\n"
            "v_cvt_pk_bf16_f32 v128, v48, v49\n"
            "v_cvt_pk_bf16_f32 v129, v50, v51\n"
            "v_cvt_pk_bf16_f32 v192, v52, v53\n"
            "v_cvt_pk_bf16_f32 v193, v54, v55\n"
            "ds_read_b64 v[84:85], v74 offset:8704\n"
            "ds_read_b64 v[86:87], v75 offset:8704\n"
            "ds_read_b64 v[88:89], v76 offset:8704\n"
            "s_lshl_b32 s78, s74, 10\n"
            "s_add_u32 m0, s78, 110592\n"
            "s_add_u32 s88, s44, 1441792\n"
            "s_addc_u32 s89, s45, 0\n"
            "global_load_lds_dwordx4 v68, s[88:89]\n"
            "ds_read_b64 v[90:91], v77 offset:8704\n"
            "ds_read_b64 v[92:93], v78 offset:8704\n"
            "ds_read_b64 v[94:95], v79 offset:8704\n"
            "ds_read_b64 v[96:97], v80 offset:8704\n"
            "ds_read_b64 v[98:99], v81 offset:8704\n"
            "s_waitcnt lgkmcnt(8)\n"
            "v_mfma_f32_16x16x32_bf16 v[48:51], v[32:35], v[0:3], 0\n"
            "v_mfma_f32_16x16x32_bf16 v[52:55], v[32:35], v[16:19], 0\n"
            "v_mfma_f32_16x16x32_bf16 v[48:51], v[36:39], v[4:7], v[48:51]\n"
            "v_mfma_f32_16x16x32_bf16 v[52:55], v[36:39], v[20:23], v[52:55]\n"
            "v_mfma_f32_16x16x32_bf16 v[48:51], v[40:43], v[8:11], v[48:51]\n"
            "v_mfma_f32_16x16x32_bf16 v[52:55], v[40:43], v[24:27], v[52:55]\n"
            "v_mfma_f32_16x16x32_bf16 v[48:51], v[44:47], v[12:15], v[48:51]\n"
            "v_mfma_f32_16x16x32_bf16 v[52:55], v[44:47], v[28:31], v[52:55]\n"
            "v_cvt_pk_bf16_f32 v130, v100, v101\n"
            "v_cvt_pk_bf16_f32 v131, v102, v103\n"
            "v_cvt_pk_bf16_f32 v194, v104, v105\n"
            "v_cvt_pk_bf16_f32 v195, v106, v107\n"
            "ds_read_b64 v[32:33], v74 offset:16384\n"
            "s_lshl_b32 s78, s74, 10\n"
            "s_add_u32 m0, s78, 114688\n"
            "s_add_u32 s88, s44, 1572864\n"
            "s_addc_u32 s89, s45, 0\n"
            "global_load_lds_dwordx4 v62, s[88:89]\n"
            "ds_read_b64 v[34:35], v75 offset:16384\n"
            "ds_read_b64 v[36:37], v76 offset:16384\n"
            "ds_read_b64 v[38:39], v77 offset:16384\n"
            "ds_read_b64 v[40:41], v78 offset:16384\n"
            "ds_read_b64 v[42:43], v79 offset:16384\n"
            "ds_read_b64 v[44:45], v80 offset:16384\n"
            "ds_read_b64 v[46:47], v81 offset:16384\n"
            "s_waitcnt lgkmcnt(8)\n"
            "v_mfma_f32_16x16x32_bf16 v[100:103], v[84:87], v[0:3], 0\n"
            "v_mfma_f32_16x16x32_bf16 v[104:107], v[84:87], v[16:19], 0\n"
            "v_mfma_f32_16x16x32_bf16 v[100:103], v[88:91], v[4:7], v[100:103]\n"
            "v_mfma_f32_16x16x32_bf16 v[104:107], v[88:91], v[20:23], v[104:107]\n"
            "v_mfma_f32_16x16x32_bf16 v[100:103], v[92:95], v[8:11], v[100:103]\n"
            "v_mfma_f32_16x16x32_bf16 v[104:107], v[92:95], v[24:27], v[104:107]\n"
            "v_mfma_f32_16x16x32_bf16 v[100:103], v[96:99], v[12:15], v[100:103]\n"
            "v_mfma_f32_16x16x32_bf16 v[104:107], v[96:99], v[28:31], v[104:107]\n"
            "v_cvt_pk_bf16_f32 v132, v48, v49\n"
            "v_cvt_pk_bf16_f32 v133, v50, v51\n"
            "v_cvt_pk_bf16_f32 v196, v52, v53\n"
            "s_lshl_b32 s78, s74, 10\n"
            "s_add_u32 m0, s78, 118784\n"
            "s_add_u32 s88, s44, 1703936\n"
            "s_addc_u32 s89, s45, 0\n"
            "global_load_lds_dwordx4 v68, s[88:89]\n"
            "v_cvt_pk_bf16_f32 v197, v54, v55\n"
            "ds_read_b64 v[84:85], v74 offset:16896\n"
            "ds_read_b64 v[86:87], v75 offset:16896\n"
            "ds_read_b64 v[88:89], v76 offset:16896\n"
            "ds_read_b64 v[90:91], v77 offset:16896\n"
            "ds_read_b64 v[92:93], v78 offset:16896\n"
            "ds_read_b64 v[94:95], v79 offset:16896\n"
            "ds_read_b64 v[96:97], v80 offset:16896\n"
            "ds_read_b64 v[98:99], v81 offset:16896\n"
            "s_waitcnt lgkmcnt(8)\n"
            "v_mfma_f32_16x16x32_bf16 v[48:51], v[32:35], v[0:3], 0\n"
            "v_mfma_f32_16x16x32_bf16 v[52:55], v[32:35], v[16:19], 0\n"
            "v_mfma_f32_16x16x32_bf16 v[48:51], v[36:39], v[4:7], v[48:51]\n"
            "v_mfma_f32_16x16x32_bf16 v[52:55], v[36:39], v[20:23], v[52:55]\n"
            "v_mfma_f32_16x16x32_bf16 v[48:51], v[40:43], v[8:11], v[48:51]\n"
            "v_mfma_f32_16x16x32_bf16 v[52:55], v[40:43], v[24:27], v[52:55]\n"
            "v_mfma_f32_16x16x32_bf16 v[48:51], v[44:47], v[12:15], v[48:51]\n"
            "v_mfma_f32_16x16x32_bf16 v[52:55], v[44:47], v[28:31], v[52:55]\n"
            "v_cvt_pk_bf16_f32 v134, v100, v101\n"
            "s_lshl_b32 s78, s74, 10\n"
            "s_add_u32 m0, s78, 122880\n"
            "s_add_u32 s88, s44, 1835008\n"
            "s_addc_u32 s89, s45, 0\n"
            "global_load_lds_dwordx4 v62, s[88:89]\n"
            "v_cvt_pk_bf16_f32 v135, v102, v103\n"
            "v_cvt_pk_bf16_f32 v198, v104, v105\n"
            "v_cvt_pk_bf16_f32 v199, v106, v107\n"
            "ds_read_b64 v[32:33], v74 offset:24576\n"
            "ds_read_b64 v[34:35], v75 offset:24576\n"
            "ds_read_b64 v[36:37], v76 offset:24576\n"
            "ds_read_b64 v[38:39], v77 offset:24576\n"
            "ds_read_b64 v[40:41], v78 offset:24576\n"
            "ds_read_b64 v[42:43], v79 offset:24576\n"
            "ds_read_b64 v[44:45], v80 offset:24576\n"
            "ds_read_b64 v[46:47], v81 offset:24576\n"
            "s_waitcnt lgkmcnt(8)\n"
            "v_mfma_f32_16x16x32_bf16 v[100:103], v[84:87], v[0:3], 0\n"
            "v_mfma_f32_16x16x32_bf16 v[104:107], v[84:87], v[16:19], 0\n"
            "v_mfma_f32_16x16x32_bf16 v[100:103], v[88:91], v[4:7], v[100:103]\n"
            "v_mfma_f32_16x16x32_bf16 v[104:107], v[88:91], v[20:23], v[104:107]\n"
            "v_mfma_f32_16x16x32_bf16 v[100:103], v[92:95], v[8:11], v[100:103]\n"
            "v_mfma_f32_16x16x32_bf16 v[104:107], v[92:95], v[24:27], v[104:107]\n"
            "v_mfma_f32_16x16x32_bf16 v[100:103], v[96:99], v[12:15], v[100:103]\n"
            "s_lshl_b32 s78, s74, 10\n"
            "s_add_u32 m0, s78, 126976\n"
            "s_add_u32 s88, s44, 1966080\n"
            "s_addc_u32 s89, s45, 0\n"
            "global_load_lds_dwordx4 v68, s[88:89]\n"
            "v_mfma_f32_16x16x32_bf16 v[104:107], v[96:99], v[28:31], v[104:107]\n"
            "v_cvt_pk_bf16_f32 v136, v48, v49\n"
            "v_cvt_pk_bf16_f32 v137, v50, v51\n"
            "v_cvt_pk_bf16_f32 v200, v52, v53\n"
            "v_cvt_pk_bf16_f32 v201, v54, v55\n"
            "ds_read_b64 v[84:85], v74 offset:25088\n"
            "ds_read_b64 v[86:87], v75 offset:25088\n"
            "ds_read_b64 v[88:89], v76 offset:25088\n"
            "ds_read_b64 v[90:91], v77 offset:25088\n"
            "ds_read_b64 v[92:93], v78 offset:25088\n"
            "ds_read_b64 v[94:95], v79 offset:25088\n"
            "ds_read_b64 v[96:97], v80 offset:25088\n"
            "ds_read_b64 v[98:99], v81 offset:25088\n"
            "s_waitcnt lgkmcnt(8)\n"
            "v_mfma_f32_16x16x32_bf16 v[48:51], v[32:35], v[0:3], 0\n"
            "v_mfma_f32_16x16x32_bf16 v[52:55], v[32:35], v[16:19], 0\n"
            "v_mfma_f32_16x16x32_bf16 v[48:51], v[36:39], v[4:7], v[48:51]\n"
            "v_mfma_f32_16x16x32_bf16 v[52:55], v[36:39], v[20:23], v[52:55]\n"
            "v_mfma_f32_16x16x32_bf16 v[48:51], v[40:43], v[8:11], v[48:51]\n"
            "v_mfma_f32_16x16x32_bf16 v[52:55], v[40:43], v[24:27], v[52:55]\n"
            "v_mfma_f32_16x16x32_bf16 v[48:51], v[44:47], v[12:15], v[48:51]\n"
            "v_mfma_f32_16x16x32_bf16 v[52:55], v[44:47], v[28:31], v[52:55]\n"
            "v_cvt_pk_bf16_f32 v138, v100, v101\n"
            "v_cvt_pk_bf16_f32 v139, v102, v103\n"
            "v_cvt_pk_bf16_f32 v202, v104, v105\n"
            "v_cvt_pk_bf16_f32 v203, v106, v107\n"
            "s_waitcnt lgkmcnt(0)\n"
            "v_mfma_f32_16x16x32_bf16 v[100:103], v[84:87], v[0:3], 0\n"
            "v_mfma_f32_16x16x32_bf16 v[104:107], v[84:87], v[16:19], 0\n"
            "v_mfma_f32_16x16x32_bf16 v[100:103], v[88:91], v[4:7], v[100:103]\n"
            "v_mfma_f32_16x16x32_bf16 v[104:107], v[88:91], v[20:23], v[104:107]\n"
            "v_mfma_f32_16x16x32_bf16 v[100:103], v[92:95], v[8:11], v[100:103]\n"
            "v_mfma_f32_16x16x32_bf16 v[104:107], v[92:95], v[24:27], v[104:107]\n"
            "v_mfma_f32_16x16x32_bf16 v[100:103], v[96:99], v[12:15], v[100:103]\n"
            "v_mfma_f32_16x16x32_bf16 v[104:107], v[96:99], v[28:31], v[104:107]\n"
            "v_cvt_pk_bf16_f32 v140, v48, v49\n"
            "v_cvt_pk_bf16_f32 v141, v50, v51\n"
            "v_cvt_pk_bf16_f32 v204, v52, v53\n"
            "v_cvt_pk_bf16_f32 v205, v54, v55\n"
            "s_nop 2\n"
            "v_cvt_pk_bf16_f32 v142, v100, v101\n"
            "v_cvt_pk_bf16_f32 v143, v102, v103\n"
            "v_cvt_pk_bf16_f32 v206, v104, v105\n"
            "v_cvt_pk_bf16_f32 v207, v106, v107\n"
            "s_waitcnt vmcnt(16)\n"
            "s_barrier\n"
            "s_lshl_b32 s78, s74, 10\n"
            "s_add_u32 m0, s78, 0\n"
            "s_add_u32 s88, s40, 1048576\n"
            "s_addc_u32 s89, s41, 0\n"
            "global_load_lds_dwordx4 v63, s[88:89]\n"
            "v_add_u32 v72, 32768, v66\n"
            "v_xor_b32 v73, 32, v72\n"
            "s_lshl_b32 s78, s74, 10\n"
            "s_add_u32 m0, s78, 4096\n"
            "s_add_u32 s88, s40, 1179648\n"
            "s_addc_u32 s89, s41, 0\n"
            "global_load_lds_dwordx4 v63, s[88:89]\n"
            "ds_read_b64_tr_b16 a[0:1], v72 offset:0\n"
            "ds_read_b64_tr_b16 a[2:3], v72 offset:512\n"
            "s_lshl_b32 s78, s74, 10\n"
            "s_add_u32 m0, s78, 8192\n"
            "s_add_u32 s88, s40, 1310720\n"
            "s_addc_u32 s89, s41, 0\n"
            "global_load_lds_dwordx4 v63, s[88:89]\n"
            "ds_read_b64_tr_b16 a[4:5], v72 offset:8192\n"
            "ds_read_b64_tr_b16 a[6:7], v72 offset:8704\n"
            "s_lshl_b32 s78, s74, 10\n"
            "s_add_u32 m0, s78, 12288\n"
            "s_add_u32 s88, s40, 1441792\n"
            "s_addc_u32 s89, s41, 0\n"
            "global_load_lds_dwordx4 v63, s[88:89]\n"
            "ds_read_b64_tr_b16 a[64:65], v73 offset:0\n"
            "ds_read_b64_tr_b16 a[66:67], v73 offset:512\n"
            "s_lshl_b32 s78, s74, 10\n"
            "s_add_u32 m0, s78, 16384\n"
            "s_add_u32 s88, s40, 1572864\n"
            "s_addc_u32 s89, s41, 0\n"
            "global_load_lds_dwordx4 v63, s[88:89]\n"
            "ds_read_b64_tr_b16 a[68:69], v73 offset:8192\n"
            "ds_read_b64_tr_b16 a[70:71], v73 offset:8704\n"
            "s_lshl_b32 s78, s74, 10\n"
            "s_add_u32 m0, s78, 20480\n"
            "s_add_u32 s88, s40, 1703936\n"
            "s_addc_u32 s89, s41, 0\n"
            "global_load_lds_dwordx4 v63, s[88:89]\n"
            "s_waitcnt lgkmcnt(0)\n"
            "ds_read_b64_tr_b16 a[8:9], v72 offset:16384\n"
            "s_lshl_b32 s78, s74, 10\n"
            "s_add_u32 m0, s78, 24576\n"
            "s_add_u32 s88, s40, 1835008\n"
            "s_addc_u32 s89, s41, 0\n"
            "global_load_lds_dwordx4 v63, s[88:89]\n"
            "ds_read_b64_tr_b16 a[10:11], v72 offset:16896\n"
            "ds_read_b64_tr_b16 a[12:13], v72 offset:24576\n"
            "s_lshl_b32 s78, s74, 10\n"
            "s_add_u32 m0, s78, 28672\n"
            "s_add_u32 s88, s40, 1966080\n"
            "s_addc_u32 s89, s41, 0\n"
            "global_load_lds_dwordx4 v63, s[88:89]\n"
            "ds_read_b64_tr_b16 a[14:15], v72 offset:25088\n"
            "ds_read_b64_tr_b16 a[72:73], v73 offset:16384\n"
            "ds_read_b64_tr_b16 a[74:75], v73 offset:16896\n"
            "ds_read_b64_tr_b16 a[76:77], v73 offset:24576\n"
            "ds_read_b64_tr_b16 a[78:79], v73 offset:25088\n"
            "s_waitcnt lgkmcnt(0)\n"
            "s_waitcnt vmcnt(16)\n"
            "s_barrier\n"
            "s_lshl_b32 s78, s74, 10\n"
            "s_add_u32 m0, s78, 32768\n"
            "s_add_u32 s88, s42, 1048576\n"
            "s_addc_u32 s89, s43, 0\n"
            "global_load_lds_dwordx4 v63, s[88:89]\n"
            "v_add_u32 v72, 65536, v66\n"
            "v_xor_b32 v73, 32, v72\n"
            "s_lshl_b32 s78, s74, 10\n"
            "s_add_u32 m0, s78, 36864\n"
            "s_add_u32 s88, s42, 1179648\n"
            "s_addc_u32 s89, s43, 0\n"
            "global_load_lds_dwordx4 v63, s[88:89]\n"
            "ds_read_b64_tr_b16 a[128:129], v72 offset:0\n"
            "ds_read_b64_tr_b16 a[130:131], v72 offset:512\n"
            "s_lshl_b32 s78, s74, 10\n"
            "s_add_u32 m0, s78, 40960\n"
            "s_add_u32 s88, s42, 1310720\n"
            "s_addc_u32 s89, s43, 0\n"
            "global_load_lds_dwordx4 v63, s[88:89]\n"
            "ds_read_b64_tr_b16 a[132:133], v72 offset:8192\n"
            "ds_read_b64_tr_b16 a[134:135], v72 offset:8704\n"
            "s_lshl_b32 s78, s74, 10\n"
            "s_add_u32 m0, s78, 45056\n"
            "s_add_u32 s88, s42, 1441792\n"
            "s_addc_u32 s89, s43, 0\n"
            "global_load_lds_dwordx4 v63, s[88:89]\n"
            "ds_read_b64_tr_b16 a[192:193], v73 offset:0\n"
            "ds_read_b64_tr_b16 a[194:195], v73 offset:512\n"
            "s_lshl_b32 s78, s74, 10\n"
            "s_add_u32 m0, s78, 49152\n"
            "s_add_u32 s88, s42, 1572864\n"
            "s_addc_u32 s89, s43, 0\n"
            "global_load_lds_dwordx4 v63, s[88:89]\n"
            "ds_read_b64_tr_b16 a[196:197], v73 offset:8192\n"
            "ds_read_b64_tr_b16 a[198:199], v73 offset:8704\n"
            "s_lshl_b32 s78, s74, 10\n"
            "s_add_u32 m0, s78, 53248\n"
            "s_add_u32 s88, s42, 1703936\n"
            "s_addc_u32 s89, s43, 0\n"
            "global_load_lds_dwordx4 v63, s[88:89]\n"
            "s_waitcnt lgkmcnt(0)\n"
            "ds_read_b64_tr_b16 a[136:137], v72 offset:16384\n"
            "s_lshl_b32 s78, s74, 10\n"
            "s_add_u32 m0, s78, 57344\n"
            "s_add_u32 s88, s42, 1835008\n"
            "s_addc_u32 s89, s43, 0\n"
            "global_load_lds_dwordx4 v63, s[88:89]\n"
            "ds_read_b64_tr_b16 a[138:139], v72 offset:16896\n"
            "ds_read_b64_tr_b16 a[140:141], v72 offset:24576\n"
            "s_lshl_b32 s78, s74, 10\n"
            "s_add_u32 m0, s78, 61440\n"
            "s_add_u32 s88, s42, 1966080\n"
            "s_addc_u32 s89, s43, 0\n"
            "global_load_lds_dwordx4 v63, s[88:89]\n"
            "ds_read_b64_tr_b16 a[142:143], v72 offset:25088\n"
            "ds_read_b64_tr_b16 a[200:201], v73 offset:16384\n"
            "ds_read_b64_tr_b16 a[202:203], v73 offset:16896\n"
            "ds_read_b64_tr_b16 a[204:205], v73 offset:24576\n"
            "ds_read_b64_tr_b16 a[206:207], v73 offset:25088\n"
            "s_waitcnt lgkmcnt(0)\n"
            "s_waitcnt vmcnt(16)\n"
            "s_barrier\n"
            "s_lshl_b32 s78, s74, 10\n"
            "s_add_u32 m0, s78, 65536\n"
            "s_add_u32 s88, s44, 2097152\n"
            "s_addc_u32 s89, s45, 0\n"
            "global_load_lds_dwordx4 v62, s[88:89]\n"
            "v_add_u32 v74, 98304, v67\n"
            "v_xor_b32 v75, 32, v74\n"
            "v_xor_b32 v76, 64, v74\n"
            "v_xor_b32 v77, 96, v74\n"
            "v_xor_b32 v78, 128, v74\n"
            "v_xor_b32 v79, 160, v74\n"
            "v_xor_b32 v80, 192, v74\n"
            "v_xor_b32 v81, 224, v74\n"
            "ds_read_b64 v[32:33], v74 offset:0\n"
            "ds_read_b64 v[34:35], v75 offset:0\n"
            "ds_read_b64 v[36:37], v76 offset:0\n"
            "ds_read_b64 v[38:39], v77 offset:0\n"
            "ds_read_b64 v[40:41], v78 offset:0\n"
            "ds_read_b64 v[42:43], v79 offset:0\n"
            "ds_read_b64 v[44:45], v80 offset:0\n"
            "ds_read_b64 v[46:47], v81 offset:0\n"
            "ds_read_b64 v[84:85], v74 offset:512\n"
            "ds_read_b64 v[86:87], v75 offset:512\n"
            "ds_read_b64 v[88:89], v76 offset:512\n"
            "s_lshl_b32 s78, s74, 10\n"
            "s_add_u32 m0, s78, 69632\n"
            "s_add_u32 s88, s44, 2228224\n"
            "s_addc_u32 s89, s45, 0\n"
            "global_load_lds_dwordx4 v68, s[88:89]\n"
            "ds_read_b64 v[90:91], v77 offset:512\n"
            "ds_read_b64 v[92:93], v78 offset:512\n"
            "ds_read_b64 v[94:95], v79 offset:512\n"
            "ds_read_b64 v[96:97], v80 offset:512\n"
            "ds_read_b64 v[98:99], v81 offset:512\n"
            "s_waitcnt lgkmcnt(8)\n"
            "v_mfma_f32_16x16x32_bf16 v[48:51], v[32:35], v[0:3], 0\n"
            "v_mfma_f32_16x16x32_bf16 v[52:55], v[32:35], v[16:19], 0\n"
            "v_mfma_f32_16x16x32_bf16 v[48:51], v[36:39], v[4:7], v[48:51]\n"
            "v_mfma_f32_16x16x32_bf16 v[52:55], v[36:39], v[20:23], v[52:55]\n"
            "v_mfma_f32_16x16x32_bf16 v[48:51], v[40:43], v[8:11], v[48:51]\n"
            "v_mfma_f32_16x16x32_bf16 v[52:55], v[40:43], v[24:27], v[52:55]\n"
            "v_mfma_f32_16x16x32_bf16 v[48:51], v[44:47], v[12:15], v[48:51]\n"
            "v_mfma_f32_16x16x32_bf16 v[52:55], v[44:47], v[28:31], v[52:55]\n"
            "ds_read_b64 v[32:33], v74 offset:8192\n"
            "ds_read_b64 v[34:35], v75 offset:8192\n"
            "ds_read_b64 v[36:37], v76 offset:8192\n"
            "ds_read_b64 v[38:39], v77 offset:8192\n"
            "ds_read_b64 v[40:41], v78 offset:8192\n"
            "s_lshl_b32 s78, s74, 10\n"
            "s_add_u32 m0, s78, 73728\n"
            "s_add_u32 s88, s44, 2359296\n"
            "s_addc_u32 s89, s45, 0\n"
            "global_load_lds_dwordx4 v62, s[88:89]\n"
            "ds_read_b64 v[42:43], v79 offset:8192\n"
            "ds_read_b64 v[44:45], v80 offset:8192\n"
            "ds_read_b64 v[46:47], v81 offset:8192\n"
            "s_waitcnt lgkmcnt(8)\n"
            "v_mfma_f32_16x16x32_bf16 v[100:103], v[84:87], v[0:3], 0\n"
            "v_mfma_f32_16x16x32_bf16 v[104:107], v[84:87], v[16:19], 0\n"
            "v_mfma_f32_16x16x32_bf16 v[100:103], v[88:91], v[4:7], v[100:103]\n"
            "v_mfma_f32_16x16x32_bf16 v[104:107], v[88:91], v[20:23], v[104:107]\n"
            "v_mfma_f32_16x16x32_bf16 v[100:103], v[92:95], v[8:11], v[100:103]\n"
            "v_mfma_f32_16x16x32_bf16 v[104:107], v[92:95], v[24:27], v[104:107]\n"
            "v_mfma_f32_16x16x32_bf16 v[100:103], v[96:99], v[12:15], v[100:103]\n"
            "v_mfma_f32_16x16x32_bf16 v[104:107], v[96:99], v[28:31], v[104:107]\n"
            "v_cvt_pk_bf16_f32 v144, v48, v49\n"
            "v_cvt_pk_bf16_f32 v145, v50, v51\n"
            "v_cvt_pk_bf16_f32 v208, v52, v53\n"
            "v_cvt_pk_bf16_f32 v209, v54, v55\n"
            "ds_read_b64 v[84:85], v74 offset:8704\n"
            "ds_read_b64 v[86:87], v75 offset:8704\n"
            "ds_read_b64 v[88:89], v76 offset:8704\n"
            "s_lshl_b32 s78, s74, 10\n"
            "s_add_u32 m0, s78, 77824\n"
            "s_add_u32 s88, s44, 2490368\n"
            "s_addc_u32 s89, s45, 0\n"
            "global_load_lds_dwordx4 v68, s[88:89]\n"
            "ds_read_b64 v[90:91], v77 offset:8704\n"
            "ds_read_b64 v[92:93], v78 offset:8704\n"
            "ds_read_b64 v[94:95], v79 offset:8704\n"
            "ds_read_b64 v[96:97], v80 offset:8704\n"
            "ds_read_b64 v[98:99], v81 offset:8704\n"
            "s_waitcnt lgkmcnt(8)\n"
            "v_mfma_f32_16x16x32_bf16 v[48:51], v[32:35], v[0:3], 0\n"
            "v_mfma_f32_16x16x32_bf16 v[52:55], v[32:35], v[16:19], 0\n"
            "v_mfma_f32_16x16x32_bf16 v[48:51], v[36:39], v[4:7], v[48:51]\n"
            "v_mfma_f32_16x16x32_bf16 v[52:55], v[36:39], v[20:23], v[52:55]\n"
            "v_mfma_f32_16x16x32_bf16 v[48:51], v[40:43], v[8:11], v[48:51]\n"
            "v_mfma_f32_16x16x32_bf16 v[52:55], v[40:43], v[24:27], v[52:55]\n"
            "v_mfma_f32_16x16x32_bf16 v[48:51], v[44:47], v[12:15], v[48:51]\n"
            "v_mfma_f32_16x16x32_bf16 v[52:55], v[44:47], v[28:31], v[52:55]\n"
            "v_cvt_pk_bf16_f32 v146, v100, v101\n"
            "v_cvt_pk_bf16_f32 v147, v102, v103\n"
            "v_cvt_pk_bf16_f32 v210, v104, v105\n"
            "v_cvt_pk_bf16_f32 v211, v106, v107\n"
            "ds_read_b64 v[32:33], v74 offset:16384\n"
            "s_lshl_b32 s78, s74, 10\n"
            "s_add_u32 m0, s78, 81920\n"
            "s_add_u32 s88, s44, 2621440\n"
            "s_addc_u32 s89, s45, 0\n"
            "global_load_lds_dwordx4 v62, s[88:89]\n"
            "ds_read_b64 v[34:35], v75 offset:16384\n"
            "ds_read_b64 v[36:37], v76 offset:16384\n"
            "ds_read_b64 v[38:39], v77 offset:16384\n"
            "ds_read_b64 v[40:41], v78 offset:16384\n"
            "ds_read_b64 v[42:43], v79 offset:16384\n"
            "ds_read_b64 v[44:45], v80 offset:16384\n"
            "ds_read_b64 v[46:47], v81 offset:16384\n"
            "s_waitcnt lgkmcnt(8)\n"
            "v_mfma_f32_16x16x32_bf16 v[100:103], v[84:87], v[0:3], 0\n"
            "v_mfma_f32_16x16x32_bf16 v[104:107], v[84:87], v[16:19], 0\n"
            "v_mfma_f32_16x16x32_bf16 v[100:103], v[88:91], v[4:7], v[100:103]\n"
            "v_mfma_f32_16x16x32_bf16 v[104:107], v[88:91], v[20:23], v[104:107]\n"
            "v_mfma_f32_16x16x32_bf16 v[100:103], v[92:95], v[8:11], v[100:103]\n"
            "v_mfma_f32_16x16x32_bf16 v[104:107], v[92:95], v[24:27], v[104:107]\n"
            "v_mfma_f32_16x16x32_bf16 v[100:103], v[96:99], v[12:15], v[100:103]\n"
            "v_mfma_f32_16x16x32_bf16 v[104:107], v[96:99], v[28:31], v[104:107]\n"
            "v_cvt_pk_bf16_f32 v148, v48, v49\n"
            "v_cvt_pk_bf16_f32 v149, v50, v51\n"
            "v_cvt_pk_bf16_f32 v212, v52, v53\n"
            "s_lshl_b32 s78, s74, 10\n"
            "s_add_u32 m0, s78, 86016\n"
            "s_add_u32 s88, s44, 2752512\n"
            "s_addc_u32 s89, s45, 0\n"
            "global_load_lds_dwordx4 v68, s[88:89]\n"
            "v_cvt_pk_bf16_f32 v213, v54, v55\n"
            "ds_read_b64 v[84:85], v74 offset:16896\n"
            "ds_read_b64 v[86:87], v75 offset:16896\n"
            "ds_read_b64 v[88:89], v76 offset:16896\n"
            "ds_read_b64 v[90:91], v77 offset:16896\n"
            "ds_read_b64 v[92:93], v78 offset:16896\n"
            "ds_read_b64 v[94:95], v79 offset:16896\n"
            "ds_read_b64 v[96:97], v80 offset:16896\n"
            "ds_read_b64 v[98:99], v81 offset:16896\n"
            "s_waitcnt lgkmcnt(8)\n"
            "v_mfma_f32_16x16x32_bf16 v[48:51], v[32:35], v[0:3], 0\n"
            "v_mfma_f32_16x16x32_bf16 v[52:55], v[32:35], v[16:19], 0\n"
            "v_mfma_f32_16x16x32_bf16 v[48:51], v[36:39], v[4:7], v[48:51]\n"
            "v_mfma_f32_16x16x32_bf16 v[52:55], v[36:39], v[20:23], v[52:55]\n"
            "v_mfma_f32_16x16x32_bf16 v[48:51], v[40:43], v[8:11], v[48:51]\n"
            "v_mfma_f32_16x16x32_bf16 v[52:55], v[40:43], v[24:27], v[52:55]\n"
            "v_mfma_f32_16x16x32_bf16 v[48:51], v[44:47], v[12:15], v[48:51]\n"
            "v_mfma_f32_16x16x32_bf16 v[52:55], v[44:47], v[28:31], v[52:55]\n"
            "v_cvt_pk_bf16_f32 v150, v100, v101\n"
            "s_lshl_b32 s78, s74, 10\n"
            "s_add_u32 m0, s78, 90112\n"
            "s_add_u32 s88, s44, 2883584\n"
            "s_addc_u32 s89, s45, 0\n"
            "global_load_lds_dwordx4 v62, s[88:89]\n"
            "v_cvt_pk_bf16_f32 v151, v102, v103\n"
            "v_cvt_pk_bf16_f32 v214, v104, v105\n"
            "v_cvt_pk_bf16_f32 v215, v106, v107\n"
            "ds_read_b64 v[32:33], v74 offset:24576\n"
            "ds_read_b64 v[34:35], v75 offset:24576\n"
            "ds_read_b64 v[36:37], v76 offset:24576\n"
            "ds_read_b64 v[38:39], v77 offset:24576\n"
            "ds_read_b64 v[40:41], v78 offset:24576\n"
            "ds_read_b64 v[42:43], v79 offset:24576\n"
            "ds_read_b64 v[44:45], v80 offset:24576\n"
            "ds_read_b64 v[46:47], v81 offset:24576\n"
            "s_waitcnt lgkmcnt(8)\n"
            "v_mfma_f32_16x16x32_bf16 v[100:103], v[84:87], v[0:3], 0\n"
            "v_mfma_f32_16x16x32_bf16 v[104:107], v[84:87], v[16:19], 0\n"
            "v_mfma_f32_16x16x32_bf16 v[100:103], v[88:91], v[4:7], v[100:103]\n"
            "v_mfma_f32_16x16x32_bf16 v[104:107], v[88:91], v[20:23], v[104:107]\n"
            "v_mfma_f32_16x16x32_bf16 v[100:103], v[92:95], v[8:11], v[100:103]\n"
            "v_mfma_f32_16x16x32_bf16 v[104:107], v[92:95], v[24:27], v[104:107]\n"
            "v_mfma_f32_16x16x32_bf16 v[100:103], v[96:99], v[12:15], v[100:103]\n"
            "s_lshl_b32 s78, s74, 10\n"
            "s_add_u32 m0, s78, 94208\n"
            "s_add_u32 s88, s44, 3014656\n"
            "s_addc_u32 s89, s45, 0\n"
            "global_load_lds_dwordx4 v68, s[88:89]\n"
            "v_mfma_f32_16x16x32_bf16 v[104:107], v[96:99], v[28:31], v[104:107]\n"
            "v_cvt_pk_bf16_f32 v152, v48, v49\n"
            "v_cvt_pk_bf16_f32 v153, v50, v51\n"
            "v_cvt_pk_bf16_f32 v216, v52, v53\n"
            "v_cvt_pk_bf16_f32 v217, v54, v55\n"
            "ds_read_b64 v[84:85], v74 offset:25088\n"
            "ds_read_b64 v[86:87], v75 offset:25088\n"
            "ds_read_b64 v[88:89], v76 offset:25088\n"
            "ds_read_b64 v[90:91], v77 offset:25088\n"
            "ds_read_b64 v[92:93], v78 offset:25088\n"
            "ds_read_b64 v[94:95], v79 offset:25088\n"
            "ds_read_b64 v[96:97], v80 offset:25088\n"
            "ds_read_b64 v[98:99], v81 offset:25088\n"
            "s_waitcnt lgkmcnt(8)\n"
            "v_mfma_f32_16x16x32_bf16 v[48:51], v[32:35], v[0:3], 0\n"
            "v_mfma_f32_16x16x32_bf16 v[52:55], v[32:35], v[16:19], 0\n"
            "v_mfma_f32_16x16x32_bf16 v[48:51], v[36:39], v[4:7], v[48:51]\n"
            "v_mfma_f32_16x16x32_bf16 v[52:55], v[36:39], v[20:23], v[52:55]\n"
            "v_mfma_f32_16x16x32_bf16 v[48:51], v[40:43], v[8:11], v[48:51]\n"
            "v_mfma_f32_16x16x32_bf16 v[52:55], v[40:43], v[24:27], v[52:55]\n"
            "v_mfma_f32_16x16x32_bf16 v[48:51], v[44:47], v[12:15], v[48:51]\n"
            "v_mfma_f32_16x16x32_bf16 v[52:55], v[44:47], v[28:31], v[52:55]\n"
            "v_cvt_pk_bf16_f32 v154, v100, v101\n"
            "v_cvt_pk_bf16_f32 v155, v102, v103\n"
            "v_cvt_pk_bf16_f32 v218, v104, v105\n"
            "v_cvt_pk_bf16_f32 v219, v106, v107\n"
            "s_waitcnt lgkmcnt(0)\n"
            "v_mfma_f32_16x16x32_bf16 v[100:103], v[84:87], v[0:3], 0\n"
            "v_mfma_f32_16x16x32_bf16 v[104:107], v[84:87], v[16:19], 0\n"
            "v_mfma_f32_16x16x32_bf16 v[100:103], v[88:91], v[4:7], v[100:103]\n"
            "v_mfma_f32_16x16x32_bf16 v[104:107], v[88:91], v[20:23], v[104:107]\n"
            "v_mfma_f32_16x16x32_bf16 v[100:103], v[92:95], v[8:11], v[100:103]\n"
            "v_mfma_f32_16x16x32_bf16 v[104:107], v[92:95], v[24:27], v[104:107]\n"
            "v_mfma_f32_16x16x32_bf16 v[100:103], v[96:99], v[12:15], v[100:103]\n"
            "v_mfma_f32_16x16x32_bf16 v[104:107], v[96:99], v[28:31], v[104:107]\n"
            "v_cvt_pk_bf16_f32 v156, v48, v49\n"
            "v_cvt_pk_bf16_f32 v157, v50, v51\n"
            "v_cvt_pk_bf16_f32 v220, v52, v53\n"
            "v_cvt_pk_bf16_f32 v221, v54, v55\n"
            "s_nop 2\n"
            "v_cvt_pk_bf16_f32 v158, v100, v101\n"
            "v_cvt_pk_bf16_f32 v159, v102, v103\n"
            "v_cvt_pk_bf16_f32 v222, v104, v105\n"
            "v_cvt_pk_bf16_f32 v223, v106, v107\n"
            "s_waitcnt vmcnt(16)\n"
            "s_barrier\n"
            "s_lshl_b32 s78, s74, 10\n"
            "s_add_u32 m0, s78, 98304\n"
            "s_add_u32 s88, s40, 2097152\n"
            "s_addc_u32 s89, s41, 0\n"
            "global_load_lds_dwordx4 v63, s[88:89]\n"
            "v_add_u32 v72, 0, v66\n"
            "v_xor_b32 v73, 32, v72\n"
            "s_lshl_b32 s78, s74, 10\n"
            "s_add_u32 m0, s78, 102400\n"
            "s_add_u32 s88, s40, 2228224\n"
            "s_addc_u32 s89, s41, 0\n"
            "global_load_lds_dwordx4 v63, s[88:89]\n"
            "ds_read_b64_tr_b16 a[16:17], v72 offset:0\n"
            "ds_read_b64_tr_b16 a[18:19], v72 offset:512\n"
            "s_lshl_b32 s78, s74, 10\n"
            "s_add_u32 m0, s78, 106496\n"
            "s_add_u32 s88, s40, 2359296\n"
            "s_addc_u32 s89, s41, 0\n"
            "global_load_lds_dwordx4 v63, s[88:89]\n"
            "ds_read_b64_tr_b16 a[20:21], v72 offset:8192\n"
            "ds_read_b64_tr_b16 a[22:23], v72 offset:8704\n"
            "s_lshl_b32 s78, s74, 10\n"
            "s_add_u32 m0, s78, 110592\n"
            "s_add_u32 s88, s40, 2490368\n"
            "s_addc_u32 s89, s41, 0\n"
            "global_load_lds_dwordx4 v63, s[88:89]\n"
            "ds_read_b64_tr_b16 a[80:81], v73 offset:0\n"
            "ds_read_b64_tr_b16 a[82:83], v73 offset:512\n"
            "s_lshl_b32 s78, s74, 10\n"
            "s_add_u32 m0, s78, 114688\n"
            "s_add_u32 s88, s40, 2621440\n"
            "s_addc_u32 s89, s41, 0\n"
            "global_load_lds_dwordx4 v63, s[88:89]\n"
            "ds_read_b64_tr_b16 a[84:85], v73 offset:8192\n"
            "ds_read_b64_tr_b16 a[86:87], v73 offset:8704\n"
            "s_lshl_b32 s78, s74, 10\n"
            "s_add_u32 m0, s78, 118784\n"
            "s_add_u32 s88, s40, 2752512\n"
            "s_addc_u32 s89, s41, 0\n"
            "global_load_lds_dwordx4 v63, s[88:89]\n"
            "s_waitcnt lgkmcnt(0)\n"
            "ds_read_b64_tr_b16 a[24:25], v72 offset:16384\n"
            "s_lshl_b32 s78, s74, 10\n"
            "s_add_u32 m0, s78, 122880\n"
            "s_add_u32 s88, s40, 2883584\n"
            "s_addc_u32 s89, s41, 0\n"
            "global_load_lds_dwordx4 v63, s[88:89]\n"
            "ds_read_b64_tr_b16 a[26:27], v72 offset:16896\n"
            "ds_read_b64_tr_b16 a[28:29], v72 offset:24576\n"
            "s_lshl_b32 s78, s74, 10\n"
            "s_add_u32 m0, s78, 126976\n"
            "s_add_u32 s88, s40, 3014656\n"
            "s_addc_u32 s89, s41, 0\n"
            "global_load_lds_dwordx4 v63, s[88:89]\n"
            "ds_read_b64_tr_b16 a[30:31], v72 offset:25088\n"
            "ds_read_b64_tr_b16 a[88:89], v73 offset:16384\n"
            "ds_read_b64_tr_b16 a[90:91], v73 offset:16896\n"
            "ds_read_b64_tr_b16 a[92:93], v73 offset:24576\n"
            "ds_read_b64_tr_b16 a[94:95], v73 offset:25088\n"
            "s_waitcnt lgkmcnt(0)\n"
            "s_waitcnt vmcnt(16)\n"
            "s_barrier\n"
            "s_lshl_b32 s78, s74, 10\n"
            "s_add_u32 m0, s78, 0\n"
            "s_add_u32 s88, s42, 2097152\n"
            "s_addc_u32 s89, s43, 0\n"
            "global_load_lds_dwordx4 v63, s[88:89]\n"
            "v_add_u32 v72, 32768, v66\n"
            "v_xor_b32 v73, 32, v72\n"
            "s_lshl_b32 s78, s74, 10\n"
            "s_add_u32 m0, s78, 4096\n"
            "s_add_u32 s88, s42, 2228224\n"
            "s_addc_u32 s89, s43, 0\n"
            "global_load_lds_dwordx4 v63, s[88:89]\n"
            "ds_read_b64_tr_b16 a[144:145], v72 offset:0\n"
            "ds_read_b64_tr_b16 a[146:147], v72 offset:512\n"
            "s_lshl_b32 s78, s74, 10\n"
            "s_add_u32 m0, s78, 8192\n"
            "s_add_u32 s88, s42, 2359296\n"
            "s_addc_u32 s89, s43, 0\n"
            "global_load_lds_dwordx4 v63, s[88:89]\n"
            "ds_read_b64_tr_b16 a[148:149], v72 offset:8192\n"
            "ds_read_b64_tr_b16 a[150:151], v72 offset:8704\n"
            "s_lshl_b32 s78, s74, 10\n"
            "s_add_u32 m0, s78, 12288\n"
            "s_add_u32 s88, s42, 2490368\n"
            "s_addc_u32 s89, s43, 0\n"
            "global_load_lds_dwordx4 v63, s[88:89]\n"
            "ds_read_b64_tr_b16 a[208:209], v73 offset:0\n"
            "ds_read_b64_tr_b16 a[210:211], v73 offset:512\n"
            "s_lshl_b32 s78, s74, 10\n"
            "s_add_u32 m0, s78, 16384\n"
            "s_add_u32 s88, s42, 2621440\n"
            "s_addc_u32 s89, s43, 0\n"
            "global_load_lds_dwordx4 v63, s[88:89]\n"
            "ds_read_b64_tr_b16 a[212:213], v73 offset:8192\n"
            "ds_read_b64_tr_b16 a[214:215], v73 offset:8704\n"
            "s_lshl_b32 s78, s74, 10\n"
            "s_add_u32 m0, s78, 20480\n"
            "s_add_u32 s88, s42, 2752512\n"
            "s_addc_u32 s89, s43, 0\n"
            "global_load_lds_dwordx4 v63, s[88:89]\n"
            "s_waitcnt lgkmcnt(0)\n"
            "ds_read_b64_tr_b16 a[152:153], v72 offset:16384\n"
            "s_lshl_b32 s78, s74, 10\n"
            "s_add_u32 m0, s78, 24576\n"
            "s_add_u32 s88, s42, 2883584\n"
            "s_addc_u32 s89, s43, 0\n"
            "global_load_lds_dwordx4 v63, s[88:89]\n"
            "ds_read_b64_tr_b16 a[154:155], v72 offset:16896\n"
            "ds_read_b64_tr_b16 a[156:157], v72 offset:24576\n"
            "s_lshl_b32 s78, s74, 10\n"
            "s_add_u32 m0, s78, 28672\n"
            "s_add_u32 s88, s42, 3014656\n"
            "s_addc_u32 s89, s43, 0\n"
            "global_load_lds_dwordx4 v63, s[88:89]\n"
            "ds_read_b64_tr_b16 a[158:159], v72 offset:25088\n"
            "ds_read_b64_tr_b16 a[216:217], v73 offset:16384\n"
            "ds_read_b64_tr_b16 a[218:219], v73 offset:16896\n"
            "ds_read_b64_tr_b16 a[220:221], v73 offset:24576\n"
            "ds_read_b64_tr_b16 a[222:223], v73 offset:25088\n"
            "s_waitcnt lgkmcnt(0)\n"
            "s_waitcnt vmcnt(16)\n"
            "s_barrier\n"
            "v_add_u32 v74, 65536, v67\n"
            "v_xor_b32 v75, 32, v74\n"
            "v_xor_b32 v76, 64, v74\n"
            "v_xor_b32 v77, 96, v74\n"
            "v_xor_b32 v78, 128, v74\n"
            "v_xor_b32 v79, 160, v74\n"
            "v_xor_b32 v80, 192, v74\n"
            "v_xor_b32 v81, 224, v74\n"
            "ds_read_b64 v[32:33], v74 offset:0\n"
            "ds_read_b64 v[34:35], v75 offset:0\n"
            "ds_read_b64 v[36:37], v76 offset:0\n"
            "ds_read_b64 v[38:39], v77 offset:0\n"
            "ds_read_b64 v[40:41], v78 offset:0\n"
            "ds_read_b64 v[42:43], v79 offset:0\n"
            "ds_read_b64 v[44:45], v80 offset:0\n"
            "ds_read_b64 v[46:47], v81 offset:0\n"
            "ds_read_b64 v[84:85], v74 offset:512\n"
            "ds_read_b64 v[86:87], v75 offset:512\n"
            "ds_read_b64 v[88:89], v76 offset:512\n"
            "ds_read_b64 v[90:91], v77 offset:512\n"
            "ds_read_b64 v[92:93], v78 offset:512\n"
            "ds_read_b64 v[94:95], v79 offset:512\n"
            "ds_read_b64 v[96:97], v80 offset:512\n"
            "ds_read_b64 v[98:99], v81 offset:512\n"
            "s_waitcnt lgkmcnt(8)\n"
            "v_mfma_f32_16x16x32_bf16 v[48:51], v[32:35], v[0:3], 0\n"
            "v_mfma_f32_16x16x32_bf16 v[52:55], v[32:35], v[16:19], 0\n"
            "v_mfma_f32_16x16x32_bf16 v[48:51], v[36:39], v[4:7], v[48:51]\n"
            "v_mfma_f32_16x16x32_bf16 v[52:55], v[36:39], v[20:23], v[52:55]\n"
            "v_mfma_f32_16x16x32_bf16 v[48:51], v[40:43], v[8:11], v[48:51]\n"
            "v_mfma_f32_16x16x32_bf16 v[52:55], v[40:43], v[24:27], v[52:55]\n"
            "v_mfma_f32_16x16x32_bf16 v[48:51], v[44:47], v[12:15], v[48:51]\n"
            "v_mfma_f32_16x16x32_bf16 v[52:55], v[44:47], v[28:31], v[52:55]\n"
            "ds_read_b64 v[32:33], v74 offset:8192\n"
            "ds_read_b64 v[34:35], v75 offset:8192\n"
            "ds_read_b64 v[36:37], v76 offset:8192\n"
            "ds_read_b64 v[38:39], v77 offset:8192\n"
            "ds_read_b64 v[40:41], v78 offset:8192\n"
            "ds_read_b64 v[42:43], v79 offset:8192\n"
            "ds_read_b64 v[44:45], v80 offset:8192\n"
            "ds_read_b64 v[46:47], v81 offset:8192\n"
            "s_waitcnt lgkmcnt(8)\n"
            "v_mfma_f32_16x16x32_bf16 v[100:103], v[84:87], v[0:3], 0\n"
            "v_mfma_f32_16x16x32_bf16 v[104:107], v[84:87], v[16:19], 0\n"
            "v_mfma_f32_16x16x32_bf16 v[100:103], v[88:91], v[4:7], v[100:103]\n"
            "v_mfma_f32_16x16x32_bf16 v[104:107], v[88:91], v[20:23], v[104:107]\n"
            "v_mfma_f32_16x16x32_bf16 v[100:103], v[92:95], v[8:11], v[100:103]\n"
            "v_mfma_f32_16x16x32_bf16 v[104:107], v[92:95], v[24:27], v[104:107]\n"
            "v_mfma_f32_16x16x32_bf16 v[100:103], v[96:99], v[12:15], v[100:103]\n"
            "v_mfma_f32_16x16x32_bf16 v[104:107], v[96:99], v[28:31], v[104:107]\n"
            "v_cvt_pk_bf16_f32 v160, v48, v49\n"
            "v_cvt_pk_bf16_f32 v161, v50, v51\n"
            "v_cvt_pk_bf16_f32 v224, v52, v53\n"
            "v_cvt_pk_bf16_f32 v225, v54, v55\n"
            "ds_read_b64 v[84:85], v74 offset:8704\n"
            "ds_read_b64 v[86:87], v75 offset:8704\n"
            "ds_read_b64 v[88:89], v76 offset:8704\n"
            "ds_read_b64 v[90:91], v77 offset:8704\n"
            "ds_read_b64 v[92:93], v78 offset:8704\n"
            "ds_read_b64 v[94:95], v79 offset:8704\n"
            "ds_read_b64 v[96:97], v80 offset:8704\n"
            "ds_read_b64 v[98:99], v81 offset:8704\n"
            "s_waitcnt lgkmcnt(8)\n"
            "v_mfma_f32_16x16x32_bf16 v[48:51], v[32:35], v[0:3], 0\n"
            "v_mfma_f32_16x16x32_bf16 v[52:55], v[32:35], v[16:19], 0\n"
            "v_mfma_f32_16x16x32_bf16 v[48:51], v[36:39], v[4:7], v[48:51]\n"
            "v_mfma_f32_16x16x32_bf16 v[52:55], v[36:39], v[20:23], v[52:55]\n"
            "v_mfma_f32_16x16x32_bf16 v[48:51], v[40:43], v[8:11], v[48:51]\n"
            "v_mfma_f32_16x16x32_bf16 v[52:55], v[40:43], v[24:27], v[52:55]\n"
            "v_mfma_f32_16x16x32_bf16 v[48:51], v[44:47], v[12:15], v[48:51]\n"
            "v_mfma_f32_16x16x32_bf16 v[52:55], v[44:47], v[28:31], v[52:55]\n"
            "v_cvt_pk_bf16_f32 v162, v100, v101\n"
            "v_cvt_pk_bf16_f32 v163, v102, v103\n"
            "v_cvt_pk_bf16_f32 v226, v104, v105\n"
            "v_cvt_pk_bf16_f32 v227, v106, v107\n"
            "ds_read_b64 v[32:33], v74 offset:16384\n"
            "ds_read_b64 v[34:35], v75 offset:16384\n"
            "ds_read_b64 v[36:37], v76 offset:16384\n"
            "ds_read_b64 v[38:39], v77 offset:16384\n"
            "ds_read_b64 v[40:41], v78 offset:16384\n"
            "ds_read_b64 v[42:43], v79 offset:16384\n"
            "ds_read_b64 v[44:45], v80 offset:16384\n"
            "ds_read_b64 v[46:47], v81 offset:16384\n"
            "s_waitcnt lgkmcnt(8)\n"
            "v_mfma_f32_16x16x32_bf16 v[100:103], v[84:87], v[0:3], 0\n"
            "v_mfma_f32_16x16x32_bf16 v[104:107], v[84:87], v[16:19], 0\n"
            "v_mfma_f32_16x16x32_bf16 v[100:103], v[88:91], v[4:7], v[100:103]\n"
            "v_mfma_f32_16x16x32_bf16 v[104:107], v[88:91], v[20:23], v[104:107]\n"
            "v_mfma_f32_16x16x32_bf16 v[100:103], v[92:95], v[8:11], v[100:103]\n"
            "v_mfma_f32_16x16x32_bf16 v[104:107], v[92:95], v[24:27], v[104:107]\n"
            "v_mfma_f32_16x16x32_bf16 v[100:103], v[96:99], v[12:15], v[100:103]\n"
            "v_mfma_f32_16x16x32_bf16 v[104:107], v[96:99], v[28:31], v[104:107]\n"
            "v_cvt_pk_bf16_f32 v164, v48, v49\n"
            "v_cvt_pk_bf16_f32 v165, v50, v51\n"
            "v_cvt_pk_bf16_f32 v228, v52, v53\n"
            "v_cvt_pk_bf16_f32 v229, v54, v55\n"
            "ds_read_b64 v[84:85], v74 offset:16896\n"
            "ds_read_b64 v[86:87], v75 offset:16896\n"
            "ds_read_b64 v[88:89], v76 offset:16896\n"
            "ds_read_b64 v[90:91], v77 offset:16896\n"
            "ds_read_b64 v[92:93], v78 offset:16896\n"
            "ds_read_b64 v[94:95], v79 offset:16896\n"
            "ds_read_b64 v[96:97], v80 offset:16896\n"
            "ds_read_b64 v[98:99], v81 offset:16896\n"
            "s_waitcnt lgkmcnt(8)\n"
            "v_mfma_f32_16x16x32_bf16 v[48:51], v[32:35], v[0:3], 0\n"
            "v_mfma_f32_16x16x32_bf16 v[52:55], v[32:35], v[16:19], 0\n"
            "v_mfma_f32_16x16x32_bf16 v[48:51], v[36:39], v[4:7], v[48:51]\n"
            "v_mfma_f32_16x16x32_bf16 v[52:55], v[36:39], v[20:23], v[52:55]\n"
            "v_mfma_f32_16x16x32_bf16 v[48:51], v[40:43], v[8:11], v[48:51]\n"
            "v_mfma_f32_16x16x32_bf16 v[52:55], v[40:43], v[24:27], v[52:55]\n"
            "v_mfma_f32_16x16x32_bf16 v[48:51], v[44:47], v[12:15], v[48:51]\n"
            "v_mfma_f32_16x16x32_bf16 v[52:55], v[44:47], v[28:31], v[52:55]\n"
            "v_cvt_pk_bf16_f32 v166, v100, v101\n"
            "v_cvt_pk_bf16_f32 v167, v102, v103\n"
            "v_cvt_pk_bf16_f32 v230, v104, v105\n"
            "v_cvt_pk_bf16_f32 v231, v106, v107\n"
            "ds_read_b64 v[32:33], v74 offset:24576\n"
            "ds_read_b64 v[34:35], v75 offset:24576\n"
            "ds_read_b64 v[36:37], v76 offset:24576\n"
            "ds_read_b64 v[38:39], v77 offset:24576\n"
            "ds_read_b64 v[40:41], v78 offset:24576\n"
            "ds_read_b64 v[42:43], v79 offset:24576\n"
            "ds_read_b64 v[44:45], v80 offset:24576\n"
            "ds_read_b64 v[46:47], v81 offset:24576\n"
            "s_waitcnt lgkmcnt(8)\n"
            "v_mfma_f32_16x16x32_bf16 v[100:103], v[84:87], v[0:3], 0\n"
            "v_mfma_f32_16x16x32_bf16 v[104:107], v[84:87], v[16:19], 0\n"
            "v_mfma_f32_16x16x32_bf16 v[100:103], v[88:91], v[4:7], v[100:103]\n"
            "v_mfma_f32_16x16x32_bf16 v[104:107], v[88:91], v[20:23], v[104:107]\n"
            "v_mfma_f32_16x16x32_bf16 v[100:103], v[92:95], v[8:11], v[100:103]\n"
            "v_mfma_f32_16x16x32_bf16 v[104:107], v[92:95], v[24:27], v[104:107]\n"
            "v_mfma_f32_16x16x32_bf16 v[100:103], v[96:99], v[12:15], v[100:103]\n"
            "v_mfma_f32_16x16x32_bf16 v[104:107], v[96:99], v[28:31], v[104:107]\n"
            "v_cvt_pk_bf16_f32 v168, v48, v49\n"
            "v_cvt_pk_bf16_f32 v169, v50, v51\n"
            "v_cvt_pk_bf16_f32 v232, v52, v53\n"
            "v_cvt_pk_bf16_f32 v233, v54, v55\n"
            "ds_read_b64 v[84:85], v74 offset:25088\n"
            "ds_read_b64 v[86:87], v75 offset:25088\n"
            "ds_read_b64 v[88:89], v76 offset:25088\n"
            "ds_read_b64 v[90:91], v77 offset:25088\n"
            "ds_read_b64 v[92:93], v78 offset:25088\n"
            "ds_read_b64 v[94:95], v79 offset:25088\n"
            "ds_read_b64 v[96:97], v80 offset:25088\n"
            "ds_read_b64 v[98:99], v81 offset:25088\n"
            "s_waitcnt lgkmcnt(8)\n"
            "v_mfma_f32_16x16x32_bf16 v[48:51], v[32:35], v[0:3], 0\n"
            "v_mfma_f32_16x16x32_bf16 v[52:55], v[32:35], v[16:19], 0\n"
            "v_mfma_f32_16x16x32_bf16 v[48:51], v[36:39], v[4:7], v[48:51]\n"
            "v_mfma_f32_16x16x32_bf16 v[52:55], v[36:39], v[20:23], v[52:55]\n"
            "v_mfma_f32_16x16x32_bf16 v[48:51], v[40:43], v[8:11], v[48:51]\n"
            "v_mfma_f32_16x16x32_bf16 v[52:55], v[40:43], v[24:27], v[52:55]\n"
            "v_mfma_f32_16x16x32_bf16 v[48:51], v[44:47], v[12:15], v[48:51]\n"
            "v_mfma_f32_16x16x32_bf16 v[52:55], v[44:47], v[28:31], v[52:55]\n"
            "v_cvt_pk_bf16_f32 v170, v100, v101\n"
            "v_cvt_pk_bf16_f32 v171, v102, v103\n"
            "v_cvt_pk_bf16_f32 v234, v104, v105\n"
            "v_cvt_pk_bf16_f32 v235, v106, v107\n"
            "s_waitcnt lgkmcnt(0)\n"
            "v_mfma_f32_16x16x32_bf16 v[100:103], v[84:87], v[0:3], 0\n"
            "v_mfma_f32_16x16x32_bf16 v[104:107], v[84:87], v[16:19], 0\n"
            "v_mfma_f32_16x16x32_bf16 v[100:103], v[88:91], v[4:7], v[100:103]\n"
            "v_mfma_f32_16x16x32_bf16 v[104:107], v[88:91], v[20:23], v[104:107]\n"
            "v_mfma_f32_16x16x32_bf16 v[100:103], v[92:95], v[8:11], v[100:103]\n"
            "v_mfma_f32_16x16x32_bf16 v[104:107], v[92:95], v[24:27], v[104:107]\n"
            "v_mfma_f32_16x16x32_bf16 v[100:103], v[96:99], v[12:15], v[100:103]\n"
            "v_mfma_f32_16x16x32_bf16 v[104:107], v[96:99], v[28:31], v[104:107]\n"
            "v_cvt_pk_bf16_f32 v172, v48, v49\n"
            "v_cvt_pk_bf16_f32 v173, v50, v51\n"
            "v_cvt_pk_bf16_f32 v236, v52, v53\n"
            "v_cvt_pk_bf16_f32 v237, v54, v55\n"
            "s_nop 2\n"
            "v_cvt_pk_bf16_f32 v174, v100, v101\n"
            "v_cvt_pk_bf16_f32 v175, v102, v103\n"
            "v_cvt_pk_bf16_f32 v238, v104, v105\n"
            "v_cvt_pk_bf16_f32 v239, v106, v107\n"
            "s_waitcnt vmcnt(8)\n"
            "s_barrier\n"
            "v_add_u32 v72, 98304, v66\n"
            "v_xor_b32 v73, 32, v72\n"
            "ds_read_b64_tr_b16 a[32:33], v72 offset:0\n"
            "ds_read_b64_tr_b16 a[34:35], v72 offset:512\n"
            "ds_read_b64_tr_b16 a[36:37], v72 offset:8192\n"
            "ds_read_b64_tr_b16 a[38:39], v72 offset:8704\n"
            "ds_read_b64_tr_b16 a[96:97], v73 offset:0\n"
            "ds_read_b64_tr_b16 a[98:99], v73 offset:512\n"
            "ds_read_b64_tr_b16 a[100:101], v73 offset:8192\n"
            "ds_read_b64_tr_b16 a[102:103], v73 offset:8704\n"
            "s_waitcnt lgkmcnt(0)\n"
            "ds_read_b64_tr_b16 a[40:41], v72 offset:16384\n"
            "ds_read_b64_tr_b16 a[42:43], v72 offset:16896\n"
            "ds_read_b64_tr_b16 a[44:45], v72 offset:24576\n"
            "ds_read_b64_tr_b16 a[46:47], v72 offset:25088\n"
            "ds_read_b64_tr_b16 a[104:105], v73 offset:16384\n"
            "ds_read_b64_tr_b16 a[106:107], v73 offset:16896\n"
            "ds_read_b64_tr_b16 a[108:109], v73 offset:24576\n"
            "ds_read_b64_tr_b16 a[110:111], v73 offset:25088\n"
            "s_waitcnt lgkmcnt(0)\n"
            "s_waitcnt vmcnt(0)\n"
            "s_barrier\n"
            "v_add_u32 v72, 0, v66\n"
            "v_xor_b32 v73, 32, v72\n"
            "ds_read_b64_tr_b16 a[160:161], v72 offset:0\n"
            "ds_read_b64_tr_b16 a[162:163], v72 offset:512\n"
            "ds_read_b64_tr_b16 a[164:165], v72 offset:8192\n"
            "ds_read_b64_tr_b16 a[166:167], v72 offset:8704\n"
            "ds_read_b64_tr_b16 a[224:225], v73 offset:0\n"
            "ds_read_b64_tr_b16 a[226:227], v73 offset:512\n"
            "ds_read_b64_tr_b16 a[228:229], v73 offset:8192\n"
            "ds_read_b64_tr_b16 a[230:231], v73 offset:8704\n"
            "s_waitcnt lgkmcnt(0)\n"
            "ds_read_b64_tr_b16 a[168:169], v72 offset:16384\n"
            "ds_read_b64_tr_b16 a[170:171], v72 offset:16896\n"
            "ds_read_b64_tr_b16 a[172:173], v72 offset:24576\n"
            "ds_read_b64_tr_b16 a[174:175], v72 offset:25088\n"
            "ds_read_b64_tr_b16 a[232:233], v73 offset:16384\n"
            "ds_read_b64_tr_b16 a[234:235], v73 offset:16896\n"
            "ds_read_b64_tr_b16 a[236:237], v73 offset:24576\n"
            "ds_read_b64_tr_b16 a[238:239], v73 offset:25088\n"
            "s_waitcnt lgkmcnt(0)\n"
            "PRO_WDONE_%=:\n"
            "s_barrier\n"
            "ds_write_b128 v114, v[0:3] offset:0\n"
            "ds_write_b128 v114, v[4:7] offset:1024\n"
            "ds_write_b128 v114, v[8:11] offset:2048\n"
            "ds_write_b128 v114, v[12:15] offset:3072\n"
            "ds_write_b128 v114, v[16:19] offset:4096\n"
            "ds_write_b128 v114, v[20:23] offset:5120\n"
            "ds_write_b128 v114, v[24:27] offset:6144\n"
            "ds_write_b128 v114, v[28:31] offset:7168\n"
            "s_cmp_eq_u32 s74, 1\n"
            "s_cbranch_scc0 PRO_N0_%=\n"
            "s_mov_b32 s91, 0\n"
            "s_cmp_gt_u32 s21, s91\n"
            "s_cbranch_scc1 PRO_FK_SKIP_%=\n"
            "s_mov_b32 s69, 0\n"
            "PRO_FK_L_%=:\n"
            "s_mov_b64 exec, 1\n"
            "global_load_dword v118, v110, s[24:25] sc1\n"
            "s_mov_b64 exec, -1\n"
            "s_waitcnt vmcnt(0)\n"
            "v_readfirstlane_b32 s21, v118\n"
            "s_nop 0\n"
            "s_cmp_gt_u32 s21, s91\n"
            "s_cbranch_scc1 PRO_FK_SKIP_%=\n"
            "s_add_u32 s69, s69, 1\n"
            "s_cmp_gt_u32 s69, 0x80000\n"
            "s_cbranch_scc1 TIMEOUT_%=\n"
            "s_sleep 2\n"
            "s_branch PRO_FK_L_%=\n"
            "PRO_FK_SKIP_%=:\n"
            "s_lshr_b32 s78, s91, 2\n"
            "s_xor_b32 s78, s78, s20\n"
            "s_and_b32 s78, s78, 1\n"
            "s_lshl_b32 s78, s78, 13\n"
            "s_add_u32 s96, s48, s78\n"
            "s_addc_u32 s97, s49, 0\n"
            "s_mul_i32 s78, s91, 524288\n"
            "s_add_u32 s96, s96, s78\n"
            "s_addc_u32 s97, s97, 0\n"
            "s_mov_b32 m0, 16384\n"
            "s_add_u32 s88, s96, 0\n"
            "s_addc_u32 s89, s97, 0\n"
            "global_load_lds_dwordx4 v110, s[88:89] sc1\n"
            "s_mov_b32 m0, 17408\n"
            "s_add_u32 s88, s96, 1024\n"
            "s_addc_u32 s89, s97, 0\n"
            "global_load_lds_dwordx4 v110, s[88:89] sc1\n"
            "s_mov_b32 m0, 18432\n"
            "s_add_u32 s88, s96, 2048\n"
            "s_addc_u32 s89, s97, 0\n"
            "global_load_lds_dwordx4 v110, s[88:89] sc1\n"
            "s_mov_b32 m0, 19456\n"
            "s_add_u32 s88, s96, 3072\n"
            "s_addc_u32 s89, s97, 0\n"
            "global_load_lds_dwordx4 v110, s[88:89] sc1\n"
            "s_mov_b32 m0, 20480\n"
            "s_add_u32 s88, s96, 4096\n"
            "s_addc_u32 s89, s97, 0\n"
            "global_load_lds_dwordx4 v110, s[88:89] sc1\n"
            "s_mov_b32 m0, 21504\n"
            "s_add_u32 s88, s96, 5120\n"
            "s_addc_u32 s89, s97, 0\n"
            "global_load_lds_dwordx4 v110, s[88:89] sc1\n"
            "s_mov_b32 m0, 22528\n"
            "s_add_u32 s88, s96, 6144\n"
            "s_addc_u32 s89, s97, 0\n"
            "global_load_lds_dwordx4 v110, s[88:89] sc1\n"
            "s_mov_b32 m0, 23552\n"
            "s_add_u32 s88, s96, 7168\n"
            "s_addc_u32 s89, s97, 0\n"
            "global_load_lds_dwordx4 v110, s[88:89] sc1\n"
            "PRO_N0_%=:\n"
            "s_cmp_eq_u32 s74, 2\n"
            "s_cbranch_scc0 PRO_N1_%=\n"
            "s_mov_b32 s91, 1\n"
            "s_cmp_gt_u32 s21, s91\n"
            "s_cbranch_scc1 PRO_FV_SKIP_%=\n"
            "s_mov_b32 s69, 0\n"
            "PRO_FV_L_%=:\n"
            "s_mov_b64 exec, 1\n"
            "global_load_dword v118, v110, s[24:25] sc1\n"
            "s_mov_b64 exec, -1\n"
            "s_waitcnt vmcnt(0)\n"
            "v_readfirstlane_b32 s21, v118\n"
            "s_nop 0\n"
            "s_cmp_gt_u32 s21, s91\n"
            "s_cbranch_scc1 PRO_FV_SKIP_%=\n"
            "s_add_u32 s69, s69, 1\n"
            "s_cmp_gt_u32 s69, 0x80000\n"
            "s_cbranch_scc1 TIMEOUT_%=\n"
            "s_sleep 2\n"
            "s_branch PRO_FV_L_%=\n"
            "PRO_FV_SKIP_%=:\n"
            "s_mov_b32 s91, 0\n"
            "s_lshr_b32 s78, s91, 2\n"
            "s_xor_b32 s78, s78, s20\n"
            "s_and_b32 s78, s78, 1\n"
            "s_lshl_b32 s78, s78, 13\n"
            "s_add_u32 s96, s50, s78\n"
            "s_addc_u32 s97, s51, 0\n"
            "s_mul_i32 s78, s91, 524288\n"
            "s_add_u32 s96, s96, s78\n"
            "s_addc_u32 s97, s97, 0\n"
            "s_mov_b32 m0, 24576\n"
            "s_add_u32 s88, s96, 0\n"
            "s_addc_u32 s89, s97, 0\n"
            "global_load_lds_dwordx4 v110, s[88:89] sc1\n"
            "s_mov_b32 m0, 25600\n"
            "s_add_u32 s88, s96, 1024\n"
            "s_addc_u32 s89, s97, 0\n"
            "global_load_lds_dwordx4 v110, s[88:89] sc1\n"
            "s_mov_b32 m0, 26624\n"
            "s_add_u32 s88, s96, 2048\n"
            "s_addc_u32 s89, s97, 0\n"
            "global_load_lds_dwordx4 v110, s[88:89] sc1\n"
            "s_mov_b32 m0, 27648\n"
            "s_add_u32 s88, s96, 3072\n"
            "s_addc_u32 s89, s97, 0\n"
            "global_load_lds_dwordx4 v110, s[88:89] sc1\n"
            "s_mov_b32 m0, 28672\n"
            "s_add_u32 s88, s96, 4096\n"
            "s_addc_u32 s89, s97, 0\n"
            "global_load_lds_dwordx4 v110, s[88:89] sc1\n"
            "s_mov_b32 m0, 29696\n"
            "s_add_u32 s88, s96, 5120\n"
            "s_addc_u32 s89, s97, 0\n"
            "global_load_lds_dwordx4 v110, s[88:89] sc1\n"
            "s_mov_b32 m0, 30720\n"
            "s_add_u32 s88, s96, 6144\n"
            "s_addc_u32 s89, s97, 0\n"
            "global_load_lds_dwordx4 v110, s[88:89] sc1\n"
            "s_mov_b32 m0, 31744\n"
            "s_add_u32 s88, s96, 7168\n"
            "s_addc_u32 s89, s97, 0\n"
            "global_load_lds_dwordx4 v110, s[88:89] sc1\n"
            "s_mov_b32 s91, 1\n"
            "s_lshr_b32 s78, s91, 2\n"
            "s_xor_b32 s78, s78, s20\n"
            "s_and_b32 s78, s78, 1\n"
            "s_lshl_b32 s78, s78, 13\n"
            "s_add_u32 s96, s50, s78\n"
            "s_addc_u32 s97, s51, 0\n"
            "s_mul_i32 s78, s91, 524288\n"
            "s_add_u32 s96, s96, s78\n"
            "s_addc_u32 s97, s97, 0\n"
            "s_mov_b32 m0, 32768\n"
            "s_add_u32 s88, s96, 0\n"
            "s_addc_u32 s89, s97, 0\n"
            "global_load_lds_dwordx4 v110, s[88:89] sc1\n"
            "s_mov_b32 m0, 33792\n"
            "s_add_u32 s88, s96, 1024\n"
            "s_addc_u32 s89, s97, 0\n"
            "global_load_lds_dwordx4 v110, s[88:89] sc1\n"
            "s_mov_b32 m0, 34816\n"
            "s_add_u32 s88, s96, 2048\n"
            "s_addc_u32 s89, s97, 0\n"
            "global_load_lds_dwordx4 v110, s[88:89] sc1\n"
            "s_mov_b32 m0, 35840\n"
            "s_add_u32 s88, s96, 3072\n"
            "s_addc_u32 s89, s97, 0\n"
            "global_load_lds_dwordx4 v110, s[88:89] sc1\n"
            "s_mov_b32 m0, 36864\n"
            "s_add_u32 s88, s96, 4096\n"
            "s_addc_u32 s89, s97, 0\n"
            "global_load_lds_dwordx4 v110, s[88:89] sc1\n"
            "s_mov_b32 m0, 37888\n"
            "s_add_u32 s88, s96, 5120\n"
            "s_addc_u32 s89, s97, 0\n"
            "global_load_lds_dwordx4 v110, s[88:89] sc1\n"
            "s_mov_b32 m0, 38912\n"
            "s_add_u32 s88, s96, 6144\n"
            "s_addc_u32 s89, s97, 0\n"
            "global_load_lds_dwordx4 v110, s[88:89] sc1\n"
            "s_mov_b32 m0, 39936\n"
            "s_add_u32 s88, s96, 7168\n"
            "s_addc_u32 s89, s97, 0\n"
            "global_load_lds_dwordx4 v110, s[88:89] sc1\n"
            "PRO_N1_%=:\n"
            "s_waitcnt vmcnt(0) lgkmcnt(0)\n"
            "s_barrier\n"
            "v_mov_b32 v124, -1\n"
            "v_mov_b32 v125, -1\n"
            "v_mov_b32 v126, -1\n"
            "v_mov_b32 v127, -1\n"
            "ds_read_b128 v[0:3], v114 offset:0\n"
            "ds_read_b128 v[4:7], v114 offset:1024\n"
            "ds_read_b128 v[8:11], v114 offset:2048\n"
            "ds_read_b128 v[12:15], v114 offset:3072\n"
            "ds_read_b128 v[16:19], v114 offset:4096\n"
            "ds_read_b128 v[20:23], v114 offset:5120\n"
            "ds_read_b128 v[24:27], v114 offset:6144\n"
            "ds_read_b128 v[28:31], v114 offset:7168\n"
            "ds_read_b128 v[56:59], v110 offset:24576\n"
            "ds_read_b128 v[60:63], v110 offset:25600\n"
            "ds_read_b128 v[64:67], v110 offset:26624\n"
            "ds_read_b128 v[68:71], v110 offset:27648\n"
            "ds_read_b128 v[72:75], v110 offset:28672\n"
            "ds_read_b128 v[76:79], v110 offset:29696\n"
            "ds_read_b128 v[80:83], v110 offset:30720\n"
            "ds_read_b128 v[84:87], v110 offset:31744\n"
            "s_waitcnt lgkmcnt(0)\n"
            "v_mfma_f32_16x16x32_bf16 v[32:35], v[56:59], v[0:3], 0\n"
            "v_mfma_f32_16x16x32_bf16 v[36:39], v[56:59], v[16:19], 0\n"
            "v_mfma_f32_16x16x32_bf16 v[40:43], v[72:75], v[0:3], 0\n"
            "v_mfma_f32_16x16x32_bf16 v[44:47], v[72:75], v[16:19], 0\n"
            "v_mfma_f32_16x16x32_bf16 v[32:35], v[60:63], v[4:7], v[32:35]\n"
            "v_mfma_f32_16x16x32_bf16 v[36:39], v[60:63], v[20:23], v[36:39]\n"
            "v_mfma_f32_16x16x32_bf16 v[40:43], v[76:79], v[4:7], v[40:43]\n"
            "v_mfma_f32_16x16x32_bf16 v[44:47], v[76:79], v[20:23], v[44:47]\n"
            "v_mfma_f32_16x16x32_bf16 v[32:35], v[64:67], v[8:11], v[32:35]\n"
            "v_mfma_f32_16x16x32_bf16 v[36:39], v[64:67], v[24:27], v[36:39]\n"
            "v_mfma_f32_16x16x32_bf16 v[40:43], v[80:83], v[8:11], v[40:43]\n"
            "v_mfma_f32_16x16x32_bf16 v[44:47], v[80:83], v[24:27], v[44:47]\n"
            "v_mfma_f32_16x16x32_bf16 v[32:35], v[68:71], v[12:15], v[32:35]\n"
            "v_mfma_f32_16x16x32_bf16 v[36:39], v[68:71], v[28:31], v[36:39]\n"
            "v_mfma_f32_16x16x32_bf16 v[40:43], v[84:87], v[12:15], v[40:43]\n"
            "v_mfma_f32_16x16x32_bf16 v[44:47], v[84:87], v[28:31], v[44:47]\n"
            "s_nop 4\n"
            "v_cvt_pk_bf16_f32 v100, v32, v33\n"
            "v_cvt_pk_bf16_f32 v101, v34, v35\n"
            "v_cvt_pk_bf16_f32 v102, v40, v41\n"
            "v_cvt_pk_bf16_f32 v103, v42, v43\n"
            "v_cvt_pk_bf16_f32 v104, v36, v37\n"
            "v_cvt_pk_bf16_f32 v105, v38, v39\n"
            "v_cvt_pk_bf16_f32 v106, v44, v45\n"
            "v_cvt_pk_bf16_f32 v107, v46, v47\n"
            "s_nop 7\n"
            "v_mov_b32 v32, 0\n"
            "v_mov_b32 v33, 0\n"
            "v_mov_b32 v34, 0\n"
            "v_mov_b32 v35, 0\n"
            "v_mov_b32 v36, 0\n"
            "v_mov_b32 v37, 0\n"
            "v_mov_b32 v38, 0\n"
            "v_mov_b32 v39, 0\n"
            "v_mov_b32 v40, 0\n"
            "v_mov_b32 v41, 0\n"
            "v_mov_b32 v42, 0\n"
            "v_mov_b32 v43, 0\n"
            "v_mov_b32 v44, 0\n"
            "v_mov_b32 v45, 0\n"
            "v_mov_b32 v46, 0\n"
            "v_mov_b32 v47, 0\n"
            "v_mov_b32 v48, 0\n"
            "v_mov_b32 v49, 0\n"
            "v_mov_b32 v50, 0\n"
            "v_mov_b32 v51, 0\n"
            "v_mov_b32 v52, 0\n"
            "v_mov_b32 v53, 0\n"
            "v_mov_b32 v54, 0\n"
            "v_mov_b32 v55, 0\n"
            "s_cmp_lt_u32 s74, 2\n"
            "s_cselect_b32 s91, 1, 2\n"
            "s_cselect_b32 s12, s48, s50\n"
            "s_cselect_b32 s13, s49, s51\n"
            "s_cmp_gt_u32 s21, s91\n"
            "s_cbranch_scc1 PRO_FKV_SKIP_%=\n"
            "s_mov_b32 s69, 0\n"
            "PRO_FKV_L_%=:\n"
            "s_mov_b64 exec, 1\n"
            "global_load_dword v118, v110, s[24:25] sc1\n"
            "s_mov_b64 exec, -1\n"
            "s_waitcnt vmcnt(0)\n"
            "v_readfirstlane_b32 s21, v118\n"
            "s_nop 0\n"
            "s_cmp_gt_u32 s21, s91\n"
            "s_cbranch_scc1 PRO_FKV_SKIP_%=\n"
            "s_add_u32 s69, s69, 1\n"
            "s_cmp_gt_u32 s69, 0x80000\n"
            "s_cbranch_scc1 TIMEOUT_%=\n"
            "s_sleep 2\n"
            "s_branch PRO_FKV_L_%=\n"
            "PRO_FKV_SKIP_%=:\n"
            "s_lshr_b32 s78, s91, 2\n"
            "s_xor_b32 s78, s78, s20\n"
            "s_and_b32 s78, s78, 1\n"
            "s_lshl_b32 s78, s78, 13\n"
            "s_add_u32 s18, s12, s78\n"
            "s_addc_u32 s19, s13, 0\n"
            "s_mul_i32 s78, s91, 524288\n"
            "s_add_u32 s18, s18, s78\n"
            "s_addc_u32 s19, s19, 0\n"
            "s_cmp_eq_u32 s74, 1\n"
            "s_cselect_b32 s78, 4096, 0\n"
            "s_add_u32 s18, s18, s78\n"
            "s_addc_u32 s19, s19, 0\n"
            "s_barrier\n"
            "s_cmp_eq_u32 s74, 0\n"
            "s_cbranch_scc0 RV_BAR_%=\n"
            "s_mov_b32 s43, s15\n"
            "s_mov_b32 s44, s16\n"
            "s_mov_b32 s46, s81\n"
            "s_memrealtime s[26:27]\n"
            "s_waitcnt lgkmcnt(0)\n"
            "s_mov_b32 s45, s26\n"
            "RV_CK_E_%=:\n"
            "s_memrealtime s[26:27]\n"
            "s_memtime s[78:79]\n"
            "s_waitcnt lgkmcnt(0)\n"
            "s_cmp_eq_u32 s26, s45\n"
            "s_cbranch_scc1 RV_CK_E_%=\n"
            "s_mov_b32 s40, s78\n"
            "s_mov_b32 s41, s26\n"
            "s_sub_u32 s78, s40, s43\n"
            "s_sub_u32 s79, s41, s44\n"
            "v_cvt_f32_u32 v118, s78\n"
            "v_cvt_f32_u32 v119, s79\n"
            "v_rcp_f32 v119, v119\n"
            "v_mul_f32 v118, 0x43800000, v118\n"
            "v_mul_f32 v118, v118, v119\n"
            "v_cvt_u32_f32 v118, v118\n"
            "s_nop 0\n"
            "v_readfirstlane_b32 s78, v118\n"
            "s_nop 0\n"
            "s_mov_b32 s42, s78\n"
            "v_mov_b32 v118, 45056\n"
            "v_mov_b32 v2, s40\n"
            "v_mov_b32 v3, s41\n"
            "v_mov_b32 v4, s42\n"
            "ds_write_b64 v118, v[2:3]\n"
            "ds_write_b32 v118, v4 offset:8\n"
            "s_mov_b32 s43, s40\n"
            "s_mov_b32 s44, s41\n"
            "v_mov_b32 v118, 128\n"
            "v_mov_b32 v119, 1\n"
            "s_mov_b64 exec, 1\n"
            "global_atomic_add v118, v119, s[22:23] sc1\n"
            "s_mov_b64 exec, -1\n"
            "s_mov_b32 s69, 0\n"
            "s_cmp_eq_u32 s29, 0\n"
            "s_cbranch_scc1 RV_WAIT_%=\n"
            "RV_L_%=:\n"
            "s_mov_b64 exec, 1\n"
            "global_load_dword v119, v118, s[22:23] sc1\n"
            "s_mov_b64 exec, -1\n"
            "s_waitcnt vmcnt(0)\n"
            "v_readfirstlane_b32 s78, v119\n"
            "s_nop 0\n"
            "s_cmp_ge_u32 s78, 128\n"
            "s_cbranch_scc1 RV_GO_%=\n"
            "s_add_u32 s69, s69, 1\n"
            "s_cmp_gt_u32 s69, 0x80000\n"
            "s_cbranch_scc1 TIMEOUT_%=\n"
            "s_branch RV_L_%=\n"
            "RV_GO_%=:\n"
            "s_memrealtime s[26:27]\n"
            "s_waitcnt lgkmcnt(0)\n"
            "s_add_u32 s26, s26, 80\n"
            "s_min_u32 s26, s26, 0xfffffffe\n"
            "v_mov_b32 v119, s26\n"
            "s_mov_b64 exec, 1\n"
            "global_store_dword v110, v119, s[30:31] sc1\n"
            "s_mov_b64 exec, -1\n"
            "RV_WAIT_%=:\n"
            "s_mov_b64 exec, 1\n"
            "global_load_dword v119, v110, s[30:31] sc1\n"
            "s_mov_b64 exec, -1\n"
            "s_waitcnt vmcnt(0)\n"
            "v_readfirstlane_b32 s32, v119\n"
            "s_nop 0\n"
            "s_cmp_eq_u32 s32, -1\n"
            "s_cbranch_scc0 RV_T_%=\n"
            "s_add_u32 s69, s69, 1\n"
            "s_cmp_gt_u32 s69, 0x100000\n"
            "s_cbranch_scc1 TIMEOUT_%=\n"
            "s_sleep 1\n"
            "s_branch RV_WAIT_%=\n"
            "RV_T_%=:\n"
            "RV_BAR_%=:\n"
            "s_barrier\n"
            "s_mov_b64 exec, 1\n"
            "global_load_dword v119, v110, s[30:31] sc1\n"
            "s_mov_b64 exec, -1\n"
            "s_waitcnt vmcnt(0)\n"
            "v_readfirstlane_b32 s32, v119\n"
            "s_cmp_eq_u32 s74, 0\n"
            "s_cbranch_scc0 RV_B2_%=\n"
            "s_sub_u32 s45, s32, s41\n"
            "s_mul_i32 s45, s45, s42\n"
            "s_ashr_i32 s45, s45, 8\n"
            "s_add_u32 s45, s45, s40\n"
            "s_memtime s[78:79]\n"
            "s_waitcnt lgkmcnt(0)\n"
            "s_sub_u32 s45, s45, s78\n"
            "s_sub_u32 s45, s45, 60\n"
            "RV_W_D_%=:\n"
            "s_sub_u32 s45, s45, 32\n"
            "s_cmp_gt_i32 s45, 0\n"
            "s_cbranch_scc1 RV_W_D_%=\n"
            "RV_B2_%=:\n"
            "s_barrier\n"
            "s_memtime s[98:99]\n"
            "s_waitcnt lgkmcnt(0)\n"
            "s_cmp_eq_u32 s74, 1\n"
            "s_cbranch_scc1 W1P0_ENTRY_%=\n"
            "s_cmp_eq_u32 s74, 2\n"
            "s_cbranch_scc1 W2P0_ENTRY_%=\n"
            "s_cmp_eq_u32 s74, 3\n"
            "s_cbranch_scc1 W3P0_ENTRY_%=\n"
            "s_nop 15\n"
            "W0P0_ENTRY_%=:\n"
            "s_mov_b32 s76, 0\n"
            "W0P0_POLL_%=:\n"
            "s_mov_b32 m0, 0\n"
            "s_nop 0\n"
            "global_load_lds_dwordx4 v109, s[52:53] sc1\n"
            "global_load_lds_dwordx4 v109, s[52:53] offset:1024 sc1\n"
            "s_waitcnt vmcnt(0)\n"
            "s_memrealtime s[26:27]\n"
            "s_waitcnt lgkmcnt(0)\n"
            "s_barrier\n"
            "W0P0_AFTERB1_%=:\n"
            "ds_read_b128 v[0:3], v110 offset:0\n"
            "ds_read_b128 v[4:7], v110 offset:1024\n"
            "ds_read_b128 v[8:11], v110 offset:2048\n"
            "ds_read_b128 v[12:15], v110 offset:3072\n"
            "ds_read_b128 v[16:19], v110 offset:4096\n"
            "ds_read_b128 v[20:23], v110 offset:5120\n"
            "ds_read_b128 v[24:27], v110 offset:6144\n"
            "ds_read_b128 v[28:31], v110 offset:7168\n"
            "s_add_u32 s30, s30, 128\n"
            "s_addc_u32 s31, s31, 0\n"
            "s_waitcnt lgkmcnt(7)\n"
            "v_smfmac_f32_16x16x64_bf16 v[32:35], v[0:3], a[0:7], v108\n"
            "v_smfmac_f32_16x16x64_bf16 v[36:39], v[0:3], a[128:135], v108\n"
            "s_waitcnt lgkmcnt(6)\n"
            "v_max_u32 v118, v1, v3\n"
            "v_smfmac_f32_16x16x64_bf16 v[32:35], v[4:7], a[8:15], v108\n"
            "ds_read_b128 v[56:59], v110 offset:16384\n"
            "v_smfmac_f32_16x16x64_bf16 v[36:39], v[4:7], a[136:143], v108\n"
            "s_waitcnt lgkmcnt(6)\n"
            "v_max3_u32 v118, v118, v5, v7\n"
            "v_smfmac_f32_16x16x64_bf16 v[32:35], v[8:11], a[16:23], v108\n"
            "ds_read_b128 v[60:63], v110 offset:17408\n"
            "v_smfmac_f32_16x16x64_bf16 v[36:39], v[8:11], a[144:151], v108\n"
            "s_waitcnt lgkmcnt(6)\n"
            "v_max3_u32 v118, v118, v9, v11\n"
            "v_smfmac_f32_16x16x64_bf16 v[32:35], v[12:15], a[24:31], v108\n"
            "ds_read_b128 v[64:67], v110 offset:18432\n"
            "v_smfmac_f32_16x16x64_bf16 v[36:39], v[12:15], a[152:159], v108\n"
            "s_waitcnt lgkmcnt(6)\n"
            "v_max3_u32 v118, v118, v13, v15\n"
            "v_smfmac_f32_16x16x64_bf16 v[32:35], v[16:19], a[32:39], v108\n"
            "ds_read_b128 v[68:71], v110 offset:19456\n"
            "v_smfmac_f32_16x16x64_bf16 v[36:39], v[16:19], a[160:167], v108\n"
            "s_waitcnt lgkmcnt(6)\n"
            "v_max3_u32 v118, v118, v17, v19\n"
            "v_smfmac_f32_16x16x64_bf16 v[32:35], v[20:23], a[40:47], v108\n"
            "ds_read_b128 v[72:75], v110 offset:20480\n"
            "v_smfmac_f32_16x16x64_bf16 v[36:39], v[20:23], a[168:175], v108\n"
            "s_waitcnt lgkmcnt(6)\n"
            "v_max3_u32 v118, v118, v21, v23\n"
            "v_smfmac_f32_16x16x64_bf16 v[32:35], v[24:27], a[48:55], v108\n"
            "ds_read_b128 v[76:79], v110 offset:21504\n"
            "v_smfmac_f32_16x16x64_bf16 v[36:39], v[24:27], a[176:183], v108\n"
            "s_waitcnt lgkmcnt(6)\n"
            "v_max3_u32 v118, v118, v25, v27\n"
            "v_smfmac_f32_16x16x64_bf16 v[32:35], v[28:31], a[56:63], v108\n"
            "ds_read_b128 v[80:83], v110 offset:22528\n"
            "v_smfmac_f32_16x16x64_bf16 v[36:39], v[28:31], a[184:191], v108\n"
            "v_max3_u32 v118, v118, v29, v31\n"
            "v_smfmac_f32_16x16x64_bf16 v[40:43], v[0:3], a[64:71], v108\n"
            "ds_read_b128 v[84:87], v110 offset:23552\n"
            "v_cmp_eq_u32 vcc, -1, v118\n"
            "v_smfmac_f32_16x16x64_bf16 v[44:47], v[0:3], a[192:199], v108\n"
            "v_smfmac_f32_16x16x64_bf16 v[40:43], v[4:7], a[72:79], v108\n"
            "v_smfmac_f32_16x16x64_bf16 v[44:47], v[4:7], a[200:207], v108\n"
            "s_cbranch_vccnz W0P0_RETRY_%=\n"
            "s_mov_b32 vcc_lo, 0xcccccccc\n"
            "s_mov_b32 vcc_hi, 0xcccccccc\n"
            "s_sub_u32 s26, s26, s32\n"
            "s_sub_u32 s88, s26, -10\n"
            "s_cmp_gt_i32 s26, 42\n"
            "s_cselect_b32 s26, s88, 0\n"
            "s_cselect_b32 s88, s77, 0\n"
            "s_add_u32 s33, s33, s88\n"
            "s_min_u32 s33, s33, 208\n"
            "s_cmp_lg_u32 s88, 0\n"
            "s_cselect_b32 s46, s82, s46\n"
            "s_sub_u32 s46, s46, 1\n"
            "s_cmp_eq_u32 s46, 0\n"
            "s_cselect_b32 s88, 1, 0\n"
            "s_sub_u32 s33, s33, s88\n"
            "s_max_u32 s33, s33, 164\n"
            "s_cmp_eq_u32 s46, 0\n"
            "s_cselect_b32 s46, s81, s46\n"
            "s_add_u32 s26, s26, s32\n"
            "s_add_u32 s26, s26, s33\n"
            "s_min_u32 s26, s26, 0xfffffffe\n"
            "v_smfmac_f32_16x16x64_bf16 v[40:43], v[8:11], a[80:87], v108\n"
            "v_mov_b32 v118, s26\n"
            "v_smfmac_f32_16x16x64_bf16 v[44:47], v[8:11], a[208:215], v108\n"
            "s_add_u32 s88, s68, 1\n"
            "v_mov_b32 v119, s88\n"
            "s_mov_b32 exec_lo, s29\n"
            "s_mov_b32 exec_hi, 0\n"
            "global_store_dword v110, v118, s[30:31] sc1\n"
            "global_store_dword v110, v119, s[22:23] sc1\n"
            "s_mov_b64 exec, -1\n"
            "v_smfmac_f32_16x16x64_bf16 v[40:43], v[12:15], a[88:95], v108\n"
            "v_smfmac_f32_16x16x64_bf16 v[44:47], v[12:15], a[216:223], v108\n"
            "v_add_f32 v36, v36, v37\n"
            "v_smfmac_f32_16x16x64_bf16 v[40:43], v[16:19], a[96:103], v108\n"
            "v_add_f32 v38, v38, v39\n"
            "v_smfmac_f32_16x16x64_bf16 v[44:47], v[16:19], a[224:231], v108\n"
            "v_add_f32 v32, v32, v33\n"
            "v_smfmac_f32_16x16x64_bf16 v[40:43], v[20:23], a[104:111], v108\n"
            "v_add_f32 v34, v34, v35\n"
            "v_smfmac_f32_16x16x64_bf16 v[44:47], v[20:23], a[232:239], v108\n"
            "v_cvt_pk_bf16_f32 v36, v36, v38\n"
            "v_smfmac_f32_16x16x64_bf16 v[40:43], v[24:27], a[112:119], v108\n"
            "v_cvt_pk_bf16_f32 v37, v32, v34\n"
            "v_smfmac_f32_16x16x64_bf16 v[44:47], v[24:27], a[240:247], v108\n"
            "ds_write_b64 v111, v[36:37] offset:0\n"
            "v_smfmac_f32_16x16x64_bf16 v[40:43], v[28:31], a[120:127], v108\n"
            "v_smfmac_f32_16x16x64_bf16 v[44:47], v[28:31], a[248:255], v108\n"
            "v_smfmac_f32_16x16x64_bf16 v[48:51], v[0:3], v[128:135], v108\n"
            "s_nop 5\n"
            "s_nop 0\n"
            "v_add_f32 v44, v44, v45\n"
            "v_add_f32 v46, v46, v47\n"
            "v_add_f32 v40, v40, v41\n"
            "v_add_f32 v42, v42, v43\n"
            "v_cvt_pk_bf16_f32 v44, v44, v46\n"
            "v_cvt_pk_bf16_f32 v45, v40, v42\n"
            "ds_write_b64 v111, v[44:45] offset:512\n"
            "v_smfmac_f32_16x16x64_bf16 v[52:55], v[0:3], v[192:199], v108\n"
            "v_smfmac_f32_16x16x64_bf16 v[48:51], v[4:7], v[136:143], v108\n"
            "v_smfmac_f32_16x16x64_bf16 v[52:55], v[4:7], v[200:207], v108\n"
            "v_smfmac_f32_16x16x64_bf16 v[48:51], v[8:11], v[144:151], v108\n"
            "v_smfmac_f32_16x16x64_bf16 v[52:55], v[8:11], v[208:215], v108\n"
            "s_waitcnt lgkmcnt(0)\n"
            "s_barrier\n"
            "ds_read_b64_tr_b16 v[32:33], v112 offset:0\n"
            "ds_read_b64_tr_b16 v[34:35], v112 offset:512\n"
            "ds_read_b64_tr_b16 v[36:37], v112 offset:1024\n"
            "ds_read_b64_tr_b16 v[38:39], v112 offset:1536\n"
            "ds_read_b64_tr_b16 v[40:41], v112 offset:2048\n"
            "ds_read_b64_tr_b16 v[42:43], v112 offset:2560\n"
            "ds_read_b64_tr_b16 v[44:45], v112 offset:3072\n"
            "ds_read_b64_tr_b16 v[46:47], v112 offset:3584\n"
            "s_mov_b32 m0, 16384\n"
            "s_nop 0\n"
            "global_load_lds_dwordx4 v110, s[18:19] offset:0 sc1\n"
            "global_load_lds_dwordx4 v110, s[18:19] offset:1024 sc1\n"
            "global_load_lds_dwordx4 v110, s[18:19] offset:2048 sc1\n"
            "global_load_lds_dwordx4 v110, s[18:19] offset:3072 sc1\n"
            "v_smfmac_f32_16x16x64_bf16 v[48:51], v[12:15], v[152:159], v108\n"
            "v_smfmac_f32_16x16x64_bf16 v[52:55], v[12:15], v[216:223], v108\n"
            "s_waitcnt lgkmcnt(6)\n"
            "v_mfma_f32_16x16x32_bf16 v[96:99], v[32:35], v[32:35], 0\n"
            "s_waitcnt lgkmcnt(4)\n"
            "v_mfma_f32_16x16x32_bf16 v[96:99], v[36:39], v[36:39], v[96:99]\n"
            "s_waitcnt lgkmcnt(2)\n"
            "v_mfma_f32_16x16x32_bf16 v[96:99], v[40:43], v[40:43], v[96:99]\n"
            "s_waitcnt lgkmcnt(0)\n"
            "v_mfma_f32_16x16x32_bf16 v[96:99], v[44:47], v[44:47], v[96:99]\n"
            "v_mfma_f32_16x16x32_bf16 v[88:91], v[56:59], v[32:35], 0\n"
            "s_mov_b64 exec, 1\n"
            "global_load_dword v117, v110, s[30:31] sc1\n"
            "global_load_dword v60, v110, s[24:25] sc1\n"
            "s_mov_b64 exec, -1\n"
            "v_mov_b32 v12, 0x3f803f80\n"
            "v_mfma_f32_16x16x32_bf16 v[88:91], v[60:63], v[36:39], v[88:91]\n"
            "v_mov_b32 v13, 0x3f803f80\n"
            "v_mov_b32 v14, 0x3f803f80\n"
            "v_mfma_f32_16x16x32_bf16 v[88:91], v[64:67], v[40:43], v[88:91]\n"
            "v_mov_b32 v15, 0x3f803f80\n"
            "v_mov_b32 v6, 0x00003f80\n"
            "v_mfma_f32_16x16x32_bf16 v[88:91], v[68:71], v[44:47], v[88:91]\n"
            "v_mov_b32 v7, 0x00003f80\n"
            "v_mfma_f32_16x16x32_bf16 v[92:95], v[72:75], v[32:35], 0\n"
            "v_cndmask_b32_dpp v96, v97, v96, vcc quad_perm:[2,3,2,3] row_mask:0xf bank_mask:0xf\n"
            "v_mfma_f32_16x16x32_bf16 v[92:95], v[76:79], v[36:39], v[92:95]\n"
            "v_mfma_f32_16x16x32_bf16 v[92:95], v[80:83], v[40:43], v[92:95]\n"
            "v_mfma_f32_16x16x32_bf16 v[92:95], v[84:87], v[44:47], v[92:95]\n"
            "v_smfmac_f32_16x16x64_bf16 v[48:51], v[16:19], v[160:167], v108\n"
            "s_nop 6\n"
            "v_cndmask_b32_dpp v88, v92, v88, vcc quad_perm:[2,3,2,3] row_mask:0xf bank_mask:0xf\n"
            "v_cndmask_b32_dpp v89, v93, v89, vcc quad_perm:[2,3,2,3] row_mask:0xf bank_mask:0xf\n"
            "v_cndmask_b32_dpp v90, v94, v90, vcc quad_perm:[2,3,2,3] row_mask:0xf bank_mask:0xf\n"
            "v_cndmask_b32_dpp v91, v95, v91, vcc quad_perm:[2,3,2,3] row_mask:0xf bank_mask:0xf\n"
            "v_max3_f32 v118, v96, v88, v89\n"
            "v_max3_f32 v118, v118, v90, v91\n"
            "v_smfmac_f32_16x16x64_bf16 v[52:55], v[16:19], v[224:231], v108\n"
            "s_nop 0\n"
            "v_max_f32_dpp v118, v118, v118 quad_perm:[2,3,0,1] row_mask:0xf bank_mask:0xf\n"
            "v_mov_b32 v119, v118\n"
            "v_smfmac_f32_16x16x64_bf16 v[48:51], v[20:23], v[168:175], v108\n"
            "s_nop 0\n"
            "v_permlane32_swap_b32 v118, v119\n"
            "v_max_f32 v118, v118, v119\n"
            "v_mov_b32 v119, v118\n"
            "v_smfmac_f32_16x16x64_bf16 v[52:55], v[20:23], v[232:239], v108\n"
            "s_nop 0\n"
            "v_permlane16_swap_b32 v118, v119\n"
            "v_max_f32 v118, v118, v119\n"
            "v_smfmac_f32_16x16x64_bf16 v[48:51], v[24:27], v[176:183], v108\n"
            "v_mul_f32 v118, s94, v118\n"
            "v_smfmac_f32_16x16x64_bf16 v[52:55], v[24:27], v[240:247], v108\n"
            "v_fma_f32 v88, v88, s94, -v118\n"
            "v_fma_f32 v89, v89, s94, -v118\n"
            "v_fma_f32 v90, v90, s94, -v118\n"
            "v_fma_f32 v91, v91, s94, -v118\n"
            "v_fma_f32 v96, v96, s94, -v118\n"
            "v_smfmac_f32_16x16x64_bf16 v[48:51], v[28:31], v[184:191], v108\n"
            "v_exp_f32 v88, v88\n"
            "v_exp_f32 v89, v89\n"
            "v_exp_f32 v90, v90\n"
            "v_exp_f32 v91, v91\n"
            "v_exp_f32 v96, v96\n"
            "v_smfmac_f32_16x16x64_bf16 v[52:55], v[28:31], v[248:255], v108\n"
            "v_cvt_pk_bf16_f32 v0, v88, v89\n"
            "v_cvt_pk_bf16_f32 v1, v90, v91\n"
            "v_cvt_pk_bf16_f32 v4, v96, v96\n"
            "v_mov_b32_dpp v2, v0 quad_perm:[0,1,0,1] row_mask:0xf bank_mask:0xf\n"
            "v_mov_b32_dpp v3, v1 quad_perm:[0,1,0,1] row_mask:0xf bank_mask:0xf\n"
            "v_mov_b32_dpp v5, v4 quad_perm:[0,1,0,1] row_mask:0xf bank_mask:0xf\n"
            "s_nop 0\n"
            "v_mfma_f32_16x16x32_bf16 v[92:95], v[12:15], v[0:3], 0\n"
            "v_cvt_pk_bf16_f32 v8, v48, v49\n"
            "v_cvt_pk_bf16_f32 v9, v50, v51\n"
            "v_mfma_f32_16x16x32_bf16 v[12:15], v[100:103], v[0:3], 0\n"
            "v_cvt_pk_bf16_f32 v10, v52, v53\n"
            "v_cvt_pk_bf16_f32 v11, v54, v55\n"
            "v_mfma_f32_16x16x16_bf16 v[92:95], v[6:7], v[4:5], v[92:95]\n"
            "v_mfma_f32_16x16x32_bf16 v[88:91], v[104:107], v[0:3], 0\n"
            "v_mfma_f32_16x16x16_bf16 v[12:15], v[8:9], v[4:5], v[12:15]\n"
            "v_mfma_f32_16x16x16_bf16 v[88:91], v[10:11], v[4:5], v[88:91]\n"
            "s_nop 4\n"
            "v_mov_b32_dpp v118, v92 quad_perm:[2,3,2,3] row_mask:0xf bank_mask:0xf\n"
            "v_rcp_f32 v118, v118\n"
            "s_nop 0\n"
            "v_cndmask_b32_dpp v12, v88, v12, vcc quad_perm:[2,3,2,3] row_mask:0xf bank_mask:0xf\n"
            "v_cndmask_b32_dpp v13, v89, v13, vcc quad_perm:[2,3,2,3] row_mask:0xf bank_mask:0xf\n"
            "v_cndmask_b32_dpp v14, v90, v14, vcc quad_perm:[2,3,2,3] row_mask:0xf bank_mask:0xf\n"
            "v_cndmask_b32_dpp v15, v91, v15, vcc quad_perm:[2,3,2,3] row_mask:0xf bank_mask:0xf\n"
            "v_mul_f32 v12, v12, v118\n"
            "v_mul_f32 v13, v13, v118\n"
            "v_mul_f32 v14, v14, v118\n"
            "v_mul_f32 v15, v15, v118\n"
            "v_cvt_pk_bf16_f32 v20, v12, v13\n"
            "v_cvt_pk_bf16_f32 v21, v14, v15\n"
            "v_min_u32 v21, -2, v21\n"
            "global_store_dwordx2 v113, v[20:21], s[54:55] sc1\n"
            "W0P0_SCH_%=:\n"
            "s_waitcnt vmcnt(1)\n"
            "v_readfirstlane_b32 s88, v117\n"
            "v_readfirstlane_b32 s21, v60\n"
            "s_add_u32 s32, s32, s33\n"
            "s_cmp_eq_u32 s88, -1\n"
            "s_cselect_b32 s32, s32, s88\n"
            "s_cselect_b32 s17, 1, 0\n"
            "s_add_u32 s91, s68, 2\n"
            "s_min_u32 s91, s91, 63\n"
            "s_cmp_gt_u32 s21, s91\n"
            "s_cbranch_scc1 W0P0_FKV_SKIP_%=\n"
            "s_mov_b32 s69, 0\n"
            "W0P0_FKV_L_%=:\n"
            "s_mov_b64 exec, 1\n"
            "global_load_dword v118, v110, s[24:25] sc1\n"
            "s_mov_b64 exec, -1\n"
            "s_waitcnt vmcnt(0)\n"
            "v_readfirstlane_b32 s21, v118\n"
            "s_nop 0\n"
            "s_cmp_gt_u32 s21, s91\n"
            "s_cbranch_scc1 W0P0_FKV_SKIP_%=\n"
            "s_add_u32 s69, s69, 1\n"
            "s_cmp_gt_u32 s69, 0x80000\n"
            "s_cbranch_scc1 TIMEOUT_%=\n"
            "s_sleep 2\n"
            "s_branch W0P0_FKV_L_%=\n"
            "W0P0_FKV_SKIP_%=:\n"
            "s_lshr_b32 s78, s91, 2\n"
            "s_xor_b32 s78, s78, s20\n"
            "s_and_b32 s78, s78, 1\n"
            "s_lshl_b32 s78, s78, 13\n"
            "s_add_u32 s18, s48, s78\n"
            "s_addc_u32 s19, s49, 0\n"
            "s_mul_i32 s78, s91, 524288\n"
            "s_add_u32 s18, s18, s78\n"
            "s_addc_u32 s19, s19, 0\n"
            "ds_read_b128 v[0:3], v114 offset:0\n"
            "ds_read_b128 v[4:7], v114 offset:1024\n"
            "ds_read_b128 v[8:11], v114 offset:2048\n"
            "ds_read_b128 v[12:15], v114 offset:3072\n"
            "ds_read_b128 v[16:19], v114 offset:4096\n"
            "ds_read_b128 v[20:23], v114 offset:5120\n"
            "ds_read_b128 v[24:27], v114 offset:6144\n"
            "ds_read_b128 v[28:31], v114 offset:7168\n"
            "ds_read_b128 v[56:59], v110 offset:32768\n"
            "ds_read_b128 v[60:63], v110 offset:33792\n"
            "ds_read_b128 v[64:67], v110 offset:34816\n"
            "ds_read_b128 v[68:71], v110 offset:35840\n"
            "ds_read_b128 v[72:75], v110 offset:36864\n"
            "ds_read_b128 v[76:79], v110 offset:37888\n"
            "ds_read_b128 v[80:83], v110 offset:38912\n"
            "ds_read_b128 v[84:87], v110 offset:39936\n"
            "s_waitcnt lgkmcnt(0)\n"
            "v_mfma_f32_16x16x32_bf16 v[32:35], v[56:59], v[0:3], 0\n"
            "v_mfma_f32_16x16x32_bf16 v[36:39], v[56:59], v[16:19], 0\n"
            "v_mfma_f32_16x16x32_bf16 v[40:43], v[72:75], v[0:3], 0\n"
            "v_mfma_f32_16x16x32_bf16 v[44:47], v[72:75], v[16:19], 0\n"
            "v_mfma_f32_16x16x32_bf16 v[32:35], v[60:63], v[4:7], v[32:35]\n"
            "v_mfma_f32_16x16x32_bf16 v[36:39], v[60:63], v[20:23], v[36:39]\n"
            "v_mfma_f32_16x16x32_bf16 v[40:43], v[76:79], v[4:7], v[40:43]\n"
            "v_mfma_f32_16x16x32_bf16 v[44:47], v[76:79], v[20:23], v[44:47]\n"
            "v_mfma_f32_16x16x32_bf16 v[32:35], v[64:67], v[8:11], v[32:35]\n"
            "v_mfma_f32_16x16x32_bf16 v[36:39], v[64:67], v[24:27], v[36:39]\n"
            "v_mfma_f32_16x16x32_bf16 v[40:43], v[80:83], v[8:11], v[40:43]\n"
            "v_mfma_f32_16x16x32_bf16 v[44:47], v[80:83], v[24:27], v[44:47]\n"
            "v_mfma_f32_16x16x32_bf16 v[32:35], v[68:71], v[12:15], v[32:35]\n"
            "v_mfma_f32_16x16x32_bf16 v[36:39], v[68:71], v[28:31], v[36:39]\n"
            "v_mfma_f32_16x16x32_bf16 v[40:43], v[84:87], v[12:15], v[40:43]\n"
            "v_mfma_f32_16x16x32_bf16 v[44:47], v[84:87], v[28:31], v[44:47]\n"
            "s_nop 4\n"
            "v_cvt_pk_bf16_f32 v100, v32, v33\n"
            "v_cvt_pk_bf16_f32 v101, v34, v35\n"
            "v_cvt_pk_bf16_f32 v102, v40, v41\n"
            "v_cvt_pk_bf16_f32 v103, v42, v43\n"
            "v_cvt_pk_bf16_f32 v104, v36, v37\n"
            "v_cvt_pk_bf16_f32 v105, v38, v39\n"
            "v_cvt_pk_bf16_f32 v106, v44, v45\n"
            "v_cvt_pk_bf16_f32 v107, v46, v47\n"
            "W0P0_ZDONE_%=:\n"
            "s_nop 7\n"
            "v_mov_b32 v32, 0\n"
            "v_mov_b32 v33, 0\n"
            "v_mov_b32 v34, 0\n"
            "v_mov_b32 v35, 0\n"
            "v_mov_b32 v36, 0\n"
            "v_mov_b32 v37, 0\n"
            "v_mov_b32 v38, 0\n"
            "v_mov_b32 v39, 0\n"
            "v_mov_b32 v40, 0\n"
            "v_mov_b32 v41, 0\n"
            "v_mov_b32 v42, 0\n"
            "v_mov_b32 v43, 0\n"
            "v_mov_b32 v44, 0\n"
            "v_mov_b32 v45, 0\n"
            "v_mov_b32 v46, 0\n"
            "v_mov_b32 v47, 0\n"
            "v_mov_b32 v48, 0\n"
            "v_mov_b32 v49, 0\n"
            "v_mov_b32 v50, 0\n"
            "v_mov_b32 v51, 0\n"
            "v_mov_b32 v52, 0\n"
            "v_mov_b32 v53, 0\n"
            "v_mov_b32 v54, 0\n"
            "v_mov_b32 v55, 0\n"
            "s_add_u32 s52, s52, 16384\n"
            "s_addc_u32 s53, s53, 0\n"
            "s_add_u32 s68, s68, 1\n"
            "s_cmp_ge_u32 s68, 63\n"
            "s_cbranch_scc1 DONE_%=\n"
            "s_memrealtime s[26:27]\n"
            "s_waitcnt lgkmcnt(0)\n"
            "s_mov_b32 s45, s26\n"
            "W0P0_CK_E_%=:\n"
            "s_memrealtime s[26:27]\n"
            "s_memtime s[78:79]\n"
            "s_waitcnt lgkmcnt(0)\n"
            "s_cmp_eq_u32 s26, s45\n"
            "s_cbranch_scc1 W0P0_CK_E_%=\n"
            "s_mov_b32 s40, s78\n"
            "s_mov_b32 s41, s26\n"
            "s_sub_u32 s78, s40, s43\n"
            "s_sub_u32 s79, s41, s44\n"
            "v_cvt_f32_u32 v118, s78\n"
            "v_cvt_f32_u32 v119, s79\n"
            "v_rcp_f32 v119, v119\n"
            "v_mul_f32 v118, 0x43800000, v118\n"
            "v_mul_f32 v118, v118, v119\n"
            "v_cvt_u32_f32 v118, v118\n"
            "s_nop 0\n"
            "v_readfirstlane_b32 s78, v118\n"
            "s_nop 0\n"
            "s_sub_i32 s78, s78, s42\n"
            "s_ashr_i32 s78, s78, 3\n"
            "s_add_i32 s42, s42, s78\n"
            "v_mov_b32 v118, 45056\n"
            "v_mov_b32 v2, s40\n"
            "v_mov_b32 v3, s41\n"
            "v_mov_b32 v4, s42\n"
            "ds_write_b64 v118, v[2:3]\n"
            "ds_write_b32 v118, v4 offset:8\n"
            "s_mov_b32 s43, s40\n"
            "s_mov_b32 s44, s41\n"
            "s_cmp_eq_u32 s17, 0\n"
            "s_cbranch_scc0 W0P0_SCHRETRY_%=\n"
            "W0P0_SCHOK_%=:\n"
            "s_sub_u32 s45, s32, s41\n"
            "s_mul_i32 s45, s45, s42\n"
            "s_ashr_i32 s45, s45, 8\n"
            "s_add_u32 s45, s45, s40\n"
            "s_memtime s[78:79]\n"
            "s_waitcnt lgkmcnt(0)\n"
            "s_sub_u32 s45, s45, s78\n"
            "s_sub_u32 s45, s45, 60\n"
            "W0P0_W2_D_%=:\n"
            "s_sub_u32 s45, s45, 32\n"
            "s_cmp_gt_i32 s45, 0\n"
            "s_cbranch_scc1 W0P0_W2_D_%=\n"
            "s_waitcnt lgkmcnt(0)\n"
            "s_barrier\n"
            "s_branch W0P1_ENTRY_%=\n"
            "W0P0_SCHRETRY_%=:\n"
            "s_add_u32 s69, s69, 1\n"
            "s_cmp_gt_u32 s69, 0x400000\n"
            "s_cbranch_scc1 TIMEOUT_%=\n"
            "s_mov_b64 exec, 1\n"
            "global_load_dword v117, v110, s[30:31] sc1\n"
            "s_mov_b64 exec, -1\n"
            "s_waitcnt vmcnt(0)\n"
            "v_readfirstlane_b32 s88, v117\n"
            "s_nop 0\n"
            "s_cmp_eq_u32 s88, -1\n"
            "s_cbranch_scc1 W0P0_SCHRETRY_%=\n"
            "s_mov_b32 s32, s88\n"
            "s_branch W0P0_SCHOK_%=\n"
            "W0P0_RETRY_%=:\n"
            "s_waitcnt lgkmcnt(0)\n"
            "s_barrier\n"
            "s_nop 7\n"
            "v_mov_b32 v32, 0\n"
            "v_mov_b32 v33, 0\n"
            "v_mov_b32 v34, 0\n"
            "v_mov_b32 v35, 0\n"
            "v_mov_b32 v36, 0\n"
            "v_mov_b32 v37, 0\n"
            "v_mov_b32 v38, 0\n"
            "v_mov_b32 v39, 0\n"
            "v_mov_b32 v40, 0\n"
            "v_mov_b32 v41, 0\n"
            "v_mov_b32 v42, 0\n"
            "v_mov_b32 v43, 0\n"
            "v_mov_b32 v44, 0\n"
            "v_mov_b32 v45, 0\n"
            "v_mov_b32 v46, 0\n"
            "v_mov_b32 v47, 0\n"
            "s_sub_u32 s30, s30, 128\n"
            "s_subb_u32 s31, s31, 0\n"
            "W0P0_RETRY2_%=:\n"
            "s_add_u32 s76, s76, 1\n"
            "s_add_u32 s84, s84, 1\n"
            "s_cmp_gt_u32 s76, 0x20000\n"
            "s_cbranch_scc1 TIMEOUT_%=\n"
            "global_load_dwordx4 v[0:3], v109, s[52:53] sc1\n"
            "global_load_dwordx4 v[4:7], v109, s[52:53] offset:1024 sc1\n"
            "s_waitcnt vmcnt(0)\n"
            "s_memrealtime s[26:27]\n"
            "v_max3_u32 v118, v1, v3, v5\n"
            "v_max_u32 v118, v118, v7\n"
            "v_cmp_eq_u32 vcc, -1, v118\n"
            "s_cbranch_vccnz W0P0_RETRY2_%=\n"
            "ds_write_b128 v110, v[0:3] offset:0\n"
            "ds_write_b128 v110, v[4:7] offset:1024\n"
            "s_waitcnt lgkmcnt(0)\n"
            "s_barrier\n"
            "s_branch W0P0_AFTERB1_%=\n"
            "s_nop 15\n"
            "W0P1_ENTRY_%=:\n"
            "s_mov_b32 s76, 0\n"
            "W0P1_POLL_%=:\n"
            "s_mov_b32 m0, 0\n"
            "s_nop 0\n"
            "global_load_lds_dwordx4 v109, s[52:53] sc1\n"
            "global_load_lds_dwordx4 v109, s[52:53] offset:1024 sc1\n"
            "s_waitcnt vmcnt(0)\n"
            "s_memrealtime s[26:27]\n"
            "s_waitcnt lgkmcnt(0)\n"
            "s_barrier\n"
            "W0P1_AFTERB1_%=:\n"
            "ds_read_b128 v[0:3], v110 offset:0\n"
            "ds_read_b128 v[4:7], v110 offset:1024\n"
            "ds_read_b128 v[8:11], v110 offset:2048\n"
            "ds_read_b128 v[12:15], v110 offset:3072\n"
            "ds_read_b128 v[16:19], v110 offset:4096\n"
            "ds_read_b128 v[20:23], v110 offset:5120\n"
            "ds_read_b128 v[24:27], v110 offset:6144\n"
            "ds_read_b128 v[28:31], v110 offset:7168\n"
            "s_add_u32 s30, s30, 128\n"
            "s_addc_u32 s31, s31, 0\n"
            "s_waitcnt lgkmcnt(7)\n"
            "v_smfmac_f32_16x16x64_bf16 v[32:35], v[0:3], a[0:7], v108\n"
            "v_smfmac_f32_16x16x64_bf16 v[36:39], v[0:3], a[128:135], v108\n"
            "s_waitcnt lgkmcnt(6)\n"
            "v_max_u32 v118, v1, v3\n"
            "v_smfmac_f32_16x16x64_bf16 v[32:35], v[4:7], a[8:15], v108\n"
            "ds_read_b128 v[56:59], v110 offset:16384\n"
            "v_smfmac_f32_16x16x64_bf16 v[36:39], v[4:7], a[136:143], v108\n"
            "s_waitcnt lgkmcnt(6)\n"
            "v_max3_u32 v118, v118, v5, v7\n"
            "v_smfmac_f32_16x16x64_bf16 v[32:35], v[8:11], a[16:23], v108\n"
            "ds_read_b128 v[60:63], v110 offset:17408\n"
            "v_smfmac_f32_16x16x64_bf16 v[36:39], v[8:11], a[144:151], v108\n"
            "s_waitcnt lgkmcnt(6)\n"
            "v_max3_u32 v118, v118, v9, v11\n"
            "v_smfmac_f32_16x16x64_bf16 v[32:35], v[12:15], a[24:31], v108\n"
            "ds_read_b128 v[64:67], v110 offset:18432\n"
            "v_smfmac_f32_16x16x64_bf16 v[36:39], v[12:15], a[152:159], v108\n"
            "s_waitcnt lgkmcnt(6)\n"
            "v_max3_u32 v118, v118, v13, v15\n"
            "v_smfmac_f32_16x16x64_bf16 v[32:35], v[16:19], a[32:39], v108\n"
            "ds_read_b128 v[68:71], v110 offset:19456\n"
            "v_smfmac_f32_16x16x64_bf16 v[36:39], v[16:19], a[160:167], v108\n"
            "s_waitcnt lgkmcnt(6)\n"
            "v_max3_u32 v118, v118, v17, v19\n"
            "v_smfmac_f32_16x16x64_bf16 v[32:35], v[20:23], a[40:47], v108\n"
            "ds_read_b128 v[72:75], v110 offset:20480\n"
            "v_smfmac_f32_16x16x64_bf16 v[36:39], v[20:23], a[168:175], v108\n"
            "s_waitcnt lgkmcnt(6)\n"
            "v_max3_u32 v118, v118, v21, v23\n"
            "v_smfmac_f32_16x16x64_bf16 v[32:35], v[24:27], a[48:55], v108\n"
            "ds_read_b128 v[76:79], v110 offset:21504\n"
            "v_smfmac_f32_16x16x64_bf16 v[36:39], v[24:27], a[176:183], v108\n"
            "s_waitcnt lgkmcnt(6)\n"
            "v_max3_u32 v118, v118, v25, v27\n"
            "v_smfmac_f32_16x16x64_bf16 v[32:35], v[28:31], a[56:63], v108\n"
            "ds_read_b128 v[80:83], v110 offset:22528\n"
            "v_smfmac_f32_16x16x64_bf16 v[36:39], v[28:31], a[184:191], v108\n"
            "v_max3_u32 v118, v118, v29, v31\n"
            "v_smfmac_f32_16x16x64_bf16 v[40:43], v[0:3], a[64:71], v108\n"
            "ds_read_b128 v[84:87], v110 offset:23552\n"
            "v_cmp_eq_u32 vcc, -1, v118\n"
            "v_smfmac_f32_16x16x64_bf16 v[44:47], v[0:3], a[192:199], v108\n"
            "v_smfmac_f32_16x16x64_bf16 v[40:43], v[4:7], a[72:79], v108\n"
            "v_smfmac_f32_16x16x64_bf16 v[44:47], v[4:7], a[200:207], v108\n"
            "s_cbranch_vccnz W0P1_RETRY_%=\n"
            "s_mov_b32 vcc_lo, 0xcccccccc\n"
            "s_mov_b32 vcc_hi, 0xcccccccc\n"
            "s_sub_u32 s26, s26, s32\n"
            "s_sub_u32 s88, s26, -10\n"
            "s_cmp_gt_i32 s26, 42\n"
            "s_cselect_b32 s26, s88, 0\n"
            "s_cselect_b32 s88, s77, 0\n"
            "s_add_u32 s33, s33, s88\n"
            "s_min_u32 s33, s33, 208\n"
            "s_cmp_lg_u32 s88, 0\n"
            "s_cselect_b32 s46, s82, s46\n"
            "s_sub_u32 s46, s46, 1\n"
            "s_cmp_eq_u32 s46, 0\n"
            "s_cselect_b32 s88, 1, 0\n"
            "s_sub_u32 s33, s33, s88\n"
            "s_max_u32 s33, s33, 164\n"
            "s_cmp_eq_u32 s46, 0\n"
            "s_cselect_b32 s46, s81, s46\n"
            "s_add_u32 s26, s26, s32\n"
            "s_add_u32 s26, s26, s33\n"
            "s_min_u32 s26, s26, 0xfffffffe\n"
            "v_smfmac_f32_16x16x64_bf16 v[40:43], v[8:11], a[80:87], v108\n"
            "v_mov_b32 v118, s26\n"
            "v_smfmac_f32_16x16x64_bf16 v[44:47], v[8:11], a[208:215], v108\n"
            "s_add_u32 s88, s68, 1\n"
            "v_mov_b32 v119, s88\n"
            "s_mov_b32 exec_lo, s29\n"
            "s_mov_b32 exec_hi, 0\n"
            "global_store_dword v110, v118, s[30:31] sc1\n"
            "global_store_dword v110, v119, s[22:23] sc1\n"
            "s_mov_b64 exec, -1\n"
            "v_smfmac_f32_16x16x64_bf16 v[40:43], v[12:15], a[88:95], v108\n"
            "v_smfmac_f32_16x16x64_bf16 v[44:47], v[12:15], a[216:223], v108\n"
            "v_add_f32 v36, v36, v37\n"
            "v_smfmac_f32_16x16x64_bf16 v[40:43], v[16:19], a[96:103], v108\n"
            "v_add_f32 v38, v38, v39\n"
            "v_smfmac_f32_16x16x64_bf16 v[44:47], v[16:19], a[224:231], v108\n"
            "v_add_f32 v32, v32, v33\n"
            "v_smfmac_f32_16x16x64_bf16 v[40:43], v[20:23], a[104:111], v108\n"
            "v_add_f32 v34, v34, v35\n"
            "v_smfmac_f32_16x16x64_bf16 v[44:47], v[20:23], a[232:239], v108\n"
            "v_cvt_pk_bf16_f32 v36, v36, v38\n"
            "v_smfmac_f32_16x16x64_bf16 v[40:43], v[24:27], a[112:119], v108\n"
            "v_cvt_pk_bf16_f32 v37, v32, v34\n"
            "v_smfmac_f32_16x16x64_bf16 v[44:47], v[24:27], a[240:247], v108\n"
            "ds_write_b64 v111, v[36:37] offset:0\n"
            "v_smfmac_f32_16x16x64_bf16 v[40:43], v[28:31], a[120:127], v108\n"
            "v_smfmac_f32_16x16x64_bf16 v[44:47], v[28:31], a[248:255], v108\n"
            "v_smfmac_f32_16x16x64_bf16 v[48:51], v[0:3], v[128:135], v108\n"
            "s_nop 5\n"
            "s_nop 0\n"
            "v_add_f32 v44, v44, v45\n"
            "v_add_f32 v46, v46, v47\n"
            "v_add_f32 v40, v40, v41\n"
            "v_add_f32 v42, v42, v43\n"
            "v_cvt_pk_bf16_f32 v44, v44, v46\n"
            "v_cvt_pk_bf16_f32 v45, v40, v42\n"
            "ds_write_b64 v111, v[44:45] offset:512\n"
            "v_smfmac_f32_16x16x64_bf16 v[52:55], v[0:3], v[192:199], v108\n"
            "v_smfmac_f32_16x16x64_bf16 v[48:51], v[4:7], v[136:143], v108\n"
            "v_smfmac_f32_16x16x64_bf16 v[52:55], v[4:7], v[200:207], v108\n"
            "v_smfmac_f32_16x16x64_bf16 v[48:51], v[8:11], v[144:151], v108\n"
            "v_smfmac_f32_16x16x64_bf16 v[52:55], v[8:11], v[208:215], v108\n"
            "s_waitcnt lgkmcnt(0)\n"
            "s_barrier\n"
            "ds_read_b64_tr_b16 v[32:33], v112 offset:0\n"
            "ds_read_b64_tr_b16 v[34:35], v112 offset:512\n"
            "ds_read_b64_tr_b16 v[36:37], v112 offset:1024\n"
            "ds_read_b64_tr_b16 v[38:39], v112 offset:1536\n"
            "ds_read_b64_tr_b16 v[40:41], v112 offset:2048\n"
            "ds_read_b64_tr_b16 v[42:43], v112 offset:2560\n"
            "ds_read_b64_tr_b16 v[44:45], v112 offset:3072\n"
            "ds_read_b64_tr_b16 v[46:47], v112 offset:3584\n"
            "s_mov_b32 m0, 16384\n"
            "s_nop 0\n"
            "global_load_lds_dwordx4 v110, s[18:19] offset:0 sc1\n"
            "global_load_lds_dwordx4 v110, s[18:19] offset:1024 sc1\n"
            "global_load_lds_dwordx4 v110, s[18:19] offset:2048 sc1\n"
            "global_load_lds_dwordx4 v110, s[18:19] offset:3072 sc1\n"
            "v_smfmac_f32_16x16x64_bf16 v[48:51], v[12:15], v[152:159], v108\n"
            "v_smfmac_f32_16x16x64_bf16 v[52:55], v[12:15], v[216:223], v108\n"
            "s_waitcnt lgkmcnt(6)\n"
            "v_mfma_f32_16x16x32_bf16 v[96:99], v[32:35], v[32:35], 0\n"
            "s_waitcnt lgkmcnt(4)\n"
            "v_mfma_f32_16x16x32_bf16 v[96:99], v[36:39], v[36:39], v[96:99]\n"
            "s_waitcnt lgkmcnt(2)\n"
            "v_mfma_f32_16x16x32_bf16 v[96:99], v[40:43], v[40:43], v[96:99]\n"
            "s_waitcnt lgkmcnt(0)\n"
            "v_mfma_f32_16x16x32_bf16 v[96:99], v[44:47], v[44:47], v[96:99]\n"
            "v_mfma_f32_16x16x32_bf16 v[88:91], v[56:59], v[32:35], 0\n"
            "s_mov_b64 exec, 1\n"
            "global_load_dword v117, v110, s[30:31] sc1\n"
            "global_load_dword v60, v110, s[24:25] sc1\n"
            "s_mov_b64 exec, -1\n"
            "v_mov_b32 v12, 0x3f803f80\n"
            "v_mfma_f32_16x16x32_bf16 v[88:91], v[60:63], v[36:39], v[88:91]\n"
            "v_mov_b32 v13, 0x3f803f80\n"
            "v_mov_b32 v14, 0x3f803f80\n"
            "v_mfma_f32_16x16x32_bf16 v[88:91], v[64:67], v[40:43], v[88:91]\n"
            "v_mov_b32 v15, 0x3f803f80\n"
            "v_mov_b32 v6, 0x00003f80\n"
            "v_mfma_f32_16x16x32_bf16 v[88:91], v[68:71], v[44:47], v[88:91]\n"
            "v_mov_b32 v7, 0x00003f80\n"
            "v_mfma_f32_16x16x32_bf16 v[92:95], v[72:75], v[32:35], 0\n"
            "v_cndmask_b32_dpp v96, v97, v96, vcc quad_perm:[2,3,2,3] row_mask:0xf bank_mask:0xf\n"
            "v_mfma_f32_16x16x32_bf16 v[92:95], v[76:79], v[36:39], v[92:95]\n"
            "v_mfma_f32_16x16x32_bf16 v[92:95], v[80:83], v[40:43], v[92:95]\n"
            "v_mfma_f32_16x16x32_bf16 v[92:95], v[84:87], v[44:47], v[92:95]\n"
            "v_smfmac_f32_16x16x64_bf16 v[48:51], v[16:19], v[160:167], v108\n"
            "s_nop 6\n"
            "v_cndmask_b32_dpp v88, v92, v88, vcc quad_perm:[2,3,2,3] row_mask:0xf bank_mask:0xf\n"
            "v_cndmask_b32_dpp v89, v93, v89, vcc quad_perm:[2,3,2,3] row_mask:0xf bank_mask:0xf\n"
            "v_cndmask_b32_dpp v90, v94, v90, vcc quad_perm:[2,3,2,3] row_mask:0xf bank_mask:0xf\n"
            "v_cndmask_b32_dpp v91, v95, v91, vcc quad_perm:[2,3,2,3] row_mask:0xf bank_mask:0xf\n"
            "v_max3_f32 v118, v96, v88, v89\n"
            "v_max3_f32 v118, v118, v90, v91\n"
            "v_smfmac_f32_16x16x64_bf16 v[52:55], v[16:19], v[224:231], v108\n"
            "s_nop 0\n"
            "v_max_f32_dpp v118, v118, v118 quad_perm:[2,3,0,1] row_mask:0xf bank_mask:0xf\n"
            "v_mov_b32 v119, v118\n"
            "v_smfmac_f32_16x16x64_bf16 v[48:51], v[20:23], v[168:175], v108\n"
            "s_nop 0\n"
            "v_permlane32_swap_b32 v118, v119\n"
            "v_max_f32 v118, v118, v119\n"
            "v_mov_b32 v119, v118\n"
            "v_smfmac_f32_16x16x64_bf16 v[52:55], v[20:23], v[232:239], v108\n"
            "s_nop 0\n"
            "v_permlane16_swap_b32 v118, v119\n"
            "v_max_f32 v118, v118, v119\n"
            "v_smfmac_f32_16x16x64_bf16 v[48:51], v[24:27], v[176:183], v108\n"
            "v_mul_f32 v118, s94, v118\n"
            "v_smfmac_f32_16x16x64_bf16 v[52:55], v[24:27], v[240:247], v108\n"
            "v_fma_f32 v88, v88, s94, -v118\n"
            "v_fma_f32 v89, v89, s94, -v118\n"
            "v_fma_f32 v90, v90, s94, -v118\n"
            "v_fma_f32 v91, v91, s94, -v118\n"
            "v_fma_f32 v96, v96, s94, -v118\n"
            "v_smfmac_f32_16x16x64_bf16 v[48:51], v[28:31], v[184:191], v108\n"
            "v_exp_f32 v88, v88\n"
            "v_exp_f32 v89, v89\n"
            "v_exp_f32 v90, v90\n"
            "v_exp_f32 v91, v91\n"
            "v_exp_f32 v96, v96\n"
            "v_smfmac_f32_16x16x64_bf16 v[52:55], v[28:31], v[248:255], v108\n"
            "v_cvt_pk_bf16_f32 v0, v88, v89\n"
            "v_cvt_pk_bf16_f32 v1, v90, v91\n"
            "v_cvt_pk_bf16_f32 v4, v96, v96\n"
            "v_mov_b32_dpp v2, v0 quad_perm:[0,1,0,1] row_mask:0xf bank_mask:0xf\n"
            "v_mov_b32_dpp v3, v1 quad_perm:[0,1,0,1] row_mask:0xf bank_mask:0xf\n"
            "v_mov_b32_dpp v5, v4 quad_perm:[0,1,0,1] row_mask:0xf bank_mask:0xf\n"
            "s_nop 0\n"
            "v_mfma_f32_16x16x32_bf16 v[92:95], v[12:15], v[0:3], 0\n"
            "v_cvt_pk_bf16_f32 v8, v48, v49\n"
            "v_cvt_pk_bf16_f32 v9, v50, v51\n"
            "v_mfma_f32_16x16x32_bf16 v[12:15], v[100:103], v[0:3], 0\n"
            "v_cvt_pk_bf16_f32 v10, v52, v53\n"
            "v_cvt_pk_bf16_f32 v11, v54, v55\n"
            "v_mfma_f32_16x16x16_bf16 v[92:95], v[6:7], v[4:5], v[92:95]\n"
            "v_mfma_f32_16x16x32_bf16 v[88:91], v[104:107], v[0:3], 0\n"
            "v_mfma_f32_16x16x16_bf16 v[12:15], v[8:9], v[4:5], v[12:15]\n"
            "v_mfma_f32_16x16x16_bf16 v[88:91], v[10:11], v[4:5], v[88:91]\n"
            "s_nop 4\n"
            "v_mov_b32_dpp v118, v92 quad_perm:[2,3,2,3] row_mask:0xf bank_mask:0xf\n"
            "v_rcp_f32 v118, v118\n"
            "s_nop 0\n"
            "v_cndmask_b32_dpp v12, v88, v12, vcc quad_perm:[2,3,2,3] row_mask:0xf bank_mask:0xf\n"
            "v_cndmask_b32_dpp v13, v89, v13, vcc quad_perm:[2,3,2,3] row_mask:0xf bank_mask:0xf\n"
            "v_cndmask_b32_dpp v14, v90, v14, vcc quad_perm:[2,3,2,3] row_mask:0xf bank_mask:0xf\n"
            "v_cndmask_b32_dpp v15, v91, v15, vcc quad_perm:[2,3,2,3] row_mask:0xf bank_mask:0xf\n"
            "v_mul_f32 v12, v12, v118\n"
            "v_mul_f32 v13, v13, v118\n"
            "v_mul_f32 v14, v14, v118\n"
            "v_mul_f32 v15, v15, v118\n"
            "v_cvt_pk_bf16_f32 v20, v12, v13\n"
            "v_cvt_pk_bf16_f32 v21, v14, v15\n"
            "v_min_u32 v21, -2, v21\n"
            "global_store_dwordx2 v113, v[20:21], s[56:57] sc1\n"
            "W0P1_SCH_%=:\n"
            "s_waitcnt vmcnt(1)\n"
            "v_readfirstlane_b32 s88, v117\n"
            "v_readfirstlane_b32 s21, v60\n"
            "s_add_u32 s32, s32, s33\n"
            "s_cmp_eq_u32 s88, -1\n"
            "s_cselect_b32 s32, s32, s88\n"
            "s_cselect_b32 s17, 1, 0\n"
            "s_add_u32 s91, s68, 2\n"
            "s_min_u32 s91, s91, 63\n"
            "s_cmp_gt_u32 s21, s91\n"
            "s_cbranch_scc1 W0P1_FKV_SKIP_%=\n"
            "s_mov_b32 s69, 0\n"
            "W0P1_FKV_L_%=:\n"
            "s_mov_b64 exec, 1\n"
            "global_load_dword v118, v110, s[24:25] sc1\n"
            "s_mov_b64 exec, -1\n"
            "s_waitcnt vmcnt(0)\n"
            "v_readfirstlane_b32 s21, v118\n"
            "s_nop 0\n"
            "s_cmp_gt_u32 s21, s91\n"
            "s_cbranch_scc1 W0P1_FKV_SKIP_%=\n"
            "s_add_u32 s69, s69, 1\n"
            "s_cmp_gt_u32 s69, 0x80000\n"
            "s_cbranch_scc1 TIMEOUT_%=\n"
            "s_sleep 2\n"
            "s_branch W0P1_FKV_L_%=\n"
            "W0P1_FKV_SKIP_%=:\n"
            "s_lshr_b32 s78, s91, 2\n"
            "s_xor_b32 s78, s78, s20\n"
            "s_and_b32 s78, s78, 1\n"
            "s_lshl_b32 s78, s78, 13\n"
            "s_add_u32 s18, s48, s78\n"
            "s_addc_u32 s19, s49, 0\n"
            "s_mul_i32 s78, s91, 524288\n"
            "s_add_u32 s18, s18, s78\n"
            "s_addc_u32 s19, s19, 0\n"
            "ds_read_b128 v[0:3], v114 offset:0\n"
            "ds_read_b128 v[4:7], v114 offset:1024\n"
            "ds_read_b128 v[8:11], v114 offset:2048\n"
            "ds_read_b128 v[12:15], v114 offset:3072\n"
            "ds_read_b128 v[16:19], v114 offset:4096\n"
            "ds_read_b128 v[20:23], v114 offset:5120\n"
            "ds_read_b128 v[24:27], v114 offset:6144\n"
            "ds_read_b128 v[28:31], v114 offset:7168\n"
            "ds_read_b128 v[56:59], v110 offset:24576\n"
            "ds_read_b128 v[60:63], v110 offset:25600\n"
            "ds_read_b128 v[64:67], v110 offset:26624\n"
            "ds_read_b128 v[68:71], v110 offset:27648\n"
            "ds_read_b128 v[72:75], v110 offset:28672\n"
            "ds_read_b128 v[76:79], v110 offset:29696\n"
            "ds_read_b128 v[80:83], v110 offset:30720\n"
            "ds_read_b128 v[84:87], v110 offset:31744\n"
            "s_waitcnt lgkmcnt(0)\n"
            "v_mfma_f32_16x16x32_bf16 v[32:35], v[56:59], v[0:3], 0\n"
            "v_mfma_f32_16x16x32_bf16 v[36:39], v[56:59], v[16:19], 0\n"
            "v_mfma_f32_16x16x32_bf16 v[40:43], v[72:75], v[0:3], 0\n"
            "v_mfma_f32_16x16x32_bf16 v[44:47], v[72:75], v[16:19], 0\n"
            "v_mfma_f32_16x16x32_bf16 v[32:35], v[60:63], v[4:7], v[32:35]\n"
            "v_mfma_f32_16x16x32_bf16 v[36:39], v[60:63], v[20:23], v[36:39]\n"
            "v_mfma_f32_16x16x32_bf16 v[40:43], v[76:79], v[4:7], v[40:43]\n"
            "v_mfma_f32_16x16x32_bf16 v[44:47], v[76:79], v[20:23], v[44:47]\n"
            "v_mfma_f32_16x16x32_bf16 v[32:35], v[64:67], v[8:11], v[32:35]\n"
            "v_mfma_f32_16x16x32_bf16 v[36:39], v[64:67], v[24:27], v[36:39]\n"
            "v_mfma_f32_16x16x32_bf16 v[40:43], v[80:83], v[8:11], v[40:43]\n"
            "v_mfma_f32_16x16x32_bf16 v[44:47], v[80:83], v[24:27], v[44:47]\n"
            "v_mfma_f32_16x16x32_bf16 v[32:35], v[68:71], v[12:15], v[32:35]\n"
            "v_mfma_f32_16x16x32_bf16 v[36:39], v[68:71], v[28:31], v[36:39]\n"
            "v_mfma_f32_16x16x32_bf16 v[40:43], v[84:87], v[12:15], v[40:43]\n"
            "v_mfma_f32_16x16x32_bf16 v[44:47], v[84:87], v[28:31], v[44:47]\n"
            "s_nop 4\n"
            "v_cvt_pk_bf16_f32 v100, v32, v33\n"
            "v_cvt_pk_bf16_f32 v101, v34, v35\n"
            "v_cvt_pk_bf16_f32 v102, v40, v41\n"
            "v_cvt_pk_bf16_f32 v103, v42, v43\n"
            "v_cvt_pk_bf16_f32 v104, v36, v37\n"
            "v_cvt_pk_bf16_f32 v105, v38, v39\n"
            "v_cvt_pk_bf16_f32 v106, v44, v45\n"
            "v_cvt_pk_bf16_f32 v107, v46, v47\n"
            "W0P1_ZDONE_%=:\n"
            "s_nop 7\n"
            "v_mov_b32 v32, 0\n"
            "v_mov_b32 v33, 0\n"
            "v_mov_b32 v34, 0\n"
            "v_mov_b32 v35, 0\n"
            "v_mov_b32 v36, 0\n"
            "v_mov_b32 v37, 0\n"
            "v_mov_b32 v38, 0\n"
            "v_mov_b32 v39, 0\n"
            "v_mov_b32 v40, 0\n"
            "v_mov_b32 v41, 0\n"
            "v_mov_b32 v42, 0\n"
            "v_mov_b32 v43, 0\n"
            "v_mov_b32 v44, 0\n"
            "v_mov_b32 v45, 0\n"
            "v_mov_b32 v46, 0\n"
            "v_mov_b32 v47, 0\n"
            "v_mov_b32 v48, 0\n"
            "v_mov_b32 v49, 0\n"
            "v_mov_b32 v50, 0\n"
            "v_mov_b32 v51, 0\n"
            "v_mov_b32 v52, 0\n"
            "v_mov_b32 v53, 0\n"
            "v_mov_b32 v54, 0\n"
            "v_mov_b32 v55, 0\n"
            "s_add_u32 s52, s52, 16384\n"
            "s_addc_u32 s53, s53, 0\n"
            "s_add_u32 s68, s68, 1\n"
            "s_cmp_ge_u32 s68, 63\n"
            "s_cbranch_scc1 DONE_%=\n"
            "s_memrealtime s[26:27]\n"
            "s_waitcnt lgkmcnt(0)\n"
            "s_mov_b32 s45, s26\n"
            "W0P1_CK_E_%=:\n"
            "s_memrealtime s[26:27]\n"
            "s_memtime s[78:79]\n"
            "s_waitcnt lgkmcnt(0)\n"
            "s_cmp_eq_u32 s26, s45\n"
            "s_cbranch_scc1 W0P1_CK_E_%=\n"
            "s_mov_b32 s40, s78\n"
            "s_mov_b32 s41, s26\n"
            "s_sub_u32 s78, s40, s43\n"
            "s_sub_u32 s79, s41, s44\n"
            "v_cvt_f32_u32 v118, s78\n"
            "v_cvt_f32_u32 v119, s79\n"
            "v_rcp_f32 v119, v119\n"
            "v_mul_f32 v118, 0x43800000, v118\n"
            "v_mul_f32 v118, v118, v119\n"
            "v_cvt_u32_f32 v118, v118\n"
            "s_nop 0\n"
            "v_readfirstlane_b32 s78, v118\n"
            "s_nop 0\n"
            "s_sub_i32 s78, s78, s42\n"
            "s_ashr_i32 s78, s78, 3\n"
            "s_add_i32 s42, s42, s78\n"
            "v_mov_b32 v118, 45056\n"
            "v_mov_b32 v2, s40\n"
            "v_mov_b32 v3, s41\n"
            "v_mov_b32 v4, s42\n"
            "ds_write_b64 v118, v[2:3]\n"
            "ds_write_b32 v118, v4 offset:8\n"
            "s_mov_b32 s43, s40\n"
            "s_mov_b32 s44, s41\n"
            "s_cmp_eq_u32 s17, 0\n"
            "s_cbranch_scc0 W0P1_SCHRETRY_%=\n"
            "W0P1_SCHOK_%=:\n"
            "s_sub_u32 s45, s32, s41\n"
            "s_mul_i32 s45, s45, s42\n"
            "s_ashr_i32 s45, s45, 8\n"
            "s_add_u32 s45, s45, s40\n"
            "s_memtime s[78:79]\n"
            "s_waitcnt lgkmcnt(0)\n"
            "s_sub_u32 s45, s45, s78\n"
            "s_sub_u32 s45, s45, 60\n"
            "W0P1_W2_D_%=:\n"
            "s_sub_u32 s45, s45, 32\n"
            "s_cmp_gt_i32 s45, 0\n"
            "s_cbranch_scc1 W0P1_W2_D_%=\n"
            "s_waitcnt lgkmcnt(0)\n"
            "s_barrier\n"
            "s_branch W0P0_ENTRY_%=\n"
            "W0P1_SCHRETRY_%=:\n"
            "s_add_u32 s69, s69, 1\n"
            "s_cmp_gt_u32 s69, 0x400000\n"
            "s_cbranch_scc1 TIMEOUT_%=\n"
            "s_mov_b64 exec, 1\n"
            "global_load_dword v117, v110, s[30:31] sc1\n"
            "s_mov_b64 exec, -1\n"
            "s_waitcnt vmcnt(0)\n"
            "v_readfirstlane_b32 s88, v117\n"
            "s_nop 0\n"
            "s_cmp_eq_u32 s88, -1\n"
            "s_cbranch_scc1 W0P1_SCHRETRY_%=\n"
            "s_mov_b32 s32, s88\n"
            "s_branch W0P1_SCHOK_%=\n"
            "W0P1_RETRY_%=:\n"
            "s_waitcnt lgkmcnt(0)\n"
            "s_barrier\n"
            "s_nop 7\n"
            "v_mov_b32 v32, 0\n"
            "v_mov_b32 v33, 0\n"
            "v_mov_b32 v34, 0\n"
            "v_mov_b32 v35, 0\n"
            "v_mov_b32 v36, 0\n"
            "v_mov_b32 v37, 0\n"
            "v_mov_b32 v38, 0\n"
            "v_mov_b32 v39, 0\n"
            "v_mov_b32 v40, 0\n"
            "v_mov_b32 v41, 0\n"
            "v_mov_b32 v42, 0\n"
            "v_mov_b32 v43, 0\n"
            "v_mov_b32 v44, 0\n"
            "v_mov_b32 v45, 0\n"
            "v_mov_b32 v46, 0\n"
            "v_mov_b32 v47, 0\n"
            "s_sub_u32 s30, s30, 128\n"
            "s_subb_u32 s31, s31, 0\n"
            "W0P1_RETRY2_%=:\n"
            "s_add_u32 s76, s76, 1\n"
            "s_add_u32 s84, s84, 1\n"
            "s_cmp_gt_u32 s76, 0x20000\n"
            "s_cbranch_scc1 TIMEOUT_%=\n"
            "global_load_dwordx4 v[0:3], v109, s[52:53] sc1\n"
            "global_load_dwordx4 v[4:7], v109, s[52:53] offset:1024 sc1\n"
            "s_waitcnt vmcnt(0)\n"
            "s_memrealtime s[26:27]\n"
            "v_max3_u32 v118, v1, v3, v5\n"
            "v_max_u32 v118, v118, v7\n"
            "v_cmp_eq_u32 vcc, -1, v118\n"
            "s_cbranch_vccnz W0P1_RETRY2_%=\n"
            "ds_write_b128 v110, v[0:3] offset:0\n"
            "ds_write_b128 v110, v[4:7] offset:1024\n"
            "s_waitcnt lgkmcnt(0)\n"
            "s_barrier\n"
            "s_branch W0P1_AFTERB1_%=\n"
            "s_nop 15\n"
            "W1P0_ENTRY_%=:\n"
            "s_mov_b32 s76, 0\n"
            "W1P0_POLL_%=:\n"
            "s_mov_b32 m0, 2048\n"
            "s_nop 0\n"
            "global_load_lds_dwordx4 v109, s[52:53] sc1\n"
            "global_load_lds_dwordx4 v109, s[52:53] offset:1024 sc1\n"
            "s_waitcnt vmcnt(0)\n"
            "s_waitcnt lgkmcnt(0)\n"
            "s_barrier\n"
            "W1P0_AFTERB1_%=:\n"
            "ds_read_b128 v[0:3], v110 offset:0\n"
            "ds_read_b128 v[4:7], v110 offset:1024\n"
            "ds_read_b128 v[8:11], v110 offset:2048\n"
            "ds_read_b128 v[12:15], v110 offset:3072\n"
            "ds_read_b128 v[16:19], v110 offset:4096\n"
            "ds_read_b128 v[20:23], v110 offset:5120\n"
            "ds_read_b128 v[24:27], v110 offset:6144\n"
            "ds_read_b128 v[28:31], v110 offset:7168\n"
            "s_add_u32 s30, s30, 128\n"
            "s_addc_u32 s31, s31, 0\n"
            "s_waitcnt lgkmcnt(7)\n"
            "v_smfmac_f32_16x16x64_bf16 v[32:35], v[0:3], a[0:7], v108\n"
            "v_smfmac_f32_16x16x64_bf16 v[36:39], v[0:3], a[128:135], v108\n"
            "s_waitcnt lgkmcnt(6)\n"
            "v_max_u32 v118, v1, v3\n"
            "v_smfmac_f32_16x16x64_bf16 v[32:35], v[4:7], a[8:15], v108\n"
            "ds_read_b128 v[56:59], v110 offset:16384\n"
            "v_smfmac_f32_16x16x64_bf16 v[36:39], v[4:7], a[136:143], v108\n"
            "s_waitcnt lgkmcnt(6)\n"
            "v_max3_u32 v118, v118, v5, v7\n"
            "v_smfmac_f32_16x16x64_bf16 v[32:35], v[8:11], a[16:23], v108\n"
            "ds_read_b128 v[60:63], v110 offset:17408\n"
            "v_smfmac_f32_16x16x64_bf16 v[36:39], v[8:11], a[144:151], v108\n"
            "s_waitcnt lgkmcnt(6)\n"
            "v_max3_u32 v118, v118, v9, v11\n"
            "v_smfmac_f32_16x16x64_bf16 v[32:35], v[12:15], a[24:31], v108\n"
            "ds_read_b128 v[64:67], v110 offset:18432\n"
            "v_smfmac_f32_16x16x64_bf16 v[36:39], v[12:15], a[152:159], v108\n"
            "s_waitcnt lgkmcnt(6)\n"
            "v_max3_u32 v118, v118, v13, v15\n"
            "v_smfmac_f32_16x16x64_bf16 v[32:35], v[16:19], a[32:39], v108\n"
            "ds_read_b128 v[68:71], v110 offset:19456\n"
            "v_smfmac_f32_16x16x64_bf16 v[36:39], v[16:19], a[160:167], v108\n"
            "s_waitcnt lgkmcnt(6)\n"
            "v_max3_u32 v118, v118, v17, v19\n"
            "v_smfmac_f32_16x16x64_bf16 v[32:35], v[20:23], a[40:47], v108\n"
            "ds_read_b128 v[72:75], v110 offset:20480\n"
            "v_smfmac_f32_16x16x64_bf16 v[36:39], v[20:23], a[168:175], v108\n"
            "s_waitcnt lgkmcnt(6)\n"
            "v_max3_u32 v118, v118, v21, v23\n"
            "v_smfmac_f32_16x16x64_bf16 v[32:35], v[24:27], a[48:55], v108\n"
            "ds_read_b128 v[76:79], v110 offset:21504\n"
            "v_smfmac_f32_16x16x64_bf16 v[36:39], v[24:27], a[176:183], v108\n"
            "s_waitcnt lgkmcnt(6)\n"
            "v_max3_u32 v118, v118, v25, v27\n"
            "v_smfmac_f32_16x16x64_bf16 v[32:35], v[28:31], a[56:63], v108\n"
            "ds_read_b128 v[80:83], v110 offset:22528\n"
            "v_smfmac_f32_16x16x64_bf16 v[36:39], v[28:31], a[184:191], v108\n"
            "v_max3_u32 v118, v118, v29, v31\n"
            "v_smfmac_f32_16x16x64_bf16 v[40:43], v[0:3], a[64:71], v108\n"
            "ds_read_b128 v[84:87], v110 offset:23552\n"
            "v_cmp_eq_u32 vcc, -1, v118\n"
            "v_smfmac_f32_16x16x64_bf16 v[44:47], v[0:3], a[192:199], v108\n"
            "v_smfmac_f32_16x16x64_bf16 v[40:43], v[4:7], a[72:79], v108\n"
            "v_smfmac_f32_16x16x64_bf16 v[44:47], v[4:7], a[200:207], v108\n"
            "s_cbranch_vccnz W1P0_RETRY_%=\n"
            "s_mov_b32 vcc_lo, 0xcccccccc\n"
            "s_mov_b32 vcc_hi, 0xcccccccc\n"
            "v_smfmac_f32_16x16x64_bf16 v[40:43], v[8:11], a[80:87], v108\n"
            "v_smfmac_f32_16x16x64_bf16 v[44:47], v[8:11], a[208:215], v108\n"
            "v_smfmac_f32_16x16x64_bf16 v[40:43], v[12:15], a[88:95], v108\n"
            "v_smfmac_f32_16x16x64_bf16 v[44:47], v[12:15], a[216:223], v108\n"
            "v_add_f32 v36, v36, v37\n"
            "v_smfmac_f32_16x16x64_bf16 v[40:43], v[16:19], a[96:103], v108\n"
            "v_add_f32 v38, v38, v39\n"
            "v_smfmac_f32_16x16x64_bf16 v[44:47], v[16:19], a[224:231], v108\n"
            "v_add_f32 v32, v32, v33\n"
            "v_smfmac_f32_16x16x64_bf16 v[40:43], v[20:23], a[104:111], v108\n"
            "v_add_f32 v34, v34, v35\n"
            "v_smfmac_f32_16x16x64_bf16 v[44:47], v[20:23], a[232:239], v108\n"
            "v_cvt_pk_bf16_f32 v36, v36, v38\n"
            "v_smfmac_f32_16x16x64_bf16 v[40:43], v[24:27], a[112:119], v108\n"
            "v_cvt_pk_bf16_f32 v37, v32, v34\n"
            "v_smfmac_f32_16x16x64_bf16 v[44:47], v[24:27], a[240:247], v108\n"
            "ds_write_b64 v111, v[36:37] offset:0\n"
            "v_smfmac_f32_16x16x64_bf16 v[40:43], v[28:31], a[120:127], v108\n"
            "v_smfmac_f32_16x16x64_bf16 v[44:47], v[28:31], a[248:255], v108\n"
            "v_smfmac_f32_16x16x64_bf16 v[48:51], v[0:3], v[128:135], v108\n"
            "s_nop 5\n"
            "s_nop 0\n"
            "v_add_f32 v44, v44, v45\n"
            "v_add_f32 v46, v46, v47\n"
            "v_add_f32 v40, v40, v41\n"
            "v_add_f32 v42, v42, v43\n"
            "v_cvt_pk_bf16_f32 v44, v44, v46\n"
            "v_cvt_pk_bf16_f32 v45, v40, v42\n"
            "ds_write_b64 v111, v[44:45] offset:512\n"
            "v_smfmac_f32_16x16x64_bf16 v[52:55], v[0:3], v[192:199], v108\n"
            "v_smfmac_f32_16x16x64_bf16 v[48:51], v[4:7], v[136:143], v108\n"
            "v_smfmac_f32_16x16x64_bf16 v[52:55], v[4:7], v[200:207], v108\n"
            "v_smfmac_f32_16x16x64_bf16 v[48:51], v[8:11], v[144:151], v108\n"
            "v_smfmac_f32_16x16x64_bf16 v[52:55], v[8:11], v[208:215], v108\n"
            "s_waitcnt lgkmcnt(0)\n"
            "s_barrier\n"
            "ds_read_b64_tr_b16 v[32:33], v112 offset:0\n"
            "ds_read_b64_tr_b16 v[34:35], v112 offset:512\n"
            "ds_read_b64_tr_b16 v[36:37], v112 offset:1024\n"
            "ds_read_b64_tr_b16 v[38:39], v112 offset:1536\n"
            "ds_read_b64_tr_b16 v[40:41], v112 offset:2048\n"
            "ds_read_b64_tr_b16 v[42:43], v112 offset:2560\n"
            "ds_read_b64_tr_b16 v[44:45], v112 offset:3072\n"
            "ds_read_b64_tr_b16 v[46:47], v112 offset:3584\n"
            "s_mov_b32 m0, 20480\n"
            "s_nop 0\n"
            "global_load_lds_dwordx4 v110, s[18:19] offset:0 sc1\n"
            "global_load_lds_dwordx4 v110, s[18:19] offset:1024 sc1\n"
            "global_load_lds_dwordx4 v110, s[18:19] offset:2048 sc1\n"
            "global_load_lds_dwordx4 v110, s[18:19] offset:3072 sc1\n"
            "v_smfmac_f32_16x16x64_bf16 v[48:51], v[12:15], v[152:159], v108\n"
            "v_smfmac_f32_16x16x64_bf16 v[52:55], v[12:15], v[216:223], v108\n"
            "s_waitcnt lgkmcnt(6)\n"
            "v_mfma_f32_16x16x32_bf16 v[96:99], v[32:35], v[32:35], 0\n"
            "s_waitcnt lgkmcnt(4)\n"
            "v_mfma_f32_16x16x32_bf16 v[96:99], v[36:39], v[36:39], v[96:99]\n"
            "s_waitcnt lgkmcnt(2)\n"
            "v_mfma_f32_16x16x32_bf16 v[96:99], v[40:43], v[40:43], v[96:99]\n"
            "s_waitcnt lgkmcnt(0)\n"
            "v_mfma_f32_16x16x32_bf16 v[96:99], v[44:47], v[44:47], v[96:99]\n"
            "v_mfma_f32_16x16x32_bf16 v[88:91], v[56:59], v[32:35], 0\n"
            "s_mov_b64 exec, 1\n"
            "global_load_dword v117, v110, s[30:31] sc1\n"
            "global_load_dword v60, v110, s[24:25] sc1\n"
            "s_mov_b64 exec, -1\n"
            "v_mov_b32 v12, 0x3f803f80\n"
            "v_mfma_f32_16x16x32_bf16 v[88:91], v[60:63], v[36:39], v[88:91]\n"
            "v_mov_b32 v13, 0x3f803f80\n"
            "v_mov_b32 v14, 0x3f803f80\n"
            "v_mfma_f32_16x16x32_bf16 v[88:91], v[64:67], v[40:43], v[88:91]\n"
            "v_mov_b32 v15, 0x3f803f80\n"
            "v_mov_b32 v6, 0x00003f80\n"
            "v_mfma_f32_16x16x32_bf16 v[88:91], v[68:71], v[44:47], v[88:91]\n"
            "v_mov_b32 v7, 0x00003f80\n"
            "v_mfma_f32_16x16x32_bf16 v[92:95], v[72:75], v[32:35], 0\n"
            "v_cndmask_b32_dpp v96, v97, v96, vcc quad_perm:[2,3,2,3] row_mask:0xf bank_mask:0xf\n"
            "v_mfma_f32_16x16x32_bf16 v[92:95], v[76:79], v[36:39], v[92:95]\n"
            "v_mfma_f32_16x16x32_bf16 v[92:95], v[80:83], v[40:43], v[92:95]\n"
            "v_mfma_f32_16x16x32_bf16 v[92:95], v[84:87], v[44:47], v[92:95]\n"
            "v_smfmac_f32_16x16x64_bf16 v[48:51], v[16:19], v[160:167], v108\n"
            "s_nop 6\n"
            "v_cndmask_b32_dpp v88, v92, v88, vcc quad_perm:[2,3,2,3] row_mask:0xf bank_mask:0xf\n"
            "v_cndmask_b32_dpp v89, v93, v89, vcc quad_perm:[2,3,2,3] row_mask:0xf bank_mask:0xf\n"
            "v_cndmask_b32_dpp v90, v94, v90, vcc quad_perm:[2,3,2,3] row_mask:0xf bank_mask:0xf\n"
            "v_cndmask_b32_dpp v91, v95, v91, vcc quad_perm:[2,3,2,3] row_mask:0xf bank_mask:0xf\n"
            "v_max3_f32 v118, v96, v88, v89\n"
            "v_max3_f32 v118, v118, v90, v91\n"
            "v_smfmac_f32_16x16x64_bf16 v[52:55], v[16:19], v[224:231], v108\n"
            "s_nop 0\n"
            "v_max_f32_dpp v118, v118, v118 quad_perm:[2,3,0,1] row_mask:0xf bank_mask:0xf\n"
            "v_mov_b32 v119, v118\n"
            "v_smfmac_f32_16x16x64_bf16 v[48:51], v[20:23], v[168:175], v108\n"
            "s_nop 0\n"
            "v_permlane32_swap_b32 v118, v119\n"
            "v_max_f32 v118, v118, v119\n"
            "v_mov_b32 v119, v118\n"
            "v_smfmac_f32_16x16x64_bf16 v[52:55], v[20:23], v[232:239], v108\n"
            "s_nop 0\n"
            "v_permlane16_swap_b32 v118, v119\n"
            "v_max_f32 v118, v118, v119\n"
            "v_smfmac_f32_16x16x64_bf16 v[48:51], v[24:27], v[176:183], v108\n"
            "v_mul_f32 v118, s94, v118\n"
            "v_smfmac_f32_16x16x64_bf16 v[52:55], v[24:27], v[240:247], v108\n"
            "v_fma_f32 v88, v88, s94, -v118\n"
            "v_fma_f32 v89, v89, s94, -v118\n"
            "v_fma_f32 v90, v90, s94, -v118\n"
            "v_fma_f32 v91, v91, s94, -v118\n"
            "v_fma_f32 v96, v96, s94, -v118\n"
            "v_smfmac_f32_16x16x64_bf16 v[48:51], v[28:31], v[184:191], v108\n"
            "v_exp_f32 v88, v88\n"
            "v_exp_f32 v89, v89\n"
            "v_exp_f32 v90, v90\n"
            "v_exp_f32 v91, v91\n"
            "v_exp_f32 v96, v96\n"
            "v_smfmac_f32_16x16x64_bf16 v[52:55], v[28:31], v[248:255], v108\n"
            "v_cvt_pk_bf16_f32 v0, v88, v89\n"
            "v_cvt_pk_bf16_f32 v1, v90, v91\n"
            "v_cvt_pk_bf16_f32 v4, v96, v96\n"
            "v_mov_b32_dpp v2, v0 quad_perm:[0,1,0,1] row_mask:0xf bank_mask:0xf\n"
            "v_mov_b32_dpp v3, v1 quad_perm:[0,1,0,1] row_mask:0xf bank_mask:0xf\n"
            "v_mov_b32_dpp v5, v4 quad_perm:[0,1,0,1] row_mask:0xf bank_mask:0xf\n"
            "s_nop 0\n"
            "v_mfma_f32_16x16x32_bf16 v[92:95], v[12:15], v[0:3], 0\n"
            "v_cvt_pk_bf16_f32 v8, v48, v49\n"
            "v_cvt_pk_bf16_f32 v9, v50, v51\n"
            "v_mfma_f32_16x16x32_bf16 v[12:15], v[100:103], v[0:3], 0\n"
            "v_cvt_pk_bf16_f32 v10, v52, v53\n"
            "v_cvt_pk_bf16_f32 v11, v54, v55\n"
            "v_mfma_f32_16x16x16_bf16 v[92:95], v[6:7], v[4:5], v[92:95]\n"
            "v_mfma_f32_16x16x32_bf16 v[88:91], v[104:107], v[0:3], 0\n"
            "v_mfma_f32_16x16x16_bf16 v[12:15], v[8:9], v[4:5], v[12:15]\n"
            "v_mfma_f32_16x16x16_bf16 v[88:91], v[10:11], v[4:5], v[88:91]\n"
            "s_nop 4\n"
            "v_mov_b32_dpp v118, v92 quad_perm:[2,3,2,3] row_mask:0xf bank_mask:0xf\n"
            "v_rcp_f32 v118, v118\n"
            "s_nop 0\n"
            "v_cndmask_b32_dpp v12, v88, v12, vcc quad_perm:[2,3,2,3] row_mask:0xf bank_mask:0xf\n"
            "v_cndmask_b32_dpp v13, v89, v13, vcc quad_perm:[2,3,2,3] row_mask:0xf bank_mask:0xf\n"
            "v_cndmask_b32_dpp v14, v90, v14, vcc quad_perm:[2,3,2,3] row_mask:0xf bank_mask:0xf\n"
            "v_cndmask_b32_dpp v15, v91, v15, vcc quad_perm:[2,3,2,3] row_mask:0xf bank_mask:0xf\n"
            "v_mul_f32 v12, v12, v118\n"
            "v_mul_f32 v13, v13, v118\n"
            "v_mul_f32 v14, v14, v118\n"
            "v_mul_f32 v15, v15, v118\n"
            "v_cvt_pk_bf16_f32 v20, v12, v13\n"
            "v_cvt_pk_bf16_f32 v21, v14, v15\n"
            "v_min_u32 v21, -2, v21\n"
            "global_store_dwordx2 v113, v[20:21], s[54:55] sc1\n"
            "W1P0_SCH_%=:\n"
            "s_waitcnt vmcnt(1)\n"
            "v_readfirstlane_b32 s88, v117\n"
            "v_readfirstlane_b32 s21, v60\n"
            "s_sub_u32 s78, s88, s32\n"
            "s_cmp_le_u32 s78, 208\n"
            "s_cselect_b32 s78, s78, s33\n"
            "s_add_u32 s32, s32, s33\n"
            "s_cmp_eq_u32 s88, -1\n"
            "s_cselect_b32 s32, s32, s88\n"
            "s_cselect_b32 s33, s33, s78\n"
            "s_add_u32 s91, s68, 2\n"
            "s_min_u32 s91, s91, 63\n"
            "s_cmp_gt_u32 s21, s91\n"
            "s_cbranch_scc1 W1P0_FKV_SKIP_%=\n"
            "s_mov_b32 s69, 0\n"
            "W1P0_FKV_L_%=:\n"
            "s_mov_b64 exec, 1\n"
            "global_load_dword v118, v110, s[24:25] sc1\n"
            "s_mov_b64 exec, -1\n"
            "s_waitcnt vmcnt(0)\n"
            "v_readfirstlane_b32 s21, v118\n"
            "s_nop 0\n"
            "s_cmp_gt_u32 s21, s91\n"
            "s_cbranch_scc1 W1P0_FKV_SKIP_%=\n"
            "s_add_u32 s69, s69, 1\n"
            "s_cmp_gt_u32 s69, 0x80000\n"
            "s_cbranch_scc1 TIMEOUT_%=\n"
            "s_sleep 2\n"
            "s_branch W1P0_FKV_L_%=\n"
            "W1P0_FKV_SKIP_%=:\n"
            "s_lshr_b32 s78, s91, 2\n"
            "s_xor_b32 s78, s78, s20\n"
            "s_and_b32 s78, s78, 1\n"
            "s_lshl_b32 s78, s78, 13\n"
            "s_add_u32 s18, s48, s78\n"
            "s_addc_u32 s19, s49, 0\n"
            "s_mul_i32 s78, s91, 524288\n"
            "s_add_u32 s18, s18, s78\n"
            "s_addc_u32 s19, s19, 0\n"
            "s_add_u32 s18, s18, 4096\n"
            "s_addc_u32 s19, s19, 0\n"
            "ds_read_b128 v[0:3], v114 offset:0\n"
            "ds_read_b128 v[4:7], v114 offset:1024\n"
            "ds_read_b128 v[8:11], v114 offset:2048\n"
            "ds_read_b128 v[12:15], v114 offset:3072\n"
            "ds_read_b128 v[16:19], v114 offset:4096\n"
            "ds_read_b128 v[20:23], v114 offset:5120\n"
            "ds_read_b128 v[24:27], v114 offset:6144\n"
            "ds_read_b128 v[28:31], v114 offset:7168\n"
            "ds_read_b128 v[56:59], v110 offset:32768\n"
            "ds_read_b128 v[60:63], v110 offset:33792\n"
            "ds_read_b128 v[64:67], v110 offset:34816\n"
            "ds_read_b128 v[68:71], v110 offset:35840\n"
            "ds_read_b128 v[72:75], v110 offset:36864\n"
            "ds_read_b128 v[76:79], v110 offset:37888\n"
            "ds_read_b128 v[80:83], v110 offset:38912\n"
            "ds_read_b128 v[84:87], v110 offset:39936\n"
            "s_waitcnt lgkmcnt(0)\n"
            "v_mfma_f32_16x16x32_bf16 v[32:35], v[56:59], v[0:3], 0\n"
            "v_mfma_f32_16x16x32_bf16 v[36:39], v[56:59], v[16:19], 0\n"
            "v_mfma_f32_16x16x32_bf16 v[40:43], v[72:75], v[0:3], 0\n"
            "v_mfma_f32_16x16x32_bf16 v[44:47], v[72:75], v[16:19], 0\n"
            "v_mfma_f32_16x16x32_bf16 v[32:35], v[60:63], v[4:7], v[32:35]\n"
            "v_mfma_f32_16x16x32_bf16 v[36:39], v[60:63], v[20:23], v[36:39]\n"
            "v_mfma_f32_16x16x32_bf16 v[40:43], v[76:79], v[4:7], v[40:43]\n"
            "v_mfma_f32_16x16x32_bf16 v[44:47], v[76:79], v[20:23], v[44:47]\n"
            "v_mfma_f32_16x16x32_bf16 v[32:35], v[64:67], v[8:11], v[32:35]\n"
            "v_mfma_f32_16x16x32_bf16 v[36:39], v[64:67], v[24:27], v[36:39]\n"
            "v_mfma_f32_16x16x32_bf16 v[40:43], v[80:83], v[8:11], v[40:43]\n"
            "v_mfma_f32_16x16x32_bf16 v[44:47], v[80:83], v[24:27], v[44:47]\n"
            "v_mfma_f32_16x16x32_bf16 v[32:35], v[68:71], v[12:15], v[32:35]\n"
            "v_mfma_f32_16x16x32_bf16 v[36:39], v[68:71], v[28:31], v[36:39]\n"
            "v_mfma_f32_16x16x32_bf16 v[40:43], v[84:87], v[12:15], v[40:43]\n"
            "v_mfma_f32_16x16x32_bf16 v[44:47], v[84:87], v[28:31], v[44:47]\n"
            "s_nop 4\n"
            "v_cvt_pk_bf16_f32 v100, v32, v33\n"
            "v_cvt_pk_bf16_f32 v101, v34, v35\n"
            "v_cvt_pk_bf16_f32 v102, v40, v41\n"
            "v_cvt_pk_bf16_f32 v103, v42, v43\n"
            "v_cvt_pk_bf16_f32 v104, v36, v37\n"
            "v_cvt_pk_bf16_f32 v105, v38, v39\n"
            "v_cvt_pk_bf16_f32 v106, v44, v45\n"
            "v_cvt_pk_bf16_f32 v107, v46, v47\n"
            "W1P0_ZDONE_%=:\n"
            "s_nop 7\n"
            "v_mov_b32 v32, 0\n"
            "v_mov_b32 v33, 0\n"
            "v_mov_b32 v34, 0\n"
            "v_mov_b32 v35, 0\n"
            "v_mov_b32 v36, 0\n"
            "v_mov_b32 v37, 0\n"
            "v_mov_b32 v38, 0\n"
            "v_mov_b32 v39, 0\n"
            "v_mov_b32 v40, 0\n"
            "v_mov_b32 v41, 0\n"
            "v_mov_b32 v42, 0\n"
            "v_mov_b32 v43, 0\n"
            "v_mov_b32 v44, 0\n"
            "v_mov_b32 v45, 0\n"
            "v_mov_b32 v46, 0\n"
            "v_mov_b32 v47, 0\n"
            "v_mov_b32 v48, 0\n"
            "v_mov_b32 v49, 0\n"
            "v_mov_b32 v50, 0\n"
            "v_mov_b32 v51, 0\n"
            "v_mov_b32 v52, 0\n"
            "v_mov_b32 v53, 0\n"
            "v_mov_b32 v54, 0\n"
            "v_mov_b32 v55, 0\n"
            "s_add_u32 s52, s52, 16384\n"
            "s_addc_u32 s53, s53, 0\n"
            "s_add_u32 s68, s68, 1\n"
            "s_cmp_ge_u32 s68, 63\n"
            "s_cbranch_scc1 DONE_%=\n"
            "s_barrier\n"
            "s_branch W1P1_ENTRY_%=\n"
            "W1P0_RETRY_%=:\n"
            "s_waitcnt lgkmcnt(0)\n"
            "s_barrier\n"
            "s_nop 7\n"
            "v_mov_b32 v32, 0\n"
            "v_mov_b32 v33, 0\n"
            "v_mov_b32 v34, 0\n"
            "v_mov_b32 v35, 0\n"
            "v_mov_b32 v36, 0\n"
            "v_mov_b32 v37, 0\n"
            "v_mov_b32 v38, 0\n"
            "v_mov_b32 v39, 0\n"
            "v_mov_b32 v40, 0\n"
            "v_mov_b32 v41, 0\n"
            "v_mov_b32 v42, 0\n"
            "v_mov_b32 v43, 0\n"
            "v_mov_b32 v44, 0\n"
            "v_mov_b32 v45, 0\n"
            "v_mov_b32 v46, 0\n"
            "v_mov_b32 v47, 0\n"
            "s_sub_u32 s30, s30, 128\n"
            "s_subb_u32 s31, s31, 0\n"
            "W1P0_RETRY2_%=:\n"
            "s_add_u32 s76, s76, 1\n"
            "s_add_u32 s84, s84, 1\n"
            "s_cmp_gt_u32 s76, 0x20000\n"
            "s_cbranch_scc1 TIMEOUT_%=\n"
            "global_load_dwordx4 v[8:11], v109, s[52:53] sc1\n"
            "global_load_dwordx4 v[12:15], v109, s[52:53] offset:1024 sc1\n"
            "s_waitcnt vmcnt(0)\n"
            "v_max3_u32 v118, v9, v11, v13\n"
            "v_max_u32 v118, v118, v15\n"
            "v_cmp_eq_u32 vcc, -1, v118\n"
            "s_cbranch_vccnz W1P0_RETRY2_%=\n"
            "ds_write_b128 v110, v[8:11] offset:2048\n"
            "ds_write_b128 v110, v[12:15] offset:3072\n"
            "s_waitcnt lgkmcnt(0)\n"
            "s_barrier\n"
            "s_branch W1P0_AFTERB1_%=\n"
            "s_nop 15\n"
            "W1P1_ENTRY_%=:\n"
            "s_mov_b32 s76, 0\n"
            "W1P1_POLL_%=:\n"
            "s_mov_b32 m0, 2048\n"
            "s_nop 0\n"
            "global_load_lds_dwordx4 v109, s[52:53] sc1\n"
            "global_load_lds_dwordx4 v109, s[52:53] offset:1024 sc1\n"
            "s_waitcnt vmcnt(0)\n"
            "s_waitcnt lgkmcnt(0)\n"
            "s_barrier\n"
            "W1P1_AFTERB1_%=:\n"
            "ds_read_b128 v[0:3], v110 offset:0\n"
            "ds_read_b128 v[4:7], v110 offset:1024\n"
            "ds_read_b128 v[8:11], v110 offset:2048\n"
            "ds_read_b128 v[12:15], v110 offset:3072\n"
            "ds_read_b128 v[16:19], v110 offset:4096\n"
            "ds_read_b128 v[20:23], v110 offset:5120\n"
            "ds_read_b128 v[24:27], v110 offset:6144\n"
            "ds_read_b128 v[28:31], v110 offset:7168\n"
            "s_add_u32 s30, s30, 128\n"
            "s_addc_u32 s31, s31, 0\n"
            "s_waitcnt lgkmcnt(7)\n"
            "v_smfmac_f32_16x16x64_bf16 v[32:35], v[0:3], a[0:7], v108\n"
            "v_smfmac_f32_16x16x64_bf16 v[36:39], v[0:3], a[128:135], v108\n"
            "s_waitcnt lgkmcnt(6)\n"
            "v_max_u32 v118, v1, v3\n"
            "v_smfmac_f32_16x16x64_bf16 v[32:35], v[4:7], a[8:15], v108\n"
            "ds_read_b128 v[56:59], v110 offset:16384\n"
            "v_smfmac_f32_16x16x64_bf16 v[36:39], v[4:7], a[136:143], v108\n"
            "s_waitcnt lgkmcnt(6)\n"
            "v_max3_u32 v118, v118, v5, v7\n"
            "v_smfmac_f32_16x16x64_bf16 v[32:35], v[8:11], a[16:23], v108\n"
            "ds_read_b128 v[60:63], v110 offset:17408\n"
            "v_smfmac_f32_16x16x64_bf16 v[36:39], v[8:11], a[144:151], v108\n"
            "s_waitcnt lgkmcnt(6)\n"
            "v_max3_u32 v118, v118, v9, v11\n"
            "v_smfmac_f32_16x16x64_bf16 v[32:35], v[12:15], a[24:31], v108\n"
            "ds_read_b128 v[64:67], v110 offset:18432\n"
            "v_smfmac_f32_16x16x64_bf16 v[36:39], v[12:15], a[152:159], v108\n"
            "s_waitcnt lgkmcnt(6)\n"
            "v_max3_u32 v118, v118, v13, v15\n"
            "v_smfmac_f32_16x16x64_bf16 v[32:35], v[16:19], a[32:39], v108\n"
            "ds_read_b128 v[68:71], v110 offset:19456\n"
            "v_smfmac_f32_16x16x64_bf16 v[36:39], v[16:19], a[160:167], v108\n"
            "s_waitcnt lgkmcnt(6)\n"
            "v_max3_u32 v118, v118, v17, v19\n"
            "v_smfmac_f32_16x16x64_bf16 v[32:35], v[20:23], a[40:47], v108\n"
            "ds_read_b128 v[72:75], v110 offset:20480\n"
            "v_smfmac_f32_16x16x64_bf16 v[36:39], v[20:23], a[168:175], v108\n"
            "s_waitcnt lgkmcnt(6)\n"
            "v_max3_u32 v118, v118, v21, v23\n"
            "v_smfmac_f32_16x16x64_bf16 v[32:35], v[24:27], a[48:55], v108\n"
            "ds_read_b128 v[76:79], v110 offset:21504\n"
            "v_smfmac_f32_16x16x64_bf16 v[36:39], v[24:27], a[176:183], v108\n"
            "s_waitcnt lgkmcnt(6)\n"
            "v_max3_u32 v118, v118, v25, v27\n"
            "v_smfmac_f32_16x16x64_bf16 v[32:35], v[28:31], a[56:63], v108\n"
            "ds_read_b128 v[80:83], v110 offset:22528\n"
            "v_smfmac_f32_16x16x64_bf16 v[36:39], v[28:31], a[184:191], v108\n"
            "v_max3_u32 v118, v118, v29, v31\n"
            "v_smfmac_f32_16x16x64_bf16 v[40:43], v[0:3], a[64:71], v108\n"
            "ds_read_b128 v[84:87], v110 offset:23552\n"
            "v_cmp_eq_u32 vcc, -1, v118\n"
            "v_smfmac_f32_16x16x64_bf16 v[44:47], v[0:3], a[192:199], v108\n"
            "v_smfmac_f32_16x16x64_bf16 v[40:43], v[4:7], a[72:79], v108\n"
            "v_smfmac_f32_16x16x64_bf16 v[44:47], v[4:7], a[200:207], v108\n"
            "s_cbranch_vccnz W1P1_RETRY_%=\n"
            "s_mov_b32 vcc_lo, 0xcccccccc\n"
            "s_mov_b32 vcc_hi, 0xcccccccc\n"
            "v_smfmac_f32_16x16x64_bf16 v[40:43], v[8:11], a[80:87], v108\n"
            "v_smfmac_f32_16x16x64_bf16 v[44:47], v[8:11], a[208:215], v108\n"
            "v_smfmac_f32_16x16x64_bf16 v[40:43], v[12:15], a[88:95], v108\n"
            "v_smfmac_f32_16x16x64_bf16 v[44:47], v[12:15], a[216:223], v108\n"
            "v_add_f32 v36, v36, v37\n"
            "v_smfmac_f32_16x16x64_bf16 v[40:43], v[16:19], a[96:103], v108\n"
            "v_add_f32 v38, v38, v39\n"
            "v_smfmac_f32_16x16x64_bf16 v[44:47], v[16:19], a[224:231], v108\n"
            "v_add_f32 v32, v32, v33\n"
            "v_smfmac_f32_16x16x64_bf16 v[40:43], v[20:23], a[104:111], v108\n"
            "v_add_f32 v34, v34, v35\n"
            "v_smfmac_f32_16x16x64_bf16 v[44:47], v[20:23], a[232:239], v108\n"
            "v_cvt_pk_bf16_f32 v36, v36, v38\n"
            "v_smfmac_f32_16x16x64_bf16 v[40:43], v[24:27], a[112:119], v108\n"
            "v_cvt_pk_bf16_f32 v37, v32, v34\n"
            "v_smfmac_f32_16x16x64_bf16 v[44:47], v[24:27], a[240:247], v108\n"
            "ds_write_b64 v111, v[36:37] offset:0\n"
            "v_smfmac_f32_16x16x64_bf16 v[40:43], v[28:31], a[120:127], v108\n"
            "v_smfmac_f32_16x16x64_bf16 v[44:47], v[28:31], a[248:255], v108\n"
            "v_smfmac_f32_16x16x64_bf16 v[48:51], v[0:3], v[128:135], v108\n"
            "s_nop 5\n"
            "s_nop 0\n"
            "v_add_f32 v44, v44, v45\n"
            "v_add_f32 v46, v46, v47\n"
            "v_add_f32 v40, v40, v41\n"
            "v_add_f32 v42, v42, v43\n"
            "v_cvt_pk_bf16_f32 v44, v44, v46\n"
            "v_cvt_pk_bf16_f32 v45, v40, v42\n"
            "ds_write_b64 v111, v[44:45] offset:512\n"
            "v_smfmac_f32_16x16x64_bf16 v[52:55], v[0:3], v[192:199], v108\n"
            "v_smfmac_f32_16x16x64_bf16 v[48:51], v[4:7], v[136:143], v108\n"
            "v_smfmac_f32_16x16x64_bf16 v[52:55], v[4:7], v[200:207], v108\n"
            "v_smfmac_f32_16x16x64_bf16 v[48:51], v[8:11], v[144:151], v108\n"
            "v_smfmac_f32_16x16x64_bf16 v[52:55], v[8:11], v[208:215], v108\n"
            "s_waitcnt lgkmcnt(0)\n"
            "s_barrier\n"
            "ds_read_b64_tr_b16 v[32:33], v112 offset:0\n"
            "ds_read_b64_tr_b16 v[34:35], v112 offset:512\n"
            "ds_read_b64_tr_b16 v[36:37], v112 offset:1024\n"
            "ds_read_b64_tr_b16 v[38:39], v112 offset:1536\n"
            "ds_read_b64_tr_b16 v[40:41], v112 offset:2048\n"
            "ds_read_b64_tr_b16 v[42:43], v112 offset:2560\n"
            "ds_read_b64_tr_b16 v[44:45], v112 offset:3072\n"
            "ds_read_b64_tr_b16 v[46:47], v112 offset:3584\n"
            "s_mov_b32 m0, 20480\n"
            "s_nop 0\n"
            "global_load_lds_dwordx4 v110, s[18:19] offset:0 sc1\n"
            "global_load_lds_dwordx4 v110, s[18:19] offset:1024 sc1\n"
            "global_load_lds_dwordx4 v110, s[18:19] offset:2048 sc1\n"
            "global_load_lds_dwordx4 v110, s[18:19] offset:3072 sc1\n"
            "v_smfmac_f32_16x16x64_bf16 v[48:51], v[12:15], v[152:159], v108\n"
            "v_smfmac_f32_16x16x64_bf16 v[52:55], v[12:15], v[216:223], v108\n"
            "s_waitcnt lgkmcnt(6)\n"
            "v_mfma_f32_16x16x32_bf16 v[96:99], v[32:35], v[32:35], 0\n"
            "s_waitcnt lgkmcnt(4)\n"
            "v_mfma_f32_16x16x32_bf16 v[96:99], v[36:39], v[36:39], v[96:99]\n"
            "s_waitcnt lgkmcnt(2)\n"
            "v_mfma_f32_16x16x32_bf16 v[96:99], v[40:43], v[40:43], v[96:99]\n"
            "s_waitcnt lgkmcnt(0)\n"
            "v_mfma_f32_16x16x32_bf16 v[96:99], v[44:47], v[44:47], v[96:99]\n"
            "v_mfma_f32_16x16x32_bf16 v[88:91], v[56:59], v[32:35], 0\n"
            "s_mov_b64 exec, 1\n"
            "global_load_dword v117, v110, s[30:31] sc1\n"
            "global_load_dword v60, v110, s[24:25] sc1\n"
            "s_mov_b64 exec, -1\n"
            "v_mov_b32 v12, 0x3f803f80\n"
            "v_mfma_f32_16x16x32_bf16 v[88:91], v[60:63], v[36:39], v[88:91]\n"
            "v_mov_b32 v13, 0x3f803f80\n"
            "v_mov_b32 v14, 0x3f803f80\n"
            "v_mfma_f32_16x16x32_bf16 v[88:91], v[64:67], v[40:43], v[88:91]\n"
            "v_mov_b32 v15, 0x3f803f80\n"
            "v_mov_b32 v6, 0x00003f80\n"
            "v_mfma_f32_16x16x32_bf16 v[88:91], v[68:71], v[44:47], v[88:91]\n"
            "v_mov_b32 v7, 0x00003f80\n"
            "v_mfma_f32_16x16x32_bf16 v[92:95], v[72:75], v[32:35], 0\n"
            "v_cndmask_b32_dpp v96, v97, v96, vcc quad_perm:[2,3,2,3] row_mask:0xf bank_mask:0xf\n"
            "v_mfma_f32_16x16x32_bf16 v[92:95], v[76:79], v[36:39], v[92:95]\n"
            "v_mfma_f32_16x16x32_bf16 v[92:95], v[80:83], v[40:43], v[92:95]\n"
            "v_mfma_f32_16x16x32_bf16 v[92:95], v[84:87], v[44:47], v[92:95]\n"
            "v_smfmac_f32_16x16x64_bf16 v[48:51], v[16:19], v[160:167], v108\n"
            "s_nop 6\n"
            "v_cndmask_b32_dpp v88, v92, v88, vcc quad_perm:[2,3,2,3] row_mask:0xf bank_mask:0xf\n"
            "v_cndmask_b32_dpp v89, v93, v89, vcc quad_perm:[2,3,2,3] row_mask:0xf bank_mask:0xf\n"
            "v_cndmask_b32_dpp v90, v94, v90, vcc quad_perm:[2,3,2,3] row_mask:0xf bank_mask:0xf\n"
            "v_cndmask_b32_dpp v91, v95, v91, vcc quad_perm:[2,3,2,3] row_mask:0xf bank_mask:0xf\n"
            "v_max3_f32 v118, v96, v88, v89\n"
            "v_max3_f32 v118, v118, v90, v91\n"
            "v_smfmac_f32_16x16x64_bf16 v[52:55], v[16:19], v[224:231], v108\n"
            "s_nop 0\n"
            "v_max_f32_dpp v118, v118, v118 quad_perm:[2,3,0,1] row_mask:0xf bank_mask:0xf\n"
            "v_mov_b32 v119, v118\n"
            "v_smfmac_f32_16x16x64_bf16 v[48:51], v[20:23], v[168:175], v108\n"
            "s_nop 0\n"
            "v_permlane32_swap_b32 v118, v119\n"
            "v_max_f32 v118, v118, v119\n"
            "v_mov_b32 v119, v118\n"
            "v_smfmac_f32_16x16x64_bf16 v[52:55], v[20:23], v[232:239], v108\n"
            "s_nop 0\n"
            "v_permlane16_swap_b32 v118, v119\n"
            "v_max_f32 v118, v118, v119\n"
            "v_smfmac_f32_16x16x64_bf16 v[48:51], v[24:27], v[176:183], v108\n"
            "v_mul_f32 v118, s94, v118\n"
            "v_smfmac_f32_16x16x64_bf16 v[52:55], v[24:27], v[240:247], v108\n"
            "v_fma_f32 v88, v88, s94, -v118\n"
            "v_fma_f32 v89, v89, s94, -v118\n"
            "v_fma_f32 v90, v90, s94, -v118\n"
            "v_fma_f32 v91, v91, s94, -v118\n"
            "v_fma_f32 v96, v96, s94, -v118\n"
            "v_smfmac_f32_16x16x64_bf16 v[48:51], v[28:31], v[184:191], v108\n"
            "v_exp_f32 v88, v88\n"
            "v_exp_f32 v89, v89\n"
            "v_exp_f32 v90, v90\n"
            "v_exp_f32 v91, v91\n"
            "v_exp_f32 v96, v96\n"
            "v_smfmac_f32_16x16x64_bf16 v[52:55], v[28:31], v[248:255], v108\n"
            "v_cvt_pk_bf16_f32 v0, v88, v89\n"
            "v_cvt_pk_bf16_f32 v1, v90, v91\n"
            "v_cvt_pk_bf16_f32 v4, v96, v96\n"
            "v_mov_b32_dpp v2, v0 quad_perm:[0,1,0,1] row_mask:0xf bank_mask:0xf\n"
            "v_mov_b32_dpp v3, v1 quad_perm:[0,1,0,1] row_mask:0xf bank_mask:0xf\n"
            "v_mov_b32_dpp v5, v4 quad_perm:[0,1,0,1] row_mask:0xf bank_mask:0xf\n"
            "s_nop 0\n"
            "v_mfma_f32_16x16x32_bf16 v[92:95], v[12:15], v[0:3], 0\n"
            "v_cvt_pk_bf16_f32 v8, v48, v49\n"
            "v_cvt_pk_bf16_f32 v9, v50, v51\n"
            "v_mfma_f32_16x16x32_bf16 v[12:15], v[100:103], v[0:3], 0\n"
            "v_cvt_pk_bf16_f32 v10, v52, v53\n"
            "v_cvt_pk_bf16_f32 v11, v54, v55\n"
            "v_mfma_f32_16x16x16_bf16 v[92:95], v[6:7], v[4:5], v[92:95]\n"
            "v_mfma_f32_16x16x32_bf16 v[88:91], v[104:107], v[0:3], 0\n"
            "v_mfma_f32_16x16x16_bf16 v[12:15], v[8:9], v[4:5], v[12:15]\n"
            "v_mfma_f32_16x16x16_bf16 v[88:91], v[10:11], v[4:5], v[88:91]\n"
            "s_nop 4\n"
            "v_mov_b32_dpp v118, v92 quad_perm:[2,3,2,3] row_mask:0xf bank_mask:0xf\n"
            "v_rcp_f32 v118, v118\n"
            "s_nop 0\n"
            "v_cndmask_b32_dpp v12, v88, v12, vcc quad_perm:[2,3,2,3] row_mask:0xf bank_mask:0xf\n"
            "v_cndmask_b32_dpp v13, v89, v13, vcc quad_perm:[2,3,2,3] row_mask:0xf bank_mask:0xf\n"
            "v_cndmask_b32_dpp v14, v90, v14, vcc quad_perm:[2,3,2,3] row_mask:0xf bank_mask:0xf\n"
            "v_cndmask_b32_dpp v15, v91, v15, vcc quad_perm:[2,3,2,3] row_mask:0xf bank_mask:0xf\n"
            "v_mul_f32 v12, v12, v118\n"
            "v_mul_f32 v13, v13, v118\n"
            "v_mul_f32 v14, v14, v118\n"
            "v_mul_f32 v15, v15, v118\n"
            "v_cvt_pk_bf16_f32 v20, v12, v13\n"
            "v_cvt_pk_bf16_f32 v21, v14, v15\n"
            "v_min_u32 v21, -2, v21\n"
            "global_store_dwordx2 v113, v[20:21], s[56:57] sc1\n"
            "W1P1_SCH_%=:\n"
            "s_waitcnt vmcnt(1)\n"
            "v_readfirstlane_b32 s88, v117\n"
            "v_readfirstlane_b32 s21, v60\n"
            "s_sub_u32 s78, s88, s32\n"
            "s_cmp_le_u32 s78, 208\n"
            "s_cselect_b32 s78, s78, s33\n"
            "s_add_u32 s32, s32, s33\n"
            "s_cmp_eq_u32 s88, -1\n"
            "s_cselect_b32 s32, s32, s88\n"
            "s_cselect_b32 s33, s33, s78\n"
            "s_add_u32 s91, s68, 2\n"
            "s_min_u32 s91, s91, 63\n"
            "s_cmp_gt_u32 s21, s91\n"
            "s_cbranch_scc1 W1P1_FKV_SKIP_%=\n"
            "s_mov_b32 s69, 0\n"
            "W1P1_FKV_L_%=:\n"
            "s_mov_b64 exec, 1\n"
            "global_load_dword v118, v110, s[24:25] sc1\n"
            "s_mov_b64 exec, -1\n"
            "s_waitcnt vmcnt(0)\n"
            "v_readfirstlane_b32 s21, v118\n"
            "s_nop 0\n"
            "s_cmp_gt_u32 s21, s91\n"
            "s_cbranch_scc1 W1P1_FKV_SKIP_%=\n"
            "s_add_u32 s69, s69, 1\n"
            "s_cmp_gt_u32 s69, 0x80000\n"
            "s_cbranch_scc1 TIMEOUT_%=\n"
            "s_sleep 2\n"
            "s_branch W1P1_FKV_L_%=\n"
            "W1P1_FKV_SKIP_%=:\n"
            "s_lshr_b32 s78, s91, 2\n"
            "s_xor_b32 s78, s78, s20\n"
            "s_and_b32 s78, s78, 1\n"
            "s_lshl_b32 s78, s78, 13\n"
            "s_add_u32 s18, s48, s78\n"
            "s_addc_u32 s19, s49, 0\n"
            "s_mul_i32 s78, s91, 524288\n"
            "s_add_u32 s18, s18, s78\n"
            "s_addc_u32 s19, s19, 0\n"
            "s_add_u32 s18, s18, 4096\n"
            "s_addc_u32 s19, s19, 0\n"
            "ds_read_b128 v[0:3], v114 offset:0\n"
            "ds_read_b128 v[4:7], v114 offset:1024\n"
            "ds_read_b128 v[8:11], v114 offset:2048\n"
            "ds_read_b128 v[12:15], v114 offset:3072\n"
            "ds_read_b128 v[16:19], v114 offset:4096\n"
            "ds_read_b128 v[20:23], v114 offset:5120\n"
            "ds_read_b128 v[24:27], v114 offset:6144\n"
            "ds_read_b128 v[28:31], v114 offset:7168\n"
            "ds_read_b128 v[56:59], v110 offset:24576\n"
            "ds_read_b128 v[60:63], v110 offset:25600\n"
            "ds_read_b128 v[64:67], v110 offset:26624\n"
            "ds_read_b128 v[68:71], v110 offset:27648\n"
            "ds_read_b128 v[72:75], v110 offset:28672\n"
            "ds_read_b128 v[76:79], v110 offset:29696\n"
            "ds_read_b128 v[80:83], v110 offset:30720\n"
            "ds_read_b128 v[84:87], v110 offset:31744\n"
            "s_waitcnt lgkmcnt(0)\n"
            "v_mfma_f32_16x16x32_bf16 v[32:35], v[56:59], v[0:3], 0\n"
            "v_mfma_f32_16x16x32_bf16 v[36:39], v[56:59], v[16:19], 0\n"
            "v_mfma_f32_16x16x32_bf16 v[40:43], v[72:75], v[0:3], 0\n"
            "v_mfma_f32_16x16x32_bf16 v[44:47], v[72:75], v[16:19], 0\n"
            "v_mfma_f32_16x16x32_bf16 v[32:35], v[60:63], v[4:7], v[32:35]\n"
            "v_mfma_f32_16x16x32_bf16 v[36:39], v[60:63], v[20:23], v[36:39]\n"
            "v_mfma_f32_16x16x32_bf16 v[40:43], v[76:79], v[4:7], v[40:43]\n"
            "v_mfma_f32_16x16x32_bf16 v[44:47], v[76:79], v[20:23], v[44:47]\n"
            "v_mfma_f32_16x16x32_bf16 v[32:35], v[64:67], v[8:11], v[32:35]\n"
            "v_mfma_f32_16x16x32_bf16 v[36:39], v[64:67], v[24:27], v[36:39]\n"
            "v_mfma_f32_16x16x32_bf16 v[40:43], v[80:83], v[8:11], v[40:43]\n"
            "v_mfma_f32_16x16x32_bf16 v[44:47], v[80:83], v[24:27], v[44:47]\n"
            "v_mfma_f32_16x16x32_bf16 v[32:35], v[68:71], v[12:15], v[32:35]\n"
            "v_mfma_f32_16x16x32_bf16 v[36:39], v[68:71], v[28:31], v[36:39]\n"
            "v_mfma_f32_16x16x32_bf16 v[40:43], v[84:87], v[12:15], v[40:43]\n"
            "v_mfma_f32_16x16x32_bf16 v[44:47], v[84:87], v[28:31], v[44:47]\n"
            "s_nop 4\n"
            "v_cvt_pk_bf16_f32 v100, v32, v33\n"
            "v_cvt_pk_bf16_f32 v101, v34, v35\n"
            "v_cvt_pk_bf16_f32 v102, v40, v41\n"
            "v_cvt_pk_bf16_f32 v103, v42, v43\n"
            "v_cvt_pk_bf16_f32 v104, v36, v37\n"
            "v_cvt_pk_bf16_f32 v105, v38, v39\n"
            "v_cvt_pk_bf16_f32 v106, v44, v45\n"
            "v_cvt_pk_bf16_f32 v107, v46, v47\n"
            "W1P1_ZDONE_%=:\n"
            "s_nop 7\n"
            "v_mov_b32 v32, 0\n"
            "v_mov_b32 v33, 0\n"
            "v_mov_b32 v34, 0\n"
            "v_mov_b32 v35, 0\n"
            "v_mov_b32 v36, 0\n"
            "v_mov_b32 v37, 0\n"
            "v_mov_b32 v38, 0\n"
            "v_mov_b32 v39, 0\n"
            "v_mov_b32 v40, 0\n"
            "v_mov_b32 v41, 0\n"
            "v_mov_b32 v42, 0\n"
            "v_mov_b32 v43, 0\n"
            "v_mov_b32 v44, 0\n"
            "v_mov_b32 v45, 0\n"
            "v_mov_b32 v46, 0\n"
            "v_mov_b32 v47, 0\n"
            "v_mov_b32 v48, 0\n"
            "v_mov_b32 v49, 0\n"
            "v_mov_b32 v50, 0\n"
            "v_mov_b32 v51, 0\n"
            "v_mov_b32 v52, 0\n"
            "v_mov_b32 v53, 0\n"
            "v_mov_b32 v54, 0\n"
            "v_mov_b32 v55, 0\n"
            "s_add_u32 s52, s52, 16384\n"
            "s_addc_u32 s53, s53, 0\n"
            "s_add_u32 s68, s68, 1\n"
            "s_cmp_ge_u32 s68, 63\n"
            "s_cbranch_scc1 DONE_%=\n"
            "s_barrier\n"
            "s_branch W1P0_ENTRY_%=\n"
            "W1P1_RETRY_%=:\n"
            "s_waitcnt lgkmcnt(0)\n"
            "s_barrier\n"
            "s_nop 7\n"
            "v_mov_b32 v32, 0\n"
            "v_mov_b32 v33, 0\n"
            "v_mov_b32 v34, 0\n"
            "v_mov_b32 v35, 0\n"
            "v_mov_b32 v36, 0\n"
            "v_mov_b32 v37, 0\n"
            "v_mov_b32 v38, 0\n"
            "v_mov_b32 v39, 0\n"
            "v_mov_b32 v40, 0\n"
            "v_mov_b32 v41, 0\n"
            "v_mov_b32 v42, 0\n"
            "v_mov_b32 v43, 0\n"
            "v_mov_b32 v44, 0\n"
            "v_mov_b32 v45, 0\n"
            "v_mov_b32 v46, 0\n"
            "v_mov_b32 v47, 0\n"
            "s_sub_u32 s30, s30, 128\n"
            "s_subb_u32 s31, s31, 0\n"
            "W1P1_RETRY2_%=:\n"
            "s_add_u32 s76, s76, 1\n"
            "s_add_u32 s84, s84, 1\n"
            "s_cmp_gt_u32 s76, 0x20000\n"
            "s_cbranch_scc1 TIMEOUT_%=\n"
            "global_load_dwordx4 v[8:11], v109, s[52:53] sc1\n"
            "global_load_dwordx4 v[12:15], v109, s[52:53] offset:1024 sc1\n"
            "s_waitcnt vmcnt(0)\n"
            "v_max3_u32 v118, v9, v11, v13\n"
            "v_max_u32 v118, v118, v15\n"
            "v_cmp_eq_u32 vcc, -1, v118\n"
            "s_cbranch_vccnz W1P1_RETRY2_%=\n"
            "ds_write_b128 v110, v[8:11] offset:2048\n"
            "ds_write_b128 v110, v[12:15] offset:3072\n"
            "s_waitcnt lgkmcnt(0)\n"
            "s_barrier\n"
            "s_branch W1P1_AFTERB1_%=\n"
            "s_nop 15\n"
            "W2P0_ENTRY_%=:\n"
            "s_mov_b32 s76, 0\n"
            "W2P0_POLL_%=:\n"
            "s_mov_b32 m0, 4096\n"
            "s_nop 0\n"
            "global_load_lds_dwordx4 v109, s[52:53] sc1\n"
            "global_load_lds_dwordx4 v109, s[52:53] offset:1024 sc1\n"
            "s_waitcnt vmcnt(0)\n"
            "s_waitcnt lgkmcnt(0)\n"
            "s_barrier\n"
            "W2P0_AFTERB1_%=:\n"
            "ds_read_b128 v[0:3], v110 offset:0\n"
            "ds_read_b128 v[4:7], v110 offset:1024\n"
            "ds_read_b128 v[8:11], v110 offset:2048\n"
            "ds_read_b128 v[12:15], v110 offset:3072\n"
            "ds_read_b128 v[16:19], v110 offset:4096\n"
            "ds_read_b128 v[20:23], v110 offset:5120\n"
            "ds_read_b128 v[24:27], v110 offset:6144\n"
            "ds_read_b128 v[28:31], v110 offset:7168\n"
            "s_add_u32 s30, s30, 128\n"
            "s_addc_u32 s31, s31, 0\n"
            "s_waitcnt lgkmcnt(7)\n"
            "v_smfmac_f32_16x16x64_bf16 v[32:35], v[0:3], a[0:7], v108\n"
            "v_smfmac_f32_16x16x64_bf16 v[36:39], v[0:3], a[128:135], v108\n"
            "s_waitcnt lgkmcnt(6)\n"
            "v_max_u32 v118, v1, v3\n"
            "v_smfmac_f32_16x16x64_bf16 v[32:35], v[4:7], a[8:15], v108\n"
            "ds_read_b128 v[56:59], v110 offset:16384\n"
            "v_smfmac_f32_16x16x64_bf16 v[36:39], v[4:7], a[136:143], v108\n"
            "s_waitcnt lgkmcnt(6)\n"
            "v_max3_u32 v118, v118, v5, v7\n"
            "v_smfmac_f32_16x16x64_bf16 v[32:35], v[8:11], a[16:23], v108\n"
            "ds_read_b128 v[60:63], v110 offset:17408\n"
            "v_smfmac_f32_16x16x64_bf16 v[36:39], v[8:11], a[144:151], v108\n"
            "s_waitcnt lgkmcnt(6)\n"
            "v_max3_u32 v118, v118, v9, v11\n"
            "v_smfmac_f32_16x16x64_bf16 v[32:35], v[12:15], a[24:31], v108\n"
            "ds_read_b128 v[64:67], v110 offset:18432\n"
            "v_smfmac_f32_16x16x64_bf16 v[36:39], v[12:15], a[152:159], v108\n"
            "s_waitcnt lgkmcnt(6)\n"
            "v_max3_u32 v118, v118, v13, v15\n"
            "v_smfmac_f32_16x16x64_bf16 v[32:35], v[16:19], a[32:39], v108\n"
            "ds_read_b128 v[68:71], v110 offset:19456\n"
            "v_smfmac_f32_16x16x64_bf16 v[36:39], v[16:19], a[160:167], v108\n"
            "s_waitcnt lgkmcnt(6)\n"
            "v_max3_u32 v118, v118, v17, v19\n"
            "v_smfmac_f32_16x16x64_bf16 v[32:35], v[20:23], a[40:47], v108\n"
            "ds_read_b128 v[72:75], v110 offset:20480\n"
            "v_smfmac_f32_16x16x64_bf16 v[36:39], v[20:23], a[168:175], v108\n"
            "s_waitcnt lgkmcnt(6)\n"
            "v_max3_u32 v118, v118, v21, v23\n"
            "v_smfmac_f32_16x16x64_bf16 v[32:35], v[24:27], a[48:55], v108\n"
            "ds_read_b128 v[76:79], v110 offset:21504\n"
            "v_smfmac_f32_16x16x64_bf16 v[36:39], v[24:27], a[176:183], v108\n"
            "s_waitcnt lgkmcnt(6)\n"
            "v_max3_u32 v118, v118, v25, v27\n"
            "v_smfmac_f32_16x16x64_bf16 v[32:35], v[28:31], a[56:63], v108\n"
            "ds_read_b128 v[80:83], v110 offset:22528\n"
            "v_smfmac_f32_16x16x64_bf16 v[36:39], v[28:31], a[184:191], v108\n"
            "v_max3_u32 v118, v118, v29, v31\n"
            "v_smfmac_f32_16x16x64_bf16 v[40:43], v[0:3], a[64:71], v108\n"
            "ds_read_b128 v[84:87], v110 offset:23552\n"
            "v_cmp_eq_u32 vcc, -1, v118\n"
            "v_smfmac_f32_16x16x64_bf16 v[44:47], v[0:3], a[192:199], v108\n"
            "v_smfmac_f32_16x16x64_bf16 v[40:43], v[4:7], a[72:79], v108\n"
            "v_smfmac_f32_16x16x64_bf16 v[44:47], v[4:7], a[200:207], v108\n"
            "s_cbranch_vccnz W2P0_RETRY_%=\n"
            "s_mov_b32 vcc_lo, 0xcccccccc\n"
            "s_mov_b32 vcc_hi, 0xcccccccc\n"
            "v_smfmac_f32_16x16x64_bf16 v[40:43], v[8:11], a[80:87], v108\n"
            "v_smfmac_f32_16x16x64_bf16 v[44:47], v[8:11], a[208:215], v108\n"
            "v_smfmac_f32_16x16x64_bf16 v[40:43], v[12:15], a[88:95], v108\n"
            "v_smfmac_f32_16x16x64_bf16 v[44:47], v[12:15], a[216:223], v108\n"
            "v_add_f32 v36, v36, v37\n"
            "v_smfmac_f32_16x16x64_bf16 v[40:43], v[16:19], a[96:103], v108\n"
            "v_add_f32 v38, v38, v39\n"
            "v_smfmac_f32_16x16x64_bf16 v[44:47], v[16:19], a[224:231], v108\n"
            "v_add_f32 v32, v32, v33\n"
            "v_smfmac_f32_16x16x64_bf16 v[40:43], v[20:23], a[104:111], v108\n"
            "v_add_f32 v34, v34, v35\n"
            "v_smfmac_f32_16x16x64_bf16 v[44:47], v[20:23], a[232:239], v108\n"
            "v_cvt_pk_bf16_f32 v36, v36, v38\n"
            "v_smfmac_f32_16x16x64_bf16 v[40:43], v[24:27], a[112:119], v108\n"
            "v_cvt_pk_bf16_f32 v37, v32, v34\n"
            "v_smfmac_f32_16x16x64_bf16 v[44:47], v[24:27], a[240:247], v108\n"
            "ds_write_b64 v111, v[36:37] offset:0\n"
            "v_smfmac_f32_16x16x64_bf16 v[40:43], v[28:31], a[120:127], v108\n"
            "v_smfmac_f32_16x16x64_bf16 v[44:47], v[28:31], a[248:255], v108\n"
            "v_smfmac_f32_16x16x64_bf16 v[48:51], v[0:3], v[128:135], v108\n"
            "s_nop 5\n"
            "s_nop 0\n"
            "v_add_f32 v44, v44, v45\n"
            "v_add_f32 v46, v46, v47\n"
            "v_add_f32 v40, v40, v41\n"
            "v_add_f32 v42, v42, v43\n"
            "v_cvt_pk_bf16_f32 v44, v44, v46\n"
            "v_cvt_pk_bf16_f32 v45, v40, v42\n"
            "ds_write_b64 v111, v[44:45] offset:512\n"
            "v_smfmac_f32_16x16x64_bf16 v[52:55], v[0:3], v[192:199], v108\n"
            "v_smfmac_f32_16x16x64_bf16 v[48:51], v[4:7], v[136:143], v108\n"
            "v_smfmac_f32_16x16x64_bf16 v[52:55], v[4:7], v[200:207], v108\n"
            "v_smfmac_f32_16x16x64_bf16 v[48:51], v[8:11], v[144:151], v108\n"
            "v_smfmac_f32_16x16x64_bf16 v[52:55], v[8:11], v[208:215], v108\n"
            "s_waitcnt lgkmcnt(0)\n"
            "s_barrier\n"
            "ds_read_b64_tr_b16 v[32:33], v112 offset:0\n"
            "ds_read_b64_tr_b16 v[34:35], v112 offset:512\n"
            "ds_read_b64_tr_b16 v[36:37], v112 offset:1024\n"
            "ds_read_b64_tr_b16 v[38:39], v112 offset:1536\n"
            "ds_read_b64_tr_b16 v[40:41], v112 offset:2048\n"
            "ds_read_b64_tr_b16 v[42:43], v112 offset:2560\n"
            "ds_read_b64_tr_b16 v[44:45], v112 offset:3072\n"
            "ds_read_b64_tr_b16 v[46:47], v112 offset:3584\n"
            "s_mov_b32 m0, 24576\n"
            "s_nop 0\n"
            "global_load_lds_dwordx4 v110, s[18:19] offset:0 sc1\n"
            "global_load_lds_dwordx4 v110, s[18:19] offset:1024 sc1\n"
            "global_load_lds_dwordx4 v110, s[18:19] offset:2048 sc1\n"
            "global_load_lds_dwordx4 v110, s[18:19] offset:3072 sc1\n"
            "s_add_u32 s18, s18, 4096\n"
            "s_addc_u32 s19, s19, 0\n"
            "s_mov_b32 m0, 28672\n"
            "s_nop 0\n"
            "global_load_lds_dwordx4 v110, s[18:19] offset:0 sc1\n"
            "global_load_lds_dwordx4 v110, s[18:19] offset:1024 sc1\n"
            "global_load_lds_dwordx4 v110, s[18:19] offset:2048 sc1\n"
            "global_load_lds_dwordx4 v110, s[18:19] offset:3072 sc1\n"
            "v_smfmac_f32_16x16x64_bf16 v[48:51], v[12:15], v[152:159], v108\n"
            "v_smfmac_f32_16x16x64_bf16 v[52:55], v[12:15], v[216:223], v108\n"
            "s_waitcnt lgkmcnt(6)\n"
            "v_mfma_f32_16x16x32_bf16 v[96:99], v[32:35], v[32:35], 0\n"
            "s_waitcnt lgkmcnt(4)\n"
            "v_mfma_f32_16x16x32_bf16 v[96:99], v[36:39], v[36:39], v[96:99]\n"
            "s_waitcnt lgkmcnt(2)\n"
            "v_mfma_f32_16x16x32_bf16 v[96:99], v[40:43], v[40:43], v[96:99]\n"
            "s_waitcnt lgkmcnt(0)\n"
            "v_mfma_f32_16x16x32_bf16 v[96:99], v[44:47], v[44:47], v[96:99]\n"
            "v_mfma_f32_16x16x32_bf16 v[88:91], v[56:59], v[32:35], 0\n"
            "s_mov_b64 exec, 1\n"
            "global_load_dword v117, v110, s[30:31] sc1\n"
            "global_load_dword v60, v110, s[24:25] sc1\n"
            "s_mov_b64 exec, -1\n"
            "v_mov_b32 v12, 0x3f803f80\n"
            "v_mfma_f32_16x16x32_bf16 v[88:91], v[60:63], v[36:39], v[88:91]\n"
            "v_mov_b32 v13, 0x3f803f80\n"
            "v_mov_b32 v14, 0x3f803f80\n"
            "v_mfma_f32_16x16x32_bf16 v[88:91], v[64:67], v[40:43], v[88:91]\n"
            "v_mov_b32 v15, 0x3f803f80\n"
            "v_mov_b32 v6, 0x00003f80\n"
            "v_mfma_f32_16x16x32_bf16 v[88:91], v[68:71], v[44:47], v[88:91]\n"
            "v_mov_b32 v7, 0x00003f80\n"
            "v_mfma_f32_16x16x32_bf16 v[92:95], v[72:75], v[32:35], 0\n"
            "v_cndmask_b32_dpp v96, v97, v96, vcc quad_perm:[2,3,2,3] row_mask:0xf bank_mask:0xf\n"
            "v_mfma_f32_16x16x32_bf16 v[92:95], v[76:79], v[36:39], v[92:95]\n"
            "v_mfma_f32_16x16x32_bf16 v[92:95], v[80:83], v[40:43], v[92:95]\n"
            "v_mfma_f32_16x16x32_bf16 v[92:95], v[84:87], v[44:47], v[92:95]\n"
            "v_smfmac_f32_16x16x64_bf16 v[48:51], v[16:19], v[160:167], v108\n"
            "s_nop 6\n"
            "v_cndmask_b32_dpp v88, v92, v88, vcc quad_perm:[2,3,2,3] row_mask:0xf bank_mask:0xf\n"
            "v_cndmask_b32_dpp v89, v93, v89, vcc quad_perm:[2,3,2,3] row_mask:0xf bank_mask:0xf\n"
            "v_cndmask_b32_dpp v90, v94, v90, vcc quad_perm:[2,3,2,3] row_mask:0xf bank_mask:0xf\n"
            "v_cndmask_b32_dpp v91, v95, v91, vcc quad_perm:[2,3,2,3] row_mask:0xf bank_mask:0xf\n"
            "v_max3_f32 v118, v96, v88, v89\n"
            "v_max3_f32 v118, v118, v90, v91\n"
            "v_smfmac_f32_16x16x64_bf16 v[52:55], v[16:19], v[224:231], v108\n"
            "s_nop 0\n"
            "v_max_f32_dpp v118, v118, v118 quad_perm:[2,3,0,1] row_mask:0xf bank_mask:0xf\n"
            "v_mov_b32 v119, v118\n"
            "v_smfmac_f32_16x16x64_bf16 v[48:51], v[20:23], v[168:175], v108\n"
            "s_nop 0\n"
            "v_permlane32_swap_b32 v118, v119\n"
            "v_max_f32 v118, v118, v119\n"
            "v_mov_b32 v119, v118\n"
            "v_smfmac_f32_16x16x64_bf16 v[52:55], v[20:23], v[232:239], v108\n"
            "s_nop 0\n"
            "v_permlane16_swap_b32 v118, v119\n"
            "v_max_f32 v118, v118, v119\n"
            "v_smfmac_f32_16x16x64_bf16 v[48:51], v[24:27], v[176:183], v108\n"
            "v_mul_f32 v118, s94, v118\n"
            "v_smfmac_f32_16x16x64_bf16 v[52:55], v[24:27], v[240:247], v108\n"
            "v_fma_f32 v88, v88, s94, -v118\n"
            "v_fma_f32 v89, v89, s94, -v118\n"
            "v_fma_f32 v90, v90, s94, -v118\n"
            "v_fma_f32 v91, v91, s94, -v118\n"
            "v_fma_f32 v96, v96, s94, -v118\n"
            "v_smfmac_f32_16x16x64_bf16 v[48:51], v[28:31], v[184:191], v108\n"
            "v_exp_f32 v88, v88\n"
            "v_exp_f32 v89, v89\n"
            "v_exp_f32 v90, v90\n"
            "v_exp_f32 v91, v91\n"
            "v_exp_f32 v96, v96\n"
            "v_smfmac_f32_16x16x64_bf16 v[52:55], v[28:31], v[248:255], v108\n"
            "v_cvt_pk_bf16_f32 v0, v88, v89\n"
            "v_cvt_pk_bf16_f32 v1, v90, v91\n"
            "v_cvt_pk_bf16_f32 v4, v96, v96\n"
            "v_mov_b32_dpp v2, v0 quad_perm:[0,1,0,1] row_mask:0xf bank_mask:0xf\n"
            "v_mov_b32_dpp v3, v1 quad_perm:[0,1,0,1] row_mask:0xf bank_mask:0xf\n"
            "v_mov_b32_dpp v5, v4 quad_perm:[0,1,0,1] row_mask:0xf bank_mask:0xf\n"
            "s_nop 0\n"
            "v_mfma_f32_16x16x32_bf16 v[92:95], v[12:15], v[0:3], 0\n"
            "v_cvt_pk_bf16_f32 v8, v48, v49\n"
            "v_cvt_pk_bf16_f32 v9, v50, v51\n"
            "v_mfma_f32_16x16x32_bf16 v[12:15], v[100:103], v[0:3], 0\n"
            "v_cvt_pk_bf16_f32 v10, v52, v53\n"
            "v_cvt_pk_bf16_f32 v11, v54, v55\n"
            "v_mfma_f32_16x16x16_bf16 v[92:95], v[6:7], v[4:5], v[92:95]\n"
            "v_mfma_f32_16x16x32_bf16 v[88:91], v[104:107], v[0:3], 0\n"
            "v_mfma_f32_16x16x16_bf16 v[12:15], v[8:9], v[4:5], v[12:15]\n"
            "v_mfma_f32_16x16x16_bf16 v[88:91], v[10:11], v[4:5], v[88:91]\n"
            "s_nop 4\n"
            "v_mov_b32_dpp v118, v92 quad_perm:[2,3,2,3] row_mask:0xf bank_mask:0xf\n"
            "v_rcp_f32 v118, v118\n"
            "s_nop 0\n"
            "v_cndmask_b32_dpp v12, v88, v12, vcc quad_perm:[2,3,2,3] row_mask:0xf bank_mask:0xf\n"
            "v_cndmask_b32_dpp v13, v89, v13, vcc quad_perm:[2,3,2,3] row_mask:0xf bank_mask:0xf\n"
            "v_cndmask_b32_dpp v14, v90, v14, vcc quad_perm:[2,3,2,3] row_mask:0xf bank_mask:0xf\n"
            "v_cndmask_b32_dpp v15, v91, v15, vcc quad_perm:[2,3,2,3] row_mask:0xf bank_mask:0xf\n"
            "v_mul_f32 v12, v12, v118\n"
            "v_mul_f32 v13, v13, v118\n"
            "v_mul_f32 v14, v14, v118\n"
            "v_mul_f32 v15, v15, v118\n"
            "v_cvt_pk_bf16_f32 v20, v12, v13\n"
            "v_cvt_pk_bf16_f32 v21, v14, v15\n"
            "v_min_u32 v21, -2, v21\n"
            "global_store_dwordx2 v113, v[20:21], s[54:55] sc1\n"
            "W2P0_SCH_%=:\n"
            "s_waitcnt vmcnt(1)\n"
            "v_readfirstlane_b32 s88, v117\n"
            "v_readfirstlane_b32 s21, v60\n"
            "s_sub_u32 s78, s88, s32\n"
            "s_cmp_le_u32 s78, 208\n"
            "s_cselect_b32 s78, s78, s33\n"
            "s_add_u32 s32, s32, s33\n"
            "s_cmp_eq_u32 s88, -1\n"
            "s_cselect_b32 s32, s32, s88\n"
            "s_cselect_b32 s33, s33, s78\n"
            "s_add_u32 s91, s68, 3\n"
            "s_min_u32 s91, s91, 63\n"
            "s_cmp_gt_u32 s21, s91\n"
            "s_cbranch_scc1 W2P0_FKV_SKIP_%=\n"
            "s_mov_b32 s69, 0\n"
            "W2P0_FKV_L_%=:\n"
            "s_mov_b64 exec, 1\n"
            "global_load_dword v118, v110, s[24:25] sc1\n"
            "s_mov_b64 exec, -1\n"
            "s_waitcnt vmcnt(0)\n"
            "v_readfirstlane_b32 s21, v118\n"
            "s_nop 0\n"
            "s_cmp_gt_u32 s21, s91\n"
            "s_cbranch_scc1 W2P0_FKV_SKIP_%=\n"
            "s_add_u32 s69, s69, 1\n"
            "s_cmp_gt_u32 s69, 0x80000\n"
            "s_cbranch_scc1 TIMEOUT_%=\n"
            "s_sleep 2\n"
            "s_branch W2P0_FKV_L_%=\n"
            "W2P0_FKV_SKIP_%=:\n"
            "s_lshr_b32 s78, s91, 2\n"
            "s_xor_b32 s78, s78, s20\n"
            "s_and_b32 s78, s78, 1\n"
            "s_lshl_b32 s78, s78, 13\n"
            "s_add_u32 s18, s50, s78\n"
            "s_addc_u32 s19, s51, 0\n"
            "s_mul_i32 s78, s91, 524288\n"
            "s_add_u32 s18, s18, s78\n"
            "s_addc_u32 s19, s19, 0\n"
            "ds_read_b128 v[0:3], v114 offset:0\n"
            "ds_read_b128 v[4:7], v114 offset:1024\n"
            "ds_read_b128 v[8:11], v114 offset:2048\n"
            "ds_read_b128 v[12:15], v114 offset:3072\n"
            "ds_read_b128 v[16:19], v114 offset:4096\n"
            "ds_read_b128 v[20:23], v114 offset:5120\n"
            "ds_read_b128 v[24:27], v114 offset:6144\n"
            "ds_read_b128 v[28:31], v114 offset:7168\n"
            "ds_read_b128 v[56:59], v110 offset:32768\n"
            "ds_read_b128 v[60:63], v110 offset:33792\n"
            "ds_read_b128 v[64:67], v110 offset:34816\n"
            "ds_read_b128 v[68:71], v110 offset:35840\n"
            "ds_read_b128 v[72:75], v110 offset:36864\n"
            "ds_read_b128 v[76:79], v110 offset:37888\n"
            "ds_read_b128 v[80:83], v110 offset:38912\n"
            "ds_read_b128 v[84:87], v110 offset:39936\n"
            "s_waitcnt lgkmcnt(0)\n"
            "v_mfma_f32_16x16x32_bf16 v[32:35], v[56:59], v[0:3], 0\n"
            "v_mfma_f32_16x16x32_bf16 v[36:39], v[56:59], v[16:19], 0\n"
            "v_mfma_f32_16x16x32_bf16 v[40:43], v[72:75], v[0:3], 0\n"
            "v_mfma_f32_16x16x32_bf16 v[44:47], v[72:75], v[16:19], 0\n"
            "v_mfma_f32_16x16x32_bf16 v[32:35], v[60:63], v[4:7], v[32:35]\n"
            "v_mfma_f32_16x16x32_bf16 v[36:39], v[60:63], v[20:23], v[36:39]\n"
            "v_mfma_f32_16x16x32_bf16 v[40:43], v[76:79], v[4:7], v[40:43]\n"
            "v_mfma_f32_16x16x32_bf16 v[44:47], v[76:79], v[20:23], v[44:47]\n"
            "v_mfma_f32_16x16x32_bf16 v[32:35], v[64:67], v[8:11], v[32:35]\n"
            "v_mfma_f32_16x16x32_bf16 v[36:39], v[64:67], v[24:27], v[36:39]\n"
            "v_mfma_f32_16x16x32_bf16 v[40:43], v[80:83], v[8:11], v[40:43]\n"
            "v_mfma_f32_16x16x32_bf16 v[44:47], v[80:83], v[24:27], v[44:47]\n"
            "v_mfma_f32_16x16x32_bf16 v[32:35], v[68:71], v[12:15], v[32:35]\n"
            "v_mfma_f32_16x16x32_bf16 v[36:39], v[68:71], v[28:31], v[36:39]\n"
            "v_mfma_f32_16x16x32_bf16 v[40:43], v[84:87], v[12:15], v[40:43]\n"
            "v_mfma_f32_16x16x32_bf16 v[44:47], v[84:87], v[28:31], v[44:47]\n"
            "s_nop 4\n"
            "v_cvt_pk_bf16_f32 v100, v32, v33\n"
            "v_cvt_pk_bf16_f32 v101, v34, v35\n"
            "v_cvt_pk_bf16_f32 v102, v40, v41\n"
            "v_cvt_pk_bf16_f32 v103, v42, v43\n"
            "v_cvt_pk_bf16_f32 v104, v36, v37\n"
            "v_cvt_pk_bf16_f32 v105, v38, v39\n"
            "v_cvt_pk_bf16_f32 v106, v44, v45\n"
            "v_cvt_pk_bf16_f32 v107, v46, v47\n"
            "W2P0_ZDONE_%=:\n"
            "s_nop 7\n"
            "v_mov_b32 v32, 0\n"
            "v_mov_b32 v33, 0\n"
            "v_mov_b32 v34, 0\n"
            "v_mov_b32 v35, 0\n"
            "v_mov_b32 v36, 0\n"
            "v_mov_b32 v37, 0\n"
            "v_mov_b32 v38, 0\n"
            "v_mov_b32 v39, 0\n"
            "v_mov_b32 v40, 0\n"
            "v_mov_b32 v41, 0\n"
            "v_mov_b32 v42, 0\n"
            "v_mov_b32 v43, 0\n"
            "v_mov_b32 v44, 0\n"
            "v_mov_b32 v45, 0\n"
            "v_mov_b32 v46, 0\n"
            "v_mov_b32 v47, 0\n"
            "v_mov_b32 v48, 0\n"
            "v_mov_b32 v49, 0\n"
            "v_mov_b32 v50, 0\n"
            "v_mov_b32 v51, 0\n"
            "v_mov_b32 v52, 0\n"
            "v_mov_b32 v53, 0\n"
            "v_mov_b32 v54, 0\n"
            "v_mov_b32 v55, 0\n"
            "s_add_u32 s52, s52, 16384\n"
            "s_addc_u32 s53, s53, 0\n"
            "s_add_u32 s68, s68, 1\n"
            "s_cmp_ge_u32 s68, 63\n"
            "s_cbranch_scc1 DONE_%=\n"
            "s_barrier\n"
            "s_branch W2P1_ENTRY_%=\n"
            "W2P0_RETRY_%=:\n"
            "s_waitcnt lgkmcnt(0)\n"
            "s_barrier\n"
            "s_nop 7\n"
            "v_mov_b32 v32, 0\n"
            "v_mov_b32 v33, 0\n"
            "v_mov_b32 v34, 0\n"
            "v_mov_b32 v35, 0\n"
            "v_mov_b32 v36, 0\n"
            "v_mov_b32 v37, 0\n"
            "v_mov_b32 v38, 0\n"
            "v_mov_b32 v39, 0\n"
            "v_mov_b32 v40, 0\n"
            "v_mov_b32 v41, 0\n"
            "v_mov_b32 v42, 0\n"
            "v_mov_b32 v43, 0\n"
            "v_mov_b32 v44, 0\n"
            "v_mov_b32 v45, 0\n"
            "v_mov_b32 v46, 0\n"
            "v_mov_b32 v47, 0\n"
            "s_sub_u32 s30, s30, 128\n"
            "s_subb_u32 s31, s31, 0\n"
            "W2P0_RETRY2_%=:\n"
            "s_add_u32 s76, s76, 1\n"
            "s_add_u32 s84, s84, 1\n"
            "s_cmp_gt_u32 s76, 0x20000\n"
            "s_cbranch_scc1 TIMEOUT_%=\n"
            "global_load_dwordx4 v[16:19], v109, s[52:53] sc1\n"
            "global_load_dwordx4 v[20:23], v109, s[52:53] offset:1024 sc1\n"
            "s_waitcnt vmcnt(0)\n"
            "v_max3_u32 v118, v17, v19, v21\n"
            "v_max_u32 v118, v118, v23\n"
            "v_cmp_eq_u32 vcc, -1, v118\n"
            "s_cbranch_vccnz W2P0_RETRY2_%=\n"
            "ds_write_b128 v110, v[16:19] offset:4096\n"
            "ds_write_b128 v110, v[20:23] offset:5120\n"
            "s_waitcnt lgkmcnt(0)\n"
            "s_barrier\n"
            "s_branch W2P0_AFTERB1_%=\n"
            "s_nop 15\n"
            "W2P1_ENTRY_%=:\n"
            "s_mov_b32 s76, 0\n"
            "W2P1_POLL_%=:\n"
            "s_mov_b32 m0, 4096\n"
            "s_nop 0\n"
            "global_load_lds_dwordx4 v109, s[52:53] sc1\n"
            "global_load_lds_dwordx4 v109, s[52:53] offset:1024 sc1\n"
            "s_waitcnt vmcnt(0)\n"
            "s_waitcnt lgkmcnt(0)\n"
            "s_barrier\n"
            "W2P1_AFTERB1_%=:\n"
            "ds_read_b128 v[0:3], v110 offset:0\n"
            "ds_read_b128 v[4:7], v110 offset:1024\n"
            "ds_read_b128 v[8:11], v110 offset:2048\n"
            "ds_read_b128 v[12:15], v110 offset:3072\n"
            "ds_read_b128 v[16:19], v110 offset:4096\n"
            "ds_read_b128 v[20:23], v110 offset:5120\n"
            "ds_read_b128 v[24:27], v110 offset:6144\n"
            "ds_read_b128 v[28:31], v110 offset:7168\n"
            "s_add_u32 s30, s30, 128\n"
            "s_addc_u32 s31, s31, 0\n"
            "s_waitcnt lgkmcnt(7)\n"
            "v_smfmac_f32_16x16x64_bf16 v[32:35], v[0:3], a[0:7], v108\n"
            "v_smfmac_f32_16x16x64_bf16 v[36:39], v[0:3], a[128:135], v108\n"
            "s_waitcnt lgkmcnt(6)\n"
            "v_max_u32 v118, v1, v3\n"
            "v_smfmac_f32_16x16x64_bf16 v[32:35], v[4:7], a[8:15], v108\n"
            "ds_read_b128 v[56:59], v110 offset:16384\n"
            "v_smfmac_f32_16x16x64_bf16 v[36:39], v[4:7], a[136:143], v108\n"
            "s_waitcnt lgkmcnt(6)\n"
            "v_max3_u32 v118, v118, v5, v7\n"
            "v_smfmac_f32_16x16x64_bf16 v[32:35], v[8:11], a[16:23], v108\n"
            "ds_read_b128 v[60:63], v110 offset:17408\n"
            "v_smfmac_f32_16x16x64_bf16 v[36:39], v[8:11], a[144:151], v108\n"
            "s_waitcnt lgkmcnt(6)\n"
            "v_max3_u32 v118, v118, v9, v11\n"
            "v_smfmac_f32_16x16x64_bf16 v[32:35], v[12:15], a[24:31], v108\n"
            "ds_read_b128 v[64:67], v110 offset:18432\n"
            "v_smfmac_f32_16x16x64_bf16 v[36:39], v[12:15], a[152:159], v108\n"
            "s_waitcnt lgkmcnt(6)\n"
            "v_max3_u32 v118, v118, v13, v15\n"
            "v_smfmac_f32_16x16x64_bf16 v[32:35], v[16:19], a[32:39], v108\n"
            "ds_read_b128 v[68:71], v110 offset:19456\n"
            "v_smfmac_f32_16x16x64_bf16 v[36:39], v[16:19], a[160:167], v108\n"
            "s_waitcnt lgkmcnt(6)\n"
            "v_max3_u32 v118, v118, v17, v19\n"
            "v_smfmac_f32_16x16x64_bf16 v[32:35], v[20:23], a[40:47], v108\n"
            "ds_read_b128 v[72:75], v110 offset:20480\n"
            "v_smfmac_f32_16x16x64_bf16 v[36:39], v[20:23], a[168:175], v108\n"
            "s_waitcnt lgkmcnt(6)\n"
            "v_max3_u32 v118, v118, v21, v23\n"
            "v_smfmac_f32_16x16x64_bf16 v[32:35], v[24:27], a[48:55], v108\n"
            "ds_read_b128 v[76:79], v110 offset:21504\n"
            "v_smfmac_f32_16x16x64_bf16 v[36:39], v[24:27], a[176:183], v108\n"
            "s_waitcnt lgkmcnt(6)\n"
            "v_max3_u32 v118, v118, v25, v27\n"
            "v_smfmac_f32_16x16x64_bf16 v[32:35], v[28:31], a[56:63], v108\n"
            "ds_read_b128 v[80:83], v110 offset:22528\n"
            "v_smfmac_f32_16x16x64_bf16 v[36:39], v[28:31], a[184:191], v108\n"
            "v_max3_u32 v118, v118, v29, v31\n"
            "v_smfmac_f32_16x16x64_bf16 v[40:43], v[0:3], a[64:71], v108\n"
            "ds_read_b128 v[84:87], v110 offset:23552\n"
            "v_cmp_eq_u32 vcc, -1, v118\n"
            "v_smfmac_f32_16x16x64_bf16 v[44:47], v[0:3], a[192:199], v108\n"
            "v_smfmac_f32_16x16x64_bf16 v[40:43], v[4:7], a[72:79], v108\n"
            "v_smfmac_f32_16x16x64_bf16 v[44:47], v[4:7], a[200:207], v108\n"
            "s_cbranch_vccnz W2P1_RETRY_%=\n"
            "s_mov_b32 vcc_lo, 0xcccccccc\n"
            "s_mov_b32 vcc_hi, 0xcccccccc\n"
            "v_smfmac_f32_16x16x64_bf16 v[40:43], v[8:11], a[80:87], v108\n"
            "v_smfmac_f32_16x16x64_bf16 v[44:47], v[8:11], a[208:215], v108\n"
            "v_smfmac_f32_16x16x64_bf16 v[40:43], v[12:15], a[88:95], v108\n"
            "v_smfmac_f32_16x16x64_bf16 v[44:47], v[12:15], a[216:223], v108\n"
            "v_add_f32 v36, v36, v37\n"
            "v_smfmac_f32_16x16x64_bf16 v[40:43], v[16:19], a[96:103], v108\n"
            "v_add_f32 v38, v38, v39\n"
            "v_smfmac_f32_16x16x64_bf16 v[44:47], v[16:19], a[224:231], v108\n"
            "v_add_f32 v32, v32, v33\n"
            "v_smfmac_f32_16x16x64_bf16 v[40:43], v[20:23], a[104:111], v108\n"
            "v_add_f32 v34, v34, v35\n"
            "v_smfmac_f32_16x16x64_bf16 v[44:47], v[20:23], a[232:239], v108\n"
            "v_cvt_pk_bf16_f32 v36, v36, v38\n"
            "v_smfmac_f32_16x16x64_bf16 v[40:43], v[24:27], a[112:119], v108\n"
            "v_cvt_pk_bf16_f32 v37, v32, v34\n"
            "v_smfmac_f32_16x16x64_bf16 v[44:47], v[24:27], a[240:247], v108\n"
            "ds_write_b64 v111, v[36:37] offset:0\n"
            "v_smfmac_f32_16x16x64_bf16 v[40:43], v[28:31], a[120:127], v108\n"
            "v_smfmac_f32_16x16x64_bf16 v[44:47], v[28:31], a[248:255], v108\n"
            "v_smfmac_f32_16x16x64_bf16 v[48:51], v[0:3], v[128:135], v108\n"
            "s_nop 5\n"
            "s_nop 0\n"
            "v_add_f32 v44, v44, v45\n"
            "v_add_f32 v46, v46, v47\n"
            "v_add_f32 v40, v40, v41\n"
            "v_add_f32 v42, v42, v43\n"
            "v_cvt_pk_bf16_f32 v44, v44, v46\n"
            "v_cvt_pk_bf16_f32 v45, v40, v42\n"
            "ds_write_b64 v111, v[44:45] offset:512\n"
            "v_smfmac_f32_16x16x64_bf16 v[52:55], v[0:3], v[192:199], v108\n"
            "v_smfmac_f32_16x16x64_bf16 v[48:51], v[4:7], v[136:143], v108\n"
            "v_smfmac_f32_16x16x64_bf16 v[52:55], v[4:7], v[200:207], v108\n"
            "v_smfmac_f32_16x16x64_bf16 v[48:51], v[8:11], v[144:151], v108\n"
            "v_smfmac_f32_16x16x64_bf16 v[52:55], v[8:11], v[208:215], v108\n"
            "s_waitcnt lgkmcnt(0)\n"
            "s_barrier\n"
            "ds_read_b64_tr_b16 v[32:33], v112 offset:0\n"
            "ds_read_b64_tr_b16 v[34:35], v112 offset:512\n"
            "ds_read_b64_tr_b16 v[36:37], v112 offset:1024\n"
            "ds_read_b64_tr_b16 v[38:39], v112 offset:1536\n"
            "ds_read_b64_tr_b16 v[40:41], v112 offset:2048\n"
            "ds_read_b64_tr_b16 v[42:43], v112 offset:2560\n"
            "ds_read_b64_tr_b16 v[44:45], v112 offset:3072\n"
            "ds_read_b64_tr_b16 v[46:47], v112 offset:3584\n"
            "s_mov_b32 m0, 32768\n"
            "s_nop 0\n"
            "global_load_lds_dwordx4 v110, s[18:19] offset:0 sc1\n"
            "global_load_lds_dwordx4 v110, s[18:19] offset:1024 sc1\n"
            "global_load_lds_dwordx4 v110, s[18:19] offset:2048 sc1\n"
            "global_load_lds_dwordx4 v110, s[18:19] offset:3072 sc1\n"
            "s_add_u32 s18, s18, 4096\n"
            "s_addc_u32 s19, s19, 0\n"
            "s_mov_b32 m0, 36864\n"
            "s_nop 0\n"
            "global_load_lds_dwordx4 v110, s[18:19] offset:0 sc1\n"
            "global_load_lds_dwordx4 v110, s[18:19] offset:1024 sc1\n"
            "global_load_lds_dwordx4 v110, s[18:19] offset:2048 sc1\n"
            "global_load_lds_dwordx4 v110, s[18:19] offset:3072 sc1\n"
            "v_smfmac_f32_16x16x64_bf16 v[48:51], v[12:15], v[152:159], v108\n"
            "v_smfmac_f32_16x16x64_bf16 v[52:55], v[12:15], v[216:223], v108\n"
            "s_waitcnt lgkmcnt(6)\n"
            "v_mfma_f32_16x16x32_bf16 v[96:99], v[32:35], v[32:35], 0\n"
            "s_waitcnt lgkmcnt(4)\n"
            "v_mfma_f32_16x16x32_bf16 v[96:99], v[36:39], v[36:39], v[96:99]\n"
            "s_waitcnt lgkmcnt(2)\n"
            "v_mfma_f32_16x16x32_bf16 v[96:99], v[40:43], v[40:43], v[96:99]\n"
            "s_waitcnt lgkmcnt(0)\n"
            "v_mfma_f32_16x16x32_bf16 v[96:99], v[44:47], v[44:47], v[96:99]\n"
            "v_mfma_f32_16x16x32_bf16 v[88:91], v[56:59], v[32:35], 0\n"
            "s_mov_b64 exec, 1\n"
            "global_load_dword v117, v110, s[30:31] sc1\n"
            "global_load_dword v60, v110, s[24:25] sc1\n"
            "s_mov_b64 exec, -1\n"
            "v_mov_b32 v12, 0x3f803f80\n"
            "v_mfma_f32_16x16x32_bf16 v[88:91], v[60:63], v[36:39], v[88:91]\n"
            "v_mov_b32 v13, 0x3f803f80\n"
            "v_mov_b32 v14, 0x3f803f80\n"
            "v_mfma_f32_16x16x32_bf16 v[88:91], v[64:67], v[40:43], v[88:91]\n"
            "v_mov_b32 v15, 0x3f803f80\n"
            "v_mov_b32 v6, 0x00003f80\n"
            "v_mfma_f32_16x16x32_bf16 v[88:91], v[68:71], v[44:47], v[88:91]\n"
            "v_mov_b32 v7, 0x00003f80\n"
            "v_mfma_f32_16x16x32_bf16 v[92:95], v[72:75], v[32:35], 0\n"
            "v_cndmask_b32_dpp v96, v97, v96, vcc quad_perm:[2,3,2,3] row_mask:0xf bank_mask:0xf\n"
            "v_mfma_f32_16x16x32_bf16 v[92:95], v[76:79], v[36:39], v[92:95]\n"
            "v_mfma_f32_16x16x32_bf16 v[92:95], v[80:83], v[40:43], v[92:95]\n"
            "v_mfma_f32_16x16x32_bf16 v[92:95], v[84:87], v[44:47], v[92:95]\n"
            "v_smfmac_f32_16x16x64_bf16 v[48:51], v[16:19], v[160:167], v108\n"
            "s_nop 6\n"
            "v_cndmask_b32_dpp v88, v92, v88, vcc quad_perm:[2,3,2,3] row_mask:0xf bank_mask:0xf\n"
            "v_cndmask_b32_dpp v89, v93, v89, vcc quad_perm:[2,3,2,3] row_mask:0xf bank_mask:0xf\n"
            "v_cndmask_b32_dpp v90, v94, v90, vcc quad_perm:[2,3,2,3] row_mask:0xf bank_mask:0xf\n"
            "v_cndmask_b32_dpp v91, v95, v91, vcc quad_perm:[2,3,2,3] row_mask:0xf bank_mask:0xf\n"
            "v_max3_f32 v118, v96, v88, v89\n"
            "v_max3_f32 v118, v118, v90, v91\n"
            "v_smfmac_f32_16x16x64_bf16 v[52:55], v[16:19], v[224:231], v108\n"
            "s_nop 0\n"
            "v_max_f32_dpp v118, v118, v118 quad_perm:[2,3,0,1] row_mask:0xf bank_mask:0xf\n"
            "v_mov_b32 v119, v118\n"
            "v_smfmac_f32_16x16x64_bf16 v[48:51], v[20:23], v[168:175], v108\n"
            "s_nop 0\n"
            "v_permlane32_swap_b32 v118, v119\n"
            "v_max_f32 v118, v118, v119\n"
            "v_mov_b32 v119, v118\n"
            "v_smfmac_f32_16x16x64_bf16 v[52:55], v[20:23], v[232:239], v108\n"
            "s_nop 0\n"
            "v_permlane16_swap_b32 v118, v119\n"
            "v_max_f32 v118, v118, v119\n"
            "v_smfmac_f32_16x16x64_bf16 v[48:51], v[24:27], v[176:183], v108\n"
            "v_mul_f32 v118, s94, v118\n"
            "v_smfmac_f32_16x16x64_bf16 v[52:55], v[24:27], v[240:247], v108\n"
            "v_fma_f32 v88, v88, s94, -v118\n"
            "v_fma_f32 v89, v89, s94, -v118\n"
            "v_fma_f32 v90, v90, s94, -v118\n"
            "v_fma_f32 v91, v91, s94, -v118\n"
            "v_fma_f32 v96, v96, s94, -v118\n"
            "v_smfmac_f32_16x16x64_bf16 v[48:51], v[28:31], v[184:191], v108\n"
            "v_exp_f32 v88, v88\n"
            "v_exp_f32 v89, v89\n"
            "v_exp_f32 v90, v90\n"
            "v_exp_f32 v91, v91\n"
            "v_exp_f32 v96, v96\n"
            "v_smfmac_f32_16x16x64_bf16 v[52:55], v[28:31], v[248:255], v108\n"
            "v_cvt_pk_bf16_f32 v0, v88, v89\n"
            "v_cvt_pk_bf16_f32 v1, v90, v91\n"
            "v_cvt_pk_bf16_f32 v4, v96, v96\n"
            "v_mov_b32_dpp v2, v0 quad_perm:[0,1,0,1] row_mask:0xf bank_mask:0xf\n"
            "v_mov_b32_dpp v3, v1 quad_perm:[0,1,0,1] row_mask:0xf bank_mask:0xf\n"
            "v_mov_b32_dpp v5, v4 quad_perm:[0,1,0,1] row_mask:0xf bank_mask:0xf\n"
            "s_nop 0\n"
            "v_mfma_f32_16x16x32_bf16 v[92:95], v[12:15], v[0:3], 0\n"
            "v_cvt_pk_bf16_f32 v8, v48, v49\n"
            "v_cvt_pk_bf16_f32 v9, v50, v51\n"
            "v_mfma_f32_16x16x32_bf16 v[12:15], v[100:103], v[0:3], 0\n"
            "v_cvt_pk_bf16_f32 v10, v52, v53\n"
            "v_cvt_pk_bf16_f32 v11, v54, v55\n"
            "v_mfma_f32_16x16x16_bf16 v[92:95], v[6:7], v[4:5], v[92:95]\n"
            "v_mfma_f32_16x16x32_bf16 v[88:91], v[104:107], v[0:3], 0\n"
            "v_mfma_f32_16x16x16_bf16 v[12:15], v[8:9], v[4:5], v[12:15]\n"
            "v_mfma_f32_16x16x16_bf16 v[88:91], v[10:11], v[4:5], v[88:91]\n"
            "s_nop 4\n"
            "v_mov_b32_dpp v118, v92 quad_perm:[2,3,2,3] row_mask:0xf bank_mask:0xf\n"
            "v_rcp_f32 v118, v118\n"
            "s_nop 0\n"
            "v_cndmask_b32_dpp v12, v88, v12, vcc quad_perm:[2,3,2,3] row_mask:0xf bank_mask:0xf\n"
            "v_cndmask_b32_dpp v13, v89, v13, vcc quad_perm:[2,3,2,3] row_mask:0xf bank_mask:0xf\n"
            "v_cndmask_b32_dpp v14, v90, v14, vcc quad_perm:[2,3,2,3] row_mask:0xf bank_mask:0xf\n"
            "v_cndmask_b32_dpp v15, v91, v15, vcc quad_perm:[2,3,2,3] row_mask:0xf bank_mask:0xf\n"
            "v_mul_f32 v12, v12, v118\n"
            "v_mul_f32 v13, v13, v118\n"
            "v_mul_f32 v14, v14, v118\n"
            "v_mul_f32 v15, v15, v118\n"
            "v_cvt_pk_bf16_f32 v20, v12, v13\n"
            "v_cvt_pk_bf16_f32 v21, v14, v15\n"
            "v_min_u32 v21, -2, v21\n"
            "global_store_dwordx2 v113, v[20:21], s[56:57] sc1\n"
            "W2P1_SCH_%=:\n"
            "s_waitcnt vmcnt(1)\n"
            "v_readfirstlane_b32 s88, v117\n"
            "v_readfirstlane_b32 s21, v60\n"
            "s_sub_u32 s78, s88, s32\n"
            "s_cmp_le_u32 s78, 208\n"
            "s_cselect_b32 s78, s78, s33\n"
            "s_add_u32 s32, s32, s33\n"
            "s_cmp_eq_u32 s88, -1\n"
            "s_cselect_b32 s32, s32, s88\n"
            "s_cselect_b32 s33, s33, s78\n"
            "s_add_u32 s91, s68, 3\n"
            "s_min_u32 s91, s91, 63\n"
            "s_cmp_gt_u32 s21, s91\n"
            "s_cbranch_scc1 W2P1_FKV_SKIP_%=\n"
            "s_mov_b32 s69, 0\n"
            "W2P1_FKV_L_%=:\n"
            "s_mov_b64 exec, 1\n"
            "global_load_dword v118, v110, s[24:25] sc1\n"
            "s_mov_b64 exec, -1\n"
            "s_waitcnt vmcnt(0)\n"
            "v_readfirstlane_b32 s21, v118\n"
            "s_nop 0\n"
            "s_cmp_gt_u32 s21, s91\n"
            "s_cbranch_scc1 W2P1_FKV_SKIP_%=\n"
            "s_add_u32 s69, s69, 1\n"
            "s_cmp_gt_u32 s69, 0x80000\n"
            "s_cbranch_scc1 TIMEOUT_%=\n"
            "s_sleep 2\n"
            "s_branch W2P1_FKV_L_%=\n"
            "W2P1_FKV_SKIP_%=:\n"
            "s_lshr_b32 s78, s91, 2\n"
            "s_xor_b32 s78, s78, s20\n"
            "s_and_b32 s78, s78, 1\n"
            "s_lshl_b32 s78, s78, 13\n"
            "s_add_u32 s18, s50, s78\n"
            "s_addc_u32 s19, s51, 0\n"
            "s_mul_i32 s78, s91, 524288\n"
            "s_add_u32 s18, s18, s78\n"
            "s_addc_u32 s19, s19, 0\n"
            "ds_read_b128 v[0:3], v114 offset:0\n"
            "ds_read_b128 v[4:7], v114 offset:1024\n"
            "ds_read_b128 v[8:11], v114 offset:2048\n"
            "ds_read_b128 v[12:15], v114 offset:3072\n"
            "ds_read_b128 v[16:19], v114 offset:4096\n"
            "ds_read_b128 v[20:23], v114 offset:5120\n"
            "ds_read_b128 v[24:27], v114 offset:6144\n"
            "ds_read_b128 v[28:31], v114 offset:7168\n"
            "ds_read_b128 v[56:59], v110 offset:24576\n"
            "ds_read_b128 v[60:63], v110 offset:25600\n"
            "ds_read_b128 v[64:67], v110 offset:26624\n"
            "ds_read_b128 v[68:71], v110 offset:27648\n"
            "ds_read_b128 v[72:75], v110 offset:28672\n"
            "ds_read_b128 v[76:79], v110 offset:29696\n"
            "ds_read_b128 v[80:83], v110 offset:30720\n"
            "ds_read_b128 v[84:87], v110 offset:31744\n"
            "s_waitcnt lgkmcnt(0)\n"
            "v_mfma_f32_16x16x32_bf16 v[32:35], v[56:59], v[0:3], 0\n"
            "v_mfma_f32_16x16x32_bf16 v[36:39], v[56:59], v[16:19], 0\n"
            "v_mfma_f32_16x16x32_bf16 v[40:43], v[72:75], v[0:3], 0\n"
            "v_mfma_f32_16x16x32_bf16 v[44:47], v[72:75], v[16:19], 0\n"
            "v_mfma_f32_16x16x32_bf16 v[32:35], v[60:63], v[4:7], v[32:35]\n"
            "v_mfma_f32_16x16x32_bf16 v[36:39], v[60:63], v[20:23], v[36:39]\n"
            "v_mfma_f32_16x16x32_bf16 v[40:43], v[76:79], v[4:7], v[40:43]\n"
            "v_mfma_f32_16x16x32_bf16 v[44:47], v[76:79], v[20:23], v[44:47]\n"
            "v_mfma_f32_16x16x32_bf16 v[32:35], v[64:67], v[8:11], v[32:35]\n"
            "v_mfma_f32_16x16x32_bf16 v[36:39], v[64:67], v[24:27], v[36:39]\n"
            "v_mfma_f32_16x16x32_bf16 v[40:43], v[80:83], v[8:11], v[40:43]\n"
            "v_mfma_f32_16x16x32_bf16 v[44:47], v[80:83], v[24:27], v[44:47]\n"
            "v_mfma_f32_16x16x32_bf16 v[32:35], v[68:71], v[12:15], v[32:35]\n"
            "v_mfma_f32_16x16x32_bf16 v[36:39], v[68:71], v[28:31], v[36:39]\n"
            "v_mfma_f32_16x16x32_bf16 v[40:43], v[84:87], v[12:15], v[40:43]\n"
            "v_mfma_f32_16x16x32_bf16 v[44:47], v[84:87], v[28:31], v[44:47]\n"
            "s_nop 4\n"
            "v_cvt_pk_bf16_f32 v100, v32, v33\n"
            "v_cvt_pk_bf16_f32 v101, v34, v35\n"
            "v_cvt_pk_bf16_f32 v102, v40, v41\n"
            "v_cvt_pk_bf16_f32 v103, v42, v43\n"
            "v_cvt_pk_bf16_f32 v104, v36, v37\n"
            "v_cvt_pk_bf16_f32 v105, v38, v39\n"
            "v_cvt_pk_bf16_f32 v106, v44, v45\n"
            "v_cvt_pk_bf16_f32 v107, v46, v47\n"
            "W2P1_ZDONE_%=:\n"
            "s_nop 7\n"
            "v_mov_b32 v32, 0\n"
            "v_mov_b32 v33, 0\n"
            "v_mov_b32 v34, 0\n"
            "v_mov_b32 v35, 0\n"
            "v_mov_b32 v36, 0\n"
            "v_mov_b32 v37, 0\n"
            "v_mov_b32 v38, 0\n"
            "v_mov_b32 v39, 0\n"
            "v_mov_b32 v40, 0\n"
            "v_mov_b32 v41, 0\n"
            "v_mov_b32 v42, 0\n"
            "v_mov_b32 v43, 0\n"
            "v_mov_b32 v44, 0\n"
            "v_mov_b32 v45, 0\n"
            "v_mov_b32 v46, 0\n"
            "v_mov_b32 v47, 0\n"
            "v_mov_b32 v48, 0\n"
            "v_mov_b32 v49, 0\n"
            "v_mov_b32 v50, 0\n"
            "v_mov_b32 v51, 0\n"
            "v_mov_b32 v52, 0\n"
            "v_mov_b32 v53, 0\n"
            "v_mov_b32 v54, 0\n"
            "v_mov_b32 v55, 0\n"
            "s_add_u32 s52, s52, 16384\n"
            "s_addc_u32 s53, s53, 0\n"
            "s_add_u32 s68, s68, 1\n"
            "s_cmp_ge_u32 s68, 63\n"
            "s_cbranch_scc1 DONE_%=\n"
            "s_barrier\n"
            "s_branch W2P0_ENTRY_%=\n"
            "W2P1_RETRY_%=:\n"
            "s_waitcnt lgkmcnt(0)\n"
            "s_barrier\n"
            "s_nop 7\n"
            "v_mov_b32 v32, 0\n"
            "v_mov_b32 v33, 0\n"
            "v_mov_b32 v34, 0\n"
            "v_mov_b32 v35, 0\n"
            "v_mov_b32 v36, 0\n"
            "v_mov_b32 v37, 0\n"
            "v_mov_b32 v38, 0\n"
            "v_mov_b32 v39, 0\n"
            "v_mov_b32 v40, 0\n"
            "v_mov_b32 v41, 0\n"
            "v_mov_b32 v42, 0\n"
            "v_mov_b32 v43, 0\n"
            "v_mov_b32 v44, 0\n"
            "v_mov_b32 v45, 0\n"
            "v_mov_b32 v46, 0\n"
            "v_mov_b32 v47, 0\n"
            "s_sub_u32 s30, s30, 128\n"
            "s_subb_u32 s31, s31, 0\n"
            "W2P1_RETRY2_%=:\n"
            "s_add_u32 s76, s76, 1\n"
            "s_add_u32 s84, s84, 1\n"
            "s_cmp_gt_u32 s76, 0x20000\n"
            "s_cbranch_scc1 TIMEOUT_%=\n"
            "global_load_dwordx4 v[16:19], v109, s[52:53] sc1\n"
            "global_load_dwordx4 v[20:23], v109, s[52:53] offset:1024 sc1\n"
            "s_waitcnt vmcnt(0)\n"
            "v_max3_u32 v118, v17, v19, v21\n"
            "v_max_u32 v118, v118, v23\n"
            "v_cmp_eq_u32 vcc, -1, v118\n"
            "s_cbranch_vccnz W2P1_RETRY2_%=\n"
            "ds_write_b128 v110, v[16:19] offset:4096\n"
            "ds_write_b128 v110, v[20:23] offset:5120\n"
            "s_waitcnt lgkmcnt(0)\n"
            "s_barrier\n"
            "s_branch W2P1_AFTERB1_%=\n"
            "s_nop 15\n"
            "W3P0_ENTRY_%=:\n"
            "s_mov_b32 s76, 0\n"
            "W3P0_POLL_%=:\n"
            "s_mov_b32 m0, 6144\n"
            "s_nop 0\n"
            "global_load_lds_dwordx4 v109, s[52:53] sc1\n"
            "global_load_lds_dwordx4 v109, s[52:53] offset:1024 sc1\n"
            "s_waitcnt vmcnt(0)\n"
            "s_waitcnt lgkmcnt(0)\n"
            "s_barrier\n"
            "W3P0_AFTERB1_%=:\n"
            "ds_read_b128 v[0:3], v110 offset:0\n"
            "ds_read_b128 v[4:7], v110 offset:1024\n"
            "ds_read_b128 v[8:11], v110 offset:2048\n"
            "ds_read_b128 v[12:15], v110 offset:3072\n"
            "ds_read_b128 v[16:19], v110 offset:4096\n"
            "ds_read_b128 v[20:23], v110 offset:5120\n"
            "ds_read_b128 v[24:27], v110 offset:6144\n"
            "ds_read_b128 v[28:31], v110 offset:7168\n"
            "s_add_u32 s30, s30, 128\n"
            "s_addc_u32 s31, s31, 0\n"
            "s_waitcnt lgkmcnt(7)\n"
            "v_smfmac_f32_16x16x64_bf16 v[32:35], v[0:3], a[0:7], v108\n"
            "v_smfmac_f32_16x16x64_bf16 v[36:39], v[0:3], a[128:135], v108\n"
            "s_waitcnt lgkmcnt(6)\n"
            "v_max_u32 v118, v1, v3\n"
            "v_smfmac_f32_16x16x64_bf16 v[32:35], v[4:7], a[8:15], v108\n"
            "ds_read_b128 v[56:59], v110 offset:16384\n"
            "v_smfmac_f32_16x16x64_bf16 v[36:39], v[4:7], a[136:143], v108\n"
            "s_waitcnt lgkmcnt(6)\n"
            "v_max3_u32 v118, v118, v5, v7\n"
            "v_smfmac_f32_16x16x64_bf16 v[32:35], v[8:11], a[16:23], v108\n"
            "ds_read_b128 v[60:63], v110 offset:17408\n"
            "v_smfmac_f32_16x16x64_bf16 v[36:39], v[8:11], a[144:151], v108\n"
            "s_waitcnt lgkmcnt(6)\n"
            "v_max3_u32 v118, v118, v9, v11\n"
            "v_smfmac_f32_16x16x64_bf16 v[32:35], v[12:15], a[24:31], v108\n"
            "ds_read_b128 v[64:67], v110 offset:18432\n"
            "v_smfmac_f32_16x16x64_bf16 v[36:39], v[12:15], a[152:159], v108\n"
            "s_waitcnt lgkmcnt(6)\n"
            "v_max3_u32 v118, v118, v13, v15\n"
            "v_smfmac_f32_16x16x64_bf16 v[32:35], v[16:19], a[32:39], v108\n"
            "ds_read_b128 v[68:71], v110 offset:19456\n"
            "v_smfmac_f32_16x16x64_bf16 v[36:39], v[16:19], a[160:167], v108\n"
            "s_waitcnt lgkmcnt(6)\n"
            "v_max3_u32 v118, v118, v17, v19\n"
            "v_smfmac_f32_16x16x64_bf16 v[32:35], v[20:23], a[40:47], v108\n"
            "ds_read_b128 v[72:75], v110 offset:20480\n"
            "v_smfmac_f32_16x16x64_bf16 v[36:39], v[20:23], a[168:175], v108\n"
            "s_waitcnt lgkmcnt(6)\n"
            "v_max3_u32 v118, v118, v21, v23\n"
            "v_smfmac_f32_16x16x64_bf16 v[32:35], v[24:27], a[48:55], v108\n"
            "ds_read_b128 v[76:79], v110 offset:21504\n"
            "v_smfmac_f32_16x16x64_bf16 v[36:39], v[24:27], a[176:183], v108\n"
            "s_waitcnt lgkmcnt(6)\n"
            "v_max3_u32 v118, v118, v25, v27\n"
            "v_smfmac_f32_16x16x64_bf16 v[32:35], v[28:31], a[56:63], v108\n"
            "ds_read_b128 v[80:83], v110 offset:22528\n"
            "v_smfmac_f32_16x16x64_bf16 v[36:39], v[28:31], a[184:191], v108\n"
            "v_max3_u32 v118, v118, v29, v31\n"
            "v_smfmac_f32_16x16x64_bf16 v[40:43], v[0:3], a[64:71], v108\n"
            "ds_read_b128 v[84:87], v110 offset:23552\n"
            "v_cmp_eq_u32 vcc, -1, v118\n"
            "v_smfmac_f32_16x16x64_bf16 v[44:47], v[0:3], a[192:199], v108\n"
            "v_smfmac_f32_16x16x64_bf16 v[40:43], v[4:7], a[72:79], v108\n"
            "v_smfmac_f32_16x16x64_bf16 v[44:47], v[4:7], a[200:207], v108\n"
            "s_cbranch_vccnz W3P0_RETRY_%=\n"
            "s_mov_b32 vcc_lo, 0xcccccccc\n"
            "s_mov_b32 vcc_hi, 0xcccccccc\n"
            "v_smfmac_f32_16x16x64_bf16 v[40:43], v[8:11], a[80:87], v108\n"
            "v_smfmac_f32_16x16x64_bf16 v[44:47], v[8:11], a[208:215], v108\n"
            "v_smfmac_f32_16x16x64_bf16 v[40:43], v[12:15], a[88:95], v108\n"
            "v_smfmac_f32_16x16x64_bf16 v[44:47], v[12:15], a[216:223], v108\n"
            "v_add_f32 v36, v36, v37\n"
            "v_smfmac_f32_16x16x64_bf16 v[40:43], v[16:19], a[96:103], v108\n"
            "v_add_f32 v38, v38, v39\n"
            "v_smfmac_f32_16x16x64_bf16 v[44:47], v[16:19], a[224:231], v108\n"
            "v_add_f32 v32, v32, v33\n"
            "v_smfmac_f32_16x16x64_bf16 v[40:43], v[20:23], a[104:111], v108\n"
            "v_add_f32 v34, v34, v35\n"
            "v_smfmac_f32_16x16x64_bf16 v[44:47], v[20:23], a[232:239], v108\n"
            "v_cvt_pk_bf16_f32 v36, v36, v38\n"
            "v_smfmac_f32_16x16x64_bf16 v[40:43], v[24:27], a[112:119], v108\n"
            "v_cvt_pk_bf16_f32 v37, v32, v34\n"
            "v_smfmac_f32_16x16x64_bf16 v[44:47], v[24:27], a[240:247], v108\n"
            "ds_write_b64 v111, v[36:37] offset:0\n"
            "v_smfmac_f32_16x16x64_bf16 v[40:43], v[28:31], a[120:127], v108\n"
            "v_smfmac_f32_16x16x64_bf16 v[44:47], v[28:31], a[248:255], v108\n"
            "v_smfmac_f32_16x16x64_bf16 v[48:51], v[0:3], v[128:135], v108\n"
            "s_nop 5\n"
            "s_nop 0\n"
            "v_add_f32 v44, v44, v45\n"
            "v_add_f32 v46, v46, v47\n"
            "v_add_f32 v40, v40, v41\n"
            "v_add_f32 v42, v42, v43\n"
            "v_cvt_pk_bf16_f32 v44, v44, v46\n"
            "v_cvt_pk_bf16_f32 v45, v40, v42\n"
            "ds_write_b64 v111, v[44:45] offset:512\n"
            "v_smfmac_f32_16x16x64_bf16 v[52:55], v[0:3], v[192:199], v108\n"
            "v_smfmac_f32_16x16x64_bf16 v[48:51], v[4:7], v[136:143], v108\n"
            "v_smfmac_f32_16x16x64_bf16 v[52:55], v[4:7], v[200:207], v108\n"
            "v_smfmac_f32_16x16x64_bf16 v[48:51], v[8:11], v[144:151], v108\n"
            "v_smfmac_f32_16x16x64_bf16 v[52:55], v[8:11], v[208:215], v108\n"
            "s_waitcnt lgkmcnt(0)\n"
            "s_barrier\n"
            "ds_read_b64_tr_b16 v[32:33], v112 offset:0\n"
            "ds_read_b64_tr_b16 v[34:35], v112 offset:512\n"
            "ds_read_b64_tr_b16 v[36:37], v112 offset:1024\n"
            "ds_read_b64_tr_b16 v[38:39], v112 offset:1536\n"
            "ds_read_b64_tr_b16 v[40:41], v112 offset:2048\n"
            "ds_read_b64_tr_b16 v[42:43], v112 offset:2560\n"
            "ds_read_b64_tr_b16 v[44:45], v112 offset:3072\n"
            "ds_read_b64_tr_b16 v[46:47], v112 offset:3584\n"
            "v_smfmac_f32_16x16x64_bf16 v[48:51], v[12:15], v[152:159], v108\n"
            "v_smfmac_f32_16x16x64_bf16 v[52:55], v[12:15], v[216:223], v108\n"
            "s_waitcnt lgkmcnt(6)\n"
            "v_mfma_f32_16x16x32_bf16 v[96:99], v[32:35], v[32:35], 0\n"
            "s_waitcnt lgkmcnt(4)\n"
            "v_mfma_f32_16x16x32_bf16 v[96:99], v[36:39], v[36:39], v[96:99]\n"
            "s_waitcnt lgkmcnt(2)\n"
            "v_mfma_f32_16x16x32_bf16 v[96:99], v[40:43], v[40:43], v[96:99]\n"
            "s_waitcnt lgkmcnt(0)\n"
            "v_mfma_f32_16x16x32_bf16 v[96:99], v[44:47], v[44:47], v[96:99]\n"
            "v_mfma_f32_16x16x32_bf16 v[88:91], v[56:59], v[32:35], 0\n"
            "s_mov_b64 exec, 1\n"
            "global_load_dword v117, v110, s[30:31] sc1\n"
            "s_mov_b64 exec, -1\n"
            "v_mov_b32 v12, 0x3f803f80\n"
            "v_mfma_f32_16x16x32_bf16 v[88:91], v[60:63], v[36:39], v[88:91]\n"
            "v_mov_b32 v13, 0x3f803f80\n"
            "v_mov_b32 v14, 0x3f803f80\n"
            "v_mfma_f32_16x16x32_bf16 v[88:91], v[64:67], v[40:43], v[88:91]\n"
            "v_mov_b32 v15, 0x3f803f80\n"
            "v_mov_b32 v6, 0x00003f80\n"
            "v_mfma_f32_16x16x32_bf16 v[88:91], v[68:71], v[44:47], v[88:91]\n"
            "v_mov_b32 v7, 0x00003f80\n"
            "v_mfma_f32_16x16x32_bf16 v[92:95], v[72:75], v[32:35], 0\n"
            "v_cndmask_b32_dpp v96, v97, v96, vcc quad_perm:[2,3,2,3] row_mask:0xf bank_mask:0xf\n"
            "v_mfma_f32_16x16x32_bf16 v[92:95], v[76:79], v[36:39], v[92:95]\n"
            "v_mfma_f32_16x16x32_bf16 v[92:95], v[80:83], v[40:43], v[92:95]\n"
            "v_mfma_f32_16x16x32_bf16 v[92:95], v[84:87], v[44:47], v[92:95]\n"
            "v_smfmac_f32_16x16x64_bf16 v[48:51], v[16:19], v[160:167], v108\n"
            "s_nop 6\n"
            "v_cndmask_b32_dpp v88, v92, v88, vcc quad_perm:[2,3,2,3] row_mask:0xf bank_mask:0xf\n"
            "v_cndmask_b32_dpp v89, v93, v89, vcc quad_perm:[2,3,2,3] row_mask:0xf bank_mask:0xf\n"
            "v_cndmask_b32_dpp v90, v94, v90, vcc quad_perm:[2,3,2,3] row_mask:0xf bank_mask:0xf\n"
            "v_cndmask_b32_dpp v91, v95, v91, vcc quad_perm:[2,3,2,3] row_mask:0xf bank_mask:0xf\n"
            "v_max3_f32 v118, v96, v88, v89\n"
            "v_max3_f32 v118, v118, v90, v91\n"
            "v_smfmac_f32_16x16x64_bf16 v[52:55], v[16:19], v[224:231], v108\n"
            "s_nop 0\n"
            "v_max_f32_dpp v118, v118, v118 quad_perm:[2,3,0,1] row_mask:0xf bank_mask:0xf\n"
            "v_mov_b32 v119, v118\n"
            "v_smfmac_f32_16x16x64_bf16 v[48:51], v[20:23], v[168:175], v108\n"
            "s_nop 0\n"
            "v_permlane32_swap_b32 v118, v119\n"
            "v_max_f32 v118, v118, v119\n"
            "v_mov_b32 v119, v118\n"
            "v_smfmac_f32_16x16x64_bf16 v[52:55], v[20:23], v[232:239], v108\n"
            "s_nop 0\n"
            "v_permlane16_swap_b32 v118, v119\n"
            "v_max_f32 v118, v118, v119\n"
            "v_smfmac_f32_16x16x64_bf16 v[48:51], v[24:27], v[176:183], v108\n"
            "v_mul_f32 v118, s94, v118\n"
            "v_smfmac_f32_16x16x64_bf16 v[52:55], v[24:27], v[240:247], v108\n"
            "v_fma_f32 v88, v88, s94, -v118\n"
            "v_fma_f32 v89, v89, s94, -v118\n"
            "v_fma_f32 v90, v90, s94, -v118\n"
            "v_fma_f32 v91, v91, s94, -v118\n"
            "v_fma_f32 v96, v96, s94, -v118\n"
            "v_smfmac_f32_16x16x64_bf16 v[48:51], v[28:31], v[184:191], v108\n"
            "v_exp_f32 v88, v88\n"
            "v_exp_f32 v89, v89\n"
            "v_exp_f32 v90, v90\n"
            "v_exp_f32 v91, v91\n"
            "v_exp_f32 v96, v96\n"
            "v_smfmac_f32_16x16x64_bf16 v[52:55], v[28:31], v[248:255], v108\n"
            "v_cvt_pk_bf16_f32 v0, v88, v89\n"
            "v_cvt_pk_bf16_f32 v1, v90, v91\n"
            "v_cvt_pk_bf16_f32 v4, v96, v96\n"
            "v_mov_b32_dpp v2, v0 quad_perm:[0,1,0,1] row_mask:0xf bank_mask:0xf\n"
            "v_mov_b32_dpp v3, v1 quad_perm:[0,1,0,1] row_mask:0xf bank_mask:0xf\n"
            "v_mov_b32_dpp v5, v4 quad_perm:[0,1,0,1] row_mask:0xf bank_mask:0xf\n"
            "s_nop 0\n"
            "v_mfma_f32_16x16x32_bf16 v[92:95], v[12:15], v[0:3], 0\n"
            "v_cvt_pk_bf16_f32 v8, v48, v49\n"
            "v_cvt_pk_bf16_f32 v9, v50, v51\n"
            "v_mfma_f32_16x16x32_bf16 v[12:15], v[100:103], v[0:3], 0\n"
            "v_cvt_pk_bf16_f32 v10, v52, v53\n"
            "v_cvt_pk_bf16_f32 v11, v54, v55\n"
            "v_mfma_f32_16x16x16_bf16 v[92:95], v[6:7], v[4:5], v[92:95]\n"
            "v_mfma_f32_16x16x32_bf16 v[88:91], v[104:107], v[0:3], 0\n"
            "v_mfma_f32_16x16x16_bf16 v[12:15], v[8:9], v[4:5], v[12:15]\n"
            "v_mfma_f32_16x16x16_bf16 v[88:91], v[10:11], v[4:5], v[88:91]\n"
            "s_nop 4\n"
            "v_mov_b32_dpp v118, v92 quad_perm:[2,3,2,3] row_mask:0xf bank_mask:0xf\n"
            "v_rcp_f32 v118, v118\n"
            "s_nop 0\n"
            "v_cndmask_b32_dpp v12, v88, v12, vcc quad_perm:[2,3,2,3] row_mask:0xf bank_mask:0xf\n"
            "v_cndmask_b32_dpp v13, v89, v13, vcc quad_perm:[2,3,2,3] row_mask:0xf bank_mask:0xf\n"
            "v_cndmask_b32_dpp v14, v90, v14, vcc quad_perm:[2,3,2,3] row_mask:0xf bank_mask:0xf\n"
            "v_cndmask_b32_dpp v15, v91, v15, vcc quad_perm:[2,3,2,3] row_mask:0xf bank_mask:0xf\n"
            "v_mul_f32 v12, v12, v118\n"
            "v_mul_f32 v13, v13, v118\n"
            "v_mul_f32 v14, v14, v118\n"
            "v_mul_f32 v15, v15, v118\n"
            "v_cvt_pk_bf16_f32 v20, v12, v13\n"
            "v_cvt_pk_bf16_f32 v21, v14, v15\n"
            "v_min_u32 v21, -2, v21\n"
            "global_store_dwordx2 v113, v[20:21], s[54:55] sc1\n"
            "W3P0_SCH_%=:\n"
            "s_waitcnt vmcnt(1)\n"
            "v_readfirstlane_b32 s88, v117\n"
            "s_nop 0\n"
            "s_sub_u32 s78, s88, s32\n"
            "s_cmp_le_u32 s78, 208\n"
            "s_cselect_b32 s78, s78, s33\n"
            "s_add_u32 s32, s32, s33\n"
            "s_cmp_eq_u32 s88, -1\n"
            "s_cselect_b32 s32, s32, s88\n"
            "s_cselect_b32 s17, 1, 0\n"
            "s_cselect_b32 s33, s33, s78\n"
            "s_cmp_lg_u32 s76, 0\n"
            "s_cselect_b32 s88, s17, 0\n"
            "s_cmp_eq_u32 s88, 0\n"
            "s_cbranch_scc0 W3P0_SCHRETRY_%=\n"
            "W3P0_SCHOK_%=:\n"
            "s_cmp_lt_i32 s75, 0\n"
            "s_cbranch_scc1 W3P0_NORED_%=\n"
            "s_add_u32 s60, s54, 0\n"
            "s_addc_u32 s61, s55, 0\n"
            "s_add_u32 s62, s54, 131072\n"
            "s_addc_u32 s63, s55, 0\n"
            "s_add_u32 s64, s54, 262144\n"
            "s_addc_u32 s65, s55, 0\n"
            "s_add_u32 s66, s54, 393216\n"
            "s_addc_u32 s67, s55, 0\n"
            "s_add_u32 s90, s52, 16384\n"
            "s_addc_u32 s91, s53, 0\n"
            "v_mov_b32 v118, 45056\n"
            "ds_read_b64 v[6:7], v118\n"
            "ds_read_b32 v119, v118 offset:8\n"
            "s_waitcnt lgkmcnt(0)\n"
            "v_readfirstlane_b32 s40, v6\n"
            "v_readfirstlane_b32 s41, v7\n"
            "v_readfirstlane_b32 s42, v119\n"
            "ds_read_b128 v[0:3], v114 offset:0\n"
            "ds_read_b128 v[4:7], v114 offset:1024\n"
            "ds_read_b128 v[8:11], v114 offset:2048\n"
            "ds_read_b128 v[12:15], v114 offset:3072\n"
            "ds_read_b128 v[16:19], v114 offset:4096\n"
            "ds_read_b128 v[20:23], v114 offset:5120\n"
            "ds_read_b128 v[24:27], v114 offset:6144\n"
            "ds_read_b128 v[28:31], v114 offset:7168\n"
            "ds_read_b128 v[56:59], v110 offset:32768\n"
            "ds_read_b128 v[60:63], v110 offset:33792\n"
            "ds_read_b128 v[64:67], v110 offset:34816\n"
            "ds_read_b128 v[68:71], v110 offset:35840\n"
            "ds_read_b128 v[72:75], v110 offset:36864\n"
            "ds_read_b128 v[76:79], v110 offset:37888\n"
            "ds_read_b128 v[80:83], v110 offset:38912\n"
            "ds_read_b128 v[84:87], v110 offset:39936\n"
            "s_sub_u32 s88, s32, s34\n"
            "s_add_u32 s88, s88, s35\n"
            "s_sub_u32 s45, s88, s41\n"
            "s_mul_i32 s45, s45, s42\n"
            "s_ashr_i32 s45, s45, 8\n"
            "s_add_u32 s45, s45, s40\n"
            "s_memtime s[78:79]\n"
            "s_waitcnt lgkmcnt(0)\n"
            "s_sub_u32 s45, s45, s78\n"
            "s_sub_u32 s45, s45, 60\n"
            "W3P0_W1_D_%=:\n"
            "s_sub_u32 s45, s45, 32\n"
            "s_cmp_gt_i32 s45, 0\n"
            "s_cbranch_scc1 W3P0_W1_D_%=\n"
            "s_mov_b32 s76, 0\n"
            "global_load_dwordx4 v[88:91], v115, s[60:61] sc1\n"
            "global_load_dwordx4 v[92:95], v115, s[62:63] sc1\n"
            "global_load_dwordx4 v[96:99], v115, s[64:65] sc1\n"
            "global_load_dwordx4 v[48:51], v115, s[66:67] sc1\n"
            "v_mfma_f32_16x16x32_bf16 v[32:35], v[56:59], v[0:3], 0\n"
            "v_mfma_f32_16x16x32_bf16 v[36:39], v[56:59], v[16:19], 0\n"
            "v_mfma_f32_16x16x32_bf16 v[40:43], v[72:75], v[0:3], 0\n"
            "v_mfma_f32_16x16x32_bf16 v[44:47], v[72:75], v[16:19], 0\n"
            "v_mfma_f32_16x16x32_bf16 v[32:35], v[60:63], v[4:7], v[32:35]\n"
            "v_mfma_f32_16x16x32_bf16 v[36:39], v[60:63], v[20:23], v[36:39]\n"
            "v_mfma_f32_16x16x32_bf16 v[40:43], v[76:79], v[4:7], v[40:43]\n"
            "v_mfma_f32_16x16x32_bf16 v[44:47], v[76:79], v[20:23], v[44:47]\n"
            "v_mfma_f32_16x16x32_bf16 v[32:35], v[64:67], v[8:11], v[32:35]\n"
            "v_mfma_f32_16x16x32_bf16 v[36:39], v[64:67], v[24:27], v[36:39]\n"
            "v_mfma_f32_16x16x32_bf16 v[40:43], v[80:83], v[8:11], v[40:43]\n"
            "v_mfma_f32_16x16x32_bf16 v[44:47], v[80:83], v[24:27], v[44:47]\n"
            "v_mfma_f32_16x16x32_bf16 v[32:35], v[68:71], v[12:15], v[32:35]\n"
            "v_mfma_f32_16x16x32_bf16 v[36:39], v[68:71], v[28:31], v[36:39]\n"
            "v_mfma_f32_16x16x32_bf16 v[40:43], v[84:87], v[12:15], v[40:43]\n"
            "v_mfma_f32_16x16x32_bf16 v[44:47], v[84:87], v[28:31], v[44:47]\n"
            "s_nop 4\n"
            "v_cvt_pk_bf16_f32 v100, v32, v33\n"
            "v_cvt_pk_bf16_f32 v101, v34, v35\n"
            "v_cvt_pk_bf16_f32 v102, v40, v41\n"
            "v_cvt_pk_bf16_f32 v103, v42, v43\n"
            "v_cvt_pk_bf16_f32 v104, v36, v37\n"
            "v_cvt_pk_bf16_f32 v105, v38, v39\n"
            "v_cvt_pk_bf16_f32 v106, v44, v45\n"
            "v_cvt_pk_bf16_f32 v107, v46, v47\n"
            "W3P0_RCHK_%=:\n"
            "s_waitcnt vmcnt(0)\n"
            "v_mfma_f32_16x16x32_bf16 v[52:55], v[120:123], v[88:91], 0\n"
            "v_max3_u32 v118, v89, v91, v93\n"
            "v_max3_u32 v119, v95, v97, v99\n"
            "v_mfma_f32_16x16x32_bf16 v[52:55], v[120:123], v[92:95], v[52:55]\n"
            "v_max3_u32 v118, v118, v49, v51\n"
            "v_max_u32 v118, v118, v119\n"
            "v_mfma_f32_16x16x32_bf16 v[52:55], v[120:123], v[96:99], v[52:55]\n"
            "v_cmp_eq_u32 vcc, -1, v118\n"
            "v_mfma_f32_16x16x32_bf16 v[52:55], v[120:123], v[48:51], v[52:55]\n"
            "s_cbranch_vccnz W3P0_RRETRY_%=\n"
            "s_nop 6\n"
            "v_add_f32_dpp v52, v52, v52 row_ror:8 row_mask:0xf bank_mask:0xf\n"
            "v_add_f32_dpp v53, v53, v53 row_ror:8 row_mask:0xf bank_mask:0xf\n"
            "v_add_f32_dpp v54, v54, v54 row_ror:8 row_mask:0xf bank_mask:0xf\n"
            "v_add_f32_dpp v55, v55, v55 row_ror:8 row_mask:0xf bank_mask:0xf\n"
            "v_cvt_pk_bf16_f32 v52, v52, v53\n"
            "v_cvt_pk_bf16_f32 v53, v54, v55\n"
            "s_mov_b32 exec_lo, 0x00ff00ff\n"
            "s_mov_b32 exec_hi, 0\n"
            "v_min_u32 v53, -2, v53\n"
            "global_store_dwordx2 v116, v[52:53], s[90:91] sc1\n"
            "s_mov_b64 exec, -1\n"
            "global_store_dwordx4 v115, v[124:127], s[60:61] sc1\n"
            "global_store_dwordx4 v115, v[124:127], s[62:63] sc1\n"
            "global_store_dwordx4 v115, v[124:127], s[64:65] sc1\n"
            "global_store_dwordx4 v115, v[124:127], s[66:67] sc1\n"
            "s_branch W3P0_ZDONE_%=\n"
            "W3P0_RRETRY_%=:\n"
            "s_add_u32 s76, s76, 1\n"
            "s_add_u32 s83, s83, 1\n"
            "s_cmp_gt_u32 s76, 0x20000\n"
            "s_cbranch_scc1 TIMEOUT_%=\n"
            "s_nop 7\n"
            "global_load_dwordx4 v[88:91], v115, s[60:61] sc1\n"
            "global_load_dwordx4 v[92:95], v115, s[62:63] sc1\n"
            "global_load_dwordx4 v[96:99], v115, s[64:65] sc1\n"
            "global_load_dwordx4 v[48:51], v115, s[66:67] sc1\n"
            "s_branch W3P0_RCHK_%=\n"
            "W3P0_NORED_%=:\n"
            "ds_read_b128 v[0:3], v114 offset:0\n"
            "ds_read_b128 v[4:7], v114 offset:1024\n"
            "ds_read_b128 v[8:11], v114 offset:2048\n"
            "ds_read_b128 v[12:15], v114 offset:3072\n"
            "ds_read_b128 v[16:19], v114 offset:4096\n"
            "ds_read_b128 v[20:23], v114 offset:5120\n"
            "ds_read_b128 v[24:27], v114 offset:6144\n"
            "ds_read_b128 v[28:31], v114 offset:7168\n"
            "ds_read_b128 v[56:59], v110 offset:32768\n"
            "ds_read_b128 v[60:63], v110 offset:33792\n"
            "ds_read_b128 v[64:67], v110 offset:34816\n"
            "ds_read_b128 v[68:71], v110 offset:35840\n"
            "ds_read_b128 v[72:75], v110 offset:36864\n"
            "ds_read_b128 v[76:79], v110 offset:37888\n"
            "ds_read_b128 v[80:83], v110 offset:38912\n"
            "ds_read_b128 v[84:87], v110 offset:39936\n"
            "s_waitcnt lgkmcnt(0)\n"
            "v_mfma_f32_16x16x32_bf16 v[32:35], v[56:59], v[0:3], 0\n"
            "v_mfma_f32_16x16x32_bf16 v[36:39], v[56:59], v[16:19], 0\n"
            "v_mfma_f32_16x16x32_bf16 v[40:43], v[72:75], v[0:3], 0\n"
            "v_mfma_f32_16x16x32_bf16 v[44:47], v[72:75], v[16:19], 0\n"
            "v_mfma_f32_16x16x32_bf16 v[32:35], v[60:63], v[4:7], v[32:35]\n"
            "v_mfma_f32_16x16x32_bf16 v[36:39], v[60:63], v[20:23], v[36:39]\n"
            "v_mfma_f32_16x16x32_bf16 v[40:43], v[76:79], v[4:7], v[40:43]\n"
            "v_mfma_f32_16x16x32_bf16 v[44:47], v[76:79], v[20:23], v[44:47]\n"
            "v_mfma_f32_16x16x32_bf16 v[32:35], v[64:67], v[8:11], v[32:35]\n"
            "v_mfma_f32_16x16x32_bf16 v[36:39], v[64:67], v[24:27], v[36:39]\n"
            "v_mfma_f32_16x16x32_bf16 v[40:43], v[80:83], v[8:11], v[40:43]\n"
            "v_mfma_f32_16x16x32_bf16 v[44:47], v[80:83], v[24:27], v[44:47]\n"
            "v_mfma_f32_16x16x32_bf16 v[32:35], v[68:71], v[12:15], v[32:35]\n"
            "v_mfma_f32_16x16x32_bf16 v[36:39], v[68:71], v[28:31], v[36:39]\n"
            "v_mfma_f32_16x16x32_bf16 v[40:43], v[84:87], v[12:15], v[40:43]\n"
            "v_mfma_f32_16x16x32_bf16 v[44:47], v[84:87], v[28:31], v[44:47]\n"
            "s_nop 4\n"
            "v_cvt_pk_bf16_f32 v100, v32, v33\n"
            "v_cvt_pk_bf16_f32 v101, v34, v35\n"
            "v_cvt_pk_bf16_f32 v102, v40, v41\n"
            "v_cvt_pk_bf16_f32 v103, v42, v43\n"
            "v_cvt_pk_bf16_f32 v104, v36, v37\n"
            "v_cvt_pk_bf16_f32 v105, v38, v39\n"
            "v_cvt_pk_bf16_f32 v106, v44, v45\n"
            "v_cvt_pk_bf16_f32 v107, v46, v47\n"
            "W3P0_ZDONE_%=:\n"
            "s_nop 7\n"
            "v_mov_b32 v32, 0\n"
            "v_mov_b32 v33, 0\n"
            "v_mov_b32 v34, 0\n"
            "v_mov_b32 v35, 0\n"
            "v_mov_b32 v36, 0\n"
            "v_mov_b32 v37, 0\n"
            "v_mov_b32 v38, 0\n"
            "v_mov_b32 v39, 0\n"
            "v_mov_b32 v40, 0\n"
            "v_mov_b32 v41, 0\n"
            "v_mov_b32 v42, 0\n"
            "v_mov_b32 v43, 0\n"
            "v_mov_b32 v44, 0\n"
            "v_mov_b32 v45, 0\n"
            "v_mov_b32 v46, 0\n"
            "v_mov_b32 v47, 0\n"
            "v_mov_b32 v48, 0\n"
            "v_mov_b32 v49, 0\n"
            "v_mov_b32 v50, 0\n"
            "v_mov_b32 v51, 0\n"
            "v_mov_b32 v52, 0\n"
            "v_mov_b32 v53, 0\n"
            "v_mov_b32 v54, 0\n"
            "v_mov_b32 v55, 0\n"
            "s_add_u32 s52, s52, 16384\n"
            "s_addc_u32 s53, s53, 0\n"
            "s_add_u32 s68, s68, 1\n"
            "s_cmp_ge_u32 s68, 63\n"
            "s_cbranch_scc1 DONE_%=\n"
            "s_barrier\n"
            "s_branch W3P1_ENTRY_%=\n"
            "W3P0_SCHRETRY_%=:\n"
            "s_add_u32 s69, s69, 1\n"
            "s_cmp_gt_u32 s69, 0x400000\n"
            "s_cbranch_scc1 TIMEOUT_%=\n"
            "s_mov_b64 exec, 1\n"
            "global_load_dword v117, v110, s[30:31] sc1\n"
            "s_mov_b64 exec, -1\n"
            "s_waitcnt vmcnt(0)\n"
            "v_readfirstlane_b32 s88, v117\n"
            "s_nop 0\n"
            "s_cmp_eq_u32 s88, -1\n"
            "s_cbranch_scc1 W3P0_SCHRETRY_%=\n"
            "s_mov_b32 s32, s88\n"
            "s_branch W3P0_SCHOK_%=\n"
            "W3P0_RETRY_%=:\n"
            "s_waitcnt lgkmcnt(0)\n"
            "s_barrier\n"
            "s_nop 7\n"
            "v_mov_b32 v32, 0\n"
            "v_mov_b32 v33, 0\n"
            "v_mov_b32 v34, 0\n"
            "v_mov_b32 v35, 0\n"
            "v_mov_b32 v36, 0\n"
            "v_mov_b32 v37, 0\n"
            "v_mov_b32 v38, 0\n"
            "v_mov_b32 v39, 0\n"
            "v_mov_b32 v40, 0\n"
            "v_mov_b32 v41, 0\n"
            "v_mov_b32 v42, 0\n"
            "v_mov_b32 v43, 0\n"
            "v_mov_b32 v44, 0\n"
            "v_mov_b32 v45, 0\n"
            "v_mov_b32 v46, 0\n"
            "v_mov_b32 v47, 0\n"
            "s_sub_u32 s30, s30, 128\n"
            "s_subb_u32 s31, s31, 0\n"
            "W3P0_RETRY2_%=:\n"
            "s_add_u32 s76, s76, 1\n"
            "s_add_u32 s84, s84, 1\n"
            "s_cmp_gt_u32 s76, 0x20000\n"
            "s_cbranch_scc1 TIMEOUT_%=\n"
            "global_load_dwordx4 v[24:27], v109, s[52:53] sc1\n"
            "global_load_dwordx4 v[28:31], v109, s[52:53] offset:1024 sc1\n"
            "s_waitcnt vmcnt(0)\n"
            "v_max3_u32 v118, v25, v27, v29\n"
            "v_max_u32 v118, v118, v31\n"
            "v_cmp_eq_u32 vcc, -1, v118\n"
            "s_cbranch_vccnz W3P0_RETRY2_%=\n"
            "ds_write_b128 v110, v[24:27] offset:6144\n"
            "ds_write_b128 v110, v[28:31] offset:7168\n"
            "s_waitcnt lgkmcnt(0)\n"
            "s_barrier\n"
            "s_branch W3P0_AFTERB1_%=\n"
            "s_nop 15\n"
            "W3P1_ENTRY_%=:\n"
            "s_mov_b32 s76, 0\n"
            "W3P1_POLL_%=:\n"
            "s_mov_b32 m0, 6144\n"
            "s_nop 0\n"
            "global_load_lds_dwordx4 v109, s[52:53] sc1\n"
            "global_load_lds_dwordx4 v109, s[52:53] offset:1024 sc1\n"
            "s_waitcnt vmcnt(0)\n"
            "s_waitcnt lgkmcnt(0)\n"
            "s_barrier\n"
            "W3P1_AFTERB1_%=:\n"
            "ds_read_b128 v[0:3], v110 offset:0\n"
            "ds_read_b128 v[4:7], v110 offset:1024\n"
            "ds_read_b128 v[8:11], v110 offset:2048\n"
            "ds_read_b128 v[12:15], v110 offset:3072\n"
            "ds_read_b128 v[16:19], v110 offset:4096\n"
            "ds_read_b128 v[20:23], v110 offset:5120\n"
            "ds_read_b128 v[24:27], v110 offset:6144\n"
            "ds_read_b128 v[28:31], v110 offset:7168\n"
            "s_add_u32 s30, s30, 128\n"
            "s_addc_u32 s31, s31, 0\n"
            "s_waitcnt lgkmcnt(7)\n"
            "v_smfmac_f32_16x16x64_bf16 v[32:35], v[0:3], a[0:7], v108\n"
            "v_smfmac_f32_16x16x64_bf16 v[36:39], v[0:3], a[128:135], v108\n"
            "s_waitcnt lgkmcnt(6)\n"
            "v_max_u32 v118, v1, v3\n"
            "v_smfmac_f32_16x16x64_bf16 v[32:35], v[4:7], a[8:15], v108\n"
            "ds_read_b128 v[56:59], v110 offset:16384\n"
            "v_smfmac_f32_16x16x64_bf16 v[36:39], v[4:7], a[136:143], v108\n"
            "s_waitcnt lgkmcnt(6)\n"
            "v_max3_u32 v118, v118, v5, v7\n"
            "v_smfmac_f32_16x16x64_bf16 v[32:35], v[8:11], a[16:23], v108\n"
            "ds_read_b128 v[60:63], v110 offset:17408\n"
            "v_smfmac_f32_16x16x64_bf16 v[36:39], v[8:11], a[144:151], v108\n"
            "s_waitcnt lgkmcnt(6)\n"
            "v_max3_u32 v118, v118, v9, v11\n"
            "v_smfmac_f32_16x16x64_bf16 v[32:35], v[12:15], a[24:31], v108\n"
            "ds_read_b128 v[64:67], v110 offset:18432\n"
            "v_smfmac_f32_16x16x64_bf16 v[36:39], v[12:15], a[152:159], v108\n"
            "s_waitcnt lgkmcnt(6)\n"
            "v_max3_u32 v118, v118, v13, v15\n"
            "v_smfmac_f32_16x16x64_bf16 v[32:35], v[16:19], a[32:39], v108\n"
            "ds_read_b128 v[68:71], v110 offset:19456\n"
            "v_smfmac_f32_16x16x64_bf16 v[36:39], v[16:19], a[160:167], v108\n"
            "s_waitcnt lgkmcnt(6)\n"
            "v_max3_u32 v118, v118, v17, v19\n"
            "v_smfmac_f32_16x16x64_bf16 v[32:35], v[20:23], a[40:47], v108\n"
            "ds_read_b128 v[72:75], v110 offset:20480\n"
            "v_smfmac_f32_16x16x64_bf16 v[36:39], v[20:23], a[168:175], v108\n"
            "s_waitcnt lgkmcnt(6)\n"
            "v_max3_u32 v118, v118, v21, v23\n"
            "v_smfmac_f32_16x16x64_bf16 v[32:35], v[24:27], a[48:55], v108\n"
            "ds_read_b128 v[76:79], v110 offset:21504\n"
            "v_smfmac_f32_16x16x64_bf16 v[36:39], v[24:27], a[176:183], v108\n"
            "s_waitcnt lgkmcnt(6)\n"
            "v_max3_u32 v118, v118, v25, v27\n"
            "v_smfmac_f32_16x16x64_bf16 v[32:35], v[28:31], a[56:63], v108\n"
            "ds_read_b128 v[80:83], v110 offset:22528\n"
            "v_smfmac_f32_16x16x64_bf16 v[36:39], v[28:31], a[184:191], v108\n"
            "v_max3_u32 v118, v118, v29, v31\n"
            "v_smfmac_f32_16x16x64_bf16 v[40:43], v[0:3], a[64:71], v108\n"
            "ds_read_b128 v[84:87], v110 offset:23552\n"
            "v_cmp_eq_u32 vcc, -1, v118\n"
            "v_smfmac_f32_16x16x64_bf16 v[44:47], v[0:3], a[192:199], v108\n"
            "v_smfmac_f32_16x16x64_bf16 v[40:43], v[4:7], a[72:79], v108\n"
            "v_smfmac_f32_16x16x64_bf16 v[44:47], v[4:7], a[200:207], v108\n"
            "s_cbranch_vccnz W3P1_RETRY_%=\n"
            "s_mov_b32 vcc_lo, 0xcccccccc\n"
            "s_mov_b32 vcc_hi, 0xcccccccc\n"
            "v_smfmac_f32_16x16x64_bf16 v[40:43], v[8:11], a[80:87], v108\n"
            "v_smfmac_f32_16x16x64_bf16 v[44:47], v[8:11], a[208:215], v108\n"
            "v_smfmac_f32_16x16x64_bf16 v[40:43], v[12:15], a[88:95], v108\n"
            "v_smfmac_f32_16x16x64_bf16 v[44:47], v[12:15], a[216:223], v108\n"
            "v_add_f32 v36, v36, v37\n"
            "v_smfmac_f32_16x16x64_bf16 v[40:43], v[16:19], a[96:103], v108\n"
            "v_add_f32 v38, v38, v39\n"
            "v_smfmac_f32_16x16x64_bf16 v[44:47], v[16:19], a[224:231], v108\n"
            "v_add_f32 v32, v32, v33\n"
            "v_smfmac_f32_16x16x64_bf16 v[40:43], v[20:23], a[104:111], v108\n"
            "v_add_f32 v34, v34, v35\n"
            "v_smfmac_f32_16x16x64_bf16 v[44:47], v[20:23], a[232:239], v108\n"
            "v_cvt_pk_bf16_f32 v36, v36, v38\n"
            "v_smfmac_f32_16x16x64_bf16 v[40:43], v[24:27], a[112:119], v108\n"
            "v_cvt_pk_bf16_f32 v37, v32, v34\n"
            "v_smfmac_f32_16x16x64_bf16 v[44:47], v[24:27], a[240:247], v108\n"
            "ds_write_b64 v111, v[36:37] offset:0\n"
            "v_smfmac_f32_16x16x64_bf16 v[40:43], v[28:31], a[120:127], v108\n"
            "v_smfmac_f32_16x16x64_bf16 v[44:47], v[28:31], a[248:255], v108\n"
            "v_smfmac_f32_16x16x64_bf16 v[48:51], v[0:3], v[128:135], v108\n"
            "s_nop 5\n"
            "s_nop 0\n"
            "v_add_f32 v44, v44, v45\n"
            "v_add_f32 v46, v46, v47\n"
            "v_add_f32 v40, v40, v41\n"
            "v_add_f32 v42, v42, v43\n"
            "v_cvt_pk_bf16_f32 v44, v44, v46\n"
            "v_cvt_pk_bf16_f32 v45, v40, v42\n"
            "ds_write_b64 v111, v[44:45] offset:512\n"
            "v_smfmac_f32_16x16x64_bf16 v[52:55], v[0:3], v[192:199], v108\n"
            "v_smfmac_f32_16x16x64_bf16 v[48:51], v[4:7], v[136:143], v108\n"
            "v_smfmac_f32_16x16x64_bf16 v[52:55], v[4:7], v[200:207], v108\n"
            "v_smfmac_f32_16x16x64_bf16 v[48:51], v[8:11], v[144:151], v108\n"
            "v_smfmac_f32_16x16x64_bf16 v[52:55], v[8:11], v[208:215], v108\n"
            "s_waitcnt lgkmcnt(0)\n"
            "s_barrier\n"
            "ds_read_b64_tr_b16 v[32:33], v112 offset:0\n"
            "ds_read_b64_tr_b16 v[34:35], v112 offset:512\n"
            "ds_read_b64_tr_b16 v[36:37], v112 offset:1024\n"
            "ds_read_b64_tr_b16 v[38:39], v112 offset:1536\n"
            "ds_read_b64_tr_b16 v[40:41], v112 offset:2048\n"
            "ds_read_b64_tr_b16 v[42:43], v112 offset:2560\n"
            "ds_read_b64_tr_b16 v[44:45], v112 offset:3072\n"
            "ds_read_b64_tr_b16 v[46:47], v112 offset:3584\n"
            "v_smfmac_f32_16x16x64_bf16 v[48:51], v[12:15], v[152:159], v108\n"
            "v_smfmac_f32_16x16x64_bf16 v[52:55], v[12:15], v[216:223], v108\n"
            "s_waitcnt lgkmcnt(6)\n"
            "v_mfma_f32_16x16x32_bf16 v[96:99], v[32:35], v[32:35], 0\n"
            "s_waitcnt lgkmcnt(4)\n"
            "v_mfma_f32_16x16x32_bf16 v[96:99], v[36:39], v[36:39], v[96:99]\n"
            "s_waitcnt lgkmcnt(2)\n"
            "v_mfma_f32_16x16x32_bf16 v[96:99], v[40:43], v[40:43], v[96:99]\n"
            "s_waitcnt lgkmcnt(0)\n"
            "v_mfma_f32_16x16x32_bf16 v[96:99], v[44:47], v[44:47], v[96:99]\n"
            "v_mfma_f32_16x16x32_bf16 v[88:91], v[56:59], v[32:35], 0\n"
            "s_mov_b64 exec, 1\n"
            "global_load_dword v117, v110, s[30:31] sc1\n"
            "s_mov_b64 exec, -1\n"
            "v_mov_b32 v12, 0x3f803f80\n"
            "v_mfma_f32_16x16x32_bf16 v[88:91], v[60:63], v[36:39], v[88:91]\n"
            "v_mov_b32 v13, 0x3f803f80\n"
            "v_mov_b32 v14, 0x3f803f80\n"
            "v_mfma_f32_16x16x32_bf16 v[88:91], v[64:67], v[40:43], v[88:91]\n"
            "v_mov_b32 v15, 0x3f803f80\n"
            "v_mov_b32 v6, 0x00003f80\n"
            "v_mfma_f32_16x16x32_bf16 v[88:91], v[68:71], v[44:47], v[88:91]\n"
            "v_mov_b32 v7, 0x00003f80\n"
            "v_mfma_f32_16x16x32_bf16 v[92:95], v[72:75], v[32:35], 0\n"
            "v_cndmask_b32_dpp v96, v97, v96, vcc quad_perm:[2,3,2,3] row_mask:0xf bank_mask:0xf\n"
            "v_mfma_f32_16x16x32_bf16 v[92:95], v[76:79], v[36:39], v[92:95]\n"
            "v_mfma_f32_16x16x32_bf16 v[92:95], v[80:83], v[40:43], v[92:95]\n"
            "v_mfma_f32_16x16x32_bf16 v[92:95], v[84:87], v[44:47], v[92:95]\n"
            "v_smfmac_f32_16x16x64_bf16 v[48:51], v[16:19], v[160:167], v108\n"
            "s_nop 6\n"
            "v_cndmask_b32_dpp v88, v92, v88, vcc quad_perm:[2,3,2,3] row_mask:0xf bank_mask:0xf\n"
            "v_cndmask_b32_dpp v89, v93, v89, vcc quad_perm:[2,3,2,3] row_mask:0xf bank_mask:0xf\n"
            "v_cndmask_b32_dpp v90, v94, v90, vcc quad_perm:[2,3,2,3] row_mask:0xf bank_mask:0xf\n"
            "v_cndmask_b32_dpp v91, v95, v91, vcc quad_perm:[2,3,2,3] row_mask:0xf bank_mask:0xf\n"
            "v_max3_f32 v118, v96, v88, v89\n"
            "v_max3_f32 v118, v118, v90, v91\n"
            "v_smfmac_f32_16x16x64_bf16 v[52:55], v[16:19], v[224:231], v108\n"
            "s_nop 0\n"
            "v_max_f32_dpp v118, v118, v118 quad_perm:[2,3,0,1] row_mask:0xf bank_mask:0xf\n"
            "v_mov_b32 v119, v118\n"
            "v_smfmac_f32_16x16x64_bf16 v[48:51], v[20:23], v[168:175], v108\n"
            "s_nop 0\n"
            "v_permlane32_swap_b32 v118, v119\n"
            "v_max_f32 v118, v118, v119\n"
            "v_mov_b32 v119, v118\n"
            "v_smfmac_f32_16x16x64_bf16 v[52:55], v[20:23], v[232:239], v108\n"
            "s_nop 0\n"
            "v_permlane16_swap_b32 v118, v119\n"
            "v_max_f32 v118, v118, v119\n"
            "v_smfmac_f32_16x16x64_bf16 v[48:51], v[24:27], v[176:183], v108\n"
            "v_mul_f32 v118, s94, v118\n"
            "v_smfmac_f32_16x16x64_bf16 v[52:55], v[24:27], v[240:247], v108\n"
            "v_fma_f32 v88, v88, s94, -v118\n"
            "v_fma_f32 v89, v89, s94, -v118\n"
            "v_fma_f32 v90, v90, s94, -v118\n"
            "v_fma_f32 v91, v91, s94, -v118\n"
            "v_fma_f32 v96, v96, s94, -v118\n"
            "v_smfmac_f32_16x16x64_bf16 v[48:51], v[28:31], v[184:191], v108\n"
            "v_exp_f32 v88, v88\n"
            "v_exp_f32 v89, v89\n"
            "v_exp_f32 v90, v90\n"
            "v_exp_f32 v91, v91\n"
            "v_exp_f32 v96, v96\n"
            "v_smfmac_f32_16x16x64_bf16 v[52:55], v[28:31], v[248:255], v108\n"
            "v_cvt_pk_bf16_f32 v0, v88, v89\n"
            "v_cvt_pk_bf16_f32 v1, v90, v91\n"
            "v_cvt_pk_bf16_f32 v4, v96, v96\n"
            "v_mov_b32_dpp v2, v0 quad_perm:[0,1,0,1] row_mask:0xf bank_mask:0xf\n"
            "v_mov_b32_dpp v3, v1 quad_perm:[0,1,0,1] row_mask:0xf bank_mask:0xf\n"
            "v_mov_b32_dpp v5, v4 quad_perm:[0,1,0,1] row_mask:0xf bank_mask:0xf\n"
            "s_nop 0\n"
            "v_mfma_f32_16x16x32_bf16 v[92:95], v[12:15], v[0:3], 0\n"
            "v_cvt_pk_bf16_f32 v8, v48, v49\n"
            "v_cvt_pk_bf16_f32 v9, v50, v51\n"
            "v_mfma_f32_16x16x32_bf16 v[12:15], v[100:103], v[0:3], 0\n"
            "v_cvt_pk_bf16_f32 v10, v52, v53\n"
            "v_cvt_pk_bf16_f32 v11, v54, v55\n"
            "v_mfma_f32_16x16x16_bf16 v[92:95], v[6:7], v[4:5], v[92:95]\n"
            "v_mfma_f32_16x16x32_bf16 v[88:91], v[104:107], v[0:3], 0\n"
            "v_mfma_f32_16x16x16_bf16 v[12:15], v[8:9], v[4:5], v[12:15]\n"
            "v_mfma_f32_16x16x16_bf16 v[88:91], v[10:11], v[4:5], v[88:91]\n"
            "s_nop 4\n"
            "v_mov_b32_dpp v118, v92 quad_perm:[2,3,2,3] row_mask:0xf bank_mask:0xf\n"
            "v_rcp_f32 v118, v118\n"
            "s_nop 0\n"
            "v_cndmask_b32_dpp v12, v88, v12, vcc quad_perm:[2,3,2,3] row_mask:0xf bank_mask:0xf\n"
            "v_cndmask_b32_dpp v13, v89, v13, vcc quad_perm:[2,3,2,3] row_mask:0xf bank_mask:0xf\n"
            "v_cndmask_b32_dpp v14, v90, v14, vcc quad_perm:[2,3,2,3] row_mask:0xf bank_mask:0xf\n"
            "v_cndmask_b32_dpp v15, v91, v15, vcc quad_perm:[2,3,2,3] row_mask:0xf bank_mask:0xf\n"
            "v_mul_f32 v12, v12, v118\n"
            "v_mul_f32 v13, v13, v118\n"
            "v_mul_f32 v14, v14, v118\n"
            "v_mul_f32 v15, v15, v118\n"
            "v_cvt_pk_bf16_f32 v20, v12, v13\n"
            "v_cvt_pk_bf16_f32 v21, v14, v15\n"
            "v_min_u32 v21, -2, v21\n"
            "global_store_dwordx2 v113, v[20:21], s[56:57] sc1\n"
            "W3P1_SCH_%=:\n"
            "s_waitcnt vmcnt(1)\n"
            "v_readfirstlane_b32 s88, v117\n"
            "s_nop 0\n"
            "s_sub_u32 s78, s88, s32\n"
            "s_cmp_le_u32 s78, 208\n"
            "s_cselect_b32 s78, s78, s33\n"
            "s_add_u32 s32, s32, s33\n"
            "s_cmp_eq_u32 s88, -1\n"
            "s_cselect_b32 s32, s32, s88\n"
            "s_cselect_b32 s17, 1, 0\n"
            "s_cselect_b32 s33, s33, s78\n"
            "s_cmp_lg_u32 s76, 0\n"
            "s_cselect_b32 s88, s17, 0\n"
            "s_cmp_eq_u32 s88, 0\n"
            "s_cbranch_scc0 W3P1_SCHRETRY_%=\n"
            "W3P1_SCHOK_%=:\n"
            "s_cmp_lt_i32 s75, 0\n"
            "s_cbranch_scc1 W3P1_NORED_%=\n"
            "s_add_u32 s60, s56, 0\n"
            "s_addc_u32 s61, s57, 0\n"
            "s_add_u32 s62, s56, 131072\n"
            "s_addc_u32 s63, s57, 0\n"
            "s_add_u32 s64, s56, 262144\n"
            "s_addc_u32 s65, s57, 0\n"
            "s_add_u32 s66, s56, 393216\n"
            "s_addc_u32 s67, s57, 0\n"
            "s_add_u32 s90, s52, 16384\n"
            "s_addc_u32 s91, s53, 0\n"
            "v_mov_b32 v118, 45056\n"
            "ds_read_b64 v[6:7], v118\n"
            "ds_read_b32 v119, v118 offset:8\n"
            "s_waitcnt lgkmcnt(0)\n"
            "v_readfirstlane_b32 s40, v6\n"
            "v_readfirstlane_b32 s41, v7\n"
            "v_readfirstlane_b32 s42, v119\n"
            "ds_read_b128 v[0:3], v114 offset:0\n"
            "ds_read_b128 v[4:7], v114 offset:1024\n"
            "ds_read_b128 v[8:11], v114 offset:2048\n"
            "ds_read_b128 v[12:15], v114 offset:3072\n"
            "ds_read_b128 v[16:19], v114 offset:4096\n"
            "ds_read_b128 v[20:23], v114 offset:5120\n"
            "ds_read_b128 v[24:27], v114 offset:6144\n"
            "ds_read_b128 v[28:31], v114 offset:7168\n"
            "ds_read_b128 v[56:59], v110 offset:24576\n"
            "ds_read_b128 v[60:63], v110 offset:25600\n"
            "ds_read_b128 v[64:67], v110 offset:26624\n"
            "ds_read_b128 v[68:71], v110 offset:27648\n"
            "ds_read_b128 v[72:75], v110 offset:28672\n"
            "ds_read_b128 v[76:79], v110 offset:29696\n"
            "ds_read_b128 v[80:83], v110 offset:30720\n"
            "ds_read_b128 v[84:87], v110 offset:31744\n"
            "s_sub_u32 s88, s32, s34\n"
            "s_add_u32 s88, s88, s35\n"
            "s_sub_u32 s45, s88, s41\n"
            "s_mul_i32 s45, s45, s42\n"
            "s_ashr_i32 s45, s45, 8\n"
            "s_add_u32 s45, s45, s40\n"
            "s_memtime s[78:79]\n"
            "s_waitcnt lgkmcnt(0)\n"
            "s_sub_u32 s45, s45, s78\n"
            "s_sub_u32 s45, s45, 60\n"
            "W3P1_W1_D_%=:\n"
            "s_sub_u32 s45, s45, 32\n"
            "s_cmp_gt_i32 s45, 0\n"
            "s_cbranch_scc1 W3P1_W1_D_%=\n"
            "s_mov_b32 s76, 0\n"
            "global_load_dwordx4 v[88:91], v115, s[60:61] sc1\n"
            "global_load_dwordx4 v[92:95], v115, s[62:63] sc1\n"
            "global_load_dwordx4 v[96:99], v115, s[64:65] sc1\n"
            "global_load_dwordx4 v[48:51], v115, s[66:67] sc1\n"
            "v_mfma_f32_16x16x32_bf16 v[32:35], v[56:59], v[0:3], 0\n"
            "v_mfma_f32_16x16x32_bf16 v[36:39], v[56:59], v[16:19], 0\n"
            "v_mfma_f32_16x16x32_bf16 v[40:43], v[72:75], v[0:3], 0\n"
            "v_mfma_f32_16x16x32_bf16 v[44:47], v[72:75], v[16:19], 0\n"
            "v_mfma_f32_16x16x32_bf16 v[32:35], v[60:63], v[4:7], v[32:35]\n"
            "v_mfma_f32_16x16x32_bf16 v[36:39], v[60:63], v[20:23], v[36:39]\n"
            "v_mfma_f32_16x16x32_bf16 v[40:43], v[76:79], v[4:7], v[40:43]\n"
            "v_mfma_f32_16x16x32_bf16 v[44:47], v[76:79], v[20:23], v[44:47]\n"
            "v_mfma_f32_16x16x32_bf16 v[32:35], v[64:67], v[8:11], v[32:35]\n"
            "v_mfma_f32_16x16x32_bf16 v[36:39], v[64:67], v[24:27], v[36:39]\n"
            "v_mfma_f32_16x16x32_bf16 v[40:43], v[80:83], v[8:11], v[40:43]\n"
            "v_mfma_f32_16x16x32_bf16 v[44:47], v[80:83], v[24:27], v[44:47]\n"
            "v_mfma_f32_16x16x32_bf16 v[32:35], v[68:71], v[12:15], v[32:35]\n"
            "v_mfma_f32_16x16x32_bf16 v[36:39], v[68:71], v[28:31], v[36:39]\n"
            "v_mfma_f32_16x16x32_bf16 v[40:43], v[84:87], v[12:15], v[40:43]\n"
            "v_mfma_f32_16x16x32_bf16 v[44:47], v[84:87], v[28:31], v[44:47]\n"
            "s_nop 4\n"
            "v_cvt_pk_bf16_f32 v100, v32, v33\n"
            "v_cvt_pk_bf16_f32 v101, v34, v35\n"
            "v_cvt_pk_bf16_f32 v102, v40, v41\n"
            "v_cvt_pk_bf16_f32 v103, v42, v43\n"
            "v_cvt_pk_bf16_f32 v104, v36, v37\n"
            "v_cvt_pk_bf16_f32 v105, v38, v39\n"
            "v_cvt_pk_bf16_f32 v106, v44, v45\n"
            "v_cvt_pk_bf16_f32 v107, v46, v47\n"
            "W3P1_RCHK_%=:\n"
            "s_waitcnt vmcnt(0)\n"
            "v_mfma_f32_16x16x32_bf16 v[52:55], v[120:123], v[88:91], 0\n"
            "v_max3_u32 v118, v89, v91, v93\n"
            "v_max3_u32 v119, v95, v97, v99\n"
            "v_mfma_f32_16x16x32_bf16 v[52:55], v[120:123], v[92:95], v[52:55]\n"
            "v_max3_u32 v118, v118, v49, v51\n"
            "v_max_u32 v118, v118, v119\n"
            "v_mfma_f32_16x16x32_bf16 v[52:55], v[120:123], v[96:99], v[52:55]\n"
            "v_cmp_eq_u32 vcc, -1, v118\n"
            "v_mfma_f32_16x16x32_bf16 v[52:55], v[120:123], v[48:51], v[52:55]\n"
            "s_cbranch_vccnz W3P1_RRETRY_%=\n"
            "s_nop 6\n"
            "v_add_f32_dpp v52, v52, v52 row_ror:8 row_mask:0xf bank_mask:0xf\n"
            "v_add_f32_dpp v53, v53, v53 row_ror:8 row_mask:0xf bank_mask:0xf\n"
            "v_add_f32_dpp v54, v54, v54 row_ror:8 row_mask:0xf bank_mask:0xf\n"
            "v_add_f32_dpp v55, v55, v55 row_ror:8 row_mask:0xf bank_mask:0xf\n"
            "v_cvt_pk_bf16_f32 v52, v52, v53\n"
            "v_cvt_pk_bf16_f32 v53, v54, v55\n"
            "s_mov_b32 exec_lo, 0x00ff00ff\n"
            "s_mov_b32 exec_hi, 0\n"
            "v_min_u32 v53, -2, v53\n"
            "global_store_dwordx2 v116, v[52:53], s[90:91] sc1\n"
            "s_mov_b64 exec, -1\n"
            "global_store_dwordx4 v115, v[124:127], s[60:61] sc1\n"
            "global_store_dwordx4 v115, v[124:127], s[62:63] sc1\n"
            "global_store_dwordx4 v115, v[124:127], s[64:65] sc1\n"
            "global_store_dwordx4 v115, v[124:127], s[66:67] sc1\n"
            "s_branch W3P1_ZDONE_%=\n"
            "W3P1_RRETRY_%=:\n"
            "s_add_u32 s76, s76, 1\n"
            "s_add_u32 s83, s83, 1\n"
            "s_cmp_gt_u32 s76, 0x20000\n"
            "s_cbranch_scc1 TIMEOUT_%=\n"
            "s_nop 7\n"
            "global_load_dwordx4 v[88:91], v115, s[60:61] sc1\n"
            "global_load_dwordx4 v[92:95], v115, s[62:63] sc1\n"
            "global_load_dwordx4 v[96:99], v115, s[64:65] sc1\n"
            "global_load_dwordx4 v[48:51], v115, s[66:67] sc1\n"
            "s_branch W3P1_RCHK_%=\n"
            "W3P1_NORED_%=:\n"
            "ds_read_b128 v[0:3], v114 offset:0\n"
            "ds_read_b128 v[4:7], v114 offset:1024\n"
            "ds_read_b128 v[8:11], v114 offset:2048\n"
            "ds_read_b128 v[12:15], v114 offset:3072\n"
            "ds_read_b128 v[16:19], v114 offset:4096\n"
            "ds_read_b128 v[20:23], v114 offset:5120\n"
            "ds_read_b128 v[24:27], v114 offset:6144\n"
            "ds_read_b128 v[28:31], v114 offset:7168\n"
            "ds_read_b128 v[56:59], v110 offset:24576\n"
            "ds_read_b128 v[60:63], v110 offset:25600\n"
            "ds_read_b128 v[64:67], v110 offset:26624\n"
            "ds_read_b128 v[68:71], v110 offset:27648\n"
            "ds_read_b128 v[72:75], v110 offset:28672\n"
            "ds_read_b128 v[76:79], v110 offset:29696\n"
            "ds_read_b128 v[80:83], v110 offset:30720\n"
            "ds_read_b128 v[84:87], v110 offset:31744\n"
            "s_waitcnt lgkmcnt(0)\n"
            "v_mfma_f32_16x16x32_bf16 v[32:35], v[56:59], v[0:3], 0\n"
            "v_mfma_f32_16x16x32_bf16 v[36:39], v[56:59], v[16:19], 0\n"
            "v_mfma_f32_16x16x32_bf16 v[40:43], v[72:75], v[0:3], 0\n"
            "v_mfma_f32_16x16x32_bf16 v[44:47], v[72:75], v[16:19], 0\n"
            "v_mfma_f32_16x16x32_bf16 v[32:35], v[60:63], v[4:7], v[32:35]\n"
            "v_mfma_f32_16x16x32_bf16 v[36:39], v[60:63], v[20:23], v[36:39]\n"
            "v_mfma_f32_16x16x32_bf16 v[40:43], v[76:79], v[4:7], v[40:43]\n"
            "v_mfma_f32_16x16x32_bf16 v[44:47], v[76:79], v[20:23], v[44:47]\n"
            "v_mfma_f32_16x16x32_bf16 v[32:35], v[64:67], v[8:11], v[32:35]\n"
            "v_mfma_f32_16x16x32_bf16 v[36:39], v[64:67], v[24:27], v[36:39]\n"
            "v_mfma_f32_16x16x32_bf16 v[40:43], v[80:83], v[8:11], v[40:43]\n"
            "v_mfma_f32_16x16x32_bf16 v[44:47], v[80:83], v[24:27], v[44:47]\n"
            "v_mfma_f32_16x16x32_bf16 v[32:35], v[68:71], v[12:15], v[32:35]\n"
            "v_mfma_f32_16x16x32_bf16 v[36:39], v[68:71], v[28:31], v[36:39]\n"
            "v_mfma_f32_16x16x32_bf16 v[40:43], v[84:87], v[12:15], v[40:43]\n"
            "v_mfma_f32_16x16x32_bf16 v[44:47], v[84:87], v[28:31], v[44:47]\n"
            "s_nop 4\n"
            "v_cvt_pk_bf16_f32 v100, v32, v33\n"
            "v_cvt_pk_bf16_f32 v101, v34, v35\n"
            "v_cvt_pk_bf16_f32 v102, v40, v41\n"
            "v_cvt_pk_bf16_f32 v103, v42, v43\n"
            "v_cvt_pk_bf16_f32 v104, v36, v37\n"
            "v_cvt_pk_bf16_f32 v105, v38, v39\n"
            "v_cvt_pk_bf16_f32 v106, v44, v45\n"
            "v_cvt_pk_bf16_f32 v107, v46, v47\n"
            "W3P1_ZDONE_%=:\n"
            "s_nop 7\n"
            "v_mov_b32 v32, 0\n"
            "v_mov_b32 v33, 0\n"
            "v_mov_b32 v34, 0\n"
            "v_mov_b32 v35, 0\n"
            "v_mov_b32 v36, 0\n"
            "v_mov_b32 v37, 0\n"
            "v_mov_b32 v38, 0\n"
            "v_mov_b32 v39, 0\n"
            "v_mov_b32 v40, 0\n"
            "v_mov_b32 v41, 0\n"
            "v_mov_b32 v42, 0\n"
            "v_mov_b32 v43, 0\n"
            "v_mov_b32 v44, 0\n"
            "v_mov_b32 v45, 0\n"
            "v_mov_b32 v46, 0\n"
            "v_mov_b32 v47, 0\n"
            "v_mov_b32 v48, 0\n"
            "v_mov_b32 v49, 0\n"
            "v_mov_b32 v50, 0\n"
            "v_mov_b32 v51, 0\n"
            "v_mov_b32 v52, 0\n"
            "v_mov_b32 v53, 0\n"
            "v_mov_b32 v54, 0\n"
            "v_mov_b32 v55, 0\n"
            "s_add_u32 s52, s52, 16384\n"
            "s_addc_u32 s53, s53, 0\n"
            "s_add_u32 s68, s68, 1\n"
            "s_cmp_ge_u32 s68, 63\n"
            "s_cbranch_scc1 DONE_%=\n"
            "s_barrier\n"
            "s_branch W3P0_ENTRY_%=\n"
            "W3P1_SCHRETRY_%=:\n"
            "s_add_u32 s69, s69, 1\n"
            "s_cmp_gt_u32 s69, 0x400000\n"
            "s_cbranch_scc1 TIMEOUT_%=\n"
            "s_mov_b64 exec, 1\n"
            "global_load_dword v117, v110, s[30:31] sc1\n"
            "s_mov_b64 exec, -1\n"
            "s_waitcnt vmcnt(0)\n"
            "v_readfirstlane_b32 s88, v117\n"
            "s_nop 0\n"
            "s_cmp_eq_u32 s88, -1\n"
            "s_cbranch_scc1 W3P1_SCHRETRY_%=\n"
            "s_mov_b32 s32, s88\n"
            "s_branch W3P1_SCHOK_%=\n"
            "W3P1_RETRY_%=:\n"
            "s_waitcnt lgkmcnt(0)\n"
            "s_barrier\n"
            "s_nop 7\n"
            "v_mov_b32 v32, 0\n"
            "v_mov_b32 v33, 0\n"
            "v_mov_b32 v34, 0\n"
            "v_mov_b32 v35, 0\n"
            "v_mov_b32 v36, 0\n"
            "v_mov_b32 v37, 0\n"
            "v_mov_b32 v38, 0\n"
            "v_mov_b32 v39, 0\n"
            "v_mov_b32 v40, 0\n"
            "v_mov_b32 v41, 0\n"
            "v_mov_b32 v42, 0\n"
            "v_mov_b32 v43, 0\n"
            "v_mov_b32 v44, 0\n"
            "v_mov_b32 v45, 0\n"
            "v_mov_b32 v46, 0\n"
            "v_mov_b32 v47, 0\n"
            "s_sub_u32 s30, s30, 128\n"
            "s_subb_u32 s31, s31, 0\n"
            "W3P1_RETRY2_%=:\n"
            "s_add_u32 s76, s76, 1\n"
            "s_add_u32 s84, s84, 1\n"
            "s_cmp_gt_u32 s76, 0x20000\n"
            "s_cbranch_scc1 TIMEOUT_%=\n"
            "global_load_dwordx4 v[24:27], v109, s[52:53] sc1\n"
            "global_load_dwordx4 v[28:31], v109, s[52:53] offset:1024 sc1\n"
            "s_waitcnt vmcnt(0)\n"
            "v_max3_u32 v118, v25, v27, v29\n"
            "v_max_u32 v118, v118, v31\n"
            "v_cmp_eq_u32 vcc, -1, v118\n"
            "s_cbranch_vccnz W3P1_RETRY2_%=\n"
            "ds_write_b128 v110, v[24:27] offset:6144\n"
            "ds_write_b128 v110, v[28:31] offset:7168\n"
            "s_waitcnt lgkmcnt(0)\n"
            "s_barrier\n"
            "s_branch W3P1_AFTERB1_%=\n"
            "TIMEOUT_%=:\n"
            "s_mov_b64 exec, -1\n"
            "v_mov_b32 v0, 0\n"
            "v_mov_b32 v1, 1\n"
            "global_store_dword v0, v1, s[92:93] sc1\n"
            "DONE_%=:\n"
            "s_memtime s[78:79]\n"
            "s_waitcnt lgkmcnt(0)\n"
            "s_sub_u32 s78, s78, s98\n"
            "s_lshl_b32 s88, s72, 2\n"
            "s_add_u32 s88, s88, s73\n"
            "s_lshl_b32 s88, s88, 2\n"
            "s_add_u32 s88, s88, s74\n"
            "s_lshl_b32 s88, s88, 5\n"
            "s_add_u32 s88, s88, 256\n"
            "v_mov_b32 v0, s88\n"
            "v_mov_b32 v2, s78\n"
            "v_mov_b32 v3, s83\n"
            "v_mov_b32 v4, s84\n"
            "v_mov_b32 v5, s35\n"
            "v_mov_b32 v6, s33\n"
            "v_mov_b32 v7, s68\n"
            "s_mov_b64 exec, 1\n"
            "global_store_dwordx4 v0, v[2:5], s[92:93]\n"
            "global_store_dwordx2 v0, v[6:7], s[92:93] offset:16\n"
            "s_waitcnt vmcnt(0)\n"
            "s_endpgm\n"
            :: "s"(ka), "s"(hs), "s"(as), "s"(w), "s"(rs), "s"(hm), "s"(hp)
            : "memory", "vcc", "v255", "a255", "s10", "s11", "s12", "s13", "s14", "s15", "s16", "s17", "s18", "s19", "s20", "s21", "s22", "s23", "s24", "s25", "s26", "s27", "s28", "s29", "s30", "s31", "s32", "s33", "s34", "s35", "s36", "s37", "s38", "s39", "s40", "s41", "s42", "s43", "s44", "s45", "s46", "s47", "s48", "s49", "s50", "s51", "s52", "s53", "s54", "s55", "s56", "s57", "s58", "s59", "s60", "s61", "s62", "s63", "s64", "s65", "s66", "s67", "s68", "s69", "s70", "s71", "s72", "s73", "s74", "s75", "s76", "s77", "s78", "s79", "s80", "s81", "s82", "s83", "s84", "s85", "s86", "s87", "s88", "s89", "s90", "s91", "s92", "s93", "s94", "s95", "s96", "s97", "s98", "s99");
        return;
    }
    const int xq = b & 3;
    if (j < 8) {
        if (!(p.roles & 4)) return;
        const int h = xq * 8 + j;
        unsigned* cst = (unsigned*)(lds + 0x20000) + tid * 24;
        const int IMG = 0x2000, QB0 = 0x8000;
        const int lq = lane >> 4, sl = lane & 15, q = c >> 2, pp = c & 3;
        cst[0] = (c & 1) ? 0xEEEEEEEEu : 0x44444444u;
        cst[1] = (2 * w) * 1024 + (g >> 1) * 512 + (c >> 2) * 128 + ((c >> 1) & 1) * 64 + (g & 1) * 32 + (c & 1) * 16;
        cst[2] = lane * 16;
        cst[3] = IMG + (32 * w + c) * 32 + ((g ^ ((c >> 2) & 3)) * 8);
        cst[4] = IMG + (4 * g + (c >> 2)) * 32 + (((c & 3) ^ g) * 8);
        cst[5] = QB0 + 0x4000 + w * 1024 + (pp * 16 + 4 * g + q) * 16;
        cst[6] = (h * 4 + w) * 16384 + lane * 16;
        cst[7] = 0;
#pragma unroll
        for (int r = 0; r < 4; ++r) cst[8 + r] = (4 * g + r > c) ? __float_as_uint(-1e30f) : 0u;
        cst[12] = (4 * w + lq) * 8192 + ((sl ^ (2 * ((lq & 1) | (w << 1)))) * 16);
        cst[13] = (4 * w + lq) * 1024 + ((sl ^ (2 * (lq | ((w & 1) << 2)))) * 16);
        cst[14] = (4 * g + q) * 256 + (((pp >> 1) ^ (2 * (4 * (g & 1) + q))) * 16) + 8 * (pp & 1);
        { const int rowp = 16 * (g >> 1) + 4 * (g & 1) + (q < 2 ? q : 6 + q), fp = 2 * ((q & 1) | (((g & 1) | ((q >> 1) << 1)) << 1));
          cst[15] = rowp * 256 + (((4 * w + (pp >> 1)) ^ fp) * 16) + 8 * (pp & 1); }
        __syncthreads();
        const int hs = __builtin_amdgcn_readfirstlane(h);
        const int hm = near_half(p.lat, 0, lane);
        asm volatile(
            "s_mov_b64 s[36:37], %0\n"
            "s_mov_b32 s65, %1\n"
            "s_mov_b32 s66, %2\n"
            "s_load_dwordx2 s[40:41], s[36:37], 8\n"
            "s_load_dwordx2 s[42:43], s[36:37], 16\n"
            "s_load_dwordx2 s[44:45], s[36:37], 24\n"
            "s_load_dwordx2 s[48:49], s[36:37], 32\n"
            "s_load_dwordx2 s[50:51], s[36:37], 40\n"
            "s_load_dwordx2 s[52:53], s[36:37], 48\n"
            "s_load_dwordx2 s[62:63], s[36:37], 64\n"
            "s_load_dwordx2 s[46:47], s[36:37], 120\n"
            "s_load_dwordx2 s[54:55], s[36:37], 128\n"
            "s_load_dwordx2 s[56:57], s[36:37], 136\n"
            "s_load_dwordx2 s[58:59], s[36:37], 144\n"
            "s_load_dwordx2 s[60:61], s[36:37], 152\n"
            "s_load_dwordx2 s[82:83], s[36:37], 232\n"
            "s_waitcnt lgkmcnt(0)\n"
            "s_lshl_b32 s10, s65, 7\n"
            "s_add_u32 s82, s82, s10\n"
            "s_addc_u32 s83, s83, 0\n"
            "s_mov_b32 s84, 0\n"
            "s_add_u32 s52, s52, %3\n"
            "s_addc_u32 s53, s53, 0\n"
            "v_mbcnt_lo_u32_b32 v0, -1, 0\n"
            "v_mbcnt_hi_u32_b32 v0, -1, v0\n"
            "s_mul_i32 s70, s66, 6144\n"
            "v_mul_u32_u24 v0, 96, v0\n"
            "v_add_u32 v0, s70, v0\n"
            "v_add_u32 v0, 131072, v0\n"
            "ds_read_b128 v[112:115], v0\n"
            "ds_read_b128 v[116:119], v0 offset:16\n"
            "ds_read_b128 v[120:123], v0 offset:32\n"
            "ds_read_b128 v[64:67], v0 offset:48\n"
            "s_waitcnt lgkmcnt(0)\n"
            "s_mov_b32 s68, 0x3e0293ee\n"
            "s_lshl_b32 s70, s65, 8\n"
            "s_add_u32 s40, s40, s70\n"
            "s_addc_u32 s41, s41, 0\n"
            "s_add_u32 s42, s42, s70\n"
            "s_addc_u32 s43, s43, 0\n"
            "s_lshl_b32 s70, s65, 17\n"
            "s_add_u32 s44, s44, s70\n"
            "s_addc_u32 s45, s45, 0\n"
            "s_lshl_b32 s70, s65, 14\n"
            "s_add_u32 s46, s48, s70\n"
            "s_addc_u32 s47, s49, 0\n"
            "s_mov_b64 s[50:51], s[54:55]\n"
            "s_lshr_b32 s27, %3, 13\n"
            "s_xor_b32 s26, s27, 1\n"
            "s_lshl_b32 s70, s66, 10\n"
            "s_add_u32 m0, s70, 0\n"
            "s_add_u32 s72, s40, 3145728\n"
            "s_addc_u32 s73, s41, 0\n"
            "global_load_lds_dwordx4 v64, s[72:73]\n"
            "s_lshl_b32 s70, s66, 10\n"
            "s_add_u32 m0, s70, 4096\n"
            "s_add_u32 s72, s40, 3276800\n"
            "s_addc_u32 s73, s41, 0\n"
            "global_load_lds_dwordx4 v64, s[72:73]\n"
            "s_lshl_b32 s70, s66, 10\n"
            "s_add_u32 m0, s70, 8192\n"
            "s_add_u32 s72, s40, 3407872\n"
            "s_addc_u32 s73, s41, 0\n"
            "global_load_lds_dwordx4 v64, s[72:73]\n"
            "s_lshl_b32 s70, s66, 10\n"
            "s_add_u32 m0, s70, 12288\n"
            "s_add_u32 s72, s40, 3538944\n"
            "s_addc_u32 s73, s41, 0\n"
            "global_load_lds_dwordx4 v64, s[72:73]\n"
            "s_lshl_b32 s70, s66, 10\n"
            "s_add_u32 m0, s70, 16384\n"
            "s_add_u32 s72, s40, 3670016\n"
            "s_addc_u32 s73, s41, 0\n"
            "global_load_lds_dwordx4 v64, s[72:73]\n"
            "s_lshl_b32 s70, s66, 10\n"
            "s_add_u32 m0, s70, 20480\n"
            "s_add_u32 s72, s40, 3801088\n"
            "s_addc_u32 s73, s41, 0\n"
            "global_load_lds_dwordx4 v64, s[72:73]\n"
            "s_lshl_b32 s70, s66, 10\n"
            "s_add_u32 m0, s70, 24576\n"
            "s_add_u32 s72, s40, 3932160\n"
            "s_addc_u32 s73, s41, 0\n"
            "global_load_lds_dwordx4 v64, s[72:73]\n"
            "s_lshl_b32 s70, s66, 10\n"
            "s_add_u32 m0, s70, 28672\n"
            "s_add_u32 s72, s40, 4063232\n"
            "s_addc_u32 s73, s41, 0\n"
            "global_load_lds_dwordx4 v64, s[72:73]\n"
            "s_lshl_b32 s70, s66, 10\n"
            "s_add_u32 m0, s70, 32768\n"
            "s_add_u32 s72, s42, 3145728\n"
            "s_addc_u32 s73, s43, 0\n"
            "global_load_lds_dwordx4 v64, s[72:73]\n"
            "s_lshl_b32 s70, s66, 10\n"
            "s_add_u32 m0, s70, 36864\n"
            "s_add_u32 s72, s42, 3276800\n"
            "s_addc_u32 s73, s43, 0\n"
            "global_load_lds_dwordx4 v64, s[72:73]\n"
            "s_lshl_b32 s70, s66, 10\n"
            "s_add_u32 m0, s70, 40960\n"
            "s_add_u32 s72, s42, 3407872\n"
            "s_addc_u32 s73, s43, 0\n"
            "global_load_lds_dwordx4 v64, s[72:73]\n"
            "s_lshl_b32 s70, s66, 10\n"
            "s_add_u32 m0, s70, 45056\n"
            "s_add_u32 s72, s42, 3538944\n"
            "s_addc_u32 s73, s43, 0\n"
            "global_load_lds_dwordx4 v64, s[72:73]\n"
            "s_lshl_b32 s70, s66, 10\n"
            "s_add_u32 m0, s70, 49152\n"
            "s_add_u32 s72, s42, 3670016\n"
            "s_addc_u32 s73, s43, 0\n"
            "global_load_lds_dwordx4 v64, s[72:73]\n"
            "s_lshl_b32 s70, s66, 10\n"
            "s_add_u32 m0, s70, 53248\n"
            "s_add_u32 s72, s42, 3801088\n"
            "s_addc_u32 s73, s43, 0\n"
            "global_load_lds_dwordx4 v64, s[72:73]\n"
            "s_lshl_b32 s70, s66, 10\n"
            "s_add_u32 m0, s70, 57344\n"
            "s_add_u32 s72, s42, 3932160\n"
            "s_addc_u32 s73, s43, 0\n"
            "global_load_lds_dwordx4 v64, s[72:73]\n"
            "s_lshl_b32 s70, s66, 10\n"
            "s_add_u32 m0, s70, 61440\n"
            "s_add_u32 s72, s42, 4063232\n"
            "s_addc_u32 s73, s43, 0\n"
            "global_load_lds_dwordx4 v64, s[72:73]\n"
            "s_lshl_b32 s70, s66, 10\n"
            "s_add_u32 m0, s70, 65536\n"
            "s_add_u32 s72, s40, 2097152\n"
            "s_addc_u32 s73, s41, 0\n"
            "global_load_lds_dwordx4 v64, s[72:73]\n"
            "s_lshl_b32 s70, s66, 10\n"
            "s_add_u32 m0, s70, 69632\n"
            "s_add_u32 s72, s40, 2228224\n"
            "s_addc_u32 s73, s41, 0\n"
            "global_load_lds_dwordx4 v64, s[72:73]\n"
            "s_lshl_b32 s70, s66, 10\n"
            "s_add_u32 m0, s70, 73728\n"
            "s_add_u32 s72, s40, 2359296\n"
            "s_addc_u32 s73, s41, 0\n"
            "global_load_lds_dwordx4 v64, s[72:73]\n"
            "s_lshl_b32 s70, s66, 10\n"
            "s_add_u32 m0, s70, 77824\n"
            "s_add_u32 s72, s40, 2490368\n"
            "s_addc_u32 s73, s41, 0\n"
            "global_load_lds_dwordx4 v64, s[72:73]\n"
            "s_lshl_b32 s70, s66, 10\n"
            "s_add_u32 m0, s70, 81920\n"
            "s_add_u32 s72, s40, 2621440\n"
            "s_addc_u32 s73, s41, 0\n"
            "global_load_lds_dwordx4 v64, s[72:73]\n"
            "s_lshl_b32 s70, s66, 10\n"
            "s_add_u32 m0, s70, 86016\n"
            "s_add_u32 s72, s40, 2752512\n"
            "s_addc_u32 s73, s41, 0\n"
            "global_load_lds_dwordx4 v64, s[72:73]\n"
            "s_lshl_b32 s70, s66, 10\n"
            "s_add_u32 m0, s70, 90112\n"
            "s_add_u32 s72, s40, 2883584\n"
            "s_addc_u32 s73, s41, 0\n"
            "global_load_lds_dwordx4 v64, s[72:73]\n"
            "s_lshl_b32 s70, s66, 10\n"
            "s_add_u32 m0, s70, 94208\n"
            "s_add_u32 s72, s40, 3014656\n"
            "s_addc_u32 s73, s41, 0\n"
            "global_load_lds_dwordx4 v64, s[72:73]\n"
            "s_waitcnt vmcnt(16)\n"
            "s_barrier\n"
            "s_lshl_b32 s70, s66, 10\n"
            "s_add_u32 m0, s70, 98304\n"
            "s_add_u32 s72, s42, 2097152\n"
            "s_addc_u32 s73, s43, 0\n"
            "global_load_lds_dwordx4 v64, s[72:73]\n"
            "v_add_u32 v72, 0, v67\n"
            "v_xor_b32 v73, 32, v72\n"
            "s_lshl_b32 s70, s66, 10\n"
            "s_add_u32 m0, s70, 102400\n"
            "s_add_u32 s72, s42, 2228224\n"
            "s_addc_u32 s73, s43, 0\n"
            "global_load_lds_dwordx4 v64, s[72:73]\n"
            "ds_read_b64_tr_b16 a[48:49], v72 offset:0\n"
            "ds_read_b64_tr_b16 a[50:51], v72 offset:512\n"
            "s_lshl_b32 s70, s66, 10\n"
            "s_add_u32 m0, s70, 106496\n"
            "s_add_u32 s72, s42, 2359296\n"
            "s_addc_u32 s73, s43, 0\n"
            "global_load_lds_dwordx4 v64, s[72:73]\n"
            "ds_read_b64_tr_b16 a[52:53], v72 offset:8192\n"
            "ds_read_b64_tr_b16 a[54:55], v72 offset:8704\n"
            "s_lshl_b32 s70, s66, 10\n"
            "s_add_u32 m0, s70, 110592\n"
            "s_add_u32 s72, s42, 2490368\n"
            "s_addc_u32 s73, s43, 0\n"
            "global_load_lds_dwordx4 v64, s[72:73]\n"
            "ds_read_b64_tr_b16 a[112:113], v73 offset:0\n"
            "ds_read_b64_tr_b16 a[114:115], v73 offset:512\n"
            "s_lshl_b32 s70, s66, 10\n"
            "s_add_u32 m0, s70, 114688\n"
            "s_add_u32 s72, s42, 2621440\n"
            "s_addc_u32 s73, s43, 0\n"
            "global_load_lds_dwordx4 v64, s[72:73]\n"
            "ds_read_b64_tr_b16 a[116:117], v73 offset:8192\n"
            "ds_read_b64_tr_b16 a[118:119], v73 offset:8704\n"
            "s_lshl_b32 s70, s66, 10\n"
            "s_add_u32 m0, s70, 118784\n"
            "s_add_u32 s72, s42, 2752512\n"
            "s_addc_u32 s73, s43, 0\n"
            "global_load_lds_dwordx4 v64, s[72:73]\n"
            "s_waitcnt lgkmcnt(0)\n"
            "ds_read_b64_tr_b16 a[56:57], v72 offset:16384\n"
            "s_lshl_b32 s70, s66, 10\n"
            "s_add_u32 m0, s70, 122880\n"
            "s_add_u32 s72, s42, 2883584\n"
            "s_addc_u32 s73, s43, 0\n"
            "global_load_lds_dwordx4 v64, s[72:73]\n"
            "ds_read_b64_tr_b16 a[58:59], v72 offset:16896\n"
            "ds_read_b64_tr_b16 a[60:61], v72 offset:24576\n"
            "s_lshl_b32 s70, s66, 10\n"
            "s_add_u32 m0, s70, 126976\n"
            "s_add_u32 s72, s42, 3014656\n"
            "s_addc_u32 s73, s43, 0\n"
            "global_load_lds_dwordx4 v64, s[72:73]\n"
            "ds_read_b64_tr_b16 a[62:63], v72 offset:25088\n"
            "ds_read_b64_tr_b16 a[120:121], v73 offset:16384\n"
            "ds_read_b64_tr_b16 a[122:123], v73 offset:16896\n"
            "ds_read_b64_tr_b16 a[124:125], v73 offset:24576\n"
            "ds_read_b64_tr_b16 a[126:127], v73 offset:25088\n"
            "s_waitcnt lgkmcnt(0)\n"
            "s_waitcnt vmcnt(16)\n"
            "s_barrier\n"
            "s_lshl_b32 s70, s66, 10\n"
            "s_add_u32 m0, s70, 0\n"
            "s_add_u32 s72, s44, 0\n"
            "s_addc_u32 s73, s45, 0\n"
            "global_load_lds_dwordx4 v65, s[72:73]\n"
            "v_add_u32 v72, 32768, v67\n"
            "v_xor_b32 v73, 32, v72\n"
            "s_lshl_b32 s70, s66, 10\n"
            "s_add_u32 m0, s70, 4096\n"
            "s_add_u32 s72, s44, 16384\n"
            "s_addc_u32 s73, s45, 0\n"
            "global_load_lds_dwordx4 v65, s[72:73]\n"
            "ds_read_b64_tr_b16 a[176:177], v72 offset:0\n"
            "ds_read_b64_tr_b16 a[178:179], v72 offset:512\n"
            "s_lshl_b32 s70, s66, 10\n"
            "s_add_u32 m0, s70, 8192\n"
            "s_add_u32 s72, s44, 32768\n"
            "s_addc_u32 s73, s45, 0\n"
            "global_load_lds_dwordx4 v65, s[72:73]\n"
            "ds_read_b64_tr_b16 a[180:181], v72 offset:8192\n"
            "ds_read_b64_tr_b16 a[182:183], v72 offset:8704\n"
            "s_lshl_b32 s70, s66, 10\n"
            "s_add_u32 m0, s70, 12288\n"
            "s_add_u32 s72, s44, 49152\n"
            "s_addc_u32 s73, s45, 0\n"
            "global_load_lds_dwordx4 v65, s[72:73]\n"
            "ds_read_b64_tr_b16 a[240:241], v73 offset:0\n"
            "ds_read_b64_tr_b16 a[242:243], v73 offset:512\n"
            "s_lshl_b32 s70, s66, 10\n"
            "s_add_u32 m0, s70, 16384\n"
            "s_add_u32 s72, s44, 65536\n"
            "s_addc_u32 s73, s45, 0\n"
            "global_load_lds_dwordx4 v65, s[72:73]\n"
            "ds_read_b64_tr_b16 a[244:245], v73 offset:8192\n"
            "ds_read_b64_tr_b16 a[246:247], v73 offset:8704\n"
            "s_lshl_b32 s70, s66, 10\n"
            "s_add_u32 m0, s70, 20480\n"
            "s_add_u32 s72, s44, 81920\n"
            "s_addc_u32 s73, s45, 0\n"
            "global_load_lds_dwordx4 v65, s[72:73]\n"
            "s_waitcnt lgkmcnt(0)\n"
            "ds_read_b64_tr_b16 a[184:185], v72 offset:16384\n"
            "s_lshl_b32 s70, s66, 10\n"
            "s_add_u32 m0, s70, 24576\n"
            "s_add_u32 s72, s44, 98304\n"
            "s_addc_u32 s73, s45, 0\n"
            "global_load_lds_dwordx4 v65, s[72:73]\n"
            "ds_read_b64_tr_b16 a[186:187], v72 offset:16896\n"
            "ds_read_b64_tr_b16 a[188:189], v72 offset:24576\n"
            "s_lshl_b32 s70, s66, 10\n"
            "s_add_u32 m0, s70, 28672\n"
            "s_add_u32 s72, s44, 114688\n"
            "s_addc_u32 s73, s45, 0\n"
            "global_load_lds_dwordx4 v65, s[72:73]\n"
            "ds_read_b64_tr_b16 a[190:191], v72 offset:25088\n"
            "ds_read_b64_tr_b16 a[248:249], v73 offset:16384\n"
            "ds_read_b64_tr_b16 a[250:251], v73 offset:16896\n"
            "ds_read_b64_tr_b16 a[252:253], v73 offset:24576\n"
            "ds_read_b64_tr_b16 a[254:255], v73 offset:25088\n"
            "s_waitcnt lgkmcnt(0)\n"
            "s_waitcnt vmcnt(16)\n"
            "s_barrier\n"
            "s_lshl_b32 s70, s66, 10\n"
            "s_add_u32 m0, s70, 32768\n"
            "s_add_u32 s72, s44, 256\n"
            "s_addc_u32 s73, s45, 0\n"
            "global_load_lds_dwordx4 v65, s[72:73]\n"
            "v_add_u32 v72, 65536, v67\n"
            "v_xor_b32 v73, 32, v72\n"
            "s_lshl_b32 s70, s66, 10\n"
            "s_add_u32 m0, s70, 36864\n"
            "s_add_u32 s72, s44, 16640\n"
            "s_addc_u32 s73, s45, 0\n"
            "global_load_lds_dwordx4 v65, s[72:73]\n"
            "ds_read_b64_tr_b16 a[32:33], v72 offset:0\n"
            "ds_read_b64_tr_b16 a[34:35], v72 offset:512\n"
            "s_lshl_b32 s70, s66, 10\n"
            "s_add_u32 m0, s70, 40960\n"
            "s_add_u32 s72, s44, 33024\n"
            "s_addc_u32 s73, s45, 0\n"
            "global_load_lds_dwordx4 v65, s[72:73]\n"
            "ds_read_b64_tr_b16 a[36:37], v72 offset:8192\n"
            "ds_read_b64_tr_b16 a[38:39], v72 offset:8704\n"
            "s_lshl_b32 s70, s66, 10\n"
            "s_add_u32 m0, s70, 45056\n"
            "s_add_u32 s72, s44, 49408\n"
            "s_addc_u32 s73, s45, 0\n"
            "global_load_lds_dwordx4 v65, s[72:73]\n"
            "ds_read_b64_tr_b16 a[96:97], v73 offset:0\n"
            "ds_read_b64_tr_b16 a[98:99], v73 offset:512\n"
            "s_lshl_b32 s70, s66, 10\n"
            "s_add_u32 m0, s70, 49152\n"
            "s_add_u32 s72, s44, 65792\n"
            "s_addc_u32 s73, s45, 0\n"
            "global_load_lds_dwordx4 v65, s[72:73]\n"
            "ds_read_b64_tr_b16 a[100:101], v73 offset:8192\n"
            "ds_read_b64_tr_b16 a[102:103], v73 offset:8704\n"
            "s_lshl_b32 s70, s66, 10\n"
            "s_add_u32 m0, s70, 53248\n"
            "s_add_u32 s72, s44, 82176\n"
            "s_addc_u32 s73, s45, 0\n"
            "global_load_lds_dwordx4 v65, s[72:73]\n"
            "s_waitcnt lgkmcnt(0)\n"
            "ds_read_b64_tr_b16 a[40:41], v72 offset:16384\n"
            "s_lshl_b32 s70, s66, 10\n"
            "s_add_u32 m0, s70, 57344\n"
            "s_add_u32 s72, s44, 98560\n"
            "s_addc_u32 s73, s45, 0\n"
            "global_load_lds_dwordx4 v65, s[72:73]\n"
            "ds_read_b64_tr_b16 a[42:43], v72 offset:16896\n"
            "ds_read_b64_tr_b16 a[44:45], v72 offset:24576\n"
            "s_lshl_b32 s70, s66, 10\n"
            "s_add_u32 m0, s70, 61440\n"
            "s_add_u32 s72, s44, 114944\n"
            "s_addc_u32 s73, s45, 0\n"
            "global_load_lds_dwordx4 v65, s[72:73]\n"
            "ds_read_b64_tr_b16 a[46:47], v72 offset:25088\n"
            "ds_read_b64_tr_b16 a[104:105], v73 offset:16384\n"
            "ds_read_b64_tr_b16 a[106:107], v73 offset:16896\n"
            "ds_read_b64_tr_b16 a[108:109], v73 offset:24576\n"
            "ds_read_b64_tr_b16 a[110:111], v73 offset:25088\n"
            "s_waitcnt lgkmcnt(0)\n"
            "s_waitcnt vmcnt(16)\n"
            "s_barrier\n"
            "s_lshl_b32 s70, s66, 10\n"
            "s_add_u32 m0, s70, 65536\n"
            "s_add_u32 s72, s44, 512\n"
            "s_addc_u32 s73, s45, 0\n"
            "global_load_lds_dwordx4 v65, s[72:73]\n"
            "v_add_u32 v72, 98304, v67\n"
            "v_xor_b32 v73, 32, v72\n"
            "s_lshl_b32 s70, s66, 10\n"
            "s_add_u32 m0, s70, 69632\n"
            "s_add_u32 s72, s44, 16896\n"
            "s_addc_u32 s73, s45, 0\n"
            "global_load_lds_dwordx4 v65, s[72:73]\n"
            "ds_read_b64_tr_b16 a[160:161], v72 offset:0\n"
            "ds_read_b64_tr_b16 a[162:163], v72 offset:512\n"
            "s_lshl_b32 s70, s66, 10\n"
            "s_add_u32 m0, s70, 73728\n"
            "s_add_u32 s72, s44, 33280\n"
            "s_addc_u32 s73, s45, 0\n"
            "global_load_lds_dwordx4 v65, s[72:73]\n"
            "ds_read_b64_tr_b16 a[164:165], v72 offset:8192\n"
            "ds_read_b64_tr_b16 a[166:167], v72 offset:8704\n"
            "s_lshl_b32 s70, s66, 10\n"
            "s_add_u32 m0, s70, 77824\n"
            "s_add_u32 s72, s44, 49664\n"
            "s_addc_u32 s73, s45, 0\n"
            "global_load_lds_dwordx4 v65, s[72:73]\n"
            "ds_read_b64_tr_b16 a[224:225], v73 offset:0\n"
            "ds_read_b64_tr_b16 a[226:227], v73 offset:512\n"
            "s_lshl_b32 s70, s66, 10\n"
            "s_add_u32 m0, s70, 81920\n"
            "s_add_u32 s72, s44, 66048\n"
            "s_addc_u32 s73, s45, 0\n"
            "global_load_lds_dwordx4 v65, s[72:73]\n"
            "ds_read_b64_tr_b16 a[228:229], v73 offset:8192\n"
            "ds_read_b64_tr_b16 a[230:231], v73 offset:8704\n"
            "s_lshl_b32 s70, s66, 10\n"
            "s_add_u32 m0, s70, 86016\n"
            "s_add_u32 s72, s44, 82432\n"
            "s_addc_u32 s73, s45, 0\n"
            "global_load_lds_dwordx4 v65, s[72:73]\n"
            "s_waitcnt lgkmcnt(0)\n"
            "ds_read_b64_tr_b16 a[168:169], v72 offset:16384\n"
            "s_lshl_b32 s70, s66, 10\n"
            "s_add_u32 m0, s70, 90112\n"
            "s_add_u32 s72, s44, 98816\n"
            "s_addc_u32 s73, s45, 0\n"
            "global_load_lds_dwordx4 v65, s[72:73]\n"
            "ds_read_b64_tr_b16 a[170:171], v72 offset:16896\n"
            "ds_read_b64_tr_b16 a[172:173], v72 offset:24576\n"
            "s_lshl_b32 s70, s66, 10\n"
            "s_add_u32 m0, s70, 94208\n"
            "s_add_u32 s72, s44, 115200\n"
            "s_addc_u32 s73, s45, 0\n"
            "global_load_lds_dwordx4 v65, s[72:73]\n"
            "ds_read_b64_tr_b16 a[174:175], v72 offset:25088\n"
            "ds_read_b64_tr_b16 a[232:233], v73 offset:16384\n"
            "ds_read_b64_tr_b16 a[234:235], v73 offset:16896\n"
            "ds_read_b64_tr_b16 a[236:237], v73 offset:24576\n"
            "ds_read_b64_tr_b16 a[238:239], v73 offset:25088\n"
            "s_waitcnt lgkmcnt(0)\n"
            "s_waitcnt vmcnt(16)\n"
            "s_barrier\n"
            "s_lshl_b32 s70, s66, 10\n"
            "s_add_u32 m0, s70, 98304\n"
            "s_add_u32 s72, s44, 768\n"
            "s_addc_u32 s73, s45, 0\n"
            "global_load_lds_dwordx4 v65, s[72:73]\n"
            "s_lshl_b32 s70, s66, 10\n"
            "s_add_u32 m0, s70, 102400\n"
            "s_add_u32 s72, s44, 17152\n"
            "s_addc_u32 s73, s45, 0\n"
            "global_load_lds_dwordx4 v65, s[72:73]\n"
            "s_lshl_b32 s70, s66, 10\n"
            "s_add_u32 m0, s70, 106496\n"
            "s_add_u32 s72, s44, 33536\n"
            "s_addc_u32 s73, s45, 0\n"
            "global_load_lds_dwordx4 v65, s[72:73]\n"
            "s_lshl_b32 s70, s66, 10\n"
            "s_add_u32 m0, s70, 110592\n"
            "s_add_u32 s72, s44, 49920\n"
            "s_addc_u32 s73, s45, 0\n"
            "global_load_lds_dwordx4 v65, s[72:73]\n"
            "s_lshl_b32 s70, s66, 10\n"
            "s_add_u32 m0, s70, 114688\n"
            "s_add_u32 s72, s44, 66304\n"
            "s_addc_u32 s73, s45, 0\n"
            "global_load_lds_dwordx4 v65, s[72:73]\n"
            "s_lshl_b32 s70, s66, 10\n"
            "s_add_u32 m0, s70, 118784\n"
            "s_add_u32 s72, s44, 82688\n"
            "s_addc_u32 s73, s45, 0\n"
            "global_load_lds_dwordx4 v65, s[72:73]\n"
            "s_lshl_b32 s70, s66, 10\n"
            "s_add_u32 m0, s70, 122880\n"
            "s_add_u32 s72, s44, 99072\n"
            "s_addc_u32 s73, s45, 0\n"
            "global_load_lds_dwordx4 v65, s[72:73]\n"
            "s_lshl_b32 s70, s66, 10\n"
            "s_add_u32 m0, s70, 126976\n"
            "s_add_u32 s72, s44, 115456\n"
            "s_addc_u32 s73, s45, 0\n"
            "global_load_lds_dwordx4 v65, s[72:73]\n"
            "s_cmp_eq_u32 s66, 0\n"
            "s_cbranch_scc0 XPO0_%=\n"
            "v_add_u32 v70, 0, v66\n"
            "v_xor_b32 v71, 0, v70\n"
            "ds_read_b64_tr_b16 v[128:129], v71 offset:0\n"
            "ds_read_b64_tr_b16 v[130:131], v71 offset:4096\n"
            "ds_read_b64_tr_b16 v[132:133], v71 offset:8192\n"
            "ds_read_b64_tr_b16 v[134:135], v71 offset:12288\n"
            "ds_read_b64_tr_b16 v[136:137], v71 offset:16384\n"
            "ds_read_b64_tr_b16 v[138:139], v71 offset:20480\n"
            "ds_read_b64_tr_b16 v[140:141], v71 offset:24576\n"
            "ds_read_b64_tr_b16 v[142:143], v71 offset:28672\n"
            "s_waitcnt lgkmcnt(0)\n"
            "v_xor_b32 v71, 32, v70\n"
            "ds_read_b64_tr_b16 v[144:145], v71 offset:0\n"
            "ds_read_b64_tr_b16 v[146:147], v71 offset:4096\n"
            "ds_read_b64_tr_b16 v[148:149], v71 offset:8192\n"
            "ds_read_b64_tr_b16 v[150:151], v71 offset:12288\n"
            "ds_read_b64_tr_b16 v[152:153], v71 offset:16384\n"
            "ds_read_b64_tr_b16 v[154:155], v71 offset:20480\n"
            "ds_read_b64_tr_b16 v[156:157], v71 offset:24576\n"
            "ds_read_b64_tr_b16 v[158:159], v71 offset:28672\n"
            "s_waitcnt lgkmcnt(0)\n"
            "v_xor_b32 v71, 64, v70\n"
            "ds_read_b64_tr_b16 v[160:161], v71 offset:0\n"
            "ds_read_b64_tr_b16 v[162:163], v71 offset:4096\n"
            "ds_read_b64_tr_b16 v[164:165], v71 offset:8192\n"
            "ds_read_b64_tr_b16 v[166:167], v71 offset:12288\n"
            "ds_read_b64_tr_b16 v[168:169], v71 offset:16384\n"
            "ds_read_b64_tr_b16 v[170:171], v71 offset:20480\n"
            "ds_read_b64_tr_b16 v[172:173], v71 offset:24576\n"
            "ds_read_b64_tr_b16 v[174:175], v71 offset:28672\n"
            "s_waitcnt lgkmcnt(0)\n"
            "v_xor_b32 v71, 96, v70\n"
            "ds_read_b64_tr_b16 v[176:177], v71 offset:0\n"
            "ds_read_b64_tr_b16 v[178:179], v71 offset:4096\n"
            "ds_read_b64_tr_b16 v[180:181], v71 offset:8192\n"
            "ds_read_b64_tr_b16 v[182:183], v71 offset:12288\n"
            "ds_read_b64_tr_b16 v[184:185], v71 offset:16384\n"
            "ds_read_b64_tr_b16 v[186:187], v71 offset:20480\n"
            "ds_read_b64_tr_b16 v[188:189], v71 offset:24576\n"
            "ds_read_b64_tr_b16 v[190:191], v71 offset:28672\n"
            "s_waitcnt lgkmcnt(0)\n"
            "v_xor_b32 v71, 128, v70\n"
            "ds_read_b64_tr_b16 v[192:193], v71 offset:0\n"
            "ds_read_b64_tr_b16 v[194:195], v71 offset:4096\n"
            "ds_read_b64_tr_b16 v[196:197], v71 offset:8192\n"
            "ds_read_b64_tr_b16 v[198:199], v71 offset:12288\n"
            "ds_read_b64_tr_b16 v[200:201], v71 offset:16384\n"
            "ds_read_b64_tr_b16 v[202:203], v71 offset:20480\n"
            "ds_read_b64_tr_b16 v[204:205], v71 offset:24576\n"
            "ds_read_b64_tr_b16 v[206:207], v71 offset:28672\n"
            "s_waitcnt lgkmcnt(0)\n"
            "v_xor_b32 v71, 160, v70\n"
            "ds_read_b64_tr_b16 v[208:209], v71 offset:0\n"
            "ds_read_b64_tr_b16 v[210:211], v71 offset:4096\n"
            "ds_read_b64_tr_b16 v[212:213], v71 offset:8192\n"
            "ds_read_b64_tr_b16 v[214:215], v71 offset:12288\n"
            "ds_read_b64_tr_b16 v[216:217], v71 offset:16384\n"
            "ds_read_b64_tr_b16 v[218:219], v71 offset:20480\n"
            "ds_read_b64_tr_b16 v[220:221], v71 offset:24576\n"
            "ds_read_b64_tr_b16 v[222:223], v71 offset:28672\n"
            "s_waitcnt lgkmcnt(0)\n"
            "v_xor_b32 v71, 192, v70\n"
            "ds_read_b64_tr_b16 v[224:225], v71 offset:0\n"
            "ds_read_b64_tr_b16 v[226:227], v71 offset:4096\n"
            "ds_read_b64_tr_b16 v[228:229], v71 offset:8192\n"
            "ds_read_b64_tr_b16 v[230:231], v71 offset:12288\n"
            "ds_read_b64_tr_b16 v[232:233], v71 offset:16384\n"
            "ds_read_b64_tr_b16 v[234:235], v71 offset:20480\n"
            "ds_read_b64_tr_b16 v[236:237], v71 offset:24576\n"
            "ds_read_b64_tr_b16 v[238:239], v71 offset:28672\n"
            "s_waitcnt lgkmcnt(0)\n"
            "v_xor_b32 v71, 224, v70\n"
            "ds_read_b64_tr_b16 v[240:241], v71 offset:0\n"
            "ds_read_b64_tr_b16 v[242:243], v71 offset:4096\n"
            "ds_read_b64_tr_b16 v[244:245], v71 offset:8192\n"
            "ds_read_b64_tr_b16 v[246:247], v71 offset:12288\n"
            "ds_read_b64_tr_b16 v[248:249], v71 offset:16384\n"
            "ds_read_b64_tr_b16 v[250:251], v71 offset:20480\n"
            "ds_read_b64_tr_b16 v[252:253], v71 offset:24576\n"
            "ds_read_b64_tr_b16 v[254:255], v71 offset:28672\n"
            "s_waitcnt lgkmcnt(0)\n"
            "XPO0_%=:\n"
            "s_waitcnt vmcnt(16)\n"
            "s_barrier\n"
            "s_lshl_b32 s70, s66, 10\n"
            "s_add_u32 m0, s70, 0\n"
            "s_add_u32 s72, s40, 1048576\n"
            "s_addc_u32 s73, s41, 0\n"
            "global_load_lds_dwordx4 v64, s[72:73]\n"
            "s_lshl_b32 s70, s66, 10\n"
            "s_add_u32 m0, s70, 4096\n"
            "s_add_u32 s72, s40, 1179648\n"
            "s_addc_u32 s73, s41, 0\n"
            "global_load_lds_dwordx4 v64, s[72:73]\n"
            "s_lshl_b32 s70, s66, 10\n"
            "s_add_u32 m0, s70, 8192\n"
            "s_add_u32 s72, s40, 1310720\n"
            "s_addc_u32 s73, s41, 0\n"
            "global_load_lds_dwordx4 v64, s[72:73]\n"
            "s_lshl_b32 s70, s66, 10\n"
            "s_add_u32 m0, s70, 12288\n"
            "s_add_u32 s72, s40, 1441792\n"
            "s_addc_u32 s73, s41, 0\n"
            "global_load_lds_dwordx4 v64, s[72:73]\n"
            "s_lshl_b32 s70, s66, 10\n"
            "s_add_u32 m0, s70, 16384\n"
            "s_add_u32 s72, s40, 1572864\n"
            "s_addc_u32 s73, s41, 0\n"
            "global_load_lds_dwordx4 v64, s[72:73]\n"
            "s_lshl_b32 s70, s66, 10\n"
            "s_add_u32 m0, s70, 20480\n"
            "s_add_u32 s72, s40, 1703936\n"
            "s_addc_u32 s73, s41, 0\n"
            "global_load_lds_dwordx4 v64, s[72:73]\n"
            "s_lshl_b32 s70, s66, 10\n"
            "s_add_u32 m0, s70, 24576\n"
            "s_add_u32 s72, s40, 1835008\n"
            "s_addc_u32 s73, s41, 0\n"
            "global_load_lds_dwordx4 v64, s[72:73]\n"
            "s_lshl_b32 s70, s66, 10\n"
            "s_add_u32 m0, s70, 28672\n"
            "s_add_u32 s72, s40, 1966080\n"
            "s_addc_u32 s73, s41, 0\n"
            "global_load_lds_dwordx4 v64, s[72:73]\n"
            "s_cmp_eq_u32 s66, 1\n"
            "s_cbranch_scc0 XPO1_%=\n"
            "v_add_u32 v70, 32768, v66\n"
            "v_xor_b32 v71, 0, v70\n"
            "ds_read_b64_tr_b16 v[128:129], v71 offset:0\n"
            "ds_read_b64_tr_b16 v[130:131], v71 offset:4096\n"
            "ds_read_b64_tr_b16 v[132:133], v71 offset:8192\n"
            "ds_read_b64_tr_b16 v[134:135], v71 offset:12288\n"
            "ds_read_b64_tr_b16 v[136:137], v71 offset:16384\n"
            "ds_read_b64_tr_b16 v[138:139], v71 offset:20480\n"
            "ds_read_b64_tr_b16 v[140:141], v71 offset:24576\n"
            "ds_read_b64_tr_b16 v[142:143], v71 offset:28672\n"
            "s_waitcnt lgkmcnt(0)\n"
            "v_xor_b32 v71, 32, v70\n"
            "ds_read_b64_tr_b16 v[144:145], v71 offset:0\n"
            "ds_read_b64_tr_b16 v[146:147], v71 offset:4096\n"
            "ds_read_b64_tr_b16 v[148:149], v71 offset:8192\n"
            "ds_read_b64_tr_b16 v[150:151], v71 offset:12288\n"
            "ds_read_b64_tr_b16 v[152:153], v71 offset:16384\n"
            "ds_read_b64_tr_b16 v[154:155], v71 offset:20480\n"
            "ds_read_b64_tr_b16 v[156:157], v71 offset:24576\n"
            "ds_read_b64_tr_b16 v[158:159], v71 offset:28672\n"
            "s_waitcnt lgkmcnt(0)\n"
            "v_xor_b32 v71, 64, v70\n"
            "ds_read_b64_tr_b16 v[160:161], v71 offset:0\n"
            "ds_read_b64_tr_b16 v[162:163], v71 offset:4096\n"
            "ds_read_b64_tr_b16 v[164:165], v71 offset:8192\n"
            "ds_read_b64_tr_b16 v[166:167], v71 offset:12288\n"
            "ds_read_b64_tr_b16 v[168:169], v71 offset:16384\n"
            "ds_read_b64_tr_b16 v[170:171], v71 offset:20480\n"
            "ds_read_b64_tr_b16 v[172:173], v71 offset:24576\n"
            "ds_read_b64_tr_b16 v[174:175], v71 offset:28672\n"
            "s_waitcnt lgkmcnt(0)\n"
            "v_xor_b32 v71, 96, v70\n"
            "ds_read_b64_tr_b16 v[176:177], v71 offset:0\n"
            "ds_read_b64_tr_b16 v[178:179], v71 offset:4096\n"
            "ds_read_b64_tr_b16 v[180:181], v71 offset:8192\n"
            "ds_read_b64_tr_b16 v[182:183], v71 offset:12288\n"
            "ds_read_b64_tr_b16 v[184:185], v71 offset:16384\n"
            "ds_read_b64_tr_b16 v[186:187], v71 offset:20480\n"
            "ds_read_b64_tr_b16 v[188:189], v71 offset:24576\n"
            "ds_read_b64_tr_b16 v[190:191], v71 offset:28672\n"
            "s_waitcnt lgkmcnt(0)\n"
            "v_xor_b32 v71, 128, v70\n"
            "ds_read_b64_tr_b16 v[192:193], v71 offset:0\n"
            "ds_read_b64_tr_b16 v[194:195], v71 offset:4096\n"
            "ds_read_b64_tr_b16 v[196:197], v71 offset:8192\n"
            "ds_read_b64_tr_b16 v[198:199], v71 offset:12288\n"
            "ds_read_b64_tr_b16 v[200:201], v71 offset:16384\n"
            "ds_read_b64_tr_b16 v[202:203], v71 offset:20480\n"
            "ds_read_b64_tr_b16 v[204:205], v71 offset:24576\n"
            "ds_read_b64_tr_b16 v[206:207], v71 offset:28672\n"
            "s_waitcnt lgkmcnt(0)\n"
            "v_xor_b32 v71, 160, v70\n"
            "ds_read_b64_tr_b16 v[208:209], v71 offset:0\n"
            "ds_read_b64_tr_b16 v[210:211], v71 offset:4096\n"
            "ds_read_b64_tr_b16 v[212:213], v71 offset:8192\n"
            "ds_read_b64_tr_b16 v[214:215], v71 offset:12288\n"
            "ds_read_b64_tr_b16 v[216:217], v71 offset:16384\n"
            "ds_read_b64_tr_b16 v[218:219], v71 offset:20480\n"
            "ds_read_b64_tr_b16 v[220:221], v71 offset:24576\n"
            "ds_read_b64_tr_b16 v[222:223], v71 offset:28672\n"
            "s_waitcnt lgkmcnt(0)\n"
            "v_xor_b32 v71, 192, v70\n"
            "ds_read_b64_tr_b16 v[224:225], v71 offset:0\n"
            "ds_read_b64_tr_b16 v[226:227], v71 offset:4096\n"
            "ds_read_b64_tr_b16 v[228:229], v71 offset:8192\n"
            "ds_read_b64_tr_b16 v[230:231], v71 offset:12288\n"
            "ds_read_b64_tr_b16 v[232:233], v71 offset:16384\n"
            "ds_read_b64_tr_b16 v[234:235], v71 offset:20480\n"
            "ds_read_b64_tr_b16 v[236:237], v71 offset:24576\n"
            "ds_read_b64_tr_b16 v[238:239], v71 offset:28672\n"
            "s_waitcnt lgkmcnt(0)\n"
            "v_xor_b32 v71, 224, v70\n"
            "ds_read_b64_tr_b16 v[240:241], v71 offset:0\n"
            "ds_read_b64_tr_b16 v[242:243], v71 offset:4096\n"
            "ds_read_b64_tr_b16 v[244:245], v71 offset:8192\n"
            "ds_read_b64_tr_b16 v[246:247], v71 offset:12288\n"
            "ds_read_b64_tr_b16 v[248:249], v71 offset:16384\n"
            "ds_read_b64_tr_b16 v[250:251], v71 offset:20480\n"
            "ds_read_b64_tr_b16 v[252:253], v71 offset:24576\n"
            "ds_read_b64_tr_b16 v[254:255], v71 offset:28672\n"
            "s_waitcnt lgkmcnt(0)\n"
            "XPO1_%=:\n"
            "s_waitcnt vmcnt(16)\n"
            "s_barrier\n"
            "s_lshl_b32 s70, s66, 10\n"
            "s_add_u32 m0, s70, 32768\n"
            "s_add_u32 s72, s42, 1048576\n"
            "s_addc_u32 s73, s43, 0\n"
            "global_load_lds_dwordx4 v64, s[72:73]\n"
            "s_lshl_b32 s70, s66, 10\n"
            "s_add_u32 m0, s70, 36864\n"
            "s_add_u32 s72, s42, 1179648\n"
            "s_addc_u32 s73, s43, 0\n"
            "global_load_lds_dwordx4 v64, s[72:73]\n"
            "s_lshl_b32 s70, s66, 10\n"
            "s_add_u32 m0, s70, 40960\n"
            "s_add_u32 s72, s42, 1310720\n"
            "s_addc_u32 s73, s43, 0\n"
            "global_load_lds_dwordx4 v64, s[72:73]\n"
            "s_lshl_b32 s70, s66, 10\n"
            "s_add_u32 m0, s70, 45056\n"
            "s_add_u32 s72, s42, 1441792\n"
            "s_addc_u32 s73, s43, 0\n"
            "global_load_lds_dwordx4 v64, s[72:73]\n"
            "s_lshl_b32 s70, s66, 10\n"
            "s_add_u32 m0, s70, 49152\n"
            "s_add_u32 s72, s42, 1572864\n"
            "s_addc_u32 s73, s43, 0\n"
            "global_load_lds_dwordx4 v64, s[72:73]\n"
            "s_lshl_b32 s70, s66, 10\n"
            "s_add_u32 m0, s70, 53248\n"
            "s_add_u32 s72, s42, 1703936\n"
            "s_addc_u32 s73, s43, 0\n"
            "global_load_lds_dwordx4 v64, s[72:73]\n"
            "s_lshl_b32 s70, s66, 10\n"
            "s_add_u32 m0, s70, 57344\n"
            "s_add_u32 s72, s42, 1835008\n"
            "s_addc_u32 s73, s43, 0\n"
            "global_load_lds_dwordx4 v64, s[72:73]\n"
            "s_lshl_b32 s70, s66, 10\n"
            "s_add_u32 m0, s70, 61440\n"
            "s_add_u32 s72, s42, 1966080\n"
            "s_addc_u32 s73, s43, 0\n"
            "global_load_lds_dwordx4 v64, s[72:73]\n"
            "s_cmp_eq_u32 s66, 2\n"
            "s_cbranch_scc0 XPO2_%=\n"
            "v_add_u32 v70, 65536, v66\n"
            "v_xor_b32 v71, 0, v70\n"
            "ds_read_b64_tr_b16 v[128:129], v71 offset:0\n"
            "ds_read_b64_tr_b16 v[130:131], v71 offset:4096\n"
            "ds_read_b64_tr_b16 v[132:133], v71 offset:8192\n"
            "ds_read_b64_tr_b16 v[134:135], v71 offset:12288\n"
            "ds_read_b64_tr_b16 v[136:137], v71 offset:16384\n"
            "ds_read_b64_tr_b16 v[138:139], v71 offset:20480\n"
            "ds_read_b64_tr_b16 v[140:141], v71 offset:24576\n"
            "ds_read_b64_tr_b16 v[142:143], v71 offset:28672\n"
            "s_waitcnt lgkmcnt(0)\n"
            "v_xor_b32 v71, 32, v70\n"
            "ds_read_b64_tr_b16 v[144:145], v71 offset:0\n"
            "ds_read_b64_tr_b16 v[146:147], v71 offset:4096\n"
            "ds_read_b64_tr_b16 v[148:149], v71 offset:8192\n"
            "ds_read_b64_tr_b16 v[150:151], v71 offset:12288\n"
            "ds_read_b64_tr_b16 v[152:153], v71 offset:16384\n"
            "ds_read_b64_tr_b16 v[154:155], v71 offset:20480\n"
            "ds_read_b64_tr_b16 v[156:157], v71 offset:24576\n"
            "ds_read_b64_tr_b16 v[158:159], v71 offset:28672\n"
            "s_waitcnt lgkmcnt(0)\n"
            "v_xor_b32 v71, 64, v70\n"
            "ds_read_b64_tr_b16 v[160:161], v71 offset:0\n"
            "ds_read_b64_tr_b16 v[162:163], v71 offset:4096\n"
            "ds_read_b64_tr_b16 v[164:165], v71 offset:8192\n"
            "ds_read_b64_tr_b16 v[166:167], v71 offset:12288\n"
            "ds_read_b64_tr_b16 v[168:169], v71 offset:16384\n"
            "ds_read_b64_tr_b16 v[170:171], v71 offset:20480\n"
            "ds_read_b64_tr_b16 v[172:173], v71 offset:24576\n"
            "ds_read_b64_tr_b16 v[174:175], v71 offset:28672\n"
            "s_waitcnt lgkmcnt(0)\n"
            "v_xor_b32 v71, 96, v70\n"
            "ds_read_b64_tr_b16 v[176:177], v71 offset:0\n"
            "ds_read_b64_tr_b16 v[178:179], v71 offset:4096\n"
            "ds_read_b64_tr_b16 v[180:181], v71 offset:8192\n"
            "ds_read_b64_tr_b16 v[182:183], v71 offset:12288\n"
            "ds_read_b64_tr_b16 v[184:185], v71 offset:16384\n"
            "ds_read_b64_tr_b16 v[186:187], v71 offset:20480\n"
            "ds_read_b64_tr_b16 v[188:189], v71 offset:24576\n"
            "ds_read_b64_tr_b16 v[190:191], v71 offset:28672\n"
            "s_waitcnt lgkmcnt(0)\n"
            "v_xor_b32 v71, 128, v70\n"
            "ds_read_b64_tr_b16 v[192:193], v71 offset:0\n"
            "ds_read_b64_tr_b16 v[194:195], v71 offset:4096\n"
            "ds_read_b64_tr_b16 v[196:197], v71 offset:8192\n"
            "ds_read_b64_tr_b16 v[198:199], v71 offset:12288\n"
            "ds_read_b64_tr_b16 v[200:201], v71 offset:16384\n"
            "ds_read_b64_tr_b16 v[202:203], v71 offset:20480\n"
            "ds_read_b64_tr_b16 v[204:205], v71 offset:24576\n"
            "ds_read_b64_tr_b16 v[206:207], v71 offset:28672\n"
            "s_waitcnt lgkmcnt(0)\n"
            "v_xor_b32 v71, 160, v70\n"
            "ds_read_b64_tr_b16 v[208:209], v71 offset:0\n"
            "ds_read_b64_tr_b16 v[210:211], v71 offset:4096\n"
            "ds_read_b64_tr_b16 v[212:213], v71 offset:8192\n"
            "ds_read_b64_tr_b16 v[214:215], v71 offset:12288\n"
            "ds_read_b64_tr_b16 v[216:217], v71 offset:16384\n"
            "ds_read_b64_tr_b16 v[218:219], v71 offset:20480\n"
            "ds_read_b64_tr_b16 v[220:221], v71 offset:24576\n"
            "ds_read_b64_tr_b16 v[222:223], v71 offset:28672\n"
            "s_waitcnt lgkmcnt(0)\n"
            "v_xor_b32 v71, 192, v70\n"
            "ds_read_b64_tr_b16 v[224:225], v71 offset:0\n"
            "ds_read_b64_tr_b16 v[226:227], v71 offset:4096\n"
            "ds_read_b64_tr_b16 v[228:229], v71 offset:8192\n"
            "ds_read_b64_tr_b16 v[230:231], v71 offset:12288\n"
            "ds_read_b64_tr_b16 v[232:233], v71 offset:16384\n"
            "ds_read_b64_tr_b16 v[234:235], v71 offset:20480\n"
            "ds_read_b64_tr_b16 v[236:237], v71 offset:24576\n"
            "ds_read_b64_tr_b16 v[238:239], v71 offset:28672\n"
            "s_waitcnt lgkmcnt(0)\n"
            "v_xor_b32 v71, 224, v70\n"
            "ds_read_b64_tr_b16 v[240:241], v71 offset:0\n"
            "ds_read_b64_tr_b16 v[242:243], v71 offset:4096\n"
            "ds_read_b64_tr_b16 v[244:245], v71 offset:8192\n"
            "ds_read_b64_tr_b16 v[246:247], v71 offset:12288\n"
            "ds_read_b64_tr_b16 v[248:249], v71 offset:16384\n"
            "ds_read_b64_tr_b16 v[250:251], v71 offset:20480\n"
            "ds_read_b64_tr_b16 v[252:253], v71 offset:24576\n"
            "ds_read_b64_tr_b16 v[254:255], v71 offset:28672\n"
            "s_waitcnt lgkmcnt(0)\n"
            "XPO2_%=:\n"
            "s_waitcnt vmcnt(16)\n"
            "s_barrier\n"
            "s_lshl_b32 s70, s66, 10\n"
            "s_add_u32 m0, s70, 65536\n"
            "s_add_u32 s72, s40, 0\n"
            "s_addc_u32 s73, s41, 0\n"
            "global_load_lds_dwordx4 v64, s[72:73]\n"
            "s_lshl_b32 s70, s66, 10\n"
            "s_add_u32 m0, s70, 69632\n"
            "s_add_u32 s72, s40, 131072\n"
            "s_addc_u32 s73, s41, 0\n"
            "global_load_lds_dwordx4 v64, s[72:73]\n"
            "s_lshl_b32 s70, s66, 10\n"
            "s_add_u32 m0, s70, 73728\n"
            "s_add_u32 s72, s40, 262144\n"
            "s_addc_u32 s73, s41, 0\n"
            "global_load_lds_dwordx4 v64, s[72:73]\n"
            "s_lshl_b32 s70, s66, 10\n"
            "s_add_u32 m0, s70, 77824\n"
            "s_add_u32 s72, s40, 393216\n"
            "s_addc_u32 s73, s41, 0\n"
            "global_load_lds_dwordx4 v64, s[72:73]\n"
            "s_lshl_b32 s70, s66, 10\n"
            "s_add_u32 m0, s70, 81920\n"
            "s_add_u32 s72, s40, 524288\n"
            "s_addc_u32 s73, s41, 0\n"
            "global_load_lds_dwordx4 v64, s[72:73]\n"
            "s_lshl_b32 s70, s66, 10\n"
            "s_add_u32 m0, s70, 86016\n"
            "s_add_u32 s72, s40, 655360\n"
            "s_addc_u32 s73, s41, 0\n"
            "global_load_lds_dwordx4 v64, s[72:73]\n"
            "s_lshl_b32 s70, s66, 10\n"
            "s_add_u32 m0, s70, 90112\n"
            "s_add_u32 s72, s40, 786432\n"
            "s_addc_u32 s73, s41, 0\n"
            "global_load_lds_dwordx4 v64, s[72:73]\n"
            "s_lshl_b32 s70, s66, 10\n"
            "s_add_u32 m0, s70, 94208\n"
            "s_add_u32 s72, s40, 917504\n"
            "s_addc_u32 s73, s41, 0\n"
            "global_load_lds_dwordx4 v64, s[72:73]\n"
            "s_cmp_eq_u32 s66, 3\n"
            "s_cbranch_scc0 XPO3_%=\n"
            "v_add_u32 v70, 98304, v66\n"
            "v_xor_b32 v71, 0, v70\n"
            "ds_read_b64_tr_b16 v[128:129], v71 offset:0\n"
            "ds_read_b64_tr_b16 v[130:131], v71 offset:4096\n"
            "ds_read_b64_tr_b16 v[132:133], v71 offset:8192\n"
            "ds_read_b64_tr_b16 v[134:135], v71 offset:12288\n"
            "ds_read_b64_tr_b16 v[136:137], v71 offset:16384\n"
            "ds_read_b64_tr_b16 v[138:139], v71 offset:20480\n"
            "ds_read_b64_tr_b16 v[140:141], v71 offset:24576\n"
            "ds_read_b64_tr_b16 v[142:143], v71 offset:28672\n"
            "s_waitcnt lgkmcnt(0)\n"
            "v_xor_b32 v71, 32, v70\n"
            "ds_read_b64_tr_b16 v[144:145], v71 offset:0\n"
            "ds_read_b64_tr_b16 v[146:147], v71 offset:4096\n"
            "ds_read_b64_tr_b16 v[148:149], v71 offset:8192\n"
            "ds_read_b64_tr_b16 v[150:151], v71 offset:12288\n"
            "ds_read_b64_tr_b16 v[152:153], v71 offset:16384\n"
            "ds_read_b64_tr_b16 v[154:155], v71 offset:20480\n"
            "ds_read_b64_tr_b16 v[156:157], v71 offset:24576\n"
            "ds_read_b64_tr_b16 v[158:159], v71 offset:28672\n"
            "s_waitcnt lgkmcnt(0)\n"
            "v_xor_b32 v71, 64, v70\n"
            "ds_read_b64_tr_b16 v[160:161], v71 offset:0\n"
            "ds_read_b64_tr_b16 v[162:163], v71 offset:4096\n"
            "ds_read_b64_tr_b16 v[164:165], v71 offset:8192\n"
            "ds_read_b64_tr_b16 v[166:167], v71 offset:12288\n"
            "ds_read_b64_tr_b16 v[168:169], v71 offset:16384\n"
            "ds_read_b64_tr_b16 v[170:171], v71 offset:20480\n"
            "ds_read_b64_tr_b16 v[172:173], v71 offset:24576\n"
            "ds_read_b64_tr_b16 v[174:175], v71 offset:28672\n"
            "s_waitcnt lgkmcnt(0)\n"
            "v_xor_b32 v71, 96, v70\n"
            "ds_read_b64_tr_b16 v[176:177], v71 offset:0\n"
            "ds_read_b64_tr_b16 v[178:179], v71 offset:4096\n"
            "ds_read_b64_tr_b16 v[180:181], v71 offset:8192\n"
            "ds_read_b64_tr_b16 v[182:183], v71 offset:12288\n"
            "ds_read_b64_tr_b16 v[184:185], v71 offset:16384\n"
            "ds_read_b64_tr_b16 v[186:187], v71 offset:20480\n"
            "ds_read_b64_tr_b16 v[188:189], v71 offset:24576\n"
            "ds_read_b64_tr_b16 v[190:191], v71 offset:28672\n"
            "s_waitcnt lgkmcnt(0)\n"
            "v_xor_b32 v71, 128, v70\n"
            "ds_read_b64_tr_b16 v[192:193], v71 offset:0\n"
            "ds_read_b64_tr_b16 v[194:195], v71 offset:4096\n"
            "ds_read_b64_tr_b16 v[196:197], v71 offset:8192\n"
            "ds_read_b64_tr_b16 v[198:199], v71 offset:12288\n"
            "ds_read_b64_tr_b16 v[200:201], v71 offset:16384\n"
            "ds_read_b64_tr_b16 v[202:203], v71 offset:20480\n"
            "ds_read_b64_tr_b16 v[204:205], v71 offset:24576\n"
            "ds_read_b64_tr_b16 v[206:207], v71 offset:28672\n"
            "s_waitcnt lgkmcnt(0)\n"
            "v_xor_b32 v71, 160, v70\n"
            "ds_read_b64_tr_b16 v[208:209], v71 offset:0\n"
            "ds_read_b64_tr_b16 v[210:211], v71 offset:4096\n"
            "ds_read_b64_tr_b16 v[212:213], v71 offset:8192\n"
            "ds_read_b64_tr_b16 v[214:215], v71 offset:12288\n"
            "ds_read_b64_tr_b16 v[216:217], v71 offset:16384\n"
            "ds_read_b64_tr_b16 v[218:219], v71 offset:20480\n"
            "ds_read_b64_tr_b16 v[220:221], v71 offset:24576\n"
            "ds_read_b64_tr_b16 v[222:223], v71 offset:28672\n"
            "s_waitcnt lgkmcnt(0)\n"
            "v_xor_b32 v71, 192, v70\n"
            "ds_read_b64_tr_b16 v[224:225], v71 offset:0\n"
            "ds_read_b64_tr_b16 v[226:227], v71 offset:4096\n"
            "ds_read_b64_tr_b16 v[228:229], v71 offset:8192\n"
            "ds_read_b64_tr_b16 v[230:231], v71 offset:12288\n"
            "ds_read_b64_tr_b16 v[232:233], v71 offset:16384\n"
            "ds_read_b64_tr_b16 v[234:235], v71 offset:20480\n"
            "ds_read_b64_tr_b16 v[236:237], v71 offset:24576\n"
            "ds_read_b64_tr_b16 v[238:239], v71 offset:28672\n"
            "s_waitcnt lgkmcnt(0)\n"
            "v_xor_b32 v71, 224, v70\n"
            "ds_read_b64_tr_b16 v[240:241], v71 offset:0\n"
            "ds_read_b64_tr_b16 v[242:243], v71 offset:4096\n"
            "ds_read_b64_tr_b16 v[244:245], v71 offset:8192\n"
            "ds_read_b64_tr_b16 v[246:247], v71 offset:12288\n"
            "ds_read_b64_tr_b16 v[248:249], v71 offset:16384\n"
            "ds_read_b64_tr_b16 v[250:251], v71 offset:20480\n"
            "ds_read_b64_tr_b16 v[252:253], v71 offset:24576\n"
            "ds_read_b64_tr_b16 v[254:255], v71 offset:28672\n"
            "s_waitcnt lgkmcnt(0)\n"
            "XPO3_%=:\n"
            "s_waitcnt vmcnt(16)\n"
            "s_barrier\n"
            "s_lshl_b32 s70, s66, 10\n"
            "s_add_u32 m0, s70, 98304\n"
            "s_add_u32 s72, s42, 0\n"
            "s_addc_u32 s73, s43, 0\n"
            "global_load_lds_dwordx4 v64, s[72:73]\n"
            "v_add_u32 v72, 0, v67\n"
            "v_xor_b32 v73, 32, v72\n"
            "s_lshl_b32 s70, s66, 10\n"
            "s_add_u32 m0, s70, 102400\n"
            "s_add_u32 s72, s42, 131072\n"
            "s_addc_u32 s73, s43, 0\n"
            "global_load_lds_dwordx4 v64, s[72:73]\n"
            "ds_read_b64_tr_b16 a[16:17], v72 offset:0\n"
            "ds_read_b64_tr_b16 a[18:19], v72 offset:512\n"
            "s_lshl_b32 s70, s66, 10\n"
            "s_add_u32 m0, s70, 106496\n"
            "s_add_u32 s72, s42, 262144\n"
            "s_addc_u32 s73, s43, 0\n"
            "global_load_lds_dwordx4 v64, s[72:73]\n"
            "ds_read_b64_tr_b16 a[20:21], v72 offset:8192\n"
            "ds_read_b64_tr_b16 a[22:23], v72 offset:8704\n"
            "s_lshl_b32 s70, s66, 10\n"
            "s_add_u32 m0, s70, 110592\n"
            "s_add_u32 s72, s42, 393216\n"
            "s_addc_u32 s73, s43, 0\n"
            "global_load_lds_dwordx4 v64, s[72:73]\n"
            "ds_read_b64_tr_b16 a[80:81], v73 offset:0\n"
            "ds_read_b64_tr_b16 a[82:83], v73 offset:512\n"
            "s_lshl_b32 s70, s66, 10\n"
            "s_add_u32 m0, s70, 114688\n"
            "s_add_u32 s72, s42, 524288\n"
            "s_addc_u32 s73, s43, 0\n"
            "global_load_lds_dwordx4 v64, s[72:73]\n"
            "ds_read_b64_tr_b16 a[84:85], v73 offset:8192\n"
            "ds_read_b64_tr_b16 a[86:87], v73 offset:8704\n"
            "s_lshl_b32 s70, s66, 10\n"
            "s_add_u32 m0, s70, 118784\n"
            "s_add_u32 s72, s42, 655360\n"
            "s_addc_u32 s73, s43, 0\n"
            "global_load_lds_dwordx4 v64, s[72:73]\n"
            "s_waitcnt lgkmcnt(0)\n"
            "ds_read_b64_tr_b16 a[24:25], v72 offset:16384\n"
            "s_lshl_b32 s70, s66, 10\n"
            "s_add_u32 m0, s70, 122880\n"
            "s_add_u32 s72, s42, 786432\n"
            "s_addc_u32 s73, s43, 0\n"
            "global_load_lds_dwordx4 v64, s[72:73]\n"
            "ds_read_b64_tr_b16 a[26:27], v72 offset:16896\n"
            "ds_read_b64_tr_b16 a[28:29], v72 offset:24576\n"
            "s_lshl_b32 s70, s66, 10\n"
            "s_add_u32 m0, s70, 126976\n"
            "s_add_u32 s72, s42, 917504\n"
            "s_addc_u32 s73, s43, 0\n"
            "global_load_lds_dwordx4 v64, s[72:73]\n"
            "ds_read_b64_tr_b16 a[30:31], v72 offset:25088\n"
            "ds_read_b64_tr_b16 a[88:89], v73 offset:16384\n"
            "ds_read_b64_tr_b16 a[90:91], v73 offset:16896\n"
            "ds_read_b64_tr_b16 a[92:93], v73 offset:24576\n"
            "ds_read_b64_tr_b16 a[94:95], v73 offset:25088\n"
            "s_waitcnt lgkmcnt(0)\n"
            "s_waitcnt vmcnt(16)\n"
            "s_barrier\n"
            "v_add_u32 v72, 32768, v67\n"
            "v_xor_b32 v73, 32, v72\n"
            "ds_read_b64_tr_b16 a[144:145], v72 offset:0\n"
            "ds_read_b64_tr_b16 a[146:147], v72 offset:512\n"
            "ds_read_b64_tr_b16 a[148:149], v72 offset:8192\n"
            "ds_read_b64_tr_b16 a[150:151], v72 offset:8704\n"
            "ds_read_b64_tr_b16 a[208:209], v73 offset:0\n"
            "ds_read_b64_tr_b16 a[210:211], v73 offset:512\n"
            "ds_read_b64_tr_b16 a[212:213], v73 offset:8192\n"
            "ds_read_b64_tr_b16 a[214:215], v73 offset:8704\n"
            "s_waitcnt lgkmcnt(0)\n"
            "ds_read_b64_tr_b16 a[152:153], v72 offset:16384\n"
            "ds_read_b64_tr_b16 a[154:155], v72 offset:16896\n"
            "ds_read_b64_tr_b16 a[156:157], v72 offset:24576\n"
            "ds_read_b64_tr_b16 a[158:159], v72 offset:25088\n"
            "ds_read_b64_tr_b16 a[216:217], v73 offset:16384\n"
            "ds_read_b64_tr_b16 a[218:219], v73 offset:16896\n"
            "ds_read_b64_tr_b16 a[220:221], v73 offset:24576\n"
            "ds_read_b64_tr_b16 a[222:223], v73 offset:25088\n"
            "s_waitcnt lgkmcnt(0)\n"
            "s_waitcnt vmcnt(8)\n"
            "s_barrier\n"
            "v_add_u32 v72, 65536, v67\n"
            "v_xor_b32 v73, 32, v72\n"
            "ds_read_b64_tr_b16 a[0:1], v72 offset:0\n"
            "ds_read_b64_tr_b16 a[2:3], v72 offset:512\n"
            "ds_read_b64_tr_b16 a[4:5], v72 offset:8192\n"
            "ds_read_b64_tr_b16 a[6:7], v72 offset:8704\n"
            "ds_read_b64_tr_b16 a[64:65], v73 offset:0\n"
            "ds_read_b64_tr_b16 a[66:67], v73 offset:512\n"
            "ds_read_b64_tr_b16 a[68:69], v73 offset:8192\n"
            "ds_read_b64_tr_b16 a[70:71], v73 offset:8704\n"
            "s_waitcnt lgkmcnt(0)\n"
            "ds_read_b64_tr_b16 a[8:9], v72 offset:16384\n"
            "ds_read_b64_tr_b16 a[10:11], v72 offset:16896\n"
            "ds_read_b64_tr_b16 a[12:13], v72 offset:24576\n"
            "ds_read_b64_tr_b16 a[14:15], v72 offset:25088\n"
            "ds_read_b64_tr_b16 a[72:73], v73 offset:16384\n"
            "ds_read_b64_tr_b16 a[74:75], v73 offset:16896\n"
            "ds_read_b64_tr_b16 a[76:77], v73 offset:24576\n"
            "ds_read_b64_tr_b16 a[78:79], v73 offset:25088\n"
            "s_waitcnt lgkmcnt(0)\n"
            "s_waitcnt vmcnt(0)\n"
            "s_barrier\n"
            "v_add_u32 v72, 98304, v67\n"
            "v_xor_b32 v73, 32, v72\n"
            "ds_read_b64_tr_b16 a[128:129], v72 offset:0\n"
            "ds_read_b64_tr_b16 a[130:131], v72 offset:512\n"
            "ds_read_b64_tr_b16 a[132:133], v72 offset:8192\n"
            "ds_read_b64_tr_b16 a[134:135], v72 offset:8704\n"
            "ds_read_b64_tr_b16 a[192:193], v73 offset:0\n"
            "ds_read_b64_tr_b16 a[194:195], v73 offset:512\n"
            "ds_read_b64_tr_b16 a[196:197], v73 offset:8192\n"
            "ds_read_b64_tr_b16 a[198:199], v73 offset:8704\n"
            "s_waitcnt lgkmcnt(0)\n"
            "ds_read_b64_tr_b16 a[136:137], v72 offset:16384\n"
            "ds_read_b64_tr_b16 a[138:139], v72 offset:16896\n"
            "ds_read_b64_tr_b16 a[140:141], v72 offset:24576\n"
            "ds_read_b64_tr_b16 a[142:143], v72 offset:25088\n"
            "ds_read_b64_tr_b16 a[200:201], v73 offset:16384\n"
            "ds_read_b64_tr_b16 a[202:203], v73 offset:16896\n"
            "ds_read_b64_tr_b16 a[204:205], v73 offset:24576\n"
            "ds_read_b64_tr_b16 a[206:207], v73 offset:25088\n"
            "s_waitcnt lgkmcnt(0)\n"
            "s_barrier\n"
            "s_mov_b32 s64, 0\n"
            "s_mov_b32 s69, 32768\n"
            "v_add_u32 v119, 32768, v114\n"
            "s_mul_i32 s70, s66, 6\n"
            "s_add_u32 s70, s70, 0\n"
            "s_lshr_b32 s71, s70, 3\n"
            "s_and_b32 s70, s70, 7\n"
            "s_lshl_b32 s70, s70, 10\n"
            "s_mov_b32 s16, 0\n"
            "s_cmp_eq_u32 s71, 2\n"
            "s_cselect_b32 s16, 33554432, s16\n"
            "s_add_u32 s16, s16, s70\n"
            "s_cmp_eq_u32 s71, 0\n"
            "s_cselect_b32 s86, 8192, 0\n"
            "s_mul_i32 s70, s66, 6\n"
            "s_add_u32 s70, s70, 1\n"
            "s_lshr_b32 s71, s70, 3\n"
            "s_and_b32 s70, s70, 7\n"
            "s_lshl_b32 s70, s70, 10\n"
            "s_mov_b32 s17, 0\n"
            "s_cmp_eq_u32 s71, 2\n"
            "s_cselect_b32 s17, 33554432, s17\n"
            "s_add_u32 s17, s17, s70\n"
            "s_cmp_eq_u32 s71, 0\n"
            "s_cselect_b32 s87, 8192, 0\n"
            "s_mul_i32 s70, s66, 6\n"
            "s_add_u32 s70, s70, 2\n"
            "s_lshr_b32 s71, s70, 3\n"
            "s_and_b32 s70, s70, 7\n"
            "s_lshl_b32 s70, s70, 10\n"
            "s_mov_b32 s18, 0\n"
            "s_cmp_eq_u32 s71, 2\n"
            "s_cselect_b32 s18, 33554432, s18\n"
            "s_add_u32 s18, s18, s70\n"
            "s_cmp_eq_u32 s71, 0\n"
            "s_cselect_b32 s88, 8192, 0\n"
            "s_mul_i32 s70, s66, 6\n"
            "s_add_u32 s70, s70, 3\n"
            "s_lshr_b32 s71, s70, 3\n"
            "s_and_b32 s70, s70, 7\n"
            "s_lshl_b32 s70, s70, 10\n"
            "s_mov_b32 s19, 0\n"
            "s_cmp_eq_u32 s71, 2\n"
            "s_cselect_b32 s19, 33554432, s19\n"
            "s_add_u32 s19, s19, s70\n"
            "s_cmp_eq_u32 s71, 0\n"
            "s_cselect_b32 s89, 8192, 0\n"
            "s_mul_i32 s70, s66, 6\n"
            "s_add_u32 s70, s70, 4\n"
            "s_lshr_b32 s71, s70, 3\n"
            "s_and_b32 s70, s70, 7\n"
            "s_lshl_b32 s70, s70, 10\n"
            "s_mov_b32 s20, 0\n"
            "s_cmp_eq_u32 s71, 2\n"
            "s_cselect_b32 s20, 33554432, s20\n"
            "s_add_u32 s20, s20, s70\n"
            "s_cmp_eq_u32 s71, 0\n"
            "s_cselect_b32 s90, 8192, 0\n"
            "s_mul_i32 s70, s66, 6\n"
            "s_add_u32 s70, s70, 5\n"
            "s_lshr_b32 s71, s70, 3\n"
            "s_and_b32 s70, s70, 7\n"
            "s_lshl_b32 s70, s70, 10\n"
            "s_mov_b32 s21, 0\n"
            "s_cmp_eq_u32 s71, 2\n"
            "s_cselect_b32 s21, 33554432, s21\n"
            "s_add_u32 s21, s21, s70\n"
            "s_cmp_eq_u32 s71, 0\n"
            "s_cselect_b32 s91, 8192, 0\n"
            "s_mul_i32 s22, s66, 6144\n"
            "s_mov_b32 s23, 0\n"
            "s_mov_b32 s24, 0\n"
            "s_mov_b32 s25, 0\n"
            "v_mov_b32 v32, 0\n"
            "v_mov_b32 v33, 0\n"
            "v_mov_b32 v34, 0\n"
            "v_mov_b32 v35, 0\n"
            "v_mov_b32 v36, 0\n"
            "v_mov_b32 v37, 0\n"
            "v_mov_b32 v38, 0\n"
            "v_mov_b32 v39, 0\n"
            "v_mov_b32 v40, 0\n"
            "v_mov_b32 v41, 0\n"
            "v_mov_b32 v42, 0\n"
            "v_mov_b32 v43, 0\n"
            "v_mov_b32 v44, 0\n"
            "v_mov_b32 v45, 0\n"
            "v_mov_b32 v46, 0\n"
            "v_mov_b32 v47, 0\n"
            "s_mov_b32 s75, 0\n"
            "s_mov_b32 s79, 32768\n"
            "s_cmp_eq_u32 s66, 0\n"
            "s_cbranch_scc0 XP0_N0_%=\n"
            "s_cmp_gt_u32 s84, s75\n"
            "s_cbranch_scc1 XP0_F0_SKIP_%=\n"
            "s_add_u32 s74, s75, 1\n"
            "s_mov_b32 s67, 0\n"
            "XP0_F0_W_%=:\n"
            "s_mov_b64 exec, 1\n"
            "global_load_dword v124, v114, s[82:83] sc1\n"
            "s_mov_b64 exec, -1\n"
            "s_waitcnt vmcnt(0)\n"
            "v_readfirstlane_b32 s70, v124\n"
            "s_nop 0\n"
            "s_cmp_ge_u32 s70, s74\n"
            "s_cbranch_scc1 XP0_F0_W_OK_%=\n"
            "s_add_u32 s67, s67, 1\n"
            "s_cmp_gt_u32 s67, 0x80000\n"
            "s_cbranch_scc1 XTIMEOUT_%=\n"
            "s_sleep 2\n"
            "s_branch XP0_F0_W_%=\n"
            "XP0_F0_W_OK_%=:\n"
            "s_mov_b32 s84, s70\n"
            "XP0_F0_SKIP_%=:\n"
            "XP0_N0_%=:\n"
            "s_cmp_eq_u32 s66, 1\n"
            "s_cbranch_scc0 XP0_N1_%=\n"
            "s_and_b32 s70, s75, 7\n"
            "s_cmp_eq_u32 s75, 4\n"
            "s_cselect_b32 s70, 0, s70\n"
            "s_cmp_eq_u32 s70, 0\n"
            "s_cbranch_scc0 XP0_F1_SKIP_%=\n"
            "s_lshr_b32 s70, s75, 3\n"
            "s_add_u32 s70, s70, 1\n"
            "s_cmp_eq_u32 s75, 0\n"
            "s_cselect_b32 s70, 0, s70\n"
            "s_add_u32 s70, s70, 18\n"
            "s_lshl_b32 s70, s70, 4\n"
            "s_lshr_b32 s71, s65, 1\n"
            "s_add_u32 s70, s70, s71\n"
            "s_lshl_b32 s70, s70, 7\n"
            "s_add_u32 s80, s56, s70\n"
            "s_addc_u32 s81, s57, 0\n"
            "s_mov_b32 s74, 2\n"
            "s_mov_b32 s67, 0\n"
            "XP0_F1_W_%=:\n"
            "s_mov_b64 exec, 1\n"
            "global_load_dword v124, v114, s[80:81] sc1\n"
            "s_mov_b64 exec, -1\n"
            "s_waitcnt vmcnt(0)\n"
            "v_readfirstlane_b32 s70, v124\n"
            "s_nop 0\n"
            "s_cmp_ge_u32 s70, s74\n"
            "s_cbranch_scc1 XP0_F1_W_OK_%=\n"
            "s_add_u32 s67, s67, 1\n"
            "s_cmp_gt_u32 s67, 0x80000\n"
            "s_cbranch_scc1 XTIMEOUT_%=\n"
            "s_sleep 2\n"
            "s_branch XP0_F1_W_%=\n"
            "XP0_F1_W_OK_%=:\n"
            "XP0_F1_SKIP_%=:\n"
            "XP0_N1_%=:\n"
            "s_barrier\n"
            "s_mov_b32 s75, 0\n"
            "s_lshr_b32 s70, s75, 2\n"
            "s_xor_b32 s70, s70, s27\n"
            "s_and_b32 s70, s70, 1\n"
            "s_lshl_b32 s70, s70, 13\n"
            "s_add_u32 s48, s46, s70\n"
            "s_addc_u32 s49, s47, 0\n"
            "s_mul_i32 s70, s75, 524288\n"
            "s_add_u32 s48, s48, s70\n"
            "s_addc_u32 s49, s49, 0\n"
            "s_add_u32 s72, s48, s16\n"
            "s_addc_u32 s73, s49, 0\n"
            "s_xor_b32 s72, s72, s86\n"
            "s_add_u32 m0, s79, s22\n"
            "s_add_u32 m0, m0, 0\n"
            "s_nop 0\n"
            "global_load_lds_dwordx4 v114, s[72:73] sc1\n"
            "s_add_u32 s72, s48, s17\n"
            "s_addc_u32 s73, s49, 0\n"
            "s_xor_b32 s72, s72, s87\n"
            "s_add_u32 m0, s79, s22\n"
            "s_add_u32 m0, m0, 1024\n"
            "s_nop 0\n"
            "global_load_lds_dwordx4 v114, s[72:73] sc1\n"
            "s_add_u32 s72, s48, s18\n"
            "s_addc_u32 s73, s49, 0\n"
            "s_xor_b32 s72, s72, s88\n"
            "s_add_u32 m0, s79, s22\n"
            "s_add_u32 m0, m0, 2048\n"
            "s_nop 0\n"
            "global_load_lds_dwordx4 v114, s[72:73] sc1\n"
            "s_add_u32 s72, s48, s19\n"
            "s_addc_u32 s73, s49, 0\n"
            "s_xor_b32 s72, s72, s89\n"
            "s_add_u32 m0, s79, s22\n"
            "s_add_u32 m0, m0, 3072\n"
            "s_nop 0\n"
            "global_load_lds_dwordx4 v114, s[72:73] sc1\n"
            "s_add_u32 s72, s48, s20\n"
            "s_addc_u32 s73, s49, 0\n"
            "s_xor_b32 s72, s72, s90\n"
            "s_add_u32 m0, s79, s22\n"
            "s_add_u32 m0, m0, 4096\n"
            "s_nop 0\n"
            "global_load_lds_dwordx4 v114, s[72:73] sc1\n"
            "s_add_u32 s72, s48, s21\n"
            "s_addc_u32 s73, s49, 0\n"
            "s_xor_b32 s72, s72, s91\n"
            "s_add_u32 m0, s79, s22\n"
            "s_add_u32 m0, m0, 5120\n"
            "s_nop 0\n"
            "global_load_lds_dwordx4 v114, s[72:73] sc1\n"
            "XLOOP_%=:\n"
            "s_add_u32 s75, s64, 1\n"
            "s_min_u32 s75, s75, 63\n"
            "s_cmp_eq_u32 s66, 0\n"
            "s_cbranch_scc0 XPN_N0_%=\n"
            "s_cmp_gt_u32 s84, s75\n"
            "s_cbranch_scc1 XPN_F0_SKIP_%=\n"
            "s_add_u32 s74, s75, 1\n"
            "s_mov_b32 s67, 0\n"
            "XPN_F0_W_%=:\n"
            "s_mov_b64 exec, 1\n"
            "global_load_dword v124, v114, s[82:83] sc1\n"
            "s_mov_b64 exec, -1\n"
            "s_waitcnt vmcnt(0)\n"
            "v_readfirstlane_b32 s70, v124\n"
            "s_nop 0\n"
            "s_cmp_ge_u32 s70, s74\n"
            "s_cbranch_scc1 XPN_F0_W_OK_%=\n"
            "s_add_u32 s67, s67, 1\n"
            "s_cmp_gt_u32 s67, 0x80000\n"
            "s_cbranch_scc1 XTIMEOUT_%=\n"
            "s_sleep 2\n"
            "s_branch XPN_F0_W_%=\n"
            "XPN_F0_W_OK_%=:\n"
            "s_mov_b32 s84, s70\n"
            "XPN_F0_SKIP_%=:\n"
            "XPN_N0_%=:\n"
            "s_cmp_eq_u32 s66, 1\n"
            "s_cbranch_scc0 XPN_N1_%=\n"
            "s_and_b32 s70, s75, 7\n"
            "s_cmp_eq_u32 s75, 4\n"
            "s_cselect_b32 s70, 0, s70\n"
            "s_cmp_eq_u32 s70, 0\n"
            "s_cbranch_scc0 XPN_F1_SKIP_%=\n"
            "s_lshr_b32 s70, s75, 3\n"
            "s_add_u32 s70, s70, 1\n"
            "s_cmp_eq_u32 s75, 0\n"
            "s_cselect_b32 s70, 0, s70\n"
            "s_add_u32 s70, s70, 18\n"
            "s_lshl_b32 s70, s70, 4\n"
            "s_lshr_b32 s71, s65, 1\n"
            "s_add_u32 s70, s70, s71\n"
            "s_lshl_b32 s70, s70, 7\n"
            "s_add_u32 s80, s56, s70\n"
            "s_addc_u32 s81, s57, 0\n"
            "s_mov_b32 s74, 2\n"
            "s_mov_b32 s67, 0\n"
            "XPN_F1_W_%=:\n"
            "s_mov_b64 exec, 1\n"
            "global_load_dword v124, v114, s[80:81] sc1\n"
            "s_mov_b64 exec, -1\n"
            "s_waitcnt vmcnt(0)\n"
            "v_readfirstlane_b32 s70, v124\n"
            "s_nop 0\n"
            "s_cmp_ge_u32 s70, s74\n"
            "s_cbranch_scc1 XPN_F1_W_OK_%=\n"
            "s_add_u32 s67, s67, 1\n"
            "s_cmp_gt_u32 s67, 0x80000\n"
            "s_cbranch_scc1 XTIMEOUT_%=\n"
            "s_sleep 2\n"
            "s_branch XPN_F1_W_%=\n"
            "XPN_F1_W_OK_%=:\n"
            "XPN_F1_SKIP_%=:\n"
            "XPN_N1_%=:\n"
            "s_cmp_eq_u32 s24, 0\n"
            "s_cbranch_scc1 XLATE_%=\n"
            "s_waitcnt vmcnt(4)\n"
            "s_sub_u32 s74, s64, 2\n"
            "s_branch XB1_%=\n"
            "XLATE_%=:\n"
            "s_cmp_eq_u32 s64, 0\n"
            "s_cbranch_scc1 XAV_%=\n"
            "s_cmp_eq_u32 s66, 2\n"
            "s_cbranch_scc0 XAV_%=\n"
            "s_add_u32 s74, s64, 1\n"
            "s_min_u32 s74, s74, 63\n"
            "s_mov_b32 s67, 0\n"
            "XPW_%=:\n"
            "s_mov_b64 exec, 1\n"
            "global_load_dword v124, v114, s[60:61] sc1\n"
            "s_mov_b64 exec, -1\n"
            "s_waitcnt vmcnt(0)\n"
            "v_readfirstlane_b32 s70, v124\n"
            "s_nop 0\n"
            "s_cmp_ge_u32 s70, s74\n"
            "s_cbranch_scc1 XPW_OK_%=\n"
            "s_add_u32 s67, s67, 1\n"
            "s_cmp_gt_u32 s67, 0x80000\n"
            "s_cbranch_scc1 XTIMEOUT_%=\n"
            "s_sleep 2\n"
            "s_branch XPW_%=\n"
            "XPW_OK_%=:\n"
            "s_load_dword s74, s[36:37], 84\n"
            "s_waitcnt lgkmcnt(0)\n"
            "XDL_%=:\n"
            "s_cmp_eq_u32 s74, 0\n"
            "s_cbranch_scc1 XAV_%=\n"
            "s_sleep 1\n"
            "s_sub_u32 s74, s74, 1\n"
            "s_branch XDL_%=\n"
            "XAV_%=:\n"
            "s_barrier\n"
            "s_lshl_b32 s70, s66, 11\n"
            "s_add_u32 m0, s70, 0\n"
            "s_nop 0\n"
            "global_load_lds_dwordx4 v113, s[52:53] sc1\n"
            "global_load_lds_dwordx4 v113, s[52:53] offset:1024 sc1\n"
            "s_waitcnt vmcnt(0)\n"
            "s_sub_u32 s74, s64, 1\n"
            "XB1_%=:\n"
            "s_waitcnt lgkmcnt(0)\n"
            "s_barrier\n"
            "s_max_i32 s74, s74, 0\n"
            "XSG_L_%=:\n"
            "s_cmp_lt_u32 s25, s74\n"
            "s_cbranch_scc0 XSG_E_%=\n"
            "s_cmp_eq_u32 s66, 0\n"
            "s_cbranch_scc0 XSG_S_%=\n"
            "s_lshl_b32 s70, s25, 7\n"
            "s_add_u32 s80, s58, s70\n"
            "s_addc_u32 s81, s59, 0\n"
            "v_mov_b32 v124, 0\n"
            "v_mov_b32 v125, 1\n"
            "s_mov_b64 exec, 1\n"
            "global_atomic_add v124, v125, s[80:81] sc1\n"
            "s_mov_b64 exec, -1\n"
            "XSG_S_%=:\n"
            "s_add_u32 s25, s25, 1\n"
            "s_branch XSG_L_%=\n"
            "XSG_E_%=:\n"
            "XAFTERB1_%=:\n"
            "ds_read_b128 v[0:3], v114 offset:0\n"
            "ds_read_b128 v[4:7], v114 offset:1024\n"
            "ds_read_b128 v[8:11], v114 offset:2048\n"
            "ds_read_b128 v[12:15], v114 offset:3072\n"
            "ds_read_b128 v[16:19], v114 offset:4096\n"
            "ds_read_b128 v[20:23], v114 offset:5120\n"
            "ds_read_b128 v[24:27], v114 offset:6144\n"
            "ds_read_b128 v[28:31], v114 offset:7168\n"
            "s_cmp_eq_u32 s66, 2\n"
            "s_cbranch_scc0 XNOPG_%=\n"
            "s_mov_b64 exec, 1\n"
            "global_load_dword v127, v114, s[60:61] sc1\n"
            "s_mov_b64 exec, -1\n"
            "XNOPG_%=:\n"
            "s_waitcnt lgkmcnt(7)\n"
            "v_smfmac_f32_16x16x64_bf16 v[32:35], v[0:3], a[0:7], v112\n"
            "v_smfmac_f32_16x16x64_bf16 v[40:43], v[0:3], a[64:71], v112\n"
            "s_waitcnt lgkmcnt(6)\n"
            "v_max_u32 v124, v1, v3\n"
            "v_smfmac_f32_16x16x64_bf16 v[32:35], v[4:7], a[8:15], v112\n"
            "ds_read_b128 v[52:55], v119 offset:8192\n"
            "v_smfmac_f32_16x16x64_bf16 v[40:43], v[4:7], a[72:79], v112\n"
            "s_waitcnt lgkmcnt(6)\n"
            "v_max3_u32 v124, v124, v5, v7\n"
            "v_smfmac_f32_16x16x64_bf16 v[32:35], v[8:11], a[16:23], v112\n"
            "ds_read_b128 v[56:59], v119 offset:9216\n"
            "v_smfmac_f32_16x16x64_bf16 v[40:43], v[8:11], a[80:87], v112\n"
            "s_waitcnt lgkmcnt(6)\n"
            "v_max3_u32 v124, v124, v9, v11\n"
            "v_smfmac_f32_16x16x64_bf16 v[32:35], v[12:15], a[24:31], v112\n"
            "ds_read_b128 v[60:63], v119 offset:10240\n"
            "v_smfmac_f32_16x16x64_bf16 v[40:43], v[12:15], a[88:95], v112\n"
            "s_waitcnt lgkmcnt(6)\n"
            "v_max3_u32 v124, v124, v13, v15\n"
            "v_smfmac_f32_16x16x64_bf16 v[32:35], v[16:19], a[32:39], v112\n"
            "ds_read_b128 v[64:67], v119 offset:11264\n"
            "v_smfmac_f32_16x16x64_bf16 v[40:43], v[16:19], a[96:103], v112\n"
            "s_waitcnt lgkmcnt(6)\n"
            "v_max3_u32 v124, v124, v17, v19\n"
            "v_smfmac_f32_16x16x64_bf16 v[32:35], v[20:23], a[40:47], v112\n"
            "ds_read_b128 v[68:71], v119 offset:12288\n"
            "v_smfmac_f32_16x16x64_bf16 v[40:43], v[20:23], a[104:111], v112\n"
            "s_waitcnt lgkmcnt(6)\n"
            "v_max3_u32 v124, v124, v21, v23\n"
            "v_smfmac_f32_16x16x64_bf16 v[32:35], v[24:27], a[48:55], v112\n"
            "ds_read_b128 v[72:75], v119 offset:13312\n"
            "v_smfmac_f32_16x16x64_bf16 v[40:43], v[24:27], a[112:119], v112\n"
            "s_waitcnt lgkmcnt(6)\n"
            "v_max3_u32 v124, v124, v25, v27\n"
            "v_smfmac_f32_16x16x64_bf16 v[32:35], v[28:31], a[56:63], v112\n"
            "ds_read_b128 v[76:79], v119 offset:14336\n"
            "v_smfmac_f32_16x16x64_bf16 v[40:43], v[28:31], a[120:127], v112\n"
            "v_max3_u32 v124, v124, v29, v31\n"
            "v_smfmac_f32_16x16x64_bf16 v[36:39], v[0:3], a[128:135], v112\n"
            "ds_read_b128 v[80:83], v119 offset:15360\n"
            "v_cmp_eq_u32 vcc, -1, v124\n"
            "v_smfmac_f32_16x16x64_bf16 v[44:47], v[0:3], a[192:199], v112\n"
            "v_smfmac_f32_16x16x64_bf16 v[36:39], v[4:7], a[136:143], v112\n"
            "v_smfmac_f32_16x16x64_bf16 v[44:47], v[4:7], a[200:207], v112\n"
            "s_cbranch_vccnz XRETRY_%=\n"
            "ds_read_b128 v[0:3], v119 offset:0\n"
            "ds_read_b128 v[4:7], v119 offset:1024\n"
            "v_smfmac_f32_16x16x64_bf16 v[36:39], v[8:11], a[144:151], v112\n"
            "v_add_f32 v32, v32, v33\n"
            "v_smfmac_f32_16x16x64_bf16 v[44:47], v[8:11], a[208:215], v112\n"
            "ds_read_b128 v[8:11], v119 offset:2048\n"
            "v_add_f32 v34, v34, v35\n"
            "v_smfmac_f32_16x16x64_bf16 v[36:39], v[12:15], a[152:159], v112\n"
            "v_cvt_pk_bf16_f32 v32, v32, v34\n"
            "v_smfmac_f32_16x16x64_bf16 v[44:47], v[12:15], a[216:223], v112\n"
            "ds_read_b128 v[12:15], v119 offset:3072\n"
            "ds_write_b32 v115, v32 offset:0\n"
            "v_smfmac_f32_16x16x64_bf16 v[36:39], v[16:19], a[160:167], v112\n"
            "v_add_f32 v40, v40, v41\n"
            "v_smfmac_f32_16x16x64_bf16 v[44:47], v[16:19], a[224:231], v112\n"
            "ds_read_b128 v[16:19], v119 offset:4096\n"
            "v_add_f32 v42, v42, v43\n"
            "v_smfmac_f32_16x16x64_bf16 v[36:39], v[20:23], a[168:175], v112\n"
            "v_cvt_pk_bf16_f32 v40, v40, v42\n"
            "v_smfmac_f32_16x16x64_bf16 v[44:47], v[20:23], a[232:239], v112\n"
            "ds_read_b128 v[20:23], v119 offset:5120\n"
            "ds_write_b32 v115, v40 offset:512\n"
            "v_smfmac_f32_16x16x64_bf16 v[36:39], v[24:27], a[176:183], v112\n"
            "v_smfmac_f32_16x16x64_bf16 v[44:47], v[24:27], a[240:247], v112\n"
            "ds_read_b128 v[24:27], v119 offset:6144\n"
            "v_smfmac_f32_16x16x64_bf16 v[36:39], v[28:31], a[184:191], v112\n"
            "v_smfmac_f32_16x16x64_bf16 v[44:47], v[28:31], a[248:255], v112\n"
            "ds_read_b128 v[28:31], v119 offset:7168\n"
            "s_cmp_eq_u32 s66, 2\n"
            "s_cbranch_scc0 XNOPG2_%=\n"
            "s_waitcnt vmcnt(0)\n"
            "v_mov_b32 v126, 20480\n"
            "s_mov_b64 exec, 1\n"
            "ds_write_b32 v126, v127\n"
            "s_mov_b64 exec, -1\n"
            "XNOPG2_%=:\n"
            "s_waitcnt lgkmcnt(0)\n"
            "s_barrier\n"
            "ds_read_b64_tr_b16 v[84:85], v116 offset:0\n"
            "ds_read_b64_tr_b16 v[86:87], v116 offset:512\n"
            "ds_read_b64_tr_b16 v[88:89], v116 offset:1024\n"
            "ds_read_b64_tr_b16 v[90:91], v116 offset:1536\n"
            "ds_read_b64_tr_b16 v[92:93], v116 offset:2048\n"
            "ds_read_b64_tr_b16 v[94:95], v116 offset:2560\n"
            "ds_read_b64_tr_b16 v[96:97], v116 offset:3072\n"
            "ds_read_b64_tr_b16 v[98:99], v116 offset:3584\n"
            "v_mov_b32 v126, 20480\n"
            "ds_read_b32 v127, v126\n"
            "v_mfma_f32_16x16x32_bf16 v[40:43], v[52:55], v[0:3], v[120:123]\n"
            "v_mfma_f32_16x16x32_bf16 v[40:43], v[56:59], v[4:7], v[40:43]\n"
            "v_mfma_f32_16x16x32_bf16 v[40:43], v[60:63], v[8:11], v[40:43]\n"
            "v_mfma_f32_16x16x32_bf16 v[40:43], v[64:67], v[12:15], v[40:43]\n"
            "v_add_f32 v36, v36, v37\n"
            "v_add_f32 v38, v38, v39\n"
            "v_cvt_pk_bf16_f32 v48, v36, v38\n"
            "v_mov_b32 v49, 0\n"
            "v_add_f32 v44, v44, v45\n"
            "v_add_f32 v46, v46, v47\n"
            "v_cvt_pk_bf16_f32 v50, v44, v46\n"
            "v_mov_b32 v51, 0\n"
            "v_mfma_f32_16x16x32_bf16 v[100:103], v[68:71], v[16:19], v[120:123]\n"
            "v_mfma_f32_16x16x32_bf16 v[100:103], v[72:75], v[20:23], v[100:103]\n"
            "v_mfma_f32_16x16x32_bf16 v[100:103], v[76:79], v[24:27], v[100:103]\n"
            "v_mfma_f32_16x16x32_bf16 v[100:103], v[80:83], v[28:31], v[100:103]\n"
            "s_waitcnt lgkmcnt(0)\n"
            "v_readfirstlane_b32 s23, v127\n"
            "v_mfma_f32_16x16x32_bf16 v[44:47], v[52:55], v[16:19], 0\n"
            "v_mfma_f32_16x16x32_bf16 v[44:47], v[56:59], v[20:23], v[44:47]\n"
            "v_mfma_f32_16x16x32_bf16 v[44:47], v[60:63], v[24:27], v[44:47]\n"
            "v_mfma_f32_16x16x32_bf16 v[44:47], v[64:67], v[28:31], v[44:47]\n"
            "v_mfma_f32_16x16x32_bf16 v[32:35], v[84:87], v[0:3], 0\n"
            "v_mfma_f32_16x16x32_bf16 v[32:35], v[88:91], v[4:7], v[32:35]\n"
            "v_mfma_f32_16x16x32_bf16 v[32:35], v[92:95], v[8:11], v[32:35]\n"
            "v_mfma_f32_16x16x32_bf16 v[32:35], v[96:99], v[12:15], v[32:35]\n"
            "ds_read_b128 v[0:3], v114 offset:12288\n"
            "ds_read_b128 v[4:7], v114 offset:13312\n"
            "ds_read_b128 v[8:11], v114 offset:14336\n"
            "ds_read_b128 v[12:15], v114 offset:15360\n"
            "v_mfma_f32_16x16x32_bf16 v[36:39], v[84:87], v[16:19], 0\n"
            "v_mfma_f32_16x16x32_bf16 v[36:39], v[88:91], v[20:23], v[36:39]\n"
            "v_mfma_f32_16x16x32_bf16 v[36:39], v[92:95], v[24:27], v[36:39]\n"
            "v_mfma_f32_16x16x32_bf16 v[36:39], v[96:99], v[28:31], v[36:39]\n"
            "ds_read_b128 v[16:19], v114 offset:16384\n"
            "ds_read_b128 v[20:23], v114 offset:17408\n"
            "ds_read_b128 v[24:27], v114 offset:18432\n"
            "ds_read_b128 v[28:31], v114 offset:19456\n"
            "s_add_u32 s52, s52, 16384\n"
            "s_addc_u32 s53, s53, 0\n"
            "s_add_u32 s70, s64, 2\n"
            "s_mov_b32 s24, 0\n"
            "s_cmp_ge_u32 s23, s70\n"
            "s_cbranch_scc0 XNOPRE_%=\n"
            "s_lshl_b32 s70, s66, 11\n"
            "s_add_u32 m0, s70, 0\n"
            "s_nop 0\n"
            "global_load_lds_dwordx4 v113, s[52:53] sc1\n"
            "global_load_lds_dwordx4 v113, s[52:53] offset:1024 sc1\n"
            "s_mov_b32 s24, 1\n"
            "XNOPRE_%=:\n"
            "ds_read_b64_tr_b16 v[76:77], v117 offset:0\n"
            "ds_read_b64_tr_b16 v[78:79], v117 offset:4096\n"
            "ds_read_b64_tr_b16 v[80:81], v117 offset:8\n"
            "ds_read_b64_tr_b16 v[82:83], v117 offset:4104\n"
            "s_add_u32 s75, s64, 1\n"
            "s_min_u32 s75, s75, 63\n"
            "s_lshr_b32 s70, s75, 2\n"
            "s_xor_b32 s70, s70, s27\n"
            "s_and_b32 s70, s70, 1\n"
            "s_lshl_b32 s70, s70, 13\n"
            "s_add_u32 s48, s46, s70\n"
            "s_addc_u32 s49, s47, 0\n"
            "s_mul_i32 s70, s75, 524288\n"
            "s_add_u32 s48, s48, s70\n"
            "s_addc_u32 s49, s49, 0\n"
            "s_sub_u32 s75, s64, 1\n"
            "s_max_i32 s75, s75, 0\n"
            "s_mov_b32 s70, s75\n"
            "s_xor_b32 s70, s70, s26\n"
            "s_and_b32 s70, s70, 1\n"
            "s_lshl_b32 s70, s70, 13\n"
            "s_add_u32 s54, s50, s70\n"
            "s_addc_u32 s55, s51, 0\n"
            "s_mul_i32 s70, s75, 2097152\n"
            "s_add_u32 s54, s54, s70\n"
            "s_addc_u32 s55, s55, 0\n"
            "s_xor_b32 s79, s69, 98304\n"
            "s_cmp_eq_u32 s64, 0\n"
            "s_cbranch_scc1 XNOWO_%=\n"
            "s_waitcnt lgkmcnt(0)\n"
            "s_add_u32 s76, s54, 4096\n"
            "s_addc_u32 s77, s55, 0\n"
            "v_mfma_f32_16x16x32_bf16 v[52:55], v[128:131], v[0:3], 0\n"
            "s_add_u32 s72, s48, s16\n"
            "s_addc_u32 s73, s49, 0\n"
            "s_xor_b32 s72, s72, s86\n"
            "s_add_u32 m0, s79, s22\n"
            "s_add_u32 m0, m0, 0\n"
            "s_nop 0\n"
            "global_load_lds_dwordx4 v114, s[72:73] sc1\n"
            "v_max3_f32 v124, v32, v33, v40\n"
            "v_max3_f32 v126, v44, v45, v46\n"
            "v_mfma_f32_16x16x32_bf16 v[56:59], v[128:131], v[16:19], 0\n"
            "v_max3_f32 v124, v124, v41, v42\n"
            "v_max3_f32 v126, v126, v47, v100\n"
            "v_mfma_f32_16x16x32_bf16 v[52:55], v[132:135], v[4:7], v[52:55]\n"
            "v_max_f32 v124, v124, v43\n"
            "v_max3_f32 v126, v126, v101, v102\n"
            "v_mfma_f32_16x16x32_bf16 v[56:59], v[132:135], v[20:23], v[56:59]\n"
            "v_mov_b32 v125, v124\n"
            "v_max3_f32 v126, v126, v103, v36\n"
            "v_mfma_f32_16x16x32_bf16 v[52:55], v[136:139], v[8:11], v[52:55]\n"
            "v_max_f32 v126, v126, v37\n"
            "v_permlane32_swap_b32 v124, v125\n"
            "v_mfma_f32_16x16x32_bf16 v[56:59], v[136:139], v[24:27], v[56:59]\n"
            "v_mov_b32 v127, v126\n"
            "v_max_f32 v124, v124, v125\n"
            "v_mfma_f32_16x16x32_bf16 v[52:55], v[140:143], v[12:15], v[52:55]\n"
            "v_mov_b32 v125, v124\n"
            "v_permlane32_swap_b32 v126, v127\n"
            "v_mfma_f32_16x16x32_bf16 v[56:59], v[140:143], v[28:31], v[56:59]\n"
            "v_max_f32 v126, v126, v127\n"
            "v_permlane16_swap_b32 v124, v125\n"
            "v_mfma_f32_16x16x32_bf16 v[60:63], v[144:147], v[0:3], 0\n"
            "s_add_u32 s72, s48, s17\n"
            "s_addc_u32 s73, s49, 0\n"
            "s_xor_b32 s72, s72, s87\n"
            "s_add_u32 m0, s79, s22\n"
            "s_add_u32 m0, m0, 1024\n"
            "s_nop 0\n"
            "global_load_lds_dwordx4 v114, s[72:73] sc1\n"
            "v_mov_b32 v127, v126\n"
            "v_max_f32 v124, v124, v125\n"
            "v_mfma_f32_16x16x32_bf16 v[64:67], v[144:147], v[16:19], 0\n"
            "v_mul_f32 v124, s68, v124\n"
            "v_permlane16_swap_b32 v126, v127\n"
            "v_mfma_f32_16x16x32_bf16 v[60:63], v[148:151], v[4:7], v[60:63]\n"
            "v_max_f32 v126, v126, v127\n"
            "v_mul_f32 v126, s68, v126\n"
            "v_mfma_f32_16x16x32_bf16 v[64:67], v[148:151], v[20:23], v[64:67]\n"
            "v_cvt_pk_bf16_f32 v68, v52, v53\n"
            "v_cvt_pk_bf16_f32 v69, v54, v55\n"
            "v_mfma_f32_16x16x32_bf16 v[60:63], v[152:155], v[8:11], v[60:63]\n"
            "v_cvt_pk_bf16_f32 v70, v56, v57\n"
            "v_cvt_pk_bf16_f32 v71, v58, v59\n"
            "v_mfma_f32_16x16x32_bf16 v[64:67], v[152:155], v[24:27], v[64:67]\n"
            "global_store_dwordx4 v118, v[68:71], s[54:55] offset:0 sc1\n"
            "v_fma_f32 v32, v32, s68, -v124\n"
            "v_fma_f32 v33, v33, s68, -v124\n"
            "v_mfma_f32_16x16x32_bf16 v[60:63], v[156:159], v[12:15], v[60:63]\n"
            "v_fma_f32 v40, v40, s68, -v124\n"
            "v_fma_f32 v41, v41, s68, -v124\n"
            "v_mfma_f32_16x16x32_bf16 v[64:67], v[156:159], v[28:31], v[64:67]\n"
            "v_fma_f32 v42, v42, s68, -v124\n"
            "v_fma_f32 v43, v43, s68, -v124\n"
            "v_mfma_f32_16x16x32_bf16 v[52:55], v[160:163], v[0:3], 0\n"
            "s_add_u32 s72, s48, s18\n"
            "s_addc_u32 s73, s49, 0\n"
            "s_xor_b32 s72, s72, s88\n"
            "s_add_u32 m0, s79, s22\n"
            "s_add_u32 m0, m0, 2048\n"
            "s_nop 0\n"
            "global_load_lds_dwordx4 v114, s[72:73] sc1\n"
            "v_fma_f32 v36, v36, s68, -v126\n"
            "v_fma_f32 v37, v37, s68, -v126\n"
            "v_mfma_f32_16x16x32_bf16 v[56:59], v[160:163], v[16:19], 0\n"
            "v_fma_f32 v44, v44, s68, -v126\n"
            "v_fma_f32 v45, v45, s68, -v126\n"
            "v_mfma_f32_16x16x32_bf16 v[52:55], v[164:167], v[4:7], v[52:55]\n"
            "v_fma_f32 v46, v46, s68, -v126\n"
            "v_fma_f32 v47, v47, s68, -v126\n"
            "v_mfma_f32_16x16x32_bf16 v[56:59], v[164:167], v[20:23], v[56:59]\n"
            "v_cvt_pk_bf16_f32 v72, v60, v61\n"
            "v_cvt_pk_bf16_f32 v73, v62, v63\n"
            "v_mfma_f32_16x16x32_bf16 v[52:55], v[168:171], v[8:11], v[52:55]\n"
            "v_cvt_pk_bf16_f32 v74, v64, v65\n"
            "v_cvt_pk_bf16_f32 v75, v66, v67\n"
            "v_mfma_f32_16x16x32_bf16 v[56:59], v[168:171], v[24:27], v[56:59]\n"
            "global_store_dwordx4 v118, v[72:75], s[54:55] offset:1024 sc1\n"
            "v_fma_f32 v100, v100, s68, -v126\n"
            "v_fma_f32 v101, v101, s68, -v126\n"
            "v_mfma_f32_16x16x32_bf16 v[52:55], v[172:175], v[12:15], v[52:55]\n"
            "v_fma_f32 v102, v102, s68, -v126\n"
            "v_fma_f32 v103, v103, s68, -v126\n"
            "v_mfma_f32_16x16x32_bf16 v[56:59], v[172:175], v[28:31], v[56:59]\n"
            "v_exp_f32 v32, v32\n"
            "v_mfma_f32_16x16x32_bf16 v[60:63], v[176:179], v[0:3], 0\n"
            "s_add_u32 s72, s48, s19\n"
            "s_addc_u32 s73, s49, 0\n"
            "s_xor_b32 s72, s72, s89\n"
            "s_add_u32 m0, s79, s22\n"
            "s_add_u32 m0, m0, 3072\n"
            "s_nop 0\n"
            "global_load_lds_dwordx4 v114, s[72:73] sc1\n"
            "v_exp_f32 v33, v33\n"
            "v_mfma_f32_16x16x32_bf16 v[64:67], v[176:179], v[16:19], 0\n"
            "v_exp_f32 v40, v40\n"
            "v_mfma_f32_16x16x32_bf16 v[60:63], v[180:183], v[4:7], v[60:63]\n"
            "v_exp_f32 v41, v41\n"
            "v_mfma_f32_16x16x32_bf16 v[64:67], v[180:183], v[20:23], v[64:67]\n"
            "v_cvt_pk_bf16_f32 v68, v52, v53\n"
            "v_cvt_pk_bf16_f32 v69, v54, v55\n"
            "v_mfma_f32_16x16x32_bf16 v[60:63], v[184:187], v[8:11], v[60:63]\n"
            "v_cvt_pk_bf16_f32 v70, v56, v57\n"
            "v_cvt_pk_bf16_f32 v71, v58, v59\n"
            "v_mfma_f32_16x16x32_bf16 v[64:67], v[184:187], v[24:27], v[64:67]\n"
            "global_store_dwordx4 v118, v[68:71], s[54:55] offset:2048 sc1\n"
            "v_exp_f32 v42, v42\n"
            "v_mfma_f32_16x16x32_bf16 v[60:63], v[188:191], v[12:15], v[60:63]\n"
            "v_exp_f32 v43, v43\n"
            "v_mfma_f32_16x16x32_bf16 v[64:67], v[188:191], v[28:31], v[64:67]\n"
            "v_exp_f32 v36, v36\n"
            "v_mfma_f32_16x16x32_bf16 v[52:55], v[192:195], v[0:3], 0\n"
            "s_add_u32 s72, s48, s20\n"
            "s_addc_u32 s73, s49, 0\n"
            "s_xor_b32 s72, s72, s90\n"
            "s_add_u32 m0, s79, s22\n"
            "s_add_u32 m0, m0, 4096\n"
            "s_nop 0\n"
            "global_load_lds_dwordx4 v114, s[72:73] sc1\n"
            "v_exp_f32 v37, v37\n"
            "v_mfma_f32_16x16x32_bf16 v[56:59], v[192:195], v[16:19], 0\n"
            "v_exp_f32 v44, v44\n"
            "v_mfma_f32_16x16x32_bf16 v[52:55], v[196:199], v[4:7], v[52:55]\n"
            "v_exp_f32 v45, v45\n"
            "v_mfma_f32_16x16x32_bf16 v[56:59], v[196:199], v[20:23], v[56:59]\n"
            "v_cvt_pk_bf16_f32 v72, v60, v61\n"
            "v_cvt_pk_bf16_f32 v73, v62, v63\n"
            "v_mfma_f32_16x16x32_bf16 v[52:55], v[200:203], v[8:11], v[52:55]\n"
            "v_cvt_pk_bf16_f32 v74, v64, v65\n"
            "v_cvt_pk_bf16_f32 v75, v66, v67\n"
            "v_mfma_f32_16x16x32_bf16 v[56:59], v[200:203], v[24:27], v[56:59]\n"
            "global_store_dwordx4 v118, v[72:75], s[54:55] offset:3072 sc1\n"
            "v_exp_f32 v46, v46\n"
            "v_mfma_f32_16x16x32_bf16 v[52:55], v[204:207], v[12:15], v[52:55]\n"
            "v_exp_f32 v47, v47\n"
            "v_mfma_f32_16x16x32_bf16 v[56:59], v[204:207], v[28:31], v[56:59]\n"
            "v_exp_f32 v100, v100\n"
            "v_mfma_f32_16x16x32_bf16 v[60:63], v[208:211], v[0:3], 0\n"
            "s_add_u32 s72, s48, s21\n"
            "s_addc_u32 s73, s49, 0\n"
            "s_xor_b32 s72, s72, s91\n"
            "s_add_u32 m0, s79, s22\n"
            "s_add_u32 m0, m0, 5120\n"
            "s_nop 0\n"
            "global_load_lds_dwordx4 v114, s[72:73] sc1\n"
            "v_exp_f32 v101, v101\n"
            "v_mfma_f32_16x16x32_bf16 v[64:67], v[208:211], v[16:19], 0\n"
            "v_exp_f32 v102, v102\n"
            "v_mfma_f32_16x16x32_bf16 v[60:63], v[212:215], v[4:7], v[60:63]\n"
            "v_exp_f32 v103, v103\n"
            "v_mfma_f32_16x16x32_bf16 v[64:67], v[212:215], v[20:23], v[64:67]\n"
            "v_cvt_pk_bf16_f32 v68, v52, v53\n"
            "v_cvt_pk_bf16_f32 v69, v54, v55\n"
            "v_mfma_f32_16x16x32_bf16 v[60:63], v[216:219], v[8:11], v[60:63]\n"
            "v_cvt_pk_bf16_f32 v70, v56, v57\n"
            "v_cvt_pk_bf16_f32 v71, v58, v59\n"
            "v_mfma_f32_16x16x32_bf16 v[64:67], v[216:219], v[24:27], v[64:67]\n"
            "global_store_dwordx4 v118, v[68:71], s[76:77] offset:0 sc1\n"
            "v_cvt_pk_bf16_f32 v84, v40, v41\n"
            "v_cvt_pk_bf16_f32 v85, v42, v43\n"
            "v_mfma_f32_16x16x32_bf16 v[60:63], v[220:223], v[12:15], v[60:63]\n"
            "v_mov_b32 v86, 0\n"
            "v_mov_b32 v87, 0\n"
            "v_mfma_f32_16x16x32_bf16 v[64:67], v[220:223], v[28:31], v[64:67]\n"
            "v_cvt_pk_bf16_f32 v92, v32, v33\n"
            "v_mov_b32 v93, 0\n"
            "v_mfma_f32_16x16x32_bf16 v[52:55], v[224:227], v[0:3], 0\n"
            "v_cvt_pk_bf16_f32 v88, v44, v45\n"
            "v_cvt_pk_bf16_f32 v89, v46, v47\n"
            "v_mfma_f32_16x16x32_bf16 v[56:59], v[224:227], v[16:19], 0\n"
            "v_cvt_pk_bf16_f32 v90, v100, v101\n"
            "v_cvt_pk_bf16_f32 v91, v102, v103\n"
            "v_mfma_f32_16x16x32_bf16 v[52:55], v[228:231], v[4:7], v[52:55]\n"
            "v_cvt_pk_bf16_f32 v94, v36, v37\n"
            "v_mov_b32 v95, 0\n"
            "v_mfma_f32_16x16x32_bf16 v[56:59], v[228:231], v[20:23], v[56:59]\n"
            "v_cvt_pk_bf16_f32 v72, v60, v61\n"
            "v_cvt_pk_bf16_f32 v73, v62, v63\n"
            "v_mfma_f32_16x16x32_bf16 v[52:55], v[232:235], v[8:11], v[52:55]\n"
            "v_cvt_pk_bf16_f32 v74, v64, v65\n"
            "v_cvt_pk_bf16_f32 v75, v66, v67\n"
            "v_mfma_f32_16x16x32_bf16 v[56:59], v[232:235], v[24:27], v[56:59]\n"
            "global_store_dwordx4 v118, v[72:75], s[76:77] offset:1024 sc1\n"
            "v_mov_b32 v96, 0x3f803f80\n"
            "v_mov_b32 v97, 0x3f803f80\n"
            "v_mfma_f32_16x16x32_bf16 v[52:55], v[236:239], v[12:15], v[52:55]\n"
            "v_mov_b32 v98, 0x3f803f80\n"
            "v_mov_b32 v99, 0x3f803f80\n"
            "v_mfma_f32_16x16x32_bf16 v[56:59], v[236:239], v[28:31], v[56:59]\n"
            "v_mfma_f32_16x16x32_bf16 v[60:63], v[240:243], v[0:3], 0\n"
            "v_mfma_f32_16x16x32_bf16 v[64:67], v[240:243], v[16:19], 0\n"
            "v_mfma_f32_16x16x32_bf16 v[60:63], v[244:247], v[4:7], v[60:63]\n"
            "v_mfma_f32_16x16x32_bf16 v[64:67], v[244:247], v[20:23], v[64:67]\n"
            "s_nop 0\n"
            "v_cvt_pk_bf16_f32 v68, v52, v53\n"
            "v_cvt_pk_bf16_f32 v69, v54, v55\n"
            "v_mfma_f32_16x16x32_bf16 v[60:63], v[248:251], v[8:11], v[60:63]\n"
            "v_cvt_pk_bf16_f32 v70, v56, v57\n"
            "v_cvt_pk_bf16_f32 v71, v58, v59\n"
            "v_mfma_f32_16x16x32_bf16 v[64:67], v[248:251], v[24:27], v[64:67]\n"
            "global_store_dwordx4 v118, v[68:71], s[76:77] offset:2048 sc1\n"
            "v_mfma_f32_16x16x32_bf16 v[60:63], v[252:255], v[12:15], v[60:63]\n"
            "v_mfma_f32_16x16x32_bf16 v[64:67], v[252:255], v[28:31], v[64:67]\n"
            "s_nop 6\n"
            "v_cvt_pk_bf16_f32 v72, v60, v61\n"
            "v_cvt_pk_bf16_f32 v73, v62, v63\n"
            "v_cvt_pk_bf16_f32 v74, v64, v65\n"
            "v_cvt_pk_bf16_f32 v75, v66, v67\n"
            "global_store_dwordx4 v118, v[72:75], s[76:77] offset:3072 sc1\n"
            "s_branch XTAIL_%=\n"
            "XNOWO_%=:\n"
            "s_add_u32 s72, s48, s16\n"
            "s_addc_u32 s73, s49, 0\n"
            "s_xor_b32 s72, s72, s86\n"
            "s_add_u32 m0, s79, s22\n"
            "s_add_u32 m0, m0, 0\n"
            "s_nop 0\n"
            "global_load_lds_dwordx4 v114, s[72:73] sc1\n"
            "s_add_u32 s72, s48, s17\n"
            "s_addc_u32 s73, s49, 0\n"
            "s_xor_b32 s72, s72, s87\n"
            "s_add_u32 m0, s79, s22\n"
            "s_add_u32 m0, m0, 1024\n"
            "s_nop 0\n"
            "global_load_lds_dwordx4 v114, s[72:73] sc1\n"
            "s_add_u32 s72, s48, s18\n"
            "s_addc_u32 s73, s49, 0\n"
            "s_xor_b32 s72, s72, s88\n"
            "s_add_u32 m0, s79, s22\n"
            "s_add_u32 m0, m0, 2048\n"
            "s_nop 0\n"
            "global_load_lds_dwordx4 v114, s[72:73] sc1\n"
            "s_add_u32 s72, s48, s19\n"
            "s_addc_u32 s73, s49, 0\n"
            "s_xor_b32 s72, s72, s89\n"
            "s_add_u32 m0, s79, s22\n"
            "s_add_u32 m0, m0, 3072\n"
            "s_nop 0\n"
            "global_load_lds_dwordx4 v114, s[72:73] sc1\n"
            "s_add_u32 s72, s48, s20\n"
            "s_addc_u32 s73, s49, 0\n"
            "s_xor_b32 s72, s72, s90\n"
            "s_add_u32 m0, s79, s22\n"
            "s_add_u32 m0, m0, 4096\n"
            "s_nop 0\n"
            "global_load_lds_dwordx4 v114, s[72:73] sc1\n"
            "s_add_u32 s72, s48, s21\n"
            "s_addc_u32 s73, s49, 0\n"
            "s_xor_b32 s72, s72, s91\n"
            "s_add_u32 m0, s79, s22\n"
            "s_add_u32 m0, m0, 5120\n"
            "s_nop 0\n"
            "global_load_lds_dwordx4 v114, s[72:73] sc1\n"
            "v_max3_f32 v124, v32, v33, v40\n"
            "v_max3_f32 v126, v44, v45, v46\n"
            "v_max3_f32 v124, v124, v41, v42\n"
            "v_max3_f32 v126, v126, v47, v100\n"
            "v_max_f32 v124, v124, v43\n"
            "v_max3_f32 v126, v126, v101, v102\n"
            "v_mov_b32 v125, v124\n"
            "v_max3_f32 v126, v126, v103, v36\n"
            "v_max_f32 v126, v126, v37\n"
            "v_permlane32_swap_b32 v124, v125\n"
            "v_mov_b32 v127, v126\n"
            "v_max_f32 v124, v124, v125\n"
            "v_mov_b32 v125, v124\n"
            "v_permlane32_swap_b32 v126, v127\n"
            "v_max_f32 v126, v126, v127\n"
            "v_permlane16_swap_b32 v124, v125\n"
            "v_mov_b32 v127, v126\n"
            "v_max_f32 v124, v124, v125\n"
            "v_mul_f32 v124, s68, v124\n"
            "v_permlane16_swap_b32 v126, v127\n"
            "v_max_f32 v126, v126, v127\n"
            "v_mul_f32 v126, s68, v126\n"
            "v_fma_f32 v32, v32, s68, -v124\n"
            "v_fma_f32 v33, v33, s68, -v124\n"
            "v_fma_f32 v40, v40, s68, -v124\n"
            "v_fma_f32 v41, v41, s68, -v124\n"
            "v_fma_f32 v42, v42, s68, -v124\n"
            "v_fma_f32 v43, v43, s68, -v124\n"
            "v_fma_f32 v36, v36, s68, -v126\n"
            "v_fma_f32 v37, v37, s68, -v126\n"
            "v_fma_f32 v44, v44, s68, -v126\n"
            "v_fma_f32 v45, v45, s68, -v126\n"
            "v_fma_f32 v46, v46, s68, -v126\n"
            "v_fma_f32 v47, v47, s68, -v126\n"
            "v_fma_f32 v100, v100, s68, -v126\n"
            "v_fma_f32 v101, v101, s68, -v126\n"
            "v_fma_f32 v102, v102, s68, -v126\n"
            "v_fma_f32 v103, v103, s68, -v126\n"
            "v_exp_f32 v32, v32\n"
            "v_exp_f32 v33, v33\n"
            "v_exp_f32 v40, v40\n"
            "v_exp_f32 v41, v41\n"
            "v_exp_f32 v42, v42\n"
            "v_exp_f32 v43, v43\n"
            "v_exp_f32 v36, v36\n"
            "v_exp_f32 v37, v37\n"
            "v_exp_f32 v44, v44\n"
            "v_exp_f32 v45, v45\n"
            "v_exp_f32 v46, v46\n"
            "v_exp_f32 v47, v47\n"
            "v_exp_f32 v100, v100\n"
            "v_exp_f32 v101, v101\n"
            "v_exp_f32 v102, v102\n"
            "v_exp_f32 v103, v103\n"
            "v_cvt_pk_bf16_f32 v84, v40, v41\n"
            "v_cvt_pk_bf16_f32 v85, v42, v43\n"
            "v_mov_b32 v86, 0\n"
            "v_mov_b32 v87, 0\n"
            "v_cvt_pk_bf16_f32 v92, v32, v33\n"
            "v_mov_b32 v93, 0\n"
            "v_cvt_pk_bf16_f32 v88, v44, v45\n"
            "v_cvt_pk_bf16_f32 v89, v46, v47\n"
            "v_cvt_pk_bf16_f32 v90, v100, v101\n"
            "v_cvt_pk_bf16_f32 v91, v102, v103\n"
            "v_cvt_pk_bf16_f32 v94, v36, v37\n"
            "v_mov_b32 v95, 0\n"
            "v_mov_b32 v96, 0x3f803f80\n"
            "v_mov_b32 v97, 0x3f803f80\n"
            "v_mov_b32 v98, 0x3f803f80\n"
            "v_mov_b32 v99, 0x3f803f80\n"
            "XTAIL_%=:\n"
            "s_waitcnt lgkmcnt(0)\n"
            "s_nop 0\n"
            "v_mfma_f32_16x16x32_bf16 v[16:19], v[96:99], v[84:87], 0\n"
            "v_mov_b32 v32, 0\n"
            "v_mov_b32 v33, 0\n"
            "v_mfma_f32_16x16x32_bf16 v[20:23], v[96:99], v[88:91], 0\n"
            "v_mov_b32 v34, 0\n"
            "v_mov_b32 v35, 0\n"
            "v_mfma_f32_16x16x16_bf16 v[16:19], v[96:97], v[92:93], v[16:19]\n"
            "v_mov_b32 v36, 0\n"
            "v_mov_b32 v37, 0\n"
            "v_mfma_f32_16x16x16_bf16 v[20:23], v[96:97], v[94:95], v[20:23]\n"
            "v_mov_b32 v38, 0\n"
            "v_mov_b32 v39, 0\n"
            "v_mfma_f32_16x16x32_bf16 v[0:3], v[76:79], v[84:87], 0\n"
            "v_mov_b32 v40, 0\n"
            "v_mov_b32 v41, 0\n"
            "v_mfma_f32_16x16x32_bf16 v[4:7], v[76:79], v[88:91], 0\n"
            "v_mov_b32 v42, 0\n"
            "v_mov_b32 v43, 0\n"
            "v_mfma_f32_16x16x32_bf16 v[8:11], v[80:83], v[84:87], 0\n"
            "v_mov_b32 v44, 0\n"
            "v_mov_b32 v45, 0\n"
            "v_mfma_f32_16x16x32_bf16 v[12:15], v[80:83], v[88:91], 0\n"
            "v_mov_b32 v46, 0\n"
            "v_mov_b32 v47, 0\n"
            "s_lshl_b32 s70, s66, 10\n"
            "v_add_u32 v126, s70, v114\n"
            "v_mfma_f32_16x16x16_bf16 v[0:3], v[48:49], v[92:93], v[0:3]\n"
            "v_mfma_f32_16x16x16_bf16 v[4:7], v[48:49], v[94:95], v[4:7]\n"
            "v_mfma_f32_16x16x16_bf16 v[8:11], v[50:51], v[92:93], v[8:11]\n"
            "v_mfma_f32_16x16x16_bf16 v[12:15], v[50:51], v[94:95], v[12:15]\n"
            "v_rcp_f32 v124, v16\n"
            "v_rcp_f32 v125, v20\n"
            "s_nop 2\n"
            "v_mul_f32 v0, v0, v124\n"
            "v_mul_f32 v1, v1, v124\n"
            "v_mul_f32 v2, v2, v124\n"
            "v_mul_f32 v3, v3, v124\n"
            "v_mul_f32 v8, v8, v124\n"
            "v_mul_f32 v9, v9, v124\n"
            "v_mul_f32 v10, v10, v124\n"
            "v_mul_f32 v11, v11, v124\n"
            "v_cvt_pk_bf16_f32 v24, v0, v1\n"
            "v_cvt_pk_bf16_f32 v25, v2, v3\n"
            "v_cvt_pk_bf16_f32 v26, v8, v9\n"
            "v_cvt_pk_bf16_f32 v27, v10, v11\n"
            "ds_write_b128 v126, v[24:27] offset:12288\n"
            "v_mul_f32 v4, v4, v125\n"
            "v_mul_f32 v5, v5, v125\n"
            "v_mul_f32 v6, v6, v125\n"
            "v_mul_f32 v7, v7, v125\n"
            "v_mul_f32 v12, v12, v125\n"
            "v_mul_f32 v13, v13, v125\n"
            "v_mul_f32 v14, v14, v125\n"
            "v_mul_f32 v15, v15, v125\n"
            "v_cvt_pk_bf16_f32 v28, v4, v5\n"
            "v_cvt_pk_bf16_f32 v29, v6, v7\n"
            "v_cvt_pk_bf16_f32 v30, v12, v13\n"
            "v_cvt_pk_bf16_f32 v31, v14, v15\n"
            "ds_write_b128 v126, v[28:31] offset:16384\n"
            "s_xor_b32 s69, s69, 98304\n"
            "v_xor_b32 v119, 98304, v119\n"
            "v_xor_b32 v117, 98304, v117\n"
            "s_add_u32 s64, s64, 1\n"
            "s_cmp_lt_u32 s64, 64\n"
            "s_cbranch_scc1 XLOOP_%=\n"
            "XLAST_%=:\n"
            "s_waitcnt lgkmcnt(0)\n"
            "s_barrier\n"
            "ds_read_b128 v[0:3], v114 offset:12288\n"
            "ds_read_b128 v[4:7], v114 offset:13312\n"
            "ds_read_b128 v[8:11], v114 offset:14336\n"
            "ds_read_b128 v[12:15], v114 offset:15360\n"
            "ds_read_b128 v[16:19], v114 offset:16384\n"
            "ds_read_b128 v[20:23], v114 offset:17408\n"
            "ds_read_b128 v[24:27], v114 offset:18432\n"
            "ds_read_b128 v[28:31], v114 offset:19456\n"
            "s_waitcnt vmcnt(0) lgkmcnt(0)\n"
            "s_barrier\n"
            "s_mov_b32 s74, 63\n"
            "XSGA_L_%=:\n"
            "s_cmp_lt_u32 s25, s74\n"
            "s_cbranch_scc0 XSGA_E_%=\n"
            "s_cmp_eq_u32 s66, 0\n"
            "s_cbranch_scc0 XSGA_S_%=\n"
            "s_lshl_b32 s70, s25, 7\n"
            "s_add_u32 s80, s58, s70\n"
            "s_addc_u32 s81, s59, 0\n"
            "v_mov_b32 v124, 0\n"
            "v_mov_b32 v125, 1\n"
            "s_mov_b64 exec, 1\n"
            "global_atomic_add v124, v125, s[80:81] sc1\n"
            "s_mov_b64 exec, -1\n"
            "XSGA_S_%=:\n"
            "s_add_u32 s25, s25, 1\n"
            "s_branch XSGA_L_%=\n"
            "XSGA_E_%=:\n"
            "s_mov_b32 s75, 63\n"
            "s_mov_b32 s70, s75\n"
            "s_xor_b32 s70, s70, s26\n"
            "s_and_b32 s70, s70, 1\n"
            "s_lshl_b32 s70, s70, 13\n"
            "s_add_u32 s54, s50, s70\n"
            "s_addc_u32 s55, s51, 0\n"
            "s_mul_i32 s70, s75, 2097152\n"
            "s_add_u32 s54, s54, s70\n"
            "s_addc_u32 s55, s55, 0\n"
            "s_add_u32 s76, s54, 4096\n"
            "s_addc_u32 s77, s55, 0\n"
            "v_mfma_f32_16x16x32_bf16 v[52:55], v[128:131], v[0:3], 0\n"
            "v_mfma_f32_16x16x32_bf16 v[56:59], v[128:131], v[16:19], 0\n"
            "v_mfma_f32_16x16x32_bf16 v[52:55], v[132:135], v[4:7], v[52:55]\n"
            "v_mfma_f32_16x16x32_bf16 v[56:59], v[132:135], v[20:23], v[56:59]\n"
            "v_mfma_f32_16x16x32_bf16 v[52:55], v[136:139], v[8:11], v[52:55]\n"
            "v_mfma_f32_16x16x32_bf16 v[56:59], v[136:139], v[24:27], v[56:59]\n"
            "v_mfma_f32_16x16x32_bf16 v[52:55], v[140:143], v[12:15], v[52:55]\n"
            "v_mfma_f32_16x16x32_bf16 v[56:59], v[140:143], v[28:31], v[56:59]\n"
            "v_mfma_f32_16x16x32_bf16 v[60:63], v[144:147], v[0:3], 0\n"
            "v_mfma_f32_16x16x32_bf16 v[64:67], v[144:147], v[16:19], 0\n"
            "v_mfma_f32_16x16x32_bf16 v[60:63], v[148:151], v[4:7], v[60:63]\n"
            "v_mfma_f32_16x16x32_bf16 v[64:67], v[148:151], v[20:23], v[64:67]\n"
            "s_nop 2\n"
            "v_cvt_pk_bf16_f32 v68, v52, v53\n"
            "v_cvt_pk_bf16_f32 v69, v54, v55\n"
            "v_mfma_f32_16x16x32_bf16 v[60:63], v[152:155], v[8:11], v[60:63]\n"
            "v_cvt_pk_bf16_f32 v70, v56, v57\n"
            "v_cvt_pk_bf16_f32 v71, v58, v59\n"
            "v_mfma_f32_16x16x32_bf16 v[64:67], v[152:155], v[24:27], v[64:67]\n"
            "global_store_dwordx4 v118, v[68:71], s[54:55] offset:0 sc1\n"
            "v_mfma_f32_16x16x32_bf16 v[60:63], v[156:159], v[12:15], v[60:63]\n"
            "v_mfma_f32_16x16x32_bf16 v[64:67], v[156:159], v[28:31], v[64:67]\n"
            "v_mfma_f32_16x16x32_bf16 v[52:55], v[160:163], v[0:3], 0\n"
            "v_mfma_f32_16x16x32_bf16 v[56:59], v[160:163], v[16:19], 0\n"
            "v_mfma_f32_16x16x32_bf16 v[52:55], v[164:167], v[4:7], v[52:55]\n"
            "v_mfma_f32_16x16x32_bf16 v[56:59], v[164:167], v[20:23], v[56:59]\n"
            "s_nop 2\n"
            "v_cvt_pk_bf16_f32 v72, v60, v61\n"
            "v_cvt_pk_bf16_f32 v73, v62, v63\n"
            "v_mfma_f32_16x16x32_bf16 v[52:55], v[168:171], v[8:11], v[52:55]\n"
            "v_cvt_pk_bf16_f32 v74, v64, v65\n"
            "v_cvt_pk_bf16_f32 v75, v66, v67\n"
            "v_mfma_f32_16x16x32_bf16 v[56:59], v[168:171], v[24:27], v[56:59]\n"
            "global_store_dwordx4 v118, v[72:75], s[54:55] offset:1024 sc1\n"
            "v_mfma_f32_16x16x32_bf16 v[52:55], v[172:175], v[12:15], v[52:55]\n"
            "v_mfma_f32_16x16x32_bf16 v[56:59], v[172:175], v[28:31], v[56:59]\n"
            "v_mfma_f32_16x16x32_bf16 v[60:63], v[176:179], v[0:3], 0\n"
            "v_mfma_f32_16x16x32_bf16 v[64:67], v[176:179], v[16:19], 0\n"
            "v_mfma_f32_16x16x32_bf16 v[60:63], v[180:183], v[4:7], v[60:63]\n"
            "v_mfma_f32_16x16x32_bf16 v[64:67], v[180:183], v[20:23], v[64:67]\n"
            "s_nop 2\n"
            "v_cvt_pk_bf16_f32 v68, v52, v53\n"
            "v_cvt_pk_bf16_f32 v69, v54, v55\n"
            "v_mfma_f32_16x16x32_bf16 v[60:63], v[184:187], v[8:11], v[60:63]\n"
            "v_cvt_pk_bf16_f32 v70, v56, v57\n"
            "v_cvt_pk_bf16_f32 v71, v58, v59\n"
            "v_mfma_f32_16x16x32_bf16 v[64:67], v[184:187], v[24:27], v[64:67]\n"
            "global_store_dwordx4 v118, v[68:71], s[54:55] offset:2048 sc1\n"
            "v_mfma_f32_16x16x32_bf16 v[60:63], v[188:191], v[12:15], v[60:63]\n"
            "v_mfma_f32_16x16x32_bf16 v[64:67], v[188:191], v[28:31], v[64:67]\n"
            "v_mfma_f32_16x16x32_bf16 v[52:55], v[192:195], v[0:3], 0\n"
            "v_mfma_f32_16x16x32_bf16 v[56:59], v[192:195], v[16:19], 0\n"
            "v_mfma_f32_16x16x32_bf16 v[52:55], v[196:199], v[4:7], v[52:55]\n"
            "v_mfma_f32_16x16x32_bf16 v[56:59], v[196:199], v[20:23], v[56:59]\n"
            "s_nop 2\n"
            "v_cvt_pk_bf16_f32 v72, v60, v61\n"
            "v_cvt_pk_bf16_f32 v73, v62, v63\n"
            "v_mfma_f32_16x16x32_bf16 v[52:55], v[200:203], v[8:11], v[52:55]\n"
            "v_cvt_pk_bf16_f32 v74, v64, v65\n"
            "v_cvt_pk_bf16_f32 v75, v66, v67\n"
            "v_mfma_f32_16x16x32_bf16 v[56:59], v[200:203], v[24:27], v[56:59]\n"
            "global_store_dwordx4 v118, v[72:75], s[54:55] offset:3072 sc1\n"
            "v_mfma_f32_16x16x32_bf16 v[52:55], v[204:207], v[12:15], v[52:55]\n"
            "v_mfma_f32_16x16x32_bf16 v[56:59], v[204:207], v[28:31], v[56:59]\n"
            "v_mfma_f32_16x16x32_bf16 v[60:63], v[208:211], v[0:3], 0\n"
            "v_mfma_f32_16x16x32_bf16 v[64:67], v[208:211], v[16:19], 0\n"
            "v_mfma_f32_16x16x32_bf16 v[60:63], v[212:215], v[4:7], v[60:63]\n"
            "v_mfma_f32_16x16x32_bf16 v[64:67], v[212:215], v[20:23], v[64:67]\n"
            "s_nop 2\n"
            "v_cvt_pk_bf16_f32 v68, v52, v53\n"
            "v_cvt_pk_bf16_f32 v69, v54, v55\n"
            "v_mfma_f32_16x16x32_bf16 v[60:63], v[216:219], v[8:11], v[60:63]\n"
            "v_cvt_pk_bf16_f32 v70, v56, v57\n"
            "v_cvt_pk_bf16_f32 v71, v58, v59\n"
            "v_mfma_f32_16x16x32_bf16 v[64:67], v[216:219], v[24:27], v[64:67]\n"
            "global_store_dwordx4 v118, v[68:71], s[76:77] offset:0 sc1\n"
            "v_mfma_f32_16x16x32_bf16 v[60:63], v[220:223], v[12:15], v[60:63]\n"
            "v_mfma_f32_16x16x32_bf16 v[64:67], v[220:223], v[28:31], v[64:67]\n"
            "v_mfma_f32_16x16x32_bf16 v[52:55], v[224:227], v[0:3], 0\n"
            "v_mfma_f32_16x16x32_bf16 v[56:59], v[224:227], v[16:19], 0\n"
            "v_mfma_f32_16x16x32_bf16 v[52:55], v[228:231], v[4:7], v[52:55]\n"
            "v_mfma_f32_16x16x32_bf16 v[56:59], v[228:231], v[20:23], v[56:59]\n"
            "s_nop 2\n"
            "v_cvt_pk_bf16_f32 v72, v60, v61\n"
            "v_cvt_pk_bf16_f32 v73, v62, v63\n"
            "v_mfma_f32_16x16x32_bf16 v[52:55], v[232:235], v[8:11], v[52:55]\n"
            "v_cvt_pk_bf16_f32 v74, v64, v65\n"
            "v_cvt_pk_bf16_f32 v75, v66, v67\n"
            "v_mfma_f32_16x16x32_bf16 v[56:59], v[232:235], v[24:27], v[56:59]\n"
            "global_store_dwordx4 v118, v[72:75], s[76:77] offset:1024 sc1\n"
            "v_mfma_f32_16x16x32_bf16 v[52:55], v[236:239], v[12:15], v[52:55]\n"
            "v_mfma_f32_16x16x32_bf16 v[56:59], v[236:239], v[28:31], v[56:59]\n"
            "v_mfma_f32_16x16x32_bf16 v[60:63], v[240:243], v[0:3], 0\n"
            "v_mfma_f32_16x16x32_bf16 v[64:67], v[240:243], v[16:19], 0\n"
            "v_mfma_f32_16x16x32_bf16 v[60:63], v[244:247], v[4:7], v[60:63]\n"
            "v_mfma_f32_16x16x32_bf16 v[64:67], v[244:247], v[20:23], v[64:67]\n"
            "s_nop 2\n"
            "v_cvt_pk_bf16_f32 v68, v52, v53\n"
            "v_cvt_pk_bf16_f32 v69, v54, v55\n"
            "v_mfma_f32_16x16x32_bf16 v[60:63], v[248:251], v[8:11], v[60:63]\n"
            "v_cvt_pk_bf16_f32 v70, v56, v57\n"
            "v_cvt_pk_bf16_f32 v71, v58, v59\n"
            "v_mfma_f32_16x16x32_bf16 v[64:67], v[248:251], v[24:27], v[64:67]\n"
            "global_store_dwordx4 v118, v[68:71], s[76:77] offset:2048 sc1\n"
            "v_mfma_f32_16x16x32_bf16 v[60:63], v[252:255], v[12:15], v[60:63]\n"
            "v_mfma_f32_16x16x32_bf16 v[64:67], v[252:255], v[28:31], v[64:67]\n"
            "s_nop 6\n"
            "v_cvt_pk_bf16_f32 v72, v60, v61\n"
            "v_cvt_pk_bf16_f32 v73, v62, v63\n"
            "v_cvt_pk_bf16_f32 v74, v64, v65\n"
            "v_cvt_pk_bf16_f32 v75, v66, v67\n"
            "global_store_dwordx4 v118, v[72:75], s[76:77] offset:3072 sc1\n"
            "s_waitcnt vmcnt(0)\n"
            "s_barrier\n"
            "s_mov_b32 s74, 64\n"
            "XSGB_L_%=:\n"
            "s_cmp_lt_u32 s25, s74\n"
            "s_cbranch_scc0 XSGB_E_%=\n"
            "s_cmp_eq_u32 s66, 0\n"
            "s_cbranch_scc0 XSGB_S_%=\n"
            "s_lshl_b32 s70, s25, 7\n"
            "s_add_u32 s80, s58, s70\n"
            "s_addc_u32 s81, s59, 0\n"
            "v_mov_b32 v124, 0\n"
            "v_mov_b32 v125, 1\n"
            "s_mov_b64 exec, 1\n"
            "global_atomic_add v124, v125, s[80:81] sc1\n"
            "s_mov_b64 exec, -1\n"
            "XSGB_S_%=:\n"
            "s_add_u32 s25, s25, 1\n"
            "s_branch XSGB_L_%=\n"
            "XSGB_E_%=:\n"
            "s_waitcnt vmcnt(0)\n"
            "s_endpgm\n"
            "XRETRY_%=:\n"
            "s_waitcnt lgkmcnt(0)\n"
            "s_barrier\n"
            "s_nop 7\n"
            "v_mov_b32 v32, 0\n"
            "v_mov_b32 v33, 0\n"
            "v_mov_b32 v34, 0\n"
            "v_mov_b32 v35, 0\n"
            "v_mov_b32 v36, 0\n"
            "v_mov_b32 v37, 0\n"
            "v_mov_b32 v38, 0\n"
            "v_mov_b32 v39, 0\n"
            "v_mov_b32 v40, 0\n"
            "v_mov_b32 v41, 0\n"
            "v_mov_b32 v42, 0\n"
            "v_mov_b32 v43, 0\n"
            "v_mov_b32 v44, 0\n"
            "v_mov_b32 v45, 0\n"
            "v_mov_b32 v46, 0\n"
            "v_mov_b32 v47, 0\n"
            "s_mov_b32 s67, 0\n"
            "XRETRY2_%=:\n"
            "s_add_u32 s67, s67, 1\n"
            "s_cmp_gt_u32 s67, 0x40000\n"
            "s_cbranch_scc1 XTIMEOUT_%=\n"
            "global_load_dwordx4 v[0:3], v113, s[52:53] sc1\n"
            "global_load_dwordx4 v[4:7], v113, s[52:53] offset:1024 sc1\n"
            "s_waitcnt vmcnt(0)\n"
            "v_max3_u32 v124, v1, v3, v5\n"
            "v_max_u32 v124, v124, v7\n"
            "v_cmp_eq_u32 vcc, -1, v124\n"
            "s_cbranch_vccnz XRETRY2_%=\n"
            "s_lshl_b32 s70, s66, 11\n"
            "v_add_u32 v125, s70, v114\n"
            "ds_write_b128 v125, v[0:3] offset:0\n"
            "ds_write_b128 v125, v[4:7] offset:1024\n"
            "s_waitcnt lgkmcnt(0)\n"
            "s_barrier\n"
            "s_branch XAFTERB1_%=\n"
            "XTIMEOUT_%=:\n"
            "s_mov_b64 exec, -1\n"
            "v_mov_b32 v0, 0\n"
            "v_mov_b32 v1, 2\n"
            "global_store_dword v0, v1, s[62:63] sc1\n"
            "s_waitcnt vmcnt(0)\n"
            "s_endpgm\n"
            :: "s"(ka), "s"(hs), "s"(w), "s"(hm)
            : "memory", "vcc", "v255", "a255", "s10", "s11", "s12", "s13", "s14", "s15", "s16", "s17", "s18", "s19", "s20", "s21", "s22", "s23", "s24", "s25", "s26", "s27", "s28", "s29", "s30", "s31", "s32", "s33", "s34", "s35", "s36", "s37", "s38", "s39", "s40", "s41", "s42", "s43", "s44", "s45", "s46", "s47", "s48", "s49", "s50", "s51", "s52", "s53", "s54", "s55", "s56", "s57", "s58", "s59", "s60", "s61", "s62", "s63", "s64", "s65", "s66", "s67", "s68", "s69", "s70", "s71", "s72", "s73", "s74", "s75", "s76", "s77", "s78", "s79", "s80", "s81", "s82", "s83", "s84", "s85", "s86", "s87", "s88", "s89", "s90", "s91", "s92", "s93", "s94", "s95", "s96", "s97", "s98", "s99");
        return;
    }
    if (j < 16) {
        if (!(p.roles & 16)) return;
        const int h = xq * 8 + (j - 8);
        unsigned* cst = (unsigned*)(lds + 0x20000) + tid * 24;
        const int lq = lane >> 4, sl = lane & 15, q = c >> 2, pp = c & 3;
        cst[0] = lane * 16;
        cst[1] = c * 1024 + g * 16;
        cst[2] = w * 1024 + lane * 16;
        cst[3] = 0;
        cst[4] = (4 * w + lq) * 8192 + ((sl ^ (2 * ((lq & 1) | (w << 1)))) * 16);
        { const int rowp = 16 * (g >> 1) + 4 * (g & 1) + (q < 2 ? q : 6 + q), fp = 2 * ((q & 1) | (((g & 1) | ((q >> 1) << 1)) << 1));
          cst[5] = rowp * 256 + (((4 * w + (pp >> 1)) ^ fp) * 16) + 8 * (pp & 1); }
        __syncthreads();
        const int hs = __builtin_amdgcn_readfirstlane(h);
        const int hm = near_half(p.lat, 0, lane);
        asm volatile(
            "s_mov_b64 s[36:37], %0\n"
            "s_mov_b32 s65, %1\n"
            "s_mov_b32 s66, %2\n"
            "s_load_dwordx2 s[40:41], s[36:37], 8\n"
            "s_load_dwordx2 s[42:43], s[36:37], 16\n"
            "s_load_dwordx2 s[48:49], s[36:37], 32\n"
            "s_load_dwordx2 s[50:51], s[36:37], 40\n"
            "s_load_dwordx2 s[62:63], s[36:37], 64\n"
            "s_load_dwordx2 s[52:53], s[36:37], 112\n"
            "s_load_dwordx2 s[60:61], s[36:37], 232\n"
            "v_mbcnt_lo_u32_b32 v0, -1, 0\n"
            "v_mbcnt_hi_u32_b32 v0, -1, v0\n"
            "s_mul_i32 s70, s66, 6144\n"
            "v_mul_u32_u24 v0, 96, v0\n"
            "v_add_u32 v0, s70, v0\n"
            "v_add_u32 v0, 131072, v0\n"
            "ds_read_b128 v[112:115], v0\n"
            "ds_read_b128 v[64:67], v0 offset:16\n"
            "s_waitcnt lgkmcnt(0)\n"
            "s_lshl_b32 s70, s65, 8\n"
            "s_add_u32 s40, s40, s70\n"
            "s_addc_u32 s41, s41, 0\n"
            "s_add_u32 s42, s42, s70\n"
            "s_addc_u32 s43, s43, 0\n"
            "s_lshl_b32 s70, s65, 14\n"
            "s_add_u32 s44, s48, s70\n"
            "s_addc_u32 s45, s49, 0\n"
            "s_add_u32 s46, s50, s70\n"
            "s_addc_u32 s47, s51, 0\n"
            "s_lshr_b32 s54, %3, 13\n"
            "s_lshl_b32 s70, s65, 7\n"
            "s_add_u32 s60, s60, s70\n"
            "s_addc_u32 s61, s61, 0\n"
            "s_lshr_b32 s70, s66, 1\n"
            "s_lshl_b32 s70, s70, 14\n"
            "s_and_b32 s71, s66, 1\n"
            "s_lshl_b32 s71, s71, 9\n"
            "s_add_u32 s70, s70, s71\n"
            "s_add_u32 s52, s52, s70\n"
            "s_addc_u32 s53, s53, 0\n"
            "s_lshl_b32 s70, s66, 10\n"
            "s_add_u32 m0, s70, 0\n"
            "s_add_u32 s72, s40, 0\n"
            "s_addc_u32 s73, s41, 0\n"
            "global_load_lds_dwordx4 v64, s[72:73]\n"
            "s_lshl_b32 s70, s66, 10\n"
            "s_add_u32 m0, s70, 4096\n"
            "s_add_u32 s72, s40, 131072\n"
            "s_addc_u32 s73, s41, 0\n"
            "global_load_lds_dwordx4 v64, s[72:73]\n"
            "s_lshl_b32 s70, s66, 10\n"
            "s_add_u32 m0, s70, 8192\n"
            "s_add_u32 s72, s40, 262144\n"
            "s_addc_u32 s73, s41, 0\n"
            "global_load_lds_dwordx4 v64, s[72:73]\n"
            "s_lshl_b32 s70, s66, 10\n"
            "s_add_u32 m0, s70, 12288\n"
            "s_add_u32 s72, s40, 393216\n"
            "s_addc_u32 s73, s41, 0\n"
            "global_load_lds_dwordx4 v64, s[72:73]\n"
            "s_lshl_b32 s70, s66, 10\n"
            "s_add_u32 m0, s70, 16384\n"
            "s_add_u32 s72, s40, 524288\n"
            "s_addc_u32 s73, s41, 0\n"
            "global_load_lds_dwordx4 v64, s[72:73]\n"
            "s_lshl_b32 s70, s66, 10\n"
            "s_add_u32 m0, s70, 20480\n"
            "s_add_u32 s72, s40, 655360\n"
            "s_addc_u32 s73, s41, 0\n"
            "global_load_lds_dwordx4 v64, s[72:73]\n"
            "s_lshl_b32 s70, s66, 10\n"
            "s_add_u32 m0, s70, 24576\n"
            "s_add_u32 s72, s40, 786432\n"
            "s_addc_u32 s73, s41, 0\n"
            "global_load_lds_dwordx4 v64, s[72:73]\n"
            "s_lshl_b32 s70, s66, 10\n"
            "s_add_u32 m0, s70, 28672\n"
            "s_add_u32 s72, s40, 917504\n"
            "s_addc_u32 s73, s41, 0\n"
            "global_load_lds_dwordx4 v64, s[72:73]\n"
            "s_lshl_b32 s70, s66, 10\n"
            "s_add_u32 m0, s70, 32768\n"
            "s_add_u32 s72, s42, 0\n"
            "s_addc_u32 s73, s43, 0\n"
            "global_load_lds_dwordx4 v64, s[72:73]\n"
            "s_lshl_b32 s70, s66, 10\n"
            "s_add_u32 m0, s70, 36864\n"
            "s_add_u32 s72, s42, 131072\n"
            "s_addc_u32 s73, s43, 0\n"
            "global_load_lds_dwordx4 v64, s[72:73]\n"
            "s_lshl_b32 s70, s66, 10\n"
            "s_add_u32 m0, s70, 40960\n"
            "s_add_u32 s72, s42, 262144\n"
            "s_addc_u32 s73, s43, 0\n"
            "global_load_lds_dwordx4 v64, s[72:73]\n"
            "s_lshl_b32 s70, s66, 10\n"
            "s_add_u32 m0, s70, 45056\n"
            "s_add_u32 s72, s42, 393216\n"
            "s_addc_u32 s73, s43, 0\n"
            "global_load_lds_dwordx4 v64, s[72:73]\n"
            "s_lshl_b32 s70, s66, 10\n"
            "s_add_u32 m0, s70, 49152\n"
            "s_add_u32 s72, s42, 524288\n"
            "s_addc_u32 s73, s43, 0\n"
            "global_load_lds_dwordx4 v64, s[72:73]\n"
            "s_lshl_b32 s70, s66, 10\n"
            "s_add_u32 m0, s70, 53248\n"
            "s_add_u32 s72, s42, 655360\n"
            "s_addc_u32 s73, s43, 0\n"
            "global_load_lds_dwordx4 v64, s[72:73]\n"
            "s_lshl_b32 s70, s66, 10\n"
            "s_add_u32 m0, s70, 57344\n"
            "s_add_u32 s72, s42, 786432\n"
            "s_addc_u32 s73, s43, 0\n"
            "global_load_lds_dwordx4 v64, s[72:73]\n"
            "s_lshl_b32 s70, s66, 10\n"
            "s_add_u32 m0, s70, 61440\n"
            "s_add_u32 s72, s42, 917504\n"
            "s_addc_u32 s73, s43, 0\n"
            "global_load_lds_dwordx4 v64, s[72:73]\n"
            "s_lshl_b32 s70, s66, 10\n"
            "s_add_u32 m0, s70, 65536\n"
            "s_add_u32 s72, s40, 1048576\n"
            "s_addc_u32 s73, s41, 0\n"
            "global_load_lds_dwordx4 v64, s[72:73]\n"
            "s_lshl_b32 s70, s66, 10\n"
            "s_add_u32 m0, s70, 69632\n"
            "s_add_u32 s72, s40, 1179648\n"
            "s_addc_u32 s73, s41, 0\n"
            "global_load_lds_dwordx4 v64, s[72:73]\n"
            "s_lshl_b32 s70, s66, 10\n"
            "s_add_u32 m0, s70, 73728\n"
            "s_add_u32 s72, s40, 1310720\n"
            "s_addc_u32 s73, s41, 0\n"
            "global_load_lds_dwordx4 v64, s[72:73]\n"
            "s_lshl_b32 s70, s66, 10\n"
            "s_add_u32 m0, s70, 77824\n"
            "s_add_u32 s72, s40, 1441792\n"
            "s_addc_u32 s73, s41, 0\n"
            "global_load_lds_dwordx4 v64, s[72:73]\n"
            "s_lshl_b32 s70, s66, 10\n"
            "s_add_u32 m0, s70, 81920\n"
            "s_add_u32 s72, s40, 1572864\n"
            "s_addc_u32 s73, s41, 0\n"
            "global_load_lds_dwordx4 v64, s[72:73]\n"
            "s_lshl_b32 s70, s66, 10\n"
            "s_add_u32 m0, s70, 86016\n"
            "s_add_u32 s72, s40, 1703936\n"
            "s_addc_u32 s73, s41, 0\n"
            "global_load_lds_dwordx4 v64, s[72:73]\n"
            "s_lshl_b32 s70, s66, 10\n"
            "s_add_u32 m0, s70, 90112\n"
            "s_add_u32 s72, s40, 1835008\n"
            "s_addc_u32 s73, s41, 0\n"
            "global_load_lds_dwordx4 v64, s[72:73]\n"
            "s_lshl_b32 s70, s66, 10\n"
            "s_add_u32 m0, s70, 94208\n"
            "s_add_u32 s72, s40, 1966080\n"
            "s_addc_u32 s73, s41, 0\n"
            "global_load_lds_dwordx4 v64, s[72:73]\n"
            "s_waitcnt vmcnt(16)\n"
            "s_barrier\n"
            "s_lshl_b32 s70, s66, 10\n"
            "s_add_u32 m0, s70, 98304\n"
            "s_add_u32 s72, s42, 1048576\n"
            "s_addc_u32 s73, s43, 0\n"
            "global_load_lds_dwordx4 v64, s[72:73]\n"
            "v_add_u32 v72, 0, v65\n"
            "v_xor_b32 v73, 32, v72\n"
            "s_lshl_b32 s70, s66, 10\n"
            "s_add_u32 m0, s70, 102400\n"
            "s_add_u32 s72, s42, 1179648\n"
            "s_addc_u32 s73, s43, 0\n"
            "global_load_lds_dwordx4 v64, s[72:73]\n"
            "ds_read_b64_tr_b16 a[0:1], v72 offset:0\n"
            "ds_read_b64_tr_b16 a[2:3], v72 offset:512\n"
            "s_lshl_b32 s70, s66, 10\n"
            "s_add_u32 m0, s70, 106496\n"
            "s_add_u32 s72, s42, 1310720\n"
            "s_addc_u32 s73, s43, 0\n"
            "global_load_lds_dwordx4 v64, s[72:73]\n"
            "ds_read_b64_tr_b16 a[4:5], v72 offset:8192\n"
            "ds_read_b64_tr_b16 a[6:7], v72 offset:8704\n"
            "s_lshl_b32 s70, s66, 10\n"
            "s_add_u32 m0, s70, 110592\n"
            "s_add_u32 s72, s42, 1441792\n"
            "s_addc_u32 s73, s43, 0\n"
            "global_load_lds_dwordx4 v64, s[72:73]\n"
            "ds_read_b64_tr_b16 a[64:65], v73 offset:0\n"
            "ds_read_b64_tr_b16 a[66:67], v73 offset:512\n"
            "s_lshl_b32 s70, s66, 10\n"
            "s_add_u32 m0, s70, 114688\n"
            "s_add_u32 s72, s42, 1572864\n"
            "s_addc_u32 s73, s43, 0\n"
            "global_load_lds_dwordx4 v64, s[72:73]\n"
            "ds_read_b64_tr_b16 a[68:69], v73 offset:8192\n"
            "ds_read_b64_tr_b16 a[70:71], v73 offset:8704\n"
            "s_lshl_b32 s70, s66, 10\n"
            "s_add_u32 m0, s70, 118784\n"
            "s_add_u32 s72, s42, 1703936\n"
            "s_addc_u32 s73, s43, 0\n"
            "global_load_lds_dwordx4 v64, s[72:73]\n"
            "s_waitcnt lgkmcnt(0)\n"
            "ds_read_b64_tr_b16 a[8:9], v72 offset:16384\n"
            "s_lshl_b32 s70, s66, 10\n"
            "s_add_u32 m0, s70, 122880\n"
            "s_add_u32 s72, s42, 1835008\n"
            "s_addc_u32 s73, s43, 0\n"
            "global_load_lds_dwordx4 v64, s[72:73]\n"
            "ds_read_b64_tr_b16 a[10:11], v72 offset:16896\n"
            "ds_read_b64_tr_b16 a[12:13], v72 offset:24576\n"
            "s_lshl_b32 s70, s66, 10\n"
            "s_add_u32 m0, s70, 126976\n"
            "s_add_u32 s72, s42, 1966080\n"
            "s_addc_u32 s73, s43, 0\n"
            "global_load_lds_dwordx4 v64, s[72:73]\n"
            "ds_read_b64_tr_b16 a[14:15], v72 offset:25088\n"
            "ds_read_b64_tr_b16 a[72:73], v73 offset:16384\n"
            "ds_read_b64_tr_b16 a[74:75], v73 offset:16896\n"
            "ds_read_b64_tr_b16 a[76:77], v73 offset:24576\n"
            "ds_read_b64_tr_b16 a[78:79], v73 offset:25088\n"
            "s_waitcnt lgkmcnt(0)\n"
            "s_waitcnt vmcnt(16)\n"
            "s_barrier\n"
            "s_lshl_b32 s70, s66, 10\n"
            "s_add_u32 m0, s70, 0\n"
            "s_add_u32 s72, s40, 2097152\n"
            "s_addc_u32 s73, s41, 0\n"
            "global_load_lds_dwordx4 v64, s[72:73]\n"
            "v_add_u32 v72, 32768, v65\n"
            "v_xor_b32 v73, 32, v72\n"
            "s_lshl_b32 s70, s66, 10\n"
            "s_add_u32 m0, s70, 4096\n"
            "s_add_u32 s72, s40, 2228224\n"
            "s_addc_u32 s73, s41, 0\n"
            "global_load_lds_dwordx4 v64, s[72:73]\n"
            "ds_read_b64_tr_b16 a[128:129], v72 offset:0\n"
            "ds_read_b64_tr_b16 a[130:131], v72 offset:512\n"
            "s_lshl_b32 s70, s66, 10\n"
            "s_add_u32 m0, s70, 8192\n"
            "s_add_u32 s72, s40, 2359296\n"
            "s_addc_u32 s73, s41, 0\n"
            "global_load_lds_dwordx4 v64, s[72:73]\n"
            "ds_read_b64_tr_b16 a[132:133], v72 offset:8192\n"
            "ds_read_b64_tr_b16 a[134:135], v72 offset:8704\n"
            "s_lshl_b32 s70, s66, 10\n"
            "s_add_u32 m0, s70, 12288\n"
            "s_add_u32 s72, s40, 2490368\n"
            "s_addc_u32 s73, s41, 0\n"
            "global_load_lds_dwordx4 v64, s[72:73]\n"
            "ds_read_b64_tr_b16 a[192:193], v73 offset:0\n"
            "ds_read_b64_tr_b16 a[194:195], v73 offset:512\n"
            "s_lshl_b32 s70, s66, 10\n"
            "s_add_u32 m0, s70, 16384\n"
            "s_add_u32 s72, s40, 2621440\n"
            "s_addc_u32 s73, s41, 0\n"
            "global_load_lds_dwordx4 v64, s[72:73]\n"
            "ds_read_b64_tr_b16 a[196:197], v73 offset:8192\n"
            "ds_read_b64_tr_b16 a[198:199], v73 offset:8704\n"
            "s_lshl_b32 s70, s66, 10\n"
            "s_add_u32 m0, s70, 20480\n"
            "s_add_u32 s72, s40, 2752512\n"
            "s_addc_u32 s73, s41, 0\n"
            "global_load_lds_dwordx4 v64, s[72:73]\n"
            "s_waitcnt lgkmcnt(0)\n"
            "ds_read_b64_tr_b16 a[136:137], v72 offset:16384\n"
            "s_lshl_b32 s70, s66, 10\n"
            "s_add_u32 m0, s70, 24576\n"
            "s_add_u32 s72, s40, 2883584\n"
            "s_addc_u32 s73, s41, 0\n"
            "global_load_lds_dwordx4 v64, s[72:73]\n"
            "ds_read_b64_tr_b16 a[138:139], v72 offset:16896\n"
            "ds_read_b64_tr_b16 a[140:141], v72 offset:24576\n"
            "s_lshl_b32 s70, s66, 10\n"
            "s_add_u32 m0, s70, 28672\n"
            "s_add_u32 s72, s40, 3014656\n"
            "s_addc_u32 s73, s41, 0\n"
            "global_load_lds_dwordx4 v64, s[72:73]\n"
            "ds_read_b64_tr_b16 a[142:143], v72 offset:25088\n"
            "ds_read_b64_tr_b16 a[200:201], v73 offset:16384\n"
            "ds_read_b64_tr_b16 a[202:203], v73 offset:16896\n"
            "ds_read_b64_tr_b16 a[204:205], v73 offset:24576\n"
            "ds_read_b64_tr_b16 a[206:207], v73 offset:25088\n"
            "s_waitcnt lgkmcnt(0)\n"
            "s_waitcnt vmcnt(16)\n"
            "s_barrier\n"
            "s_lshl_b32 s70, s66, 10\n"
            "s_add_u32 m0, s70, 32768\n"
            "s_add_u32 s72, s42, 2097152\n"
            "s_addc_u32 s73, s43, 0\n"
            "global_load_lds_dwordx4 v64, s[72:73]\n"
            "v_add_u32 v72, 65536, v65\n"
            "v_xor_b32 v73, 32, v72\n"
            "s_lshl_b32 s70, s66, 10\n"
            "s_add_u32 m0, s70, 36864\n"
            "s_add_u32 s72, s42, 2228224\n"
            "s_addc_u32 s73, s43, 0\n"
            "global_load_lds_dwordx4 v64, s[72:73]\n"
            "ds_read_b64_tr_b16 a[16:17], v72 offset:0\n"
            "ds_read_b64_tr_b16 a[18:19], v72 offset:512\n"
            "s_lshl_b32 s70, s66, 10\n"
            "s_add_u32 m0, s70, 40960\n"
            "s_add_u32 s72, s42, 2359296\n"
            "s_addc_u32 s73, s43, 0\n"
            "global_load_lds_dwordx4 v64, s[72:73]\n"
            "ds_read_b64_tr_b16 a[20:21], v72 offset:8192\n"
            "ds_read_b64_tr_b16 a[22:23], v72 offset:8704\n"
            "s_lshl_b32 s70, s66, 10\n"
            "s_add_u32 m0, s70, 45056\n"
            "s_add_u32 s72, s42, 2490368\n"
            "s_addc_u32 s73, s43, 0\n"
            "global_load_lds_dwordx4 v64, s[72:73]\n"
            "ds_read_b64_tr_b16 a[80:81], v73 offset:0\n"
            "ds_read_b64_tr_b16 a[82:83], v73 offset:512\n"
            "s_lshl_b32 s70, s66, 10\n"
            "s_add_u32 m0, s70, 49152\n"
            "s_add_u32 s72, s42, 2621440\n"
            "s_addc_u32 s73, s43, 0\n"
            "global_load_lds_dwordx4 v64, s[72:73]\n"
            "ds_read_b64_tr_b16 a[84:85], v73 offset:8192\n"
            "ds_read_b64_tr_b16 a[86:87], v73 offset:8704\n"
            "s_lshl_b32 s70, s66, 10\n"
            "s_add_u32 m0, s70, 53248\n"
            "s_add_u32 s72, s42, 2752512\n"
            "s_addc_u32 s73, s43, 0\n"
            "global_load_lds_dwordx4 v64, s[72:73]\n"
            "s_waitcnt lgkmcnt(0)\n"
            "ds_read_b64_tr_b16 a[24:25], v72 offset:16384\n"
            "s_lshl_b32 s70, s66, 10\n"
            "s_add_u32 m0, s70, 57344\n"
            "s_add_u32 s72, s42, 2883584\n"
            "s_addc_u32 s73, s43, 0\n"
            "global_load_lds_dwordx4 v64, s[72:73]\n"
            "ds_read_b64_tr_b16 a[26:27], v72 offset:16896\n"
            "ds_read_b64_tr_b16 a[28:29], v72 offset:24576\n"
            "s_lshl_b32 s70, s66, 10\n"
            "s_add_u32 m0, s70, 61440\n"
            "s_add_u32 s72, s42, 3014656\n"
            "s_addc_u32 s73, s43, 0\n"
            "global_load_lds_dwordx4 v64, s[72:73]\n"
            "ds_read_b64_tr_b16 a[30:31], v72 offset:25088\n"
            "ds_read_b64_tr_b16 a[88:89], v73 offset:16384\n"
            "ds_read_b64_tr_b16 a[90:91], v73 offset:16896\n"
            "ds_read_b64_tr_b16 a[92:93], v73 offset:24576\n"
            "ds_read_b64_tr_b16 a[94:95], v73 offset:25088\n"
            "s_waitcnt lgkmcnt(0)\n"
            "s_waitcnt vmcnt(16)\n"
            "s_barrier\n"
            "s_lshl_b32 s70, s66, 10\n"
            "s_add_u32 m0, s70, 65536\n"
            "s_add_u32 s72, s40, 3145728\n"
            "s_addc_u32 s73, s41, 0\n"
            "global_load_lds_dwordx4 v64, s[72:73]\n"
            "v_add_u32 v72, 98304, v65\n"
            "v_xor_b32 v73, 32, v72\n"
            "s_lshl_b32 s70, s66, 10\n"
            "s_add_u32 m0, s70, 69632\n"
            "s_add_u32 s72, s40, 3276800\n"
            "s_addc_u32 s73, s41, 0\n"
            "global_load_lds_dwordx4 v64, s[72:73]\n"
            "ds_read_b64_tr_b16 a[144:145], v72 offset:0\n"
            "ds_read_b64_tr_b16 a[146:147], v72 offset:512\n"
            "s_lshl_b32 s70, s66, 10\n"
            "s_add_u32 m0, s70, 73728\n"
            "s_add_u32 s72, s40, 3407872\n"
            "s_addc_u32 s73, s41, 0\n"
            "global_load_lds_dwordx4 v64, s[72:73]\n"
            "ds_read_b64_tr_b16 a[148:149], v72 offset:8192\n"
            "ds_read_b64_tr_b16 a[150:151], v72 offset:8704\n"
            "s_lshl_b32 s70, s66, 10\n"
            "s_add_u32 m0, s70, 77824\n"
            "s_add_u32 s72, s40, 3538944\n"
            "s_addc_u32 s73, s41, 0\n"
            "global_load_lds_dwordx4 v64, s[72:73]\n"
            "ds_read_b64_tr_b16 a[208:209], v73 offset:0\n"
            "ds_read_b64_tr_b16 a[210:211], v73 offset:512\n"
            "s_lshl_b32 s70, s66, 10\n"
            "s_add_u32 m0, s70, 81920\n"
            "s_add_u32 s72, s40, 3670016\n"
            "s_addc_u32 s73, s41, 0\n"
            "global_load_lds_dwordx4 v64, s[72:73]\n"
            "ds_read_b64_tr_b16 a[212:213], v73 offset:8192\n"
            "ds_read_b64_tr_b16 a[214:215], v73 offset:8704\n"
            "s_lshl_b32 s70, s66, 10\n"
            "s_add_u32 m0, s70, 86016\n"
            "s_add_u32 s72, s40, 3801088\n"
            "s_addc_u32 s73, s41, 0\n"
            "global_load_lds_dwordx4 v64, s[72:73]\n"
            "s_waitcnt lgkmcnt(0)\n"
            "ds_read_b64_tr_b16 a[152:153], v72 offset:16384\n"
            "s_lshl_b32 s70, s66, 10\n"
            "s_add_u32 m0, s70, 90112\n"
            "s_add_u32 s72, s40, 3932160\n"
            "s_addc_u32 s73, s41, 0\n"
            "global_load_lds_dwordx4 v64, s[72:73]\n"
            "ds_read_b64_tr_b16 a[154:155], v72 offset:16896\n"
            "ds_read_b64_tr_b16 a[156:157], v72 offset:24576\n"
            "s_lshl_b32 s70, s66, 10\n"
            "s_add_u32 m0, s70, 94208\n"
            "s_add_u32 s72, s40, 4063232\n"
            "s_addc_u32 s73, s41, 0\n"
            "global_load_lds_dwordx4 v64, s[72:73]\n"
            "ds_read_b64_tr_b16 a[158:159], v72 offset:25088\n"
            "ds_read_b64_tr_b16 a[216:217], v73 offset:16384\n"
            "ds_read_b64_tr_b16 a[218:219], v73 offset:16896\n"
            "ds_read_b64_tr_b16 a[220:221], v73 offset:24576\n"
            "ds_read_b64_tr_b16 a[222:223], v73 offset:25088\n"
            "s_waitcnt lgkmcnt(0)\n"
            "s_waitcnt vmcnt(16)\n"
            "s_barrier\n"
            "s_lshl_b32 s70, s66, 10\n"
            "s_add_u32 m0, s70, 98304\n"
            "s_add_u32 s72, s42, 3145728\n"
            "s_addc_u32 s73, s43, 0\n"
            "global_load_lds_dwordx4 v64, s[72:73]\n"
            "v_add_u32 v72, 0, v65\n"
            "v_xor_b32 v73, 32, v72\n"
            "s_lshl_b32 s70, s66, 10\n"
            "s_add_u32 m0, s70, 102400\n"
            "s_add_u32 s72, s42, 3276800\n"
            "s_addc_u32 s73, s43, 0\n"
            "global_load_lds_dwordx4 v64, s[72:73]\n"
            "ds_read_b64_tr_b16 a[32:33], v72 offset:0\n"
            "ds_read_b64_tr_b16 a[34:35], v72 offset:512\n"
            "s_lshl_b32 s70, s66, 10\n"
            "s_add_u32 m0, s70, 106496\n"
            "s_add_u32 s72, s42, 3407872\n"
            "s_addc_u32 s73, s43, 0\n"
            "global_load_lds_dwordx4 v64, s[72:73]\n"
            "ds_read_b64_tr_b16 a[36:37], v72 offset:8192\n"
            "ds_read_b64_tr_b16 a[38:39], v72 offset:8704\n"
            "s_lshl_b32 s70, s66, 10\n"
            "s_add_u32 m0, s70, 110592\n"
            "s_add_u32 s72, s42, 3538944\n"
            "s_addc_u32 s73, s43, 0\n"
            "global_load_lds_dwordx4 v64, s[72:73]\n"
            "ds_read_b64_tr_b16 a[96:97], v73 offset:0\n"
            "ds_read_b64_tr_b16 a[98:99], v73 offset:512\n"
            "s_lshl_b32 s70, s66, 10\n"
            "s_add_u32 m0, s70, 114688\n"
            "s_add_u32 s72, s42, 3670016\n"
            "s_addc_u32 s73, s43, 0\n"
            "global_load_lds_dwordx4 v64, s[72:73]\n"
            "ds_read_b64_tr_b16 a[100:101], v73 offset:8192\n"
            "ds_read_b64_tr_b16 a[102:103], v73 offset:8704\n"
            "s_lshl_b32 s70, s66, 10\n"
            "s_add_u32 m0, s70, 118784\n"
            "s_add_u32 s72, s42, 3801088\n"
            "s_addc_u32 s73, s43, 0\n"
            "global_load_lds_dwordx4 v64, s[72:73]\n"
            "s_waitcnt lgkmcnt(0)\n"
            "ds_read_b64_tr_b16 a[40:41], v72 offset:16384\n"
            "s_lshl_b32 s70, s66, 10\n"
            "s_add_u32 m0, s70, 122880\n"
            "s_add_u32 s72, s42, 3932160\n"
            "s_addc_u32 s73, s43, 0\n"
            "global_load_lds_dwordx4 v64, s[72:73]\n"
            "ds_read_b64_tr_b16 a[42:43], v72 offset:16896\n"
            "ds_read_b64_tr_b16 a[44:45], v72 offset:24576\n"
            "s_lshl_b32 s70, s66, 10\n"
            "s_add_u32 m0, s70, 126976\n"
            "s_add_u32 s72, s42, 4063232\n"
            "s_addc_u32 s73, s43, 0\n"
            "global_load_lds_dwordx4 v64, s[72:73]\n"
            "ds_read_b64_tr_b16 a[46:47], v72 offset:25088\n"
            "ds_read_b64_tr_b16 a[104:105], v73 offset:16384\n"
            "ds_read_b64_tr_b16 a[106:107], v73 offset:16896\n"
            "ds_read_b64_tr_b16 a[108:109], v73 offset:24576\n"
            "ds_read_b64_tr_b16 a[110:111], v73 offset:25088\n"
            "s_waitcnt lgkmcnt(0)\n"
            "s_waitcnt vmcnt(16)\n"
            "s_barrier\n"
            "v_add_u32 v72, 32768, v65\n"
            "v_xor_b32 v73, 32, v72\n"
            "ds_read_b64_tr_b16 a[160:161], v72 offset:0\n"
            "ds_read_b64_tr_b16 a[162:163], v72 offset:512\n"
            "ds_read_b64_tr_b16 a[164:165], v72 offset:8192\n"
            "ds_read_b64_tr_b16 a[166:167], v72 offset:8704\n"
            "ds_read_b64_tr_b16 a[224:225], v73 offset:0\n"
            "ds_read_b64_tr_b16 a[226:227], v73 offset:512\n"
            "ds_read_b64_tr_b16 a[228:229], v73 offset:8192\n"
            "ds_read_b64_tr_b16 a[230:231], v73 offset:8704\n"
            "s_waitcnt lgkmcnt(0)\n"
            "ds_read_b64_tr_b16 a[168:169], v72 offset:16384\n"
            "ds_read_b64_tr_b16 a[170:171], v72 offset:16896\n"
            "ds_read_b64_tr_b16 a[172:173], v72 offset:24576\n"
            "ds_read_b64_tr_b16 a[174:175], v72 offset:25088\n"
            "ds_read_b64_tr_b16 a[232:233], v73 offset:16384\n"
            "ds_read_b64_tr_b16 a[234:235], v73 offset:16896\n"
            "ds_read_b64_tr_b16 a[236:237], v73 offset:24576\n"
            "ds_read_b64_tr_b16 a[238:239], v73 offset:25088\n"
            "s_waitcnt lgkmcnt(0)\n"
            "s_waitcnt vmcnt(8)\n"
            "s_barrier\n"
            "v_add_u32 v72, 65536, v65\n"
            "v_xor_b32 v73, 32, v72\n"
            "ds_read_b64_tr_b16 a[48:49], v72 offset:0\n"
            "ds_read_b64_tr_b16 a[50:51], v72 offset:512\n"
            "ds_read_b64_tr_b16 a[52:53], v72 offset:8192\n"
            "ds_read_b64_tr_b16 a[54:55], v72 offset:8704\n"
            "ds_read_b64_tr_b16 a[112:113], v73 offset:0\n"
            "ds_read_b64_tr_b16 a[114:115], v73 offset:512\n"
            "ds_read_b64_tr_b16 a[116:117], v73 offset:8192\n"
            "ds_read_b64_tr_b16 a[118:119], v73 offset:8704\n"
            "s_waitcnt lgkmcnt(0)\n"
            "ds_read_b64_tr_b16 a[56:57], v72 offset:16384\n"
            "ds_read_b64_tr_b16 a[58:59], v72 offset:16896\n"
            "ds_read_b64_tr_b16 a[60:61], v72 offset:24576\n"
            "ds_read_b64_tr_b16 a[62:63], v72 offset:25088\n"
            "ds_read_b64_tr_b16 a[120:121], v73 offset:16384\n"
            "ds_read_b64_tr_b16 a[122:123], v73 offset:16896\n"
            "ds_read_b64_tr_b16 a[124:125], v73 offset:24576\n"
            "ds_read_b64_tr_b16 a[126:127], v73 offset:25088\n"
            "s_waitcnt lgkmcnt(0)\n"
            "s_waitcnt vmcnt(0)\n"
            "s_barrier\n"
            "v_add_u32 v72, 98304, v65\n"
            "v_xor_b32 v73, 32, v72\n"
            "ds_read_b64_tr_b16 a[176:177], v72 offset:0\n"
            "ds_read_b64_tr_b16 a[178:179], v72 offset:512\n"
            "ds_read_b64_tr_b16 a[180:181], v72 offset:8192\n"
            "ds_read_b64_tr_b16 a[182:183], v72 offset:8704\n"
            "ds_read_b64_tr_b16 a[240:241], v73 offset:0\n"
            "ds_read_b64_tr_b16 a[242:243], v73 offset:512\n"
            "ds_read_b64_tr_b16 a[244:245], v73 offset:8192\n"
            "ds_read_b64_tr_b16 a[246:247], v73 offset:8704\n"
            "s_waitcnt lgkmcnt(0)\n"
            "ds_read_b64_tr_b16 a[184:185], v72 offset:16384\n"
            "ds_read_b64_tr_b16 a[186:187], v72 offset:16896\n"
            "ds_read_b64_tr_b16 a[188:189], v72 offset:24576\n"
            "ds_read_b64_tr_b16 a[190:191], v72 offset:25088\n"
            "ds_read_b64_tr_b16 a[248:249], v73 offset:16384\n"
            "ds_read_b64_tr_b16 a[250:251], v73 offset:16896\n"
            "ds_read_b64_tr_b16 a[252:253], v73 offset:24576\n"
            "ds_read_b64_tr_b16 a[254:255], v73 offset:25088\n"
            "s_waitcnt lgkmcnt(0)\n"
            "s_barrier\n"
            "s_mov_b32 s64, 0\n"
            "v_add_u32 v115, 4096, v114\n"
            "s_lshl_b32 s70, s66, 13\n"
            "s_add_u32 m0, s70, 0\n"
            "s_add_u32 s72, s52, 0\n"
            "s_addc_u32 s73, s53, 0\n"
            "global_load_lds_dwordx4 v113, s[72:73]\n"
            "s_lshl_b32 s70, s66, 13\n"
            "s_add_u32 m0, s70, 1024\n"
            "s_add_u32 s72, s52, 64\n"
            "s_addc_u32 s73, s53, 0\n"
            "global_load_lds_dwordx4 v113, s[72:73]\n"
            "s_lshl_b32 s70, s66, 13\n"
            "s_add_u32 m0, s70, 2048\n"
            "s_add_u32 s72, s52, 128\n"
            "s_addc_u32 s73, s53, 0\n"
            "global_load_lds_dwordx4 v113, s[72:73]\n"
            "s_lshl_b32 s70, s66, 13\n"
            "s_add_u32 m0, s70, 3072\n"
            "s_add_u32 s72, s52, 192\n"
            "s_addc_u32 s73, s53, 0\n"
            "global_load_lds_dwordx4 v113, s[72:73]\n"
            "s_lshl_b32 s70, s66, 13\n"
            "s_add_u32 m0, s70, 4096\n"
            "s_add_u32 s72, s52, 256\n"
            "s_addc_u32 s73, s53, 0\n"
            "global_load_lds_dwordx4 v113, s[72:73]\n"
            "s_lshl_b32 s70, s66, 13\n"
            "s_add_u32 m0, s70, 5120\n"
            "s_add_u32 s72, s52, 320\n"
            "s_addc_u32 s73, s53, 0\n"
            "global_load_lds_dwordx4 v113, s[72:73]\n"
            "s_lshl_b32 s70, s66, 13\n"
            "s_add_u32 m0, s70, 6144\n"
            "s_add_u32 s72, s52, 384\n"
            "s_addc_u32 s73, s53, 0\n"
            "global_load_lds_dwordx4 v113, s[72:73]\n"
            "s_lshl_b32 s70, s66, 13\n"
            "s_add_u32 m0, s70, 7168\n"
            "s_add_u32 s72, s52, 448\n"
            "s_addc_u32 s73, s53, 0\n"
            "global_load_lds_dwordx4 v113, s[72:73]\n"
            "KV0_TOP_%=:\n"
            "s_cmp_le_u32 s64, 2\n"
            "s_cbranch_scc0 KV0_LATE_%=\n"
            "s_waitcnt vmcnt(0)\n"
            "s_mov_b32 s74, s64\n"
            "s_branch KV0_W_%=\n"
            "KV0_LATE_%=:\n"
            "s_waitcnt vmcnt(4)\n"
            "s_sub_u32 s74, s64, 1\n"
            "KV0_W_%=:\n"
            "s_barrier\n"
            "s_cmp_eq_u32 s64, 0\n"
            "s_cbranch_scc1 KV0_PUB_%=\n"
            "s_cmp_eq_u32 s66, 0\n"
            "s_cbranch_scc0 KV0_PUB_%=\n"
            "v_mov_b32 v124, s74\n"
            "v_mov_b32 v125, 0\n"
            "s_mov_b64 exec, 1\n"
            "global_store_dword v125, v124, s[60:61] sc1\n"
            "s_mov_b64 exec, -1\n"
            "KV0_PUB_%=:\n"
            "s_cmp_lt_u32 s64, 63\n"
            "s_cselect_b32 s70, 0x8000, 0\n"
            "s_add_u32 s52, s52, s70\n"
            "s_addc_u32 s53, s53, 0\n"
            "ds_read_b128 v[0:3], v112 offset:0\n"
            "ds_read_b128 v[4:7], v112 offset:16384\n"
            "ds_read_b128 v[8:11], v112 offset:1024\n"
            "ds_read_b128 v[12:15], v112 offset:17408\n"
            "s_waitcnt lgkmcnt(2)\n"
            "v_mfma_f32_16x16x32_bf16 v[32:35], a[0:3], v[0:3], 0\n"
            "v_mfma_f32_16x16x32_bf16 v[36:39], a[64:67], v[0:3], 0\n"
            "s_lshl_b32 s70, s66, 13\n"
            "s_add_u32 m0, s70, 32768\n"
            "s_add_u32 s72, s52, 0\n"
            "s_addc_u32 s73, s53, 0\n"
            "global_load_lds_dwordx4 v113, s[72:73]\n"
            "v_mfma_f32_16x16x32_bf16 v[48:51], a[128:131], v[0:3], 0\n"
            "v_mfma_f32_16x16x32_bf16 v[52:55], a[192:195], v[0:3], 0\n"
            "v_mfma_f32_16x16x32_bf16 v[40:43], a[0:3], v[4:7], 0\n"
            "v_mfma_f32_16x16x32_bf16 v[44:47], a[64:67], v[4:7], 0\n"
            "v_mfma_f32_16x16x32_bf16 v[56:59], a[128:131], v[4:7], 0\n"
            "v_mfma_f32_16x16x32_bf16 v[60:63], a[192:195], v[4:7], 0\n"
            "ds_read_b128 v[0:3], v112 offset:2048\n"
            "ds_read_b128 v[4:7], v112 offset:18432\n"
            "s_waitcnt lgkmcnt(2)\n"
            "v_mfma_f32_16x16x32_bf16 v[32:35], a[4:7], v[8:11], v[32:35]\n"
            "v_mfma_f32_16x16x32_bf16 v[36:39], a[68:71], v[8:11], v[36:39]\n"
            "v_mfma_f32_16x16x32_bf16 v[48:51], a[132:135], v[8:11], v[48:51]\n"
            "v_mfma_f32_16x16x32_bf16 v[52:55], a[196:199], v[8:11], v[52:55]\n"
            "v_mfma_f32_16x16x32_bf16 v[40:43], a[4:7], v[12:15], v[40:43]\n"
            "v_mfma_f32_16x16x32_bf16 v[44:47], a[68:71], v[12:15], v[44:47]\n"
            "v_mfma_f32_16x16x32_bf16 v[56:59], a[132:135], v[12:15], v[56:59]\n"
            "v_mfma_f32_16x16x32_bf16 v[60:63], a[196:199], v[12:15], v[60:63]\n"
            "ds_read_b128 v[8:11], v112 offset:3072\n"
            "ds_read_b128 v[12:15], v112 offset:19456\n"
            "s_waitcnt lgkmcnt(2)\n"
            "v_mfma_f32_16x16x32_bf16 v[32:35], a[8:11], v[0:3], v[32:35]\n"
            "v_mfma_f32_16x16x32_bf16 v[36:39], a[72:75], v[0:3], v[36:39]\n"
            "s_lshl_b32 s70, s66, 13\n"
            "s_add_u32 m0, s70, 33792\n"
            "s_add_u32 s72, s52, 64\n"
            "s_addc_u32 s73, s53, 0\n"
            "global_load_lds_dwordx4 v113, s[72:73]\n"
            "v_mfma_f32_16x16x32_bf16 v[48:51], a[136:139], v[0:3], v[48:51]\n"
            "v_mfma_f32_16x16x32_bf16 v[52:55], a[200:203], v[0:3], v[52:55]\n"
            "v_mfma_f32_16x16x32_bf16 v[40:43], a[8:11], v[4:7], v[40:43]\n"
            "v_mfma_f32_16x16x32_bf16 v[44:47], a[72:75], v[4:7], v[44:47]\n"
            "v_mfma_f32_16x16x32_bf16 v[56:59], a[136:139], v[4:7], v[56:59]\n"
            "v_mfma_f32_16x16x32_bf16 v[60:63], a[200:203], v[4:7], v[60:63]\n"
            "ds_read_b128 v[0:3], v112 offset:4096\n"
            "ds_read_b128 v[4:7], v112 offset:20480\n"
            "s_waitcnt lgkmcnt(2)\n"
            "v_mfma_f32_16x16x32_bf16 v[32:35], a[12:15], v[8:11], v[32:35]\n"
            "v_mfma_f32_16x16x32_bf16 v[36:39], a[76:79], v[8:11], v[36:39]\n"
            "v_mfma_f32_16x16x32_bf16 v[48:51], a[140:143], v[8:11], v[48:51]\n"
            "v_mfma_f32_16x16x32_bf16 v[52:55], a[204:207], v[8:11], v[52:55]\n"
            "v_mfma_f32_16x16x32_bf16 v[40:43], a[12:15], v[12:15], v[40:43]\n"
            "v_mfma_f32_16x16x32_bf16 v[44:47], a[76:79], v[12:15], v[44:47]\n"
            "v_mfma_f32_16x16x32_bf16 v[56:59], a[140:143], v[12:15], v[56:59]\n"
            "v_mfma_f32_16x16x32_bf16 v[60:63], a[204:207], v[12:15], v[60:63]\n"
            "ds_read_b128 v[8:11], v112 offset:5120\n"
            "ds_read_b128 v[12:15], v112 offset:21504\n"
            "s_waitcnt lgkmcnt(2)\n"
            "v_mfma_f32_16x16x32_bf16 v[32:35], a[16:19], v[0:3], v[32:35]\n"
            "v_mfma_f32_16x16x32_bf16 v[36:39], a[80:83], v[0:3], v[36:39]\n"
            "s_lshl_b32 s70, s66, 13\n"
            "s_add_u32 m0, s70, 34816\n"
            "s_add_u32 s72, s52, 128\n"
            "s_addc_u32 s73, s53, 0\n"
            "global_load_lds_dwordx4 v113, s[72:73]\n"
            "v_mfma_f32_16x16x32_bf16 v[48:51], a[144:147], v[0:3], v[48:51]\n"
            "v_mfma_f32_16x16x32_bf16 v[52:55], a[208:211], v[0:3], v[52:55]\n"
            "v_mfma_f32_16x16x32_bf16 v[40:43], a[16:19], v[4:7], v[40:43]\n"
            "v_mfma_f32_16x16x32_bf16 v[44:47], a[80:83], v[4:7], v[44:47]\n"
            "v_mfma_f32_16x16x32_bf16 v[56:59], a[144:147], v[4:7], v[56:59]\n"
            "v_mfma_f32_16x16x32_bf16 v[60:63], a[208:211], v[4:7], v[60:63]\n"
            "ds_read_b128 v[0:3], v112 offset:6144\n"
            "ds_read_b128 v[4:7], v112 offset:22528\n"
            "s_waitcnt lgkmcnt(2)\n"
            "v_mfma_f32_16x16x32_bf16 v[32:35], a[20:23], v[8:11], v[32:35]\n"
            "v_mfma_f32_16x16x32_bf16 v[36:39], a[84:87], v[8:11], v[36:39]\n"
            "v_mfma_f32_16x16x32_bf16 v[48:51], a[148:151], v[8:11], v[48:51]\n"
            "v_mfma_f32_16x16x32_bf16 v[52:55], a[212:215], v[8:11], v[52:55]\n"
            "v_mfma_f32_16x16x32_bf16 v[40:43], a[20:23], v[12:15], v[40:43]\n"
            "v_mfma_f32_16x16x32_bf16 v[44:47], a[84:87], v[12:15], v[44:47]\n"
            "v_mfma_f32_16x16x32_bf16 v[56:59], a[148:151], v[12:15], v[56:59]\n"
            "v_mfma_f32_16x16x32_bf16 v[60:63], a[212:215], v[12:15], v[60:63]\n"
            "ds_read_b128 v[8:11], v112 offset:7168\n"
            "ds_read_b128 v[12:15], v112 offset:23552\n"
            "s_waitcnt lgkmcnt(2)\n"
            "v_mfma_f32_16x16x32_bf16 v[32:35], a[24:27], v[0:3], v[32:35]\n"
            "v_mfma_f32_16x16x32_bf16 v[36:39], a[88:91], v[0:3], v[36:39]\n"
            "s_lshl_b32 s70, s66, 13\n"
            "s_add_u32 m0, s70, 35840\n"
            "s_add_u32 s72, s52, 192\n"
            "s_addc_u32 s73, s53, 0\n"
            "global_load_lds_dwordx4 v113, s[72:73]\n"
            "v_mfma_f32_16x16x32_bf16 v[48:51], a[152:155], v[0:3], v[48:51]\n"
            "v_mfma_f32_16x16x32_bf16 v[52:55], a[216:219], v[0:3], v[52:55]\n"
            "v_mfma_f32_16x16x32_bf16 v[40:43], a[24:27], v[4:7], v[40:43]\n"
            "v_mfma_f32_16x16x32_bf16 v[44:47], a[88:91], v[4:7], v[44:47]\n"
            "v_mfma_f32_16x16x32_bf16 v[56:59], a[152:155], v[4:7], v[56:59]\n"
            "v_mfma_f32_16x16x32_bf16 v[60:63], a[216:219], v[4:7], v[60:63]\n"
            "ds_read_b128 v[0:3], v112 offset:8192\n"
            "ds_read_b128 v[4:7], v112 offset:24576\n"
            "s_waitcnt lgkmcnt(2)\n"
            "v_mfma_f32_16x16x32_bf16 v[32:35], a[28:31], v[8:11], v[32:35]\n"
            "v_mfma_f32_16x16x32_bf16 v[36:39], a[92:95], v[8:11], v[36:39]\n"
            "v_mfma_f32_16x16x32_bf16 v[48:51], a[156:159], v[8:11], v[48:51]\n"
            "v_mfma_f32_16x16x32_bf16 v[52:55], a[220:223], v[8:11], v[52:55]\n"
            "v_mfma_f32_16x16x32_bf16 v[40:43], a[28:31], v[12:15], v[40:43]\n"
            "v_mfma_f32_16x16x32_bf16 v[44:47], a[92:95], v[12:15], v[44:47]\n"
            "v_mfma_f32_16x16x32_bf16 v[56:59], a[156:159], v[12:15], v[56:59]\n"
            "v_mfma_f32_16x16x32_bf16 v[60:63], a[220:223], v[12:15], v[60:63]\n"
            "ds_read_b128 v[8:11], v112 offset:9216\n"
            "ds_read_b128 v[12:15], v112 offset:25600\n"
            "s_waitcnt lgkmcnt(2)\n"
            "v_mfma_f32_16x16x32_bf16 v[32:35], a[32:35], v[0:3], v[32:35]\n"
            "v_mfma_f32_16x16x32_bf16 v[36:39], a[96:99], v[0:3], v[36:39]\n"
            "s_lshl_b32 s70, s66, 13\n"
            "s_add_u32 m0, s70, 36864\n"
            "s_add_u32 s72, s52, 256\n"
            "s_addc_u32 s73, s53, 0\n"
            "global_load_lds_dwordx4 v113, s[72:73]\n"
            "v_mfma_f32_16x16x32_bf16 v[48:51], a[160:163], v[0:3], v[48:51]\n"
            "v_mfma_f32_16x16x32_bf16 v[52:55], a[224:227], v[0:3], v[52:55]\n"
            "v_mfma_f32_16x16x32_bf16 v[40:43], a[32:35], v[4:7], v[40:43]\n"
            "v_mfma_f32_16x16x32_bf16 v[44:47], a[96:99], v[4:7], v[44:47]\n"
            "v_mfma_f32_16x16x32_bf16 v[56:59], a[160:163], v[4:7], v[56:59]\n"
            "v_mfma_f32_16x16x32_bf16 v[60:63], a[224:227], v[4:7], v[60:63]\n"
            "ds_read_b128 v[0:3], v112 offset:10240\n"
            "ds_read_b128 v[4:7], v112 offset:26624\n"
            "s_waitcnt lgkmcnt(2)\n"
            "v_mfma_f32_16x16x32_bf16 v[32:35], a[36:39], v[8:11], v[32:35]\n"
            "v_mfma_f32_16x16x32_bf16 v[36:39], a[100:103], v[8:11], v[36:39]\n"
            "v_mfma_f32_16x16x32_bf16 v[48:51], a[164:167], v[8:11], v[48:51]\n"
            "v_mfma_f32_16x16x32_bf16 v[52:55], a[228:231], v[8:11], v[52:55]\n"
            "v_mfma_f32_16x16x32_bf16 v[40:43], a[36:39], v[12:15], v[40:43]\n"
            "v_mfma_f32_16x16x32_bf16 v[44:47], a[100:103], v[12:15], v[44:47]\n"
            "v_mfma_f32_16x16x32_bf16 v[56:59], a[164:167], v[12:15], v[56:59]\n"
            "v_mfma_f32_16x16x32_bf16 v[60:63], a[228:231], v[12:15], v[60:63]\n"
            "ds_read_b128 v[8:11], v112 offset:11264\n"
            "ds_read_b128 v[12:15], v112 offset:27648\n"
            "s_waitcnt lgkmcnt(2)\n"
            "v_mfma_f32_16x16x32_bf16 v[32:35], a[40:43], v[0:3], v[32:35]\n"
            "v_mfma_f32_16x16x32_bf16 v[36:39], a[104:107], v[0:3], v[36:39]\n"
            "s_lshl_b32 s70, s66, 13\n"
            "s_add_u32 m0, s70, 37888\n"
            "s_add_u32 s72, s52, 320\n"
            "s_addc_u32 s73, s53, 0\n"
            "global_load_lds_dwordx4 v113, s[72:73]\n"
            "v_mfma_f32_16x16x32_bf16 v[48:51], a[168:171], v[0:3], v[48:51]\n"
            "v_mfma_f32_16x16x32_bf16 v[52:55], a[232:235], v[0:3], v[52:55]\n"
            "v_mfma_f32_16x16x32_bf16 v[40:43], a[40:43], v[4:7], v[40:43]\n"
            "v_mfma_f32_16x16x32_bf16 v[44:47], a[104:107], v[4:7], v[44:47]\n"
            "v_mfma_f32_16x16x32_bf16 v[56:59], a[168:171], v[4:7], v[56:59]\n"
            "v_mfma_f32_16x16x32_bf16 v[60:63], a[232:235], v[4:7], v[60:63]\n"
            "ds_read_b128 v[0:3], v112 offset:12288\n"
            "ds_read_b128 v[4:7], v112 offset:28672\n"
            "s_waitcnt lgkmcnt(2)\n"
            "v_mfma_f32_16x16x32_bf16 v[32:35], a[44:47], v[8:11], v[32:35]\n"
            "v_mfma_f32_16x16x32_bf16 v[36:39], a[108:111], v[8:11], v[36:39]\n"
            "v_mfma_f32_16x16x32_bf16 v[48:51], a[172:175], v[8:11], v[48:51]\n"
            "v_mfma_f32_16x16x32_bf16 v[52:55], a[236:239], v[8:11], v[52:55]\n"
            "v_mfma_f32_16x16x32_bf16 v[40:43], a[44:47], v[12:15], v[40:43]\n"
            "v_mfma_f32_16x16x32_bf16 v[44:47], a[108:111], v[12:15], v[44:47]\n"
            "v_mfma_f32_16x16x32_bf16 v[56:59], a[172:175], v[12:15], v[56:59]\n"
            "v_mfma_f32_16x16x32_bf16 v[60:63], a[236:239], v[12:15], v[60:63]\n"
            "ds_read_b128 v[8:11], v112 offset:13312\n"
            "ds_read_b128 v[12:15], v112 offset:29696\n"
            "s_waitcnt lgkmcnt(2)\n"
            "v_mfma_f32_16x16x32_bf16 v[32:35], a[48:51], v[0:3], v[32:35]\n"
            "v_mfma_f32_16x16x32_bf16 v[36:39], a[112:115], v[0:3], v[36:39]\n"
            "s_lshl_b32 s70, s66, 13\n"
            "s_add_u32 m0, s70, 38912\n"
            "s_add_u32 s72, s52, 384\n"
            "s_addc_u32 s73, s53, 0\n"
            "global_load_lds_dwordx4 v113, s[72:73]\n"
            "v_mfma_f32_16x16x32_bf16 v[48:51], a[176:179], v[0:3], v[48:51]\n"
            "v_mfma_f32_16x16x32_bf16 v[52:55], a[240:243], v[0:3], v[52:55]\n"
            "v_mfma_f32_16x16x32_bf16 v[40:43], a[48:51], v[4:7], v[40:43]\n"
            "v_mfma_f32_16x16x32_bf16 v[44:47], a[112:115], v[4:7], v[44:47]\n"
            "v_mfma_f32_16x16x32_bf16 v[56:59], a[176:179], v[4:7], v[56:59]\n"
            "v_mfma_f32_16x16x32_bf16 v[60:63], a[240:243], v[4:7], v[60:63]\n"
            "ds_read_b128 v[0:3], v112 offset:14336\n"
            "ds_read_b128 v[4:7], v112 offset:30720\n"
            "s_waitcnt lgkmcnt(2)\n"
            "v_mfma_f32_16x16x32_bf16 v[32:35], a[52:55], v[8:11], v[32:35]\n"
            "v_mfma_f32_16x16x32_bf16 v[36:39], a[116:119], v[8:11], v[36:39]\n"
            "v_mfma_f32_16x16x32_bf16 v[48:51], a[180:183], v[8:11], v[48:51]\n"
            "v_mfma_f32_16x16x32_bf16 v[52:55], a[244:247], v[8:11], v[52:55]\n"
            "v_mfma_f32_16x16x32_bf16 v[40:43], a[52:55], v[12:15], v[40:43]\n"
            "v_mfma_f32_16x16x32_bf16 v[44:47], a[116:119], v[12:15], v[44:47]\n"
            "v_mfma_f32_16x16x32_bf16 v[56:59], a[180:183], v[12:15], v[56:59]\n"
            "v_mfma_f32_16x16x32_bf16 v[60:63], a[244:247], v[12:15], v[60:63]\n"
            "ds_read_b128 v[8:11], v112 offset:15360\n"
            "ds_read_b128 v[12:15], v112 offset:31744\n"
            "s_waitcnt lgkmcnt(2)\n"
            "v_mfma_f32_16x16x32_bf16 v[32:35], a[56:59], v[0:3], v[32:35]\n"
            "v_mfma_f32_16x16x32_bf16 v[36:39], a[120:123], v[0:3], v[36:39]\n"
            "s_lshl_b32 s70, s66, 13\n"
            "s_add_u32 m0, s70, 39936\n"
            "s_add_u32 s72, s52, 448\n"
            "s_addc_u32 s73, s53, 0\n"
            "global_load_lds_dwordx4 v113, s[72:73]\n"
            "v_mfma_f32_16x16x32_bf16 v[48:51], a[184:187], v[0:3], v[48:51]\n"
            "v_mfma_f32_16x16x32_bf16 v[52:55], a[248:251], v[0:3], v[52:55]\n"
            "v_mfma_f32_16x16x32_bf16 v[40:43], a[56:59], v[4:7], v[40:43]\n"
            "v_mfma_f32_16x16x32_bf16 v[44:47], a[120:123], v[4:7], v[44:47]\n"
            "v_mfma_f32_16x16x32_bf16 v[56:59], a[184:187], v[4:7], v[56:59]\n"
            "v_mfma_f32_16x16x32_bf16 v[60:63], a[248:251], v[4:7], v[60:63]\n"
            "s_waitcnt lgkmcnt(0)\n"
            "v_mfma_f32_16x16x32_bf16 v[32:35], a[60:63], v[8:11], v[32:35]\n"
            "v_mfma_f32_16x16x32_bf16 v[36:39], a[124:127], v[8:11], v[36:39]\n"
            "v_mfma_f32_16x16x32_bf16 v[48:51], a[188:191], v[8:11], v[48:51]\n"
            "v_mfma_f32_16x16x32_bf16 v[52:55], a[252:255], v[8:11], v[52:55]\n"
            "v_mfma_f32_16x16x32_bf16 v[40:43], a[60:63], v[12:15], v[40:43]\n"
            "v_mfma_f32_16x16x32_bf16 v[44:47], a[124:127], v[12:15], v[44:47]\n"
            "v_mfma_f32_16x16x32_bf16 v[56:59], a[188:191], v[12:15], v[56:59]\n"
            "v_mfma_f32_16x16x32_bf16 v[60:63], a[252:255], v[12:15], v[60:63]\n"
            "s_lshr_b32 s70, s64, 2\n"
            "s_xor_b32 s70, s70, s54\n"
            "s_and_b32 s70, s70, 1\n"
            "s_lshl_b32 s70, s70, 13\n"
            "s_add_u32 s48, s44, s70\n"
            "s_addc_u32 s49, s45, 0\n"
            "s_mul_i32 s70, s64, 524288\n"
            "s_add_u32 s48, s48, s70\n"
            "s_addc_u32 s49, s49, 0\n"
            "s_lshr_b32 s70, s64, 2\n"
            "s_xor_b32 s70, s70, s54\n"
            "s_and_b32 s70, s70, 1\n"
            "s_lshl_b32 s70, s70, 13\n"
            "s_add_u32 s50, s46, s70\n"
            "s_addc_u32 s51, s47, 0\n"
            "s_mul_i32 s70, s64, 524288\n"
            "s_add_u32 s50, s50, s70\n"
            "s_addc_u32 s51, s51, 0\n"
            "v_cvt_pk_bf16_f32 v96, v32, v33\n"
            "v_cvt_pk_bf16_f32 v97, v34, v35\n"
            "v_cvt_pk_bf16_f32 v98, v36, v37\n"
            "v_cvt_pk_bf16_f32 v99, v38, v39\n"
            "global_store_dwordx4 v114, v[96:99], s[48:49] sc1\n"
            "v_cvt_pk_bf16_f32 v100, v40, v41\n"
            "v_cvt_pk_bf16_f32 v101, v42, v43\n"
            "v_cvt_pk_bf16_f32 v102, v44, v45\n"
            "v_cvt_pk_bf16_f32 v103, v46, v47\n"
            "global_store_dwordx4 v115, v[100:103], s[48:49] sc1\n"
            "v_cvt_pk_bf16_f32 v104, v48, v49\n"
            "v_cvt_pk_bf16_f32 v105, v50, v51\n"
            "v_cvt_pk_bf16_f32 v106, v52, v53\n"
            "v_cvt_pk_bf16_f32 v107, v54, v55\n"
            "global_store_dwordx4 v114, v[104:107], s[50:51] sc1\n"
            "v_cvt_pk_bf16_f32 v108, v56, v57\n"
            "v_cvt_pk_bf16_f32 v109, v58, v59\n"
            "v_cvt_pk_bf16_f32 v110, v60, v61\n"
            "v_cvt_pk_bf16_f32 v111, v62, v63\n"
            "global_store_dwordx4 v115, v[108:111], s[50:51] sc1\n"
            "s_add_u32 s64, s64, 1\n"
            "s_cmp_lt_u32 s64, 64\n"
            "s_cbranch_scc0 KVEND_%=\n"
            "KV1_TOP_%=:\n"
            "s_cmp_le_u32 s64, 2\n"
            "s_cbranch_scc0 KV1_LATE_%=\n"
            "s_waitcnt vmcnt(0)\n"
            "s_mov_b32 s74, s64\n"
            "s_branch KV1_W_%=\n"
            "KV1_LATE_%=:\n"
            "s_waitcnt vmcnt(4)\n"
            "s_sub_u32 s74, s64, 1\n"
            "KV1_W_%=:\n"
            "s_barrier\n"
            "s_cmp_eq_u32 s64, 0\n"
            "s_cbranch_scc1 KV1_PUB_%=\n"
            "s_cmp_eq_u32 s66, 0\n"
            "s_cbranch_scc0 KV1_PUB_%=\n"
            "v_mov_b32 v124, s74\n"
            "v_mov_b32 v125, 0\n"
            "s_mov_b64 exec, 1\n"
            "global_store_dword v125, v124, s[60:61] sc1\n"
            "s_mov_b64 exec, -1\n"
            "KV1_PUB_%=:\n"
            "s_cmp_lt_u32 s64, 63\n"
            "s_cselect_b32 s70, 0x8000, 0\n"
            "s_add_u32 s52, s52, s70\n"
            "s_addc_u32 s53, s53, 0\n"
            "ds_read_b128 v[0:3], v112 offset:32768\n"
            "ds_read_b128 v[4:7], v112 offset:49152\n"
            "ds_read_b128 v[8:11], v112 offset:33792\n"
            "ds_read_b128 v[12:15], v112 offset:50176\n"
            "s_waitcnt lgkmcnt(2)\n"
            "v_mfma_f32_16x16x32_bf16 v[32:35], a[0:3], v[0:3], 0\n"
            "v_mfma_f32_16x16x32_bf16 v[36:39], a[64:67], v[0:3], 0\n"
            "s_lshl_b32 s70, s66, 13\n"
            "s_add_u32 m0, s70, 0\n"
            "s_add_u32 s72, s52, 0\n"
            "s_addc_u32 s73, s53, 0\n"
            "global_load_lds_dwordx4 v113, s[72:73]\n"
            "v_mfma_f32_16x16x32_bf16 v[48:51], a[128:131], v[0:3], 0\n"
            "v_mfma_f32_16x16x32_bf16 v[52:55], a[192:195], v[0:3], 0\n"
            "v_mfma_f32_16x16x32_bf16 v[40:43], a[0:3], v[4:7], 0\n"
            "v_mfma_f32_16x16x32_bf16 v[44:47], a[64:67], v[4:7], 0\n"
            "v_mfma_f32_16x16x32_bf16 v[56:59], a[128:131], v[4:7], 0\n"
            "v_mfma_f32_16x16x32_bf16 v[60:63], a[192:195], v[4:7], 0\n"
            "ds_read_b128 v[0:3], v112 offset:34816\n"
            "ds_read_b128 v[4:7], v112 offset:51200\n"
            "s_waitcnt lgkmcnt(2)\n"
            "v_mfma_f32_16x16x32_bf16 v[32:35], a[4:7], v[8:11], v[32:35]\n"
            "v_mfma_f32_16x16x32_bf16 v[36:39], a[68:71], v[8:11], v[36:39]\n"
            "v_mfma_f32_16x16x32_bf16 v[48:51], a[132:135], v[8:11], v[48:51]\n"
            "v_mfma_f32_16x16x32_bf16 v[52:55], a[196:199], v[8:11], v[52:55]\n"
            "v_mfma_f32_16x16x32_bf16 v[40:43], a[4:7], v[12:15], v[40:43]\n"
            "v_mfma_f32_16x16x32_bf16 v[44:47], a[68:71], v[12:15], v[44:47]\n"
            "v_mfma_f32_16x16x32_bf16 v[56:59], a[132:135], v[12:15], v[56:59]\n"
            "v_mfma_f32_16x16x32_bf16 v[60:63], a[196:199], v[12:15], v[60:63]\n"
            "ds_read_b128 v[8:11], v112 offset:35840\n"
            "ds_read_b128 v[12:15], v112 offset:52224\n"
            "s_waitcnt lgkmcnt(2)\n"
            "v_mfma_f32_16x16x32_bf16 v[32:35], a[8:11], v[0:3], v[32:35]\n"
            "v_mfma_f32_16x16x32_bf16 v[36:39], a[72:75], v[0:3], v[36:39]\n"
            "s_lshl_b32 s70, s66, 13\n"
            "s_add_u32 m0, s70, 1024\n"
            "s_add_u32 s72, s52, 64\n"
            "s_addc_u32 s73, s53, 0\n"
            "global_load_lds_dwordx4 v113, s[72:73]\n"
            "v_mfma_f32_16x16x32_bf16 v[48:51], a[136:139], v[0:3], v[48:51]\n"
            "v_mfma_f32_16x16x32_bf16 v[52:55], a[200:203], v[0:3], v[52:55]\n"
            "v_mfma_f32_16x16x32_bf16 v[40:43], a[8:11], v[4:7], v[40:43]\n"
            "v_mfma_f32_16x16x32_bf16 v[44:47], a[72:75], v[4:7], v[44:47]\n"
            "v_mfma_f32_16x16x32_bf16 v[56:59], a[136:139], v[4:7], v[56:59]\n"
            "v_mfma_f32_16x16x32_bf16 v[60:63], a[200:203], v[4:7], v[60:63]\n"
            "ds_read_b128 v[0:3], v112 offset:36864\n"
            "ds_read_b128 v[4:7], v112 offset:53248\n"
            "s_waitcnt lgkmcnt(2)\n"
            "v_mfma_f32_16x16x32_bf16 v[32:35], a[12:15], v[8:11], v[32:35]\n"
            "v_mfma_f32_16x16x32_bf16 v[36:39], a[76:79], v[8:11], v[36:39]\n"
            "v_mfma_f32_16x16x32_bf16 v[48:51], a[140:143], v[8:11], v[48:51]\n"
            "v_mfma_f32_16x16x32_bf16 v[52:55], a[204:207], v[8:11], v[52:55]\n"
            "v_mfma_f32_16x16x32_bf16 v[40:43], a[12:15], v[12:15], v[40:43]\n"
            "v_mfma_f32_16x16x32_bf16 v[44:47], a[76:79], v[12:15], v[44:47]\n"
            "v_mfma_f32_16x16x32_bf16 v[56:59], a[140:143], v[12:15], v[56:59]\n"
            "v_mfma_f32_16x16x32_bf16 v[60:63], a[204:207], v[12:15], v[60:63]\n"
            "ds_read_b128 v[8:11], v112 offset:37888\n"
            "ds_read_b128 v[12:15], v112 offset:54272\n"
            "s_waitcnt lgkmcnt(2)\n"
            "v_mfma_f32_16x16x32_bf16 v[32:35], a[16:19], v[0:3], v[32:35]\n"
            "v_mfma_f32_16x16x32_bf16 v[36:39], a[80:83], v[0:3], v[36:39]\n"
            "s_lshl_b32 s70, s66, 13\n"
            "s_add_u32 m0, s70, 2048\n"
            "s_add_u32 s72, s52, 128\n"
            "s_addc_u32 s73, s53, 0\n"
            "global_load_lds_dwordx4 v113, s[72:73]\n"
            "v_mfma_f32_16x16x32_bf16 v[48:51], a[144:147], v[0:3], v[48:51]\n"
            "v_mfma_f32_16x16x32_bf16 v[52:55], a[208:211], v[0:3], v[52:55]\n"
            "v_mfma_f32_16x16x32_bf16 v[40:43], a[16:19], v[4:7], v[40:43]\n"
            "v_mfma_f32_16x16x32_bf16 v[44:47], a[80:83], v[4:7], v[44:47]\n"
            "v_mfma_f32_16x16x32_bf16 v[56:59], a[144:147], v[4:7], v[56:59]\n"
            "v_mfma_f32_16x16x32_bf16 v[60:63], a[208:211], v[4:7], v[60:63]\n"
            "ds_read_b128 v[0:3], v112 offset:38912\n"
            "ds_read_b128 v[4:7], v112 offset:55296\n"
            "s_waitcnt lgkmcnt(2)\n"
            "v_mfma_f32_16x16x32_bf16 v[32:35], a[20:23], v[8:11], v[32:35]\n"
            "v_mfma_f32_16x16x32_bf16 v[36:39], a[84:87], v[8:11], v[36:39]\n"
            "v_mfma_f32_16x16x32_bf16 v[48:51], a[148:151], v[8:11], v[48:51]\n"
            "v_mfma_f32_16x16x32_bf16 v[52:55], a[212:215], v[8:11], v[52:55]\n"
            "v_mfma_f32_16x16x32_bf16 v[40:43], a[20:23], v[12:15], v[40:43]\n"
            "v_mfma_f32_16x16x32_bf16 v[44:47], a[84:87], v[12:15], v[44:47]\n"
            "v_mfma_f32_16x16x32_bf16 v[56:59], a[148:151], v[12:15], v[56:59]\n"
            "v_mfma_f32_16x16x32_bf16 v[60:63], a[212:215], v[12:15], v[60:63]\n"
            "ds_read_b128 v[8:11], v112 offset:39936\n"
            "ds_read_b128 v[12:15], v112 offset:56320\n"
            "s_waitcnt lgkmcnt(2)\n"
            "v_mfma_f32_16x16x32_bf16 v[32:35], a[24:27], v[0:3], v[32:35]\n"
            "v_mfma_f32_16x16x32_bf16 v[36:39], a[88:91], v[0:3], v[36:39]\n"
            "s_lshl_b32 s70, s66, 13\n"
            "s_add_u32 m0, s70, 3072\n"
            "s_add_u32 s72, s52, 192\n"
            "s_addc_u32 s73, s53, 0\n"
            "global_load_lds_dwordx4 v113, s[72:73]\n"
            "v_mfma_f32_16x16x32_bf16 v[48:51], a[152:155], v[0:3], v[48:51]\n"
            "v_mfma_f32_16x16x32_bf16 v[52:55], a[216:219], v[0:3], v[52:55]\n"
            "v_mfma_f32_16x16x32_bf16 v[40:43], a[24:27], v[4:7], v[40:43]\n"
            "v_mfma_f32_16x16x32_bf16 v[44:47], a[88:91], v[4:7], v[44:47]\n"
            "v_mfma_f32_16x16x32_bf16 v[56:59], a[152:155], v[4:7], v[56:59]\n"
            "v_mfma_f32_16x16x32_bf16 v[60:63], a[216:219], v[4:7], v[60:63]\n"
            "ds_read_b128 v[0:3], v112 offset:40960\n"
            "ds_read_b128 v[4:7], v112 offset:57344\n"
            "s_waitcnt lgkmcnt(2)\n"
            "v_mfma_f32_16x16x32_bf16 v[32:35], a[28:31], v[8:11], v[32:35]\n"
            "v_mfma_f32_16x16x32_bf16 v[36:39], a[92:95], v[8:11], v[36:39]\n"
            "v_mfma_f32_16x16x32_bf16 v[48:51], a[156:159], v[8:11], v[48:51]\n"
            "v_mfma_f32_16x16x32_bf16 v[52:55], a[220:223], v[8:11], v[52:55]\n"
            "v_mfma_f32_16x16x32_bf16 v[40:43], a[28:31], v[12:15], v[40:43]\n"
            "v_mfma_f32_16x16x32_bf16 v[44:47], a[92:95], v[12:15], v[44:47]\n"
            "v_mfma_f32_16x16x32_bf16 v[56:59], a[156:159], v[12:15], v[56:59]\n"
            "v_mfma_f32_16x16x32_bf16 v[60:63], a[220:223], v[12:15], v[60:63]\n"
            "ds_read_b128 v[8:11], v112 offset:41984\n"
            "ds_read_b128 v[12:15], v112 offset:58368\n"
            "s_waitcnt lgkmcnt(2)\n"
            "v_mfma_f32_16x16x32_bf16 v[32:35], a[32:35], v[0:3], v[32:35]\n"
            "v_mfma_f32_16x16x32_bf16 v[36:39], a[96:99], v[0:3], v[36:39]\n"
            "s_lshl_b32 s70, s66, 13\n"
            "s_add_u32 m0, s70, 4096\n"
            "s_add_u32 s72, s52, 256\n"
            "s_addc_u32 s73, s53, 0\n"
            "global_load_lds_dwordx4 v113, s[72:73]\n"
            "v_mfma_f32_16x16x32_bf16 v[48:51], a[160:163], v[0:3], v[48:51]\n"
            "v_mfma_f32_16x16x32_bf16 v[52:55], a[224:227], v[0:3], v[52:55]\n"
            "v_mfma_f32_16x16x32_bf16 v[40:43], a[32:35], v[4:7], v[40:43]\n"
            "v_mfma_f32_16x16x32_bf16 v[44:47], a[96:99], v[4:7], v[44:47]\n"
            "v_mfma_f32_16x16x32_bf16 v[56:59], a[160:163], v[4:7], v[56:59]\n"
            "v_mfma_f32_16x16x32_bf16 v[60:63], a[224:227], v[4:7], v[60:63]\n"
            "ds_read_b128 v[0:3], v112 offset:43008\n"
            "ds_read_b128 v[4:7], v112 offset:59392\n"
            "s_waitcnt lgkmcnt(2)\n"
            "v_mfma_f32_16x16x32_bf16 v[32:35], a[36:39], v[8:11], v[32:35]\n"
            "v_mfma_f32_16x16x32_bf16 v[36:39], a[100:103], v[8:11], v[36:39]\n"
            "v_mfma_f32_16x16x32_bf16 v[48:51], a[164:167], v[8:11], v[48:51]\n"
            "v_mfma_f32_16x16x32_bf16 v[52:55], a[228:231], v[8:11], v[52:55]\n"
            "v_mfma_f32_16x16x32_bf16 v[40:43], a[36:39], v[12:15], v[40:43]\n"
            "v_mfma_f32_16x16x32_bf16 v[44:47], a[100:103], v[12:15], v[44:47]\n"
            "v_mfma_f32_16x16x32_bf16 v[56:59], a[164:167], v[12:15], v[56:59]\n"
            "v_mfma_f32_16x16x32_bf16 v[60:63], a[228:231], v[12:15], v[60:63]\n"
            "ds_read_b128 v[8:11], v112 offset:44032\n"
            "ds_read_b128 v[12:15], v112 offset:60416\n"
            "s_waitcnt lgkmcnt(2)\n"
            "v_mfma_f32_16x16x32_bf16 v[32:35], a[40:43], v[0:3], v[32:35]\n"
            "v_mfma_f32_16x16x32_bf16 v[36:39], a[104:107], v[0:3], v[36:39]\n"
            "s_lshl_b32 s70, s66, 13\n"
            "s_add_u32 m0, s70, 5120\n"
            "s_add_u32 s72, s52, 320\n"
            "s_addc_u32 s73, s53, 0\n"
            "global_load_lds_dwordx4 v113, s[72:73]\n"
            "v_mfma_f32_16x16x32_bf16 v[48:51], a[168:171], v[0:3], v[48:51]\n"
            "v_mfma_f32_16x16x32_bf16 v[52:55], a[232:235], v[0:3], v[52:55]\n"
            "v_mfma_f32_16x16x32_bf16 v[40:43], a[40:43], v[4:7], v[40:43]\n"
            "v_mfma_f32_16x16x32_bf16 v[44:47], a[104:107], v[4:7], v[44:47]\n"
            "v_mfma_f32_16x16x32_bf16 v[56:59], a[168:171], v[4:7], v[56:59]\n"
            "v_mfma_f32_16x16x32_bf16 v[60:63], a[232:235], v[4:7], v[60:63]\n"
            "ds_read_b128 v[0:3], v112 offset:45056\n"
            "ds_read_b128 v[4:7], v112 offset:61440\n"
            "s_waitcnt lgkmcnt(2)\n"
            "v_mfma_f32_16x16x32_bf16 v[32:35], a[44:47], v[8:11], v[32:35]\n"
            "v_mfma_f32_16x16x32_bf16 v[36:39], a[108:111], v[8:11], v[36:39]\n"
            "v_mfma_f32_16x16x32_bf16 v[48:51], a[172:175], v[8:11], v[48:51]\n"
            "v_mfma_f32_16x16x32_bf16 v[52:55], a[236:239], v[8:11], v[52:55]\n"
            "v_mfma_f32_16x16x32_bf16 v[40:43], a[44:47], v[12:15], v[40:43]\n"
            "v_mfma_f32_16x16x32_bf16 v[44:47], a[108:111], v[12:15], v[44:47]\n"
            "v_mfma_f32_16x16x32_bf16 v[56:59], a[172:175], v[12:15], v[56:59]\n"
            "v_mfma_f32_16x16x32_bf16 v[60:63], a[236:239], v[12:15], v[60:63]\n"
            "ds_read_b128 v[8:11], v112 offset:46080\n"
            "ds_read_b128 v[12:15], v112 offset:62464\n"
            "s_waitcnt lgkmcnt(2)\n"
            "v_mfma_f32_16x16x32_bf16 v[32:35], a[48:51], v[0:3], v[32:35]\n"
            "v_mfma_f32_16x16x32_bf16 v[36:39], a[112:115], v[0:3], v[36:39]\n"
            "s_lshl_b32 s70, s66, 13\n"
            "s_add_u32 m0, s70, 6144\n"
            "s_add_u32 s72, s52, 384\n"
            "s_addc_u32 s73, s53, 0\n"
            "global_load_lds_dwordx4 v113, s[72:73]\n"
            "v_mfma_f32_16x16x32_bf16 v[48:51], a[176:179], v[0:3], v[48:51]\n"
            "v_mfma_f32_16x16x32_bf16 v[52:55], a[240:243], v[0:3], v[52:55]\n"
            "v_mfma_f32_16x16x32_bf16 v[40:43], a[48:51], v[4:7], v[40:43]\n"
            "v_mfma_f32_16x16x32_bf16 v[44:47], a[112:115], v[4:7], v[44:47]\n"
            "v_mfma_f32_16x16x32_bf16 v[56:59], a[176:179], v[4:7], v[56:59]\n"
            "v_mfma_f32_16x16x32_bf16 v[60:63], a[240:243], v[4:7], v[60:63]\n"
            "ds_read_b128 v[0:3], v112 offset:47104\n"
            "ds_read_b128 v[4:7], v112 offset:63488\n"
            "s_waitcnt lgkmcnt(2)\n"
            "v_mfma_f32_16x16x32_bf16 v[32:35], a[52:55], v[8:11], v[32:35]\n"
            "v_mfma_f32_16x16x32_bf16 v[36:39], a[116:119], v[8:11], v[36:39]\n"
            "v_mfma_f32_16x16x32_bf16 v[48:51], a[180:183], v[8:11], v[48:51]\n"
            "v_mfma_f32_16x16x32_bf16 v[52:55], a[244:247], v[8:11], v[52:55]\n"
            "v_mfma_f32_16x16x32_bf16 v[40:43], a[52:55], v[12:15], v[40:43]\n"
            "v_mfma_f32_16x16x32_bf16 v[44:47], a[116:119], v[12:15], v[44:47]\n"
            "v_mfma_f32_16x16x32_bf16 v[56:59], a[180:183], v[12:15], v[56:59]\n"
            "v_mfma_f32_16x16x32_bf16 v[60:63], a[244:247], v[12:15], v[60:63]\n"
            "ds_read_b128 v[8:11], v112 offset:48128\n"
            "ds_read_b128 v[12:15], v112 offset:64512\n"
            "s_waitcnt lgkmcnt(2)\n"
            "v_mfma_f32_16x16x32_bf16 v[32:35], a[56:59], v[0:3], v[32:35]\n"
            "v_mfma_f32_16x16x32_bf16 v[36:39], a[120:123], v[0:3], v[36:39]\n"
            "s_lshl_b32 s70, s66, 13\n"
            "s_add_u32 m0, s70, 7168\n"
            "s_add_u32 s72, s52, 448\n"
            "s_addc_u32 s73, s53, 0\n"
            "global_load_lds_dwordx4 v113, s[72:73]\n"
            "v_mfma_f32_16x16x32_bf16 v[48:51], a[184:187], v[0:3], v[48:51]\n"
            "v_mfma_f32_16x16x32_bf16 v[52:55], a[248:251], v[0:3], v[52:55]\n"
            "v_mfma_f32_16x16x32_bf16 v[40:43], a[56:59], v[4:7], v[40:43]\n"
            "v_mfma_f32_16x16x32_bf16 v[44:47], a[120:123], v[4:7], v[44:47]\n"
            "v_mfma_f32_16x16x32_bf16 v[56:59], a[184:187], v[4:7], v[56:59]\n"
            "v_mfma_f32_16x16x32_bf16 v[60:63], a[248:251], v[4:7], v[60:63]\n"
            "s_waitcnt lgkmcnt(0)\n"
            "v_mfma_f32_16x16x32_bf16 v[32:35], a[60:63], v[8:11], v[32:35]\n"
            "v_mfma_f32_16x16x32_bf16 v[36:39], a[124:127], v[8:11], v[36:39]\n"
            "v_mfma_f32_16x16x32_bf16 v[48:51], a[188:191], v[8:11], v[48:51]\n"
            "v_mfma_f32_16x16x32_bf16 v[52:55], a[252:255], v[8:11], v[52:55]\n"
            "v_mfma_f32_16x16x32_bf16 v[40:43], a[60:63], v[12:15], v[40:43]\n"
            "v_mfma_f32_16x16x32_bf16 v[44:47], a[124:127], v[12:15], v[44:47]\n"
            "v_mfma_f32_16x16x32_bf16 v[56:59], a[188:191], v[12:15], v[56:59]\n"
            "v_mfma_f32_16x16x32_bf16 v[60:63], a[252:255], v[12:15], v[60:63]\n"
            "s_lshr_b32 s70, s64, 2\n"
            "s_xor_b32 s70, s70, s54\n"
            "s_and_b32 s70, s70, 1\n"
            "s_lshl_b32 s70, s70, 13\n"
            "s_add_u32 s48, s44, s70\n"
            "s_addc_u32 s49, s45, 0\n"
            "s_mul_i32 s70, s64, 524288\n"
            "s_add_u32 s48, s48, s70\n"
            "s_addc_u32 s49, s49, 0\n"
            "s_lshr_b32 s70, s64, 2\n"
            "s_xor_b32 s70, s70, s54\n"
            "s_and_b32 s70, s70, 1\n"
            "s_lshl_b32 s70, s70, 13\n"
            "s_add_u32 s50, s46, s70\n"
            "s_addc_u32 s51, s47, 0\n"
            "s_mul_i32 s70, s64, 524288\n"
            "s_add_u32 s50, s50, s70\n"
            "s_addc_u32 s51, s51, 0\n"
            "v_cvt_pk_bf16_f32 v96, v32, v33\n"
            "v_cvt_pk_bf16_f32 v97, v34, v35\n"
            "v_cvt_pk_bf16_f32 v98, v36, v37\n"
            "v_cvt_pk_bf16_f32 v99, v38, v39\n"
            "global_store_dwordx4 v114, v[96:99], s[48:49] sc1\n"
            "v_cvt_pk_bf16_f32 v100, v40, v41\n"
            "v_cvt_pk_bf16_f32 v101, v42, v43\n"
            "v_cvt_pk_bf16_f32 v102, v44, v45\n"
            "v_cvt_pk_bf16_f32 v103, v46, v47\n"
            "global_store_dwordx4 v115, v[100:103], s[48:49] sc1\n"
            "v_cvt_pk_bf16_f32 v104, v48, v49\n"
            "v_cvt_pk_bf16_f32 v105, v50, v51\n"
            "v_cvt_pk_bf16_f32 v106, v52, v53\n"
            "v_cvt_pk_bf16_f32 v107, v54, v55\n"
            "global_store_dwordx4 v114, v[104:107], s[50:51] sc1\n"
            "v_cvt_pk_bf16_f32 v108, v56, v57\n"
            "v_cvt_pk_bf16_f32 v109, v58, v59\n"
            "v_cvt_pk_bf16_f32 v110, v60, v61\n"
            "v_cvt_pk_bf16_f32 v111, v62, v63\n"
            "global_store_dwordx4 v115, v[108:111], s[50:51] sc1\n"
            "s_add_u32 s64, s64, 1\n"
            "s_cmp_lt_u32 s64, 64\n"
            "s_cbranch_scc0 KVEND_%=\n"
            "s_branch KV0_TOP_%=\n"
            "KVEND_%=:\n"
            "s_waitcnt vmcnt(0)\n"
            "s_barrier\n"
            "s_mov_b32 s74, 64\n"
            "s_cmp_eq_u32 s66, 0\n"
            "s_cbranch_scc0 KVPUBE_%=\n"
            "v_mov_b32 v124, s74\n"
            "v_mov_b32 v125, 0\n"
            "s_mov_b64 exec, 1\n"
            "global_store_dword v125, v124, s[60:61] sc1\n"
            "s_mov_b64 exec, -1\n"
            "KVPUBE_%=:\n"
            "s_waitcnt vmcnt(0)\n"
            "s_endpgm\n"
            :: "s"(ka), "s"(hs), "s"(w), "s"(hm)
            : "memory", "vcc", "v255", "a255", "s10", "s11", "s12", "s13", "s14", "s15", "s16", "s17", "s18", "s19", "s20", "s21", "s22", "s23", "s24", "s25", "s26", "s27", "s28", "s29", "s30", "s31", "s32", "s33", "s34", "s35", "s36", "s37", "s38", "s39", "s40", "s41", "s42", "s43", "s44", "s45", "s46", "s47", "s48", "s49", "s50", "s51", "s52", "s53", "s54", "s55", "s56", "s57", "s58", "s59", "s60", "s61", "s62", "s63", "s64", "s65", "s66", "s67", "s68", "s69", "s70", "s71", "s72", "s73", "s74", "s75", "s76", "s77", "s78", "s79", "s80", "s81", "s82", "s83", "s84", "s85", "s86", "s87", "s88", "s89", "s90", "s91", "s92", "s93", "s94", "s95", "s96", "s97", "s98", "s99");
        return;
    }
    worker_role((KParams)ka, xq, j - 16, w, lane);
}

static void fill_params(Params& p, void* const* d_in, void* d_out, void* d_ws) {
    char* ws = (char*)(((uintptr_t)d_ws + ((size_t)2 << 20) - 1) & ~(((uintptr_t)2 << 20) - 1));
    p.x = (const char*)d_in[0]; p.mem0 = (const char*)d_in[1];
    p.Wq = (const char*)d_in[2]; p.Wk = (const char*)d_in[3]; p.Wv = (const char*)d_in[4]; p.Wo = (const char*)d_in[5];
    p.Wg = (const char*)d_in[8]; p.Wu = (const char*)d_in[9]; p.Wd = (const char*)d_in[10];
    p.ctl = (unsigned*)ws;
    p.flg = (unsigned*)(ws + 65536);
    p.f2f = (unsigned*)(ws + 65536 + 432 * 128);
    p.f1c = (unsigned*)(ws + 65536 + 496 * 128);
    p.prog = (unsigned*)(ws + 65536 + 504 * 128);
    p.kvp = (unsigned*)(ws + 139264);
    p.lat = (unsigned*)(ws + 143360);

    p.cnt = (unsigned*)(ws + 131072);
    p.memb = ws + (4 << 20); p.part = ws + (6 << 20);
    p.xn = ws + (8 << 20);
    p.Kf = ws + (24 << 20); p.Vf = ws + (56 << 20); p.Qf = p.Kf; p.h = p.Vf; p.Bo = p.Vf; p.PO = ws + (88 << 20);
    p.out = (char*)d_out;
}

extern "C" void kernel_launch(void* const* d_in, const int* in_sizes, int n_in, void* d_out, int out_size, void* d_ws, size_t ws_size, hipStream_t stream) {
    Params p; fill_params(p, d_in, d_out, d_ws);
    p.D1 = 48; p.D2 = 180; p.up1 = 3; p.up2 = 20; p.dn1 = 4; p.dn2 = 20; p.roles = 31; p.pad0 = 0; p.halfM = 0; p.halfP = 0;
    k_prep<<<256, 256, 0, stream>>>(p);
    (void)hipFuncSetAttribute((const void*)k_main, hipFuncAttributeMaxDynamicSharedMemorySize, 160 * 1024);
    k_main<<<256, 256, 160 * 1024, stream>>>(p);
}
```
